```python
import torch

def _softmax(x, axis=-1):
    return torch.softmax(x, dim=axis)
def _gelu(x):
    return torch.nn.functional.gelu(x, approximate="tanh")
def _silu(x):
    return torch.nn.functional.silu(x)
def _sigmoid(x):
    return torch.sigmoid(x)
def _layernorm(x, axis=-1, eps=1e-5):
    m = x.mean(dim=axis, keepdim=True)
    v = x.var(dim=axis, keepdim=True, unbiased=False)
    return (x - m) / torch.sqrt(v + eps)
def _scatter_add(tgt, idx, src, axis=0):
    return tgt.clone().index_add_(axis, idx, src)
def _segment_sum(src, seg, n):
    out = torch.zeros((n,) + src.shape[1:], dtype=src.dtype)
    return out.index_add_(0, seg, src)
def _pad(x, pw):
    flat = tuple(v for pair in reversed(pw) for v in pair)
    return torch.nn.functional.pad(x, flat)

kernel_name = 'recurrent_scan__ssm_conv_s5__dense_activation_gelu__prompt'

def setup_inputs():

    _w = (lambda a: (lambda t: t.to(torch.bfloat16) if t.is_floating_point() else t)(torch.as_tensor(a)))
    def _A_setup():
        B, S, D = 1, 2048, 576
        P = D
        Lambda_re = -0.5 * (lambda _x: torch.exp(torch.as_tensor(_x)))((lambda *s: torch.randn(*s, dtype=torch.bfloat16) * 0.4)(P))
        Lambda_im = (lambda *s: torch.randn(*s, dtype=torch.bfloat16) * 0.8)(P) * 1.0
        B_re = (lambda *s: torch.randn(*s, dtype=torch.bfloat16) * 0.8)(P, D) * (D ** -0.5)
        B_im = (lambda *s: torch.randn(*s, dtype=torch.bfloat16) * 0.8)(P, D) * (D ** -0.5)
        C_re = (lambda *s: torch.randn(*s, dtype=torch.bfloat16) * 0.8)(D, P) * (P ** -0.5)
        C_im = (lambda *s: torch.randn(*s, dtype=torch.bfloat16) * 0.8)(D, P) * (P ** -0.5)
        log_dt = (lambda *s: torch.randn(*s, dtype=torch.bfloat16) * 0.4)(P) * 0.3 - 2.0
        Dskip = (lambda *s: torch.randn(*s, dtype=torch.bfloat16) * 0.2)(D)
        return {
            "x": _w((lambda *s: torch.randn(*s, dtype=torch.bfloat16) * 0.05)(B, S, D)),
            "Lambda_re": _w(Lambda_re),
            "Lambda_im": _w(Lambda_im),
            "B_re": _w(B_re),
            "B_im": _w(B_im),
            "C_re": _w(C_re),
            "C_im": _w(C_im),
            "log_dt": _w(log_dt),
            "Dskip": _w(Dskip),
        }
    _A_in = _A_setup()

    def _B_setup():
        B, S, D, F = 1, 2048, 576, 4608
        return {
            "x": _w((lambda *s: torch.randn(*s, dtype=torch.bfloat16) * 0.05)(B, S, D)),
            "w1": _w((lambda *s: torch.randn(*s, dtype=torch.bfloat16) * 0.8)(D, F) * (D ** -0.5)),
            "b1": _w((lambda *s: torch.randn(*s, dtype=torch.bfloat16) * 12.8)(F) * 0.01),
            "w2": _w((lambda *s: torch.randn(*s, dtype=torch.bfloat16) * 0.8)(F, D) * (F ** -0.5)),
            "b2": _w((lambda *s: torch.randn(*s, dtype=torch.bfloat16) * 6.4)(D) * 0.01),
        }
    _B_in = _B_setup()

    return {
        "A_x": _A_in["x"],
        "A_Lambda_re": _A_in["Lambda_re"],
        "A_Lambda_im": _A_in["Lambda_im"],
        "A_B_re": _A_in["B_re"],
        "A_B_im": _A_in["B_im"],
        "A_C_re": _A_in["C_re"],
        "A_C_im": _A_in["C_im"],
        "A_log_dt": _A_in["log_dt"],
        "A_Dskip": _A_in["Dskip"],
        "B_x": _B_in["x"],
        "B_w1": _B_in["w1"],
        "B_b1": _B_in["b1"],
        "B_w2": _B_in["w2"],
        "B_b2": _B_in["b2"],
    }

def reference(A_x, A_Lambda_re, A_Lambda_im, A_B_re, A_B_im, A_C_re, A_C_im, A_log_dt, A_Dskip, B_x, B_w1, B_b1, B_w2, B_b2):
    def _A_ref(x, Lambda_re, Lambda_im, B_re, B_im, C_re, C_im, log_dt, Dskip):
        Bsz, L, H = x.shape
        Lam = Lambda_re + 1j * Lambda_im
        Bmat = B_re + 1j * B_im
        Cmat = C_re + 1j * C_im
        dt = torch.exp(log_dt)
        Lam_bar = torch.exp(dt * Lam)
        B_bar = ((Lam_bar - 1.0) / Lam)[:, None] * Bmat
        Bu = torch.einsum("bld,pd->blp", x + 0j, B_bar)
        h = torch.zeros_like(Bu[:, 0, :])
        hs = []
        for t in range(L):
            h = Lam_bar[None, :] * h + Bu[:, t, :]
            hs.append(h)
        hseq = torch.stack(hs, 1)
        y = 2.0 * torch.einsum("blp,dp->bld", hseq, Cmat).real + x * Dskip[None, None, :]
        return y

    def _B_ref(x, w1, b1, w2, b2):
        h = torch.einsum("bsd,df->bsf", x, w1) + b1
        h = _gelu(h)
        return torch.einsum("bsf,fd->bsd", h, w2) + b2

    x = A_x
    xn = _layernorm(x, axis=-1)
    s = _A_ref(xn, A_Lambda_re, A_Lambda_im, A_B_re, A_B_im, A_C_re, A_C_im, A_log_dt, A_Dskip)
    g = _silu(_B_ref(xn, B_w1, B_b1, B_w2, B_b2))
    return x + s * g
```

```cpp
#include <hip/hip_runtime.h>
#include <hip/hip_bf16.h>
#include <stdint.h>

typedef unsigned short u16;
static __device__ __forceinline__ float bf2f(u16 v) { return __uint_as_float(((unsigned)v) << 16); }
static __device__ __forceinline__ u16 f2bf(float x) { unsigned u = __float_as_uint(x); u += 0x7fffu + ((u >> 16) & 1u); return (u16)(u >> 16); }

constexpr int S = 2048, D = 576, P = 576, F = 4608;

__global__ void ln_kernel(const u16* x, float* xn) {
  int row = blockIdx.x; int t = threadIdx.x;
  float v[9]; float s = 0;
  for (int i = 0; i < 9; i++) { v[i] = bf2f(x[row * D + t + 64 * i]); s += v[i]; }
  for (int o = 32; o; o >>= 1) s += __shfl_xor(s, o);
  float m = s / D; float q = 0;
  for (int i = 0; i < 9; i++) { float d = v[i] - m; q += d * d; }
  for (int o = 32; o; o >>= 1) q += __shfl_xor(q, o);
  float r = rsqrtf(q / D + 1e-5f);
  for (int i = 0; i < 9; i++) xn[row * D + t + 64 * i] = bf2f(f2bf((v[i] - m) * r));
}
__global__ void g1_kernel(const float* xn, const u16* w1, const u16* b1, float* H) {
  int row = blockIdx.y; int c = blockIdx.x * 256 + threadIdx.x;
  float a = 0;
  for (int k = 0; k < D; k++) a += xn[row * D + k] * bf2f(w1[(size_t)k * F + c]);
  a += bf2f(b1[c]);
  float u = 0.7978845608028654f * (a + 0.044715f * a * a * a);
  float g = 0.5f * a * (1.f + tanhf(u));
  H[(size_t)row * F + c] = bf2f(f2bf(g));
}
__global__ void bu_kernel(const float* xn, const u16* Bre, const u16* Bim, float* U) {
  int row = blockIdx.y; int p = blockIdx.x * 64 + threadIdx.x;
  float a = 0, b = 0;
  for (int k = 0; k < D; k++) { float xv = xn[row * D + k]; a += xv * bf2f(Bre[p * D + k]); b += xv * bf2f(Bim[p * D + k]); }
  U[(size_t)row * 2 * P + p] = a; U[(size_t)row * 2 * P + P + p] = b;
}
__global__ void scan_kernel(float* U, const u16* Lre, const u16* Lim, const u16* logdt) {
  int p = blockIdx.x * 64 + threadIdx.x;
  float lr = bf2f(Lre[p]), li = bf2f(Lim[p]);
  float dt = bf2f(f2bf(expf(bf2f(logdt[p]))));
  float e = expf(dt * lr); float br = e * cosf(dt * li), bi = e * sinf(dt * li);
  float nr = br - 1.f, ni = bi; float den = lr * lr + li * li;
  float cr = (nr * lr + ni * li) / den, ci = (ni * lr - nr * li) / den;
  float hr = 0, hi = 0;
  for (int t = 0; t < S; t++) {
    float ur = U[(size_t)t * 2 * P + p], ui = U[(size_t)t * 2 * P + P + p];
    float bur = cr * ur - ci * ui, bui = cr * ui + ci * ur;
    float nhr = br * hr - bi * hi + bur, nhi = br * hi + bi * hr + bui;
    hr = nhr; hi = nhi;
    U[(size_t)t * 2 * P + p] = bf2f(f2bf(hr)); U[(size_t)t * 2 * P + P + p] = bf2f(f2bf(hi));
  }
}
__global__ void fin_kernel(const u16* x, const float* xn, const float* H, const float* U, const u16* w2, const u16* b2,
                           const u16* Cre, const u16* Cim, const u16* Dskip, float* out) {
  int row = blockIdx.y; int c = blockIdx.x * 64 + threadIdx.x;
  float g = 0;
  for (int k = 0; k < F; k++) g += H[(size_t)row * F + k] * bf2f(w2[(size_t)k * D + c]);
  g += bf2f(b2[c]);
  float y = 0;
  for (int k = 0; k < P; k++) y += U[(size_t)row * 2 * P + k] * bf2f(Cre[c * P + k]) - U[(size_t)row * 2 * P + P + k] * bf2f(Cim[c * P + k]);
  float s = 2.f * y + xn[row * D + c] * bf2f(Dskip[c]);
  float sg = g / (1.f + expf(-g));
  out[row * D + c] = bf2f(x[row * D + c]) + s * sg;
}

extern "C" void kernel_launch(void* const* d_in, const int* in_sizes, int n_in,
                              void* d_out, int out_size, void* d_ws, size_t ws_size,
                              hipStream_t stream) {
  const u16* x = (const u16*)d_in[0];
  const u16* Lre = (const u16*)d_in[1];
  const u16* Lim = (const u16*)d_in[2];
  const u16* Bre = (const u16*)d_in[3];
  const u16* Bim = (const u16*)d_in[4];
  const u16* Cre = (const u16*)d_in[5];
  const u16* Cim = (const u16*)d_in[6];
  const u16* logdt = (const u16*)d_in[7];
  const u16* Dskip = (const u16*)d_in[8];
  const u16* w1 = (const u16*)d_in[10];
  const u16* b1 = (const u16*)d_in[11];
  const u16* w2 = (const u16*)d_in[12];
  const u16* b2 = (const u16*)d_in[13];
  float* xn = (float*)d_ws;
  float* H = xn + S * D;
  float* U = H + (size_t)S * F;
  ln_kernel<<<S, 64, 0, stream>>>(x, xn);
  g1_kernel<<<dim3(F / 256, S), 256, 0, stream>>>(xn, w1, b1, H);
  bu_kernel<<<dim3(P / 64, S), 64, 0, stream>>>(xn, Bre, Bim, U);
  scan_kernel<<<P / 64, 64, 0, stream>>>(U, Lre, Lim, logdt);
  fin_kernel<<<dim3(D / 64, S), 64, 0, stream>>>(x, xn, H, U, w2, b2, Cre, Cim, Dskip, (float*)d_out);
}
```

```cpp
#define AMP 0
#include <hip/hip_runtime.h>
#include <hip/hip_bf16.h>
#include <stdint.h>

typedef unsigned short u16;
static __device__ __forceinline__ float bf2f(u16 v) { return __uint_as_float(((unsigned)v) << 16); }
static __device__ __forceinline__ u16 f2bf(float x) { unsigned u = __float_as_uint(x); u += 0x7fffu + ((u >> 16) & 1u); return (u16)(u >> 16); }

constexpr int S = 2048, D = 576, P = 576, F = 4608;

__global__ void ln_kernel(const u16* x, float* xn) {
  int row = blockIdx.x; int t = threadIdx.x;
  float v[9]; float s = 0;
  for (int i = 0; i < 9; i++) { v[i] = bf2f(x[row * D + t + 64 * i]); s += v[i]; }
  for (int o = 32; o; o >>= 1) s += __shfl_xor(s, o);
  float m = s / D; float q = 0;
  for (int i = 0; i < 9; i++) { float d = v[i] - m; q += d * d; }
  for (int o = 32; o; o >>= 1) q += __shfl_xor(q, o);
  float r = rsqrtf(q / D + 1e-5f);
  for (int i = 0; i < 9; i++) xn[row * D + t + 64 * i] = bf2f(f2bf((v[i] - m) * r));
}
__global__ void g1_kernel(const float* xn, const u16* w1, const u16* b1, float* H) {
  int row = blockIdx.y; int c = blockIdx.x * 256 + threadIdx.x;
  float a = 0;
  for (int k = 0; k < D; k++) a += xn[row * D + k] * bf2f(w1[(size_t)k * F + c]);
  a += bf2f(b1[c]);
  float u = 0.7978845608028654f * (a + 0.044715f * a * a * a);
  float g = 0.5f * a * (1.f + tanhf(u));
  H[(size_t)row * F + c] = bf2f(f2bf(g));
}
__global__ void bu_kernel(const float* xn, const u16* Bre, const u16* Bim, float* U) {
  int row = blockIdx.y; int p = blockIdx.x * 64 + threadIdx.x;
  float a = 0, b = 0;
  for (int k = 0; k < D; k++) { float xv = xn[row * D + k]; a += xv * bf2f(Bre[p * D + k]); b += xv * bf2f(Bim[p * D + k]); }
  U[(size_t)row * 2 * P + p] = a; U[(size_t)row * 2 * P + P + p] = b;
}
__global__ void scan_kernel(float* U, const u16* Lre, const u16* Lim, const u16* logdt) {
  int p = blockIdx.x * 64 + threadIdx.x;
  float lr = bf2f(Lre[p]), li = bf2f(Lim[p]);
  float dt = bf2f(f2bf(expf(bf2f(logdt[p]))));
  float e = expf(dt * lr); float br = e * cosf(dt * li), bi = e * sinf(dt * li);
  float nr = br - 1.f, ni = bi; float den = lr * lr + li * li;
  float cr = (nr * lr + ni * li) / den, ci = (ni * lr - nr * li) / den;
  float hr = 0, hi = 0;
  for (int t = 0; t < S; t++) {
    float ur = U[(size_t)t * 2 * P + p], ui = U[(size_t)t * 2 * P + P + p];
    float bur = cr * ur - ci * ui, bui = cr * ui + ci * ur;
    float nhr = br * hr - bi * hi + bur, nhi = br * hi + bi * hr + bui;
    hr = nhr; hi = nhi;
    U[(size_t)t * 2 * P + p] = bf2f(f2bf(hr)); U[(size_t)t * 2 * P + P + p] = bf2f(f2bf(hi));
  }
}
__global__ void fin_kernel(const u16* x, const float* xn, const float* H, const float* U, const u16* w2, const u16* b2,
                           const u16* Cre, const u16* Cim, const u16* Dskip, float* out) {
  int row = blockIdx.y; int c = blockIdx.x * 64 + threadIdx.x;
  float g = 0;
  for (int k = 0; k < F; k++) g += H[(size_t)row * F + k] * bf2f(w2[(size_t)k * D + c]);
  g += bf2f(b2[c]);
  float y = 0;
  for (int k = 0; k < P; k++) y += U[(size_t)row * 2 * P + k] * bf2f(Cre[c * P + k]) - U[(size_t)row * 2 * P + P + k] * bf2f(Cim[c * P + k]);
  float s = 2.f * y + xn[row * D + c] * bf2f(Dskip[c]);
  float sg = g / (1.f + expf(-g));
  out[row * D + c] = bf2f(x[row * D + c]) + s * sg;
}


typedef unsigned u32x4 __attribute__((ext_vector_type(4)));
typedef unsigned long long u64;
__device__ __forceinline__ u64 rt() { return __builtin_amdgcn_s_memrealtime(); }
struct Ptrs { const char* p[7]; int bytes[7]; int pad; };
__global__ __launch_bounds__(512) void probe_k(Ptrs P, u64* stamps, unsigned* sink) {
  u64 t0 = rt();
  u32x4 acc = {0,0,0,0};
  u32x4 r[16];
  int cnt = 0;
#pragma unroll
  for (int a = 0; a < 7; a++) {
    int per = P.bytes[a] / 256;
    const char* b = P.p[a] + (size_t)blockIdx.x * per;
#pragma unroll
    for (int i = 0; i < 3; i++) { int off = (threadIdx.x + i * 512) * 16; if (off < per) acc ^= *(const u32x4*)(b + off); }
  }
  __syncthreads();
  u64 t1 = rt();
  if ((acc.x ^ acc.y ^ acc.z ^ acc.w) == 0x12345) sink[0] = 1;
  if (threadIdx.x == 0) { stamps[blockIdx.x * 2] = t0; stamps[blockIdx.x * 2 + 1] = t1; }
}
__global__ void amp_k(u64* stamps, int amp) {
  __shared__ u64 mn[256], mx[256];
  mn[threadIdx.x] = stamps[threadIdx.x * 2]; mx[threadIdx.x] = stamps[threadIdx.x * 2 + 1];
  __syncthreads();
  if (threadIdx.x == 0) {
    u64 a = ~0ull, b = 0; for (int i = 0; i < 256; i++) { a = a < mn[i] ? a : mn[i]; b = b > mx[i] ? b : mx[i]; }
    u64 span = b - a; if (span > 5000) span = 5000;
    u64 t0 = rt(); while (rt() - t0 < span * amp) __builtin_amdgcn_s_sleep(8);
  }
}

extern "C" void kernel_launch(void* const* d_in, const int* in_sizes, int n_in,
                              void* d_out, int out_size, void* d_ws, size_t ws_size,
                              hipStream_t stream) {
  const u16* x = (const u16*)d_in[0];
  const u16* Lre = (const u16*)d_in[1];
  const u16* Lim = (const u16*)d_in[2];
  const u16* Bre = (const u16*)d_in[3];
  const u16* Bim = (const u16*)d_in[4];
  const u16* Cre = (const u16*)d_in[5];
  const u16* Cim = (const u16*)d_in[6];
  const u16* logdt = (const u16*)d_in[7];
  const u16* Dskip = (const u16*)d_in[8];
  const u16* w1 = (const u16*)d_in[10];
  const u16* b1 = (const u16*)d_in[11];
  const u16* w2 = (const u16*)d_in[12];
  const u16* b2 = (const u16*)d_in[13];
  float* xn = (float*)d_ws;
  float* H = xn + S * D;
  float* U = H + (size_t)S * F;
  Ptrs Pp; Pp.p[0] = (const char*)x; Pp.bytes[0] = S*D*2; Pp.p[1] = (const char*)Bre; Pp.bytes[1] = P*D*2; Pp.p[2] = (const char*)Bim; Pp.bytes[2] = P*D*2;
  Pp.p[3] = (const char*)Cre; Pp.bytes[3] = P*D*2; Pp.p[4] = (const char*)Cim; Pp.bytes[4] = P*D*2; Pp.p[5] = (const char*)w1; Pp.bytes[5] = D*F*2; Pp.p[6] = (const char*)w2; Pp.bytes[6] = D*F*2; Pp.pad = 0;
  u64* stamps = (u64*)((char*)d_ws + (200u<<20));
  probe_k<<<256, 512, 0, stream>>>(Pp, stamps, (unsigned*)(stamps + 1024));
  amp_k<<<1, 256, 0, stream>>>(stamps, AMP);
  ln_kernel<<<S, 64, 0, stream>>>(x, xn);
  g1_kernel<<<dim3(F / 256, S), 256, 0, stream>>>(xn, w1, b1, H);
  bu_kernel<<<dim3(P / 64, S), 64, 0, stream>>>(xn, Bre, Bim, U);
  scan_kernel<<<P / 64, 64, 0, stream>>>(U, Lre, Lim, logdt);
  fin_kernel<<<dim3(D / 64, S), 64, 0, stream>>>(x, xn, H, U, w2, b2, Cre, Cim, Dskip, (float*)d_out);
}
```

```cpp
#include <hip/hip_runtime.h>
#include <hip/hip_bf16.h>
#include <stdint.h>

typedef unsigned short u16;
static __device__ __forceinline__ float bf2f(u16 v) { return __uint_as_float(((unsigned)v) << 16); }
static __device__ __forceinline__ u16 f2bf(float x) { unsigned u = __float_as_uint(x); u += 0x7fffu + ((u >> 16) & 1u); return (u16)(u >> 16); }

constexpr int S = 2048, D = 576, P = 576, F = 4608;

__global__ void ln_kernel(const u16* x, float* xn) {
  int row = blockIdx.x; int t = threadIdx.x;
  float v[9]; float s = 0;
  for (int i = 0; i < 9; i++) { v[i] = bf2f(x[row * D + t + 64 * i]); s += v[i]; }
  for (int o = 32; o; o >>= 1) s += __shfl_xor(s, o);
  float m = s / D; float q = 0;
  for (int i = 0; i < 9; i++) { float d = v[i] - m; q += d * d; }
  for (int o = 32; o; o >>= 1) q += __shfl_xor(q, o);
  float r = rsqrtf(q / D + 1e-5f);
  for (int i = 0; i < 9; i++) xn[row * D + t + 64 * i] = bf2f(f2bf((v[i] - m) * r));
}
__global__ void g1_kernel(const float* xn, const u16* w1, const u16* b1, float* H) {
  int row = blockIdx.y; int c = blockIdx.x * 256 + threadIdx.x;
  float a = 0;
  for (int k = 0; k < D; k++) a += xn[row * D + k] * bf2f(w1[(size_t)k * F + c]);
  a += bf2f(b1[c]);
  float u = 0.7978845608028654f * (a + 0.044715f * a * a * a);
  float g = 0.5f * a * (1.f + tanhf(u));
  H[(size_t)row * F + c] = bf2f(f2bf(g));
}
__global__ void bu_kernel(const float* xn, const u16* Bre, const u16* Bim, float* U) {
  int row = blockIdx.y; int p = blockIdx.x * 64 + threadIdx.x;
  float a = 0, b = 0;
  for (int k = 0; k < D; k++) { float xv = xn[row * D + k]; a += xv * bf2f(Bre[p * D + k]); b += xv * bf2f(Bim[p * D + k]); }
  U[(size_t)row * 2 * P + p] = a; U[(size_t)row * 2 * P + P + p] = b;
}
__global__ void scan_kernel(float* U, const u16* Lre, const u16* Lim, const u16* logdt) {
  int p = blockIdx.x * 64 + threadIdx.x;
  float lr = bf2f(Lre[p]), li = bf2f(Lim[p]);
  float dt = bf2f(f2bf(expf(bf2f(logdt[p]))));
  float e = expf(dt * lr); float br = e * cosf(dt * li), bi = e * sinf(dt * li);
  float nr = br - 1.f, ni = bi; float den = lr * lr + li * li;
  float cr = (nr * lr + ni * li) / den, ci = (ni * lr - nr * li) / den;
  float hr = 0, hi = 0;
  for (int t = 0; t < S; t++) {
    float ur = U[(size_t)t * 2 * P + p], ui = U[(size_t)t * 2 * P + P + p];
    float bur = cr * ur - ci * ui, bui = cr * ui + ci * ur;
    float nhr = br * hr - bi * hi + bur, nhi = br * hi + bi * hr + bui;
    hr = nhr; hi = nhi;
    U[(size_t)t * 2 * P + p] = bf2f(f2bf(hr)); U[(size_t)t * 2 * P + P + p] = bf2f(f2bf(hi));
  }
}
__global__ void fin_kernel(const u16* x, const float* xn, const float* H, const float* U, const u16* w2, const u16* b2,
                           const u16* Cre, const u16* Cim, const u16* Dskip, float* out) {
  int row = blockIdx.y; int c = blockIdx.x * 64 + threadIdx.x;
  float g = 0;
  for (int k = 0; k < F; k++) g += H[(size_t)row * F + k] * bf2f(w2[(size_t)k * D + c]);
  g += bf2f(b2[c]);
  float y = 0;
  for (int k = 0; k < P; k++) y += U[(size_t)row * 2 * P + k] * bf2f(Cre[c * P + k]) - U[(size_t)row * 2 * P + P + k] * bf2f(Cim[c * P + k]);
  float s = 2.f * y + xn[row * D + c] * bf2f(Dskip[c]);
  float sg = g / (1.f + expf(-g));
  out[row * D + c] = bf2f(x[row * D + c]) + s * sg;
}


typedef unsigned u32x4 __attribute__((ext_vector_type(4)));
typedef unsigned long long u64;
__device__ __forceinline__ u64 rt() { return __builtin_amdgcn_s_memrealtime(); }
__global__ __launch_bounds__(512) void probe_a(const char* x, const char* b0, const char* b1, const char* c0, const char* c1, const char* w1, const char* w2, u64* stamps, unsigned* sink) {
  u64 t0 = rt();
  u32x4 r[12];
  int t = threadIdx.x * 16;
#define LD(i, base, per, k) { int off = t + (k) * 8192; off = off < (per) ? off : (per) - 16; r[i] = *(const u32x4*)(base + (size_t)blockIdx.x * (per) + off); }
  LD(0, w1, 20736, 0) LD(1, w1, 20736, 1) LD(2, w1, 20736, 2) LD(3, w2, 20736, 0) LD(4, w2, 20736, 1) LD(5, w2, 20736, 2)
  LD(6, x, 9216, 0) LD(7, x, 9216, 1) LD(8, b0, 2592, 0) LD(9, b1, 2592, 0) LD(10, c0, 2592, 0) LD(11, c1, 2592, 0)
  u32x4 acc = {0,0,0,0};
#pragma unroll
  for (int i = 0; i < 12; i++) acc ^= r[i];
  __syncthreads();
  u64 t1 = rt();
  if ((acc.x ^ acc.y ^ acc.z ^ acc.w) == 0x12345) sink[0] = 1;
  if (threadIdx.x == 0) { stamps[blockIdx.x * 2] = t0; stamps[blockIdx.x * 2 + 1] = t1; }
}
template <int ROT>
__global__ __launch_bounds__(256) void probe_b(const char* w1, const char* w2, u64* stamps, unsigned* sink) {
  extern __shared__ __attribute__((aligned(16))) char smem[];
  u64 t0 = rt();
  int wid = __builtin_amdgcn_readfirstlane(threadIdx.x >> 6), lane = threadIdx.x & 63;
  const int RB = 640 * 1024;
  char* l = smem + wid * 16 * 1024;
  int rot = ROT ? (blockIdx.x >> 3) % 10 : 0;
  for (int ph = 0; ph < 2; ph++) {
    const char* p = (ph ? w2 : w1) + (size_t)(blockIdx.x % 8) * RB + wid * 1024 + lane * 16;
    for (int i = 0; i < 10; i++) {
      int ii = i + rot; if (ii >= 10) ii -= 10;
#pragma unroll
      for (int d = 0; d < 16; ++d) {
        __builtin_amdgcn_global_load_lds((const unsigned*)(p + (size_t)(ii * 16 + d) * 4096), (__attribute__((address_space(3))) unsigned*)(l + d * 1024), 16, 0, 0);
        asm volatile("s_waitcnt vmcnt(15)" ::: "memory");
      }
    }
  }
  asm volatile("s_waitcnt vmcnt(0)" ::: "memory");
  __syncthreads();
  u64 t1 = rt();
  if (threadIdx.x == 0) { stamps[blockIdx.x * 2] = t0; stamps[blockIdx.x * 2 + 1] = t1; if (*(unsigned*)smem == 0x12345) sink[0] = 1; }
}
template <int AUX>
__global__ __launch_bounds__(512) void store_k(char* dst, int bytes_per_block, u64* stamps) {
  u64 t0 = rt();
  int n = bytes_per_block / (1024 * 8);
  char* p = dst + (size_t)blockIdx.x * bytes_per_block;
  auto rsrc = __builtin_amdgcn_make_buffer_rsrc(p, 0, bytes_per_block, 0x00020000);
  u32x4 v = {threadIdx.x, blockIdx.x, 3, 4};
  for (int i = 0; i < n; ++i) __builtin_amdgcn_raw_buffer_store_b128(v, rsrc, threadIdx.x * 16 + i * 8192, 0, AUX);
  asm volatile("s_waitcnt vmcnt(0)" ::: "memory");
  __syncthreads();
  if (threadIdx.x == 0) { stamps[blockIdx.x * 2] = t0; }
}
__global__ __launch_bounds__(512) void read_k(const char* src, int bytes_per_block, int shift, u64* stamps, unsigned* sink) {
  int n = bytes_per_block / (1024 * 8);
  const char* p = src + (size_t)((blockIdx.x + shift) % 256) * bytes_per_block + threadIdx.x * 16;
  u32x4 acc = {0, 0, 0, 0};
#pragma unroll 6
  for (int i = 0; i < n; ++i) acc ^= *(const u32x4*)(p + (size_t)i * 8192);
  __syncthreads();
  u64 t1 = rt();
  if ((acc.x ^ acc.y ^ acc.z ^ acc.w) == 0x12345) sink[0] = 1;
  if (threadIdx.x == 0) { stamps[blockIdx.x * 2 + 1] = t1; }
}
__global__ void e0_k(u64* stamps) { if (threadIdx.x == 0) stamps[blockIdx.x * 2] = rt(); }
__global__ void e1_k(u64* stamps) { if (threadIdx.x == 0) stamps[blockIdx.x * 2 + 1] = rt(); }
__global__ void amp_k(u64* stamps, int amp) {
  __shared__ u64 mn[256], mx[256];
  mn[threadIdx.x] = stamps[threadIdx.x * 2]; mx[threadIdx.x] = stamps[threadIdx.x * 2 + 1];
  __syncthreads();
  if (threadIdx.x == 0) {
    u64 a = ~0ull, b = 0; for (int i = 0; i < 256; i++) { a = a < mn[i] ? a : mn[i]; b = b > mx[i] ? b : mx[i]; }
    u64 span = b - a; if (span > 5000) span = 5000;
    u64 t0 = rt(); while (rt() - t0 < span * amp) __builtin_amdgcn_s_sleep(8);
  }
}

extern "C" void kernel_launch(void* const* d_in, const int* in_sizes, int n_in,
                              void* d_out, int out_size, void* d_ws, size_t ws_size,
                              hipStream_t stream) {
  const u16* x = (const u16*)d_in[0];
  const u16* Lre = (const u16*)d_in[1];
  const u16* Lim = (const u16*)d_in[2];
  const u16* Bre = (const u16*)d_in[3];
  const u16* Bim = (const u16*)d_in[4];
  const u16* Cre = (const u16*)d_in[5];
  const u16* Cim = (const u16*)d_in[6];
  const u16* logdt = (const u16*)d_in[7];
  const u16* Dskip = (const u16*)d_in[8];
  const u16* w1 = (const u16*)d_in[10];
  const u16* b1 = (const u16*)d_in[11];
  const u16* w2 = (const u16*)d_in[12];
  const u16* b2 = (const u16*)d_in[13];
  float* xn = (float*)d_ws;
  float* H = xn + S * D;
  float* U = H + (size_t)S * F;
  u64* stamps = (u64*)((char*)d_ws + (200u<<20)); unsigned* sink = (unsigned*)(stamps + 1024); char* dst = (char*)d_ws + (128u<<20);
  probe_a<<<256, 512, 0, stream>>>((const char*)x, (const char*)Bre, (const char*)Bim, (const char*)Cre, (const char*)Cim, (const char*)w1, (const char*)w2, stamps, sink);
  amp_k<<<1, 256, 0, stream>>>(stamps, 500);
  ln_kernel<<<S, 64, 0, stream>>>(x, xn);
  g1_kernel<<<dim3(F / 256, S), 256, 0, stream>>>(xn, w1, b1, H);
  bu_kernel<<<dim3(P / 64, S), 64, 0, stream>>>(xn, Bre, Bim, U);
  scan_kernel<<<P / 64, 64, 0, stream>>>(U, Lre, Lim, logdt);
  fin_kernel<<<dim3(D / 64, S), 64, 0, stream>>>(x, xn, H, U, w2, b2, Cre, Cim, Dskip, (float*)d_out);
}
```

```cpp
#include <hip/hip_runtime.h>
#include <hip/hip_bf16.h>
#include <stdint.h>

typedef unsigned short u16;
static __device__ __forceinline__ float bf2f(u16 v) { return __uint_as_float(((unsigned)v) << 16); }
static __device__ __forceinline__ u16 f2bf(float x) { unsigned u = __float_as_uint(x); u += 0x7fffu + ((u >> 16) & 1u); return (u16)(u >> 16); }

constexpr int S = 2048, D = 576, P = 576, F = 4608;

__global__ void ln_kernel(const u16* x, float* xn) {
  int row = blockIdx.x; int t = threadIdx.x;
  float v[9]; float s = 0;
  for (int i = 0; i < 9; i++) { v[i] = bf2f(x[row * D + t + 64 * i]); s += v[i]; }
  for (int o = 32; o; o >>= 1) s += __shfl_xor(s, o);
  float m = s / D; float q = 0;
  for (int i = 0; i < 9; i++) { float d = v[i] - m; q += d * d; }
  for (int o = 32; o; o >>= 1) q += __shfl_xor(q, o);
  float r = rsqrtf(q / D + 1e-5f);
  for (int i = 0; i < 9; i++) xn[row * D + t + 64 * i] = bf2f(f2bf((v[i] - m) * r));
}
__global__ void g1_kernel(const float* xn, const u16* w1, const u16* b1, float* H) {
  int row = blockIdx.y; int c = blockIdx.x * 256 + threadIdx.x;
  float a = 0;
  for (int k = 0; k < D; k++) a += xn[row * D + k] * bf2f(w1[(size_t)k * F + c]);
  a += bf2f(b1[c]);
  float u = 0.7978845608028654f * (a + 0.044715f * a * a * a);
  float g = 0.5f * a * (1.f + tanhf(u));
  H[(size_t)row * F + c] = bf2f(f2bf(g));
}
__global__ void bu_kernel(const float* xn, const u16* Bre, const u16* Bim, float* U) {
  int row = blockIdx.y; int p = blockIdx.x * 64 + threadIdx.x;
  float a = 0, b = 0;
  for (int k = 0; k < D; k++) { float xv = xn[row * D + k]; a += xv * bf2f(Bre[p * D + k]); b += xv * bf2f(Bim[p * D + k]); }
  U[(size_t)row * 2 * P + p] = a; U[(size_t)row * 2 * P + P + p] = b;
}
__global__ void scan_kernel(float* U, const u16* Lre, const u16* Lim, const u16* logdt) {
  int p = blockIdx.x * 64 + threadIdx.x;
  float lr = bf2f(Lre[p]), li = bf2f(Lim[p]);
  float dt = bf2f(f2bf(expf(bf2f(logdt[p]))));
  float e = expf(dt * lr); float br = e * cosf(dt * li), bi = e * sinf(dt * li);
  float nr = br - 1.f, ni = bi; float den = lr * lr + li * li;
  float cr = (nr * lr + ni * li) / den, ci = (ni * lr - nr * li) / den;
  float hr = 0, hi = 0;
  for (int t = 0; t < S; t++) {
    float ur = U[(size_t)t * 2 * P + p], ui = U[(size_t)t * 2 * P + P + p];
    float bur = cr * ur - ci * ui, bui = cr * ui + ci * ur;
    float nhr = br * hr - bi * hi + bur, nhi = br * hi + bi * hr + bui;
    hr = nhr; hi = nhi;
    U[(size_t)t * 2 * P + p] = bf2f(f2bf(hr)); U[(size_t)t * 2 * P + P + p] = bf2f(f2bf(hi));
  }
}
__global__ void fin_kernel(const u16* x, const float* xn, const float* H, const float* U, const u16* w2, const u16* b2,
                           const u16* Cre, const u16* Cim, const u16* Dskip, float* out) {
  int row = blockIdx.y; int c = blockIdx.x * 64 + threadIdx.x;
  float g = 0;
  for (int k = 0; k < F; k++) g += H[(size_t)row * F + k] * bf2f(w2[(size_t)k * D + c]);
  g += bf2f(b2[c]);
  float y = 0;
  for (int k = 0; k < P; k++) y += U[(size_t)row * 2 * P + k] * bf2f(Cre[c * P + k]) - U[(size_t)row * 2 * P + P + k] * bf2f(Cim[c * P + k]);
  float s = 2.f * y + xn[row * D + c] * bf2f(Dskip[c]);
  float sg = g / (1.f + expf(-g));
  out[row * D + c] = bf2f(x[row * D + c]) + s * sg;
}


typedef unsigned long long u64;
__device__ __forceinline__ u64 rt() { return __builtin_amdgcn_s_memrealtime(); }
__global__ void e0_k(u64* stamps, int ticks) { u64 t0 = rt(); if (threadIdx.x == 0) stamps[blockIdx.x * 2] = t0; while (rt() - t0 < ticks) __builtin_amdgcn_s_sleep(1); }
__global__ void em_k(u64* stamps, int ticks) { u64 t0 = rt(); while (rt() - t0 < ticks) __builtin_amdgcn_s_sleep(1); }
__global__ void e1_k(u64* stamps) { if (threadIdx.x == 0) stamps[blockIdx.x * 2 + 1] = rt(); }
__global__ void amp_k(u64* stamps, int amp) {
  __shared__ u64 mn[256], mx[256];
  mn[threadIdx.x] = stamps[threadIdx.x * 2]; mx[threadIdx.x] = stamps[threadIdx.x * 2 + 1];
  __syncthreads();
  if (threadIdx.x == 0) {
    u64 a = ~0ull, b = 0; for (int i = 0; i < 256; i++) { a = a < mn[i] ? a : mn[i]; b = b > mx[i] ? b : mx[i]; }
    u64 span = b - a; if (span > 5000) span = 5000;
    u64 t0 = rt(); while (rt() - t0 < span * amp) __builtin_amdgcn_s_sleep(8);
  }
}

extern "C" void kernel_launch(void* const* d_in, const int* in_sizes, int n_in,
                              void* d_out, int out_size, void* d_ws, size_t ws_size,
                              hipStream_t stream) {
  const u16* x = (const u16*)d_in[0];
  const u16* Lre = (const u16*)d_in[1];
  const u16* Lim = (const u16*)d_in[2];
  const u16* Bre = (const u16*)d_in[3];
  const u16* Bim = (const u16*)d_in[4];
  const u16* Cre = (const u16*)d_in[5];
  const u16* Cim = (const u16*)d_in[6];
  const u16* logdt = (const u16*)d_in[7];
  const u16* Dskip = (const u16*)d_in[8];
  const u16* w1 = (const u16*)d_in[10];
  const u16* b1 = (const u16*)d_in[11];
  const u16* w2 = (const u16*)d_in[12];
  const u16* b2 = (const u16*)d_in[13];
  float* xn = (float*)d_ws;
  float* H = xn + S * D;
  float* U = H + (size_t)S * F;
  u64* stamps = (u64*)((char*)d_ws + (200u<<20));
  em_k<<<256, 256, 0, stream>>>(stamps, 2000); e0_k<<<256, 256, 0, stream>>>(stamps, 0); e1_k<<<256, 256, 0, stream>>>(stamps);
  amp_k<<<1, 256, 0, stream>>>(stamps, 500);
  ln_kernel<<<S, 64, 0, stream>>>(x, xn);
  g1_kernel<<<dim3(F / 256, S), 256, 0, stream>>>(xn, w1, b1, H);
  bu_kernel<<<dim3(P / 64, S), 64, 0, stream>>>(xn, Bre, Bim, U);
  scan_kernel<<<P / 64, 64, 0, stream>>>(U, Lre, Lim, logdt);
  fin_kernel<<<dim3(D / 64, S), 64, 0, stream>>>(x, xn, H, U, w2, b2, Cre, Cim, Dskip, (float*)d_out);
}
```

```cpp
#include <hip/hip_runtime.h>
#include <utility>

#define LAS __attribute__((address_space(3)))
#define GAS __attribute__((address_space(1)))
#define DI __device__ __forceinline__
typedef unsigned short u16;
typedef unsigned u32;
typedef unsigned long long u64;
typedef unsigned u32x4 __attribute__((ext_vector_type(4)));
typedef unsigned u32x2 __attribute__((ext_vector_type(2)));
typedef short s16x4 __attribute__((ext_vector_type(4)));
typedef short bf16x8 __attribute__((ext_vector_type(8)));
typedef float f32x4 __attribute__((ext_vector_type(4)));
typedef float f32x2 __attribute__((ext_vector_type(2)));
typedef __bf16 bf16x2 __attribute__((ext_vector_type(2)));
typedef __amdgpu_buffer_rsrc_t rsrc_t;

constexpr int S = 2048, D = 576, F = 4608;
constexpr int ROWB = D * 2;
constexpr int WSLOT = 24576, TOFF = 4 * WSLOT, TSLOT = 16384, LDS_BYTES = 163840;
constexpr int AX_W = 0, AX_T = 6144, AX_S = 8192, AX_TAB = 10240;
constexpr u32 WS_FLAGS = 0;
constexpr u32 FL_S = 0, FL_C = 16, FL_P = 32, FL_O = 32 + 240, NFLAG = 32 + 480;
constexpr u32 WS_EG = NFLAG * 128;
constexpr u32 WS_ZERO_BYTES = WS_EG + 16 * 576 * 16;
constexpr u32 WS_AUXW = 256 * 1024;
constexpr u32 WS_TAB = WS_AUXW + 15 * 6144;
constexpr u32 WS_P = 1u << 20;
constexpr u32 XBW = 5 * 1536;
constexpr u32 WS_XB = WS_P + 240 * 98304;
constexpr u32 WS_END = WS_XB + 48 * 32 * XBW;
constexpr u32 SPIN_CAP = 1u << 18;

struct Params {
  const u16 *x, *lre, *lim, *bre, *bim, *cre, *cim, *logdt, *dskip, *w1, *b1, *w2, *b2;
  float* out; char* ws;
};

template <int N> DI void waitv() { asm volatile("s_waitcnt vmcnt(%0)" :: "n"(N) : "memory"); }
#define WAITL() asm volatile("s_waitcnt lgkmcnt(0)" ::: "memory")
#define BAR() do { asm volatile("" ::: "memory"); __builtin_amdgcn_s_barrier(); asm volatile("" ::: "memory"); } while (0)
#define SB() __builtin_amdgcn_sched_barrier(0)
#define MFMA(a, b, c) __builtin_amdgcn_mfma_f32_16x16x32_bf16((a), (b), (c), 0, 0, 0)
#define STAMP(i)

DI void dma(rsrc_t r, u32 lds, u32 voff, u32 soff) {
  asm volatile("s_mov_b32 m0, %0\n\ts_nop 0\n\tbuffer_load_dwordx4 %1, %2, %3 offen lds" :: "s"(lds), "v"(voff), "s"(r), "s"(soff) : "memory");
}
DI void dma_sc1(rsrc_t r, u32 lds, u32 voff, u32 soff) {
  asm volatile("s_mov_b32 m0, %0\n\ts_nop 0\n\tbuffer_load_dwordx4 %1, %2, %3 offen sc1 lds" :: "s"(lds), "v"(voff), "s"(r), "s"(soff) : "memory");
}
DI rsrc_t mk(const void* ptr, u32 bytes) { return __builtin_amdgcn_make_buffer_rsrc((void*)ptr, 0, bytes, 0x00020000); }
DI s16x4 rd_tr1(u32 a, int off) { return __builtin_amdgcn_ds_read_tr16_b64_v4i16((LAS s16x4*)(size_t)(a + off)); }
DI bf16x8 cat(s16x4 lo, s16x4 hi) { return __builtin_shufflevector(lo, hi, 0, 1, 2, 3, 4, 5, 6, 7); }
DI bf16x8 rd_tr(u32 a, int off) { return cat(rd_tr1(a, off), rd_tr1(a, off + 2048)); }
DI bf16x8 rd128(u32 a, int off) { return *(LAS bf16x8*)(size_t)(a + off); }
DI f32x4 rdf4(u32 a, int off) { return *(LAS f32x4*)(size_t)(a + off); }
DI float rdf(u32 a, int off) { return *(LAS float*)(size_t)(a + off); }
DI void wr128(u32 a, int off, u32x4 v) { *(LAS u32x4*)(size_t)(a + off) = v; }
DI u32 cvtpk(float lo, float hi) { f32x2 v = {lo, hi}; bf16x2 b = __builtin_convertvector(v, bf16x2); return __builtin_bit_cast(u32, b); }
DI float bflo(u32 v) { return __uint_as_float(v << 16); }
DI float bfhi(u32 v) { return __uint_as_float(v & 0xffff0000u); }
DI float bf2f(u16 v) { return __uint_as_float((u32)v << 16); }
DI float dot2(u32 a, u32 b, float c) { return __builtin_amdgcn_fdot2_f32_bf16(__builtin_bit_cast(bf16x2, a), __builtin_bit_cast(bf16x2, b), c, false); }
constexpr u32 ONES = 0x3f803f80u;
DI float rbf(float v) { return bflo(cvtpk(v, 0.f)); }
DI void split3(float v, float& a, float& b, float& c) { a = rbf(v); const float r = v - a; b = rbf(r); c = rbf(r - b); }
template <int C> DI float dppx(float v) { return __uint_as_float(__builtin_amdgcn_update_dpp(0u, __float_as_uint(v), C, 0xf, 0xf, true)); }
template <int C> DI double dppd(double v) { u32x2 t = __builtin_bit_cast(u32x2, v); t[0] = __builtin_amdgcn_update_dpp(0u, t[0], C, 0xf, 0xf, true); t[1] = __builtin_amdgcn_update_dpp(0u, t[1], C, 0xf, 0xf, true); return __builtin_bit_cast(double, t); }
DI void st_flag(char* ws, u32 idx, u32 tag) { __hip_atomic_store((GAS u32*)(ws + WS_FLAGS + idx * 128), tag, __ATOMIC_RELAXED, __HIP_MEMORY_SCOPE_AGENT); }
DI u32 ld_flag(const char* a) { return __hip_atomic_load((GAS u32*)a, __ATOMIC_RELAXED, __HIP_MEMORY_SCOPE_AGENT); }
DI void spin_all(const char* a, u32 tag) {
  for (u32 i = 0; i < SPIN_CAP; i++) { if (__all(ld_flag(a) == tag)) break; __builtin_amdgcn_s_sleep(1); }
  asm volatile("" ::: "memory");
}
DI u32 poll_issue(const char* a) { u32 v; asm volatile("global_load_dword %0, %1, off sc1" : "=v"(v) : "v"(a) : "memory"); return v; }

DI f32x2 gelu2(f32x2 a, float k1, float k2, float sg) {
  f32x2 t = a * a; t = t * k2 + k1; t = t * a;
  t.x = __builtin_amdgcn_exp2f(t.x); t.y = __builtin_amdgcn_exp2f(t.y);
  t = t * sg + sg;
  t.x = __builtin_amdgcn_rcpf(t.x); t.y = __builtin_amdgcn_rcpf(t.y);
  return t * a;
}
DI u32x4 gelu_frag(f32x4 a, f32x4 b, float k1, float k2, float sg) {
  f32x2 g0 = gelu2((f32x2){a[0], a[1]}, k1, k2, sg), g1 = gelu2((f32x2){a[2], a[3]}, k1, k2, sg);
  f32x2 g2 = gelu2((f32x2){b[0], b[1]}, k1, k2, sg), g3 = gelu2((f32x2){b[2], b[3]}, k1, k2, sg);
  return (u32x4){cvtpk(g0.x, g0.y), cvtpk(g1.x, g1.y), cvtpk(g2.x, g2.y), cvtpk(g3.x, g3.y)};
}

template <int H>
DI void stepA(f32x4 (&acc)[6][4], bf16x8 (&wp)[2], bf16x8 (&t)[4], const u32 (&wc)[4], u32 n0, u32 n1, u32 xn) {
  bf16x8 w[8], nx[4];
  w[0] = wp[0]; w[1] = wp[1];
#pragma unroll
  for (int n = 0; n < 6; n++) {
    const int f = n + 2, ff = f < 6 ? f : f - 6;
    const u32 a = (f < 6 || H == 0) ? wc[ff & 3] : (ff ? n1 : n0); const int off = (f < 6 ? H : 1 - H) * WSLOT + (ff >> 2) * 4096;
    acc[n][0] = MFMA(w[n], t[0], acc[n][0]); SB();
    s16x4 lo = rd_tr1(a, off); SB();
    acc[n][1] = MFMA(w[n], t[1], acc[n][1]); SB();
    s16x4 hi = rd_tr1(a, off + 2048); SB();
    acc[n][2] = MFMA(w[n], t[2], acc[n][2]); SB();
    if (n >= 1 && n <= 4) { nx[n - 1] = rd128(xn, (n - 1) * 2048); SB(); }
    acc[n][3] = MFMA(w[n], t[3], acc[n][3]); SB();
    w[f] = cat(lo, hi);
  }
  wp[0] = w[6]; wp[1] = w[7];
#pragma unroll
  for (int i = 0; i < 4; i++) t[i] = nx[i];
}

template <int KIND, class FW, class FT, class FN, class MID>
DI void half(f32x4 (&acc)[3][4], bf16x8 (&w)[3], bf16x8 (&tp)[2], FW&& rw, FT&& rt_, FN&& rn, MID&& mid) {
  bf16x8 t[4], wn[3], tn[2];
  t[0] = tp[0]; t[1] = tp[1];
  s16x4 lo;
#pragma unroll
  for (int i = 0; i < 12; i++) {
    const int T = i / 3, n = i % 3;
    if constexpr (KIND == 2) acc[n][T] = MFMA(t[T], w[n], acc[n][T]); else acc[n][T] = MFMA(w[n], t[T], acc[n][T]);
    SB();
    if constexpr (KIND == 0) {
      if (i == 0) t[2] = rt_(2, 0); if (i == 1) t[3] = rt_(3, 0);
      if (i >= 2 && i <= 7) { if ((i & 1) == 0) lo = rw((i - 2) >> 1, 0); else wn[(i - 2) >> 1] = cat(lo, rw((i - 2) >> 1, 1)); }
      if (i == 8) tn[0] = rn(0, 0); if (i == 9) tn[1] = rn(1, 0);
    } else if constexpr (KIND == 1) {
      if (i <= 3) { if ((i & 1) == 0) lo = rt_(2 + (i >> 1), 0); else t[2 + (i >> 1)] = cat(lo, rt_(2 + (i >> 1), 1)); }
      if (i >= 4 && i <= 6) wn[i - 4] = rw(i - 4, 0);
      if (i >= 7 && i <= 10) { if ((i & 1) == 1) lo = rn((i - 7) >> 1, 0); else tn[(i - 7) >> 1] = cat(lo, rn((i - 7) >> 1, 1)); }
    } else {
      if (i == 0) t[2] = rt_(2, 0); if (i == 2) t[3] = rt_(3, 0);
      if (i == 3 || i == 5 || i == 6) wn[i == 3 ? 0 : i - 4] = rw(i == 3 ? 0 : i - 4, 0);
      if (i == 8) tn[0] = rn(0, 0); if (i == 9) tn[1] = rn(1, 0);
    }
    mid(i);
    SB();
  }
#pragma unroll
  for (int i = 0; i < 3; i++) w[i] = wn[i];
  tp[0] = tn[0]; tp[1] = tn[1];
}

template <int I, int N, class FN> DI void sfor(FN&& f) { if constexpr (I < N) { f(std::integral_constant<int, I>{}); sfor<I + 1, N>(f); } }

DI void ssm_carry(const Params& p, int rt, int j, int c, u32 lds0, u32 tag) {
  const u32 ax = lds0 + (18 & 3) * WSLOT, scr = lds0 + TOFF + (22 & 3) * TSLOT;
  {
    f32x4 pw = rdf4(ax + AX_TAB + c * 32, 16);
    { const float t = pw[0] * pw[0] - pw[1] * pw[1]; pw[1] = 2.f * pw[0] * pw[1]; pw[0] = t; }
    f32x2 es[8];
#pragma unroll
    for (int s = 0; s < 8; s++) es[s] = *(LAS f32x2*)(size_t)(scr + (s * 192 + c) * 8);
    float sr = 0.f, si = 0.f;
#pragma unroll
    for (int s = 0; s < 8; s++) { const float nr = pw[0] * sr - pw[1] * si + es[s].x, ni = pw[0] * si + pw[1] * sr + es[s].y; sr = nr; si = ni; }
    const rsrc_t rWS = mk(p.ws, WS_END);
    __builtin_amdgcn_raw_buffer_store_b128((u32x4){__float_as_uint(sr), tag, __float_as_uint(si), tag}, rWS, (192 * j + c) * 16, WS_EG + rt * 576 * 16, 16);
    float cr = 0.f, ci = 0.f;
    for (u32 spin = 0; spin < SPIN_CAP; spin++) {
      u32x4 v[15]; bool ok = true;
#pragma unroll
      for (int i = 0; i < 15; i++) v[i] = __builtin_amdgcn_raw_buffer_load_b128(rWS, (192 * j + c) * 16, WS_EG + (i < rt ? i : 0) * 576 * 16, 16);
      cr = 0.f; ci = 0.f;
#pragma unroll
      for (int i = 0; i < 15; i++) {
        if (i < rt) {
          ok = ok && v[i][1] == tag && v[i][3] == tag;
          const float nr = pw[2] * cr - pw[3] * ci + __uint_as_float(v[i][0]), ni = pw[2] * ci + pw[3] * cr + __uint_as_float(v[i][2]); cr = nr; ci = ni;
        }
      }
      if (__all(ok)) break;
      __builtin_amdgcn_s_sleep(1);
    }
#pragma unroll
    for (int s = 0; s < 8; s++) {
      *(LAS f32x2*)(size_t)(scr + (s * 192 + c) * 8) = (f32x2){cr, ci};
      const float nr = pw[0] * cr - pw[1] * ci + es[s].x, ni = pw[0] * ci + pw[1] * cr + es[s].y; cr = nr; ci = ni;
    }
  }
}

template <bool SSM>
DI void loader(const Params& p, int rt, int g, int j, int u, int lane, u32 lds0, u32 tag) {
  const rsrc_t rX = mk(p.x, S * ROWB), rWS = mk(p.ws, WS_END);
  const rsrc_t rA0 = SSM ? mk(p.bre, D * ROWB) : mk(p.w1, D * F * 2), rA1 = SSM ? mk(p.bim, D * ROWB) : rA0;
  const rsrc_t rB0 = SSM ? mk(p.cre, D * ROWB) : mk(p.w2, D * F * 2), rB1 = SSM ? mk(p.cim, D * ROWB) : rB0;
  const int r8 = lane >> 3, gp = lane & 7;
  const int cgr = ((((gp >> 1) ^ ((r8 >> 1) & 3)) << 1) | (gp & 1));
  const int rgr = gp ^ (4 * (u & 1) + (r8 >> 1));
  const int slice = 3 * g + j;
  const int kA = 16 * (u & 1) + 8 * (r8 >> 2) + 4 * (u >> 1) + (r8 & 3);
  const u32 voffA = SSM ? (192 * j + 8 * u + r8) * ROWB + rgr * 16 : kA * (F * 2) + slice * 768 + cgr * 16;
  const u32 voffB = SSM ? voffA : (8 * u + r8) * ROWB + j * 384 + cgr * 16;
  const int R = 8 * u + r8;
  const int tok = SSM ? 16 * ((R >> 2) & 3) + 8 * (R & 1) + 2 * (R >> 4) + ((R >> 1) & 1) : R;
  const u32 voffX = (rt * 128 + tok) * ROWB + rgr * 16;
  const u32 voffL = lane * 16;
  const u32 ldsW = lds0 + u * 1024, ldsT = lds0 + TOFF;
  const int rot = rt % 9;
  const int j1 = j == 2 ? 0 : j + 1, j2 = j == 0 ? 2 : j - 1;
  auto issueA = [&](int s, auto&& fill) {
    int kt = (s >> 1) + rot; if (kt >= 9) kt -= 9;
    const u32 l = ldsW + (s & 3) * WSLOT;
    const u32 so = SSM ? kt * 128 : (kt * 64 + (s & 1) * 32) * (F * 2);
#pragma unroll
    for (int i = 0; i < 6; i++) { dma((s & 1) ? rA1 : rA0, l + i * 4096, voffA, so + i * (SSM ? 32 * ROWB : 128)); SB(); fill(i); SB(); }
  };
  auto nofill = [](int) {};
  auto issueX = [&](int kap, int h) {
    int kt = kap + rot; if (kt >= 9) kt -= 9;
    const u32 l = ldsT + (kap & 3) * TSLOT + h * 8192 + u * 1024, so = h * 64 * ROWB + kt * 128;
    dma(rX, l, voffX, so); dma(rX, l + 4096, voffX, so + (SSM ? 4 : 32) * ROWB);
  };
  auto issueB = [&](int s) {
    const int i = s < 25 ? s - 19 : (s - 25) >> 1;
    const int jj = s < 25 ? j : (((s - 25) & 1) ? j2 : j1);
    const u32 l = ldsW + (s & 3) * WSLOT;
    if (SSM) {
      const u32 so = (192 * jj + 64 * (i >> 1)) * 2;
#pragma unroll
      for (int k = 0; k < 6; k++) dma((i & 1) ? rB1 : rB0, l + k * 4096, voffB, k * 32 * ROWB + so);
    } else {
      const u32 so = ((3 * g + jj) * 384 + 192 * (i & 1) + 32 * (i >> 1)) * ROWB;
#pragma unroll
      for (int k = 0; k < 6; k++) dma(rB0, l + k * 4096, voffB, so + (k / 3) * 96 * ROWB + (k % 3) * 128);
    }
    if (s >= 25) {
      const u32 sp = WS_P + (rt * 15 + 3 * g + jj) * 98304 + i * 16384 + u * 4096, lp = ldsT + (s & 3) * TSLOT + u * 4096;
#pragma unroll
      for (int k = 0; k < 4; k++) dma_sc1(rWS, lp + k * 1024, voffL, sp + k * 1024);
    }
  };
  auto issueAux = [&]() {
    const u32 l = lds0 + (18 & 3) * WSLOT;
#pragma unroll
    for (int k = 0; k < (SSM ? 3 : 2); k++) {
      int pc = SSM ? u * 3 + k : u * 2 + k; if (!SSM && pc >= 6) pc -= 6;
      const u32 so = pc < 6 ? WS_AUXW + slice * 6144 + pc * 1024 : WS_TAB + j * 6144 + (pc - 6) * 1024;
      dma_sc1(rWS, l + (pc < 6 ? AX_W + pc * 1024 : AX_TAB + (pc - 6) * 1024), voffL, so);
    }
  };
  float s1[4] = {0.f, 0.f, 0.f, 0.f}, s2[4] = {0.f, 0.f, 0.f, 0.f};
  u32x4 sv[2];
  auto statRead = [&](int hs) {
    sv[0] = __builtin_bit_cast(u32x4, rd128(ldsT + u * 1024 + voffL, ((hs >> 1) & 3) * TSLOT + (hs & 1) * 8192));
    sv[1] = __builtin_bit_cast(u32x4, rd128(ldsT + u * 1024 + voffL, ((hs >> 1) & 3) * TSLOT + (hs & 1) * 8192 + 4096));
  };
  auto statAcc = [&](int hs, int c) {
#pragma unroll
    for (int i = 0; i < 2; i++) { const int k = (hs & 1) * 2 + i; s1[k] = dot2(sv[i][c], ONES, s1[k]); s2[k] = dot2(sv[i][c], sv[i][c], s2[k]); }
  };
  auto statFinish = [&]() {
    const u32 ax = lds0 + (18 & 3) * WSLOT;
    const bool o1 = gp & 1, o2 = gp & 2;
    double a[2], b[2];
#pragma unroll
    for (int i = 0; i < 2; i++) {
      a[i] = (double)(o1 ? s1[2 * i + 1] : s1[2 * i]) + (double)dppx<0xb1>(o1 ? s1[2 * i] : s1[2 * i + 1]);
      b[i] = (double)(o1 ? s2[2 * i + 1] : s2[2 * i]) + (double)dppx<0xb1>(o1 ? s2[2 * i] : s2[2 * i + 1]);
    }
    double A = (o2 ? a[1] : a[0]) + dppd<0x4e>(o2 ? a[0] : a[1]), B = (o2 ? b[1] : b[0]) + dppd<0x4e>(o2 ? b[0] : b[1]);
    A += dppd<0x1b>(dppd<0x141>(A)); B += dppd<0x1b>(dppd<0x141>(B));
    const double m = A * (1.0 / D);
    const float mean = (float)m, ve = fmaxf((float)(B * (1.0 / D) - m * m), 0.f) + 1e-5f;
    const float rstd = __builtin_amdgcn_rsqf(ve), sig = ve * rstd;
    float m1, m2, m3; split3(-mean, m1, m2, m3);
    const float g1 = rbf(sig), g2 = sig - g1;
    const int tk = R + (gp & 3) * 32;
    if (gp < 4) wr128(ax + AX_T + tk * 16, 0, (u32x4){cvtpk(m1, m1), cvtpk(m2, m2), cvtpk(m1, m3), cvtpk(g1, g2)});
    else { LAS float* sp = (LAS float*)(size_t)(ax + AX_S + tk * 4); sp[0] = mean; sp[128] = rstd; sp[256] = sig; }
  };
  const char* fAux = p.ws + WS_FLAGS + (FL_C + slice) * 128;
  const char* fP = p.ws + WS_FLAGS + (FL_P + rt * 15 + 3 * g + ((lane & 1) ? j1 : j2)) * 128;
  __builtin_amdgcn_s_setprio(3);
  issueA(0, nofill); issueX(0, 0); issueX(0, 1); issueA(1, nofill); issueX(1, 0); issueX(1, 1); issueA(2, nofill);
  u32 pv = 0;
  waitv<16>(); BAR();
  sfor<0, 37>([&](auto sc) {
    constexpr int s = decltype(sc)::value;
    constexpr int prev = s == 0 ? 10 : (s <= 14 ? 8 : (s == 15 ? 6 : (s == 16 ? 0 : (s == 17 ? (SSM ? 9 : 8) : (s <= 22 ? 6 : (s <= 34 ? 10 : 0))))));
    waitv<prev>();
    if constexpr (SSM && s == 19) { BAR(); if (u < 3) ssm_carry(p, rt, j, u * 64 + lane, lds0, tag); WAITL(); BAR(); }
    BAR();
    if constexpr (s == 20) { if (u == 0 && lane == 0) st_flag(p.ws, FL_P + rt * 15 + slice, tag); }
    if constexpr (s == 16) { asm volatile("" : "+v"(pv)); if (!__all(pv == tag)) spin_all(fAux, tag); issueAux(); }
    if constexpr (s == 22) { asm volatile("" : "+v"(pv)); if (!__all(pv == tag)) spin_all(fP, tag); }
    if constexpr (s == 15) pv = poll_issue(fAux);
    if constexpr (s == 21) pv = poll_issue(fP);
    if constexpr (s + 3 < 18) { statRead(s); SB(); issueA(s + 3, [&](int i) { if (i >= 1 && i <= 4) statAcc(s, i - 1); }); }
    else if constexpr (s + 3 > 18 && s + 3 < 37) issueB(s + 3);
    if constexpr ((s >> 1) + 2 < 9) issueX((s >> 1) + 2, s & 1);
    if constexpr (s == 15) {
#pragma unroll
      for (int hs = 15; hs < 18; hs++) { statRead(hs);
#pragma unroll
        for (int c = 0; c < 4; c++) statAcc(hs, c); }
    }
    if constexpr (s == 16) { statFinish(); WAITL(); }
  });
  BAR();
}

template <bool SSM, class OWN>
DI void phaseB(const Params& p, int rt, int g, int j, int w, int lane, u32 lds0, u32 tag, OWN&& own) {
  asm volatile("" : "+v"(lane));
  const int rg = w >> 2, cg = w & 3, q = lane >> 4, e = (lane >> 2) & 3, pp = lane & 3, jl = lane & 15;
  const int z = 2 * (q & 1) + (e >> 1);
  u32 wlo[3], whi[3], tb[2];
#pragma unroll
  for (int n = 0; n < 3; n++) {
    const int t = 3 * cg + n;
    wlo[n] = SSM ? lds0 + (16 * t + jl) * 128 + ((q ^ (jl >> 1)) * 16) : lds0 + (t >> 2) * 4096 + (4 * q + e) * 128 + (((t & 3) ^ z) * 32) + pp * 8;
    whi[n] = SSM ? (wlo[n] ^ 64) : wlo[n] + 2 * WSLOT;
  }
  tb[0] = SSM ? lds0 + TOFF + rg * 8192 + (q >> 1) * 2048 + (pp >> 1) * 1024 + (((8 * (q & 1) + e) * 16) ^ ((pp >> 1) * 64)) + 8 * (pp & 1) : lds0 + TOFF + rg * 4096 + lane * 16;
  tb[1] = tb[0] ^ 64;
  float mean = 0.f, rstd = 0.f;
  if constexpr (!SSM) { const u32 sa = lds0 + (18 & 3) * WSLOT + AX_S + (64 * rg + 16 * g + jl) * 4; mean = rdf(sa, 0); rstd = rdf(sa, 512); }
  f32x4 ob[3][4];
#pragma unroll
  for (int n = 0; n < 3; n++)
#pragma unroll
    for (int T = 0; T < 4; T++) ob[n][T] = (f32x4){0.f, 0.f, 0.f, 0.f};
  auto RW = [&](int sl, int kb, int n, int part) {
    if constexpr (SSM) return rd128((kb ? whi[n] : wlo[n]) + (sl >> 1) * 2 * WSLOT, (sl & 1) * WSLOT);
    else return rd_tr1(sl < 2 ? wlo[n] : whi[n], (sl & 1) * WSLOT + kb * 12288 + part * 2048);
  };
  auto RT = [&](int sl, int kb, int T, int part) {
    if constexpr (SSM) return rd_tr1(tb[part], sl * TSLOT + kb * 4096 + T * 256);
    else return rd128(tb[0], sl * TSLOT + kb * 8192 + T * 1024);
  };
  bf16x8 wf[3], tp[2];
  auto step = [&](auto slc, auto&& mid) {
    constexpr int SL = decltype(slc)::value, NS = (SL + 1) & 3;
    half<SSM ? 1 : 0>(ob, wf, tp, [&](int n, int part) { return RW(SL, 1, n, part); }, [&](int T, int part) { return RT(SL, 0, T, part); }, [&](int T, int part) { return RT(SL, 1, T, part); }, mid);
    half<SSM ? 1 : 0>(ob, wf, tp, [&](int n, int part) { return RW(NS, 0, n, part); }, [&](int T, int part) { return RT(SL, 1, T, part); }, [&](int T, int part) { return RT(NS, 0, T, part); }, [](int) {});
  };
  auto nop = [](int) {};
  using I0 = std::integral_constant<int, 0>; using I1 = std::integral_constant<int, 1>; using I2 = std::integral_constant<int, 2>; using I3 = std::integral_constant<int, 3>;
  WAITL(); BAR();
  STAMP(4);
#pragma unroll
  for (int n = 0; n < 3; n++) { if constexpr (SSM) wf[n] = RW(3, 0, n, 0); else wf[n] = cat(RW(3, 0, n, 0), RW(3, 0, n, 1)); }
#pragma unroll
  for (int T = 0; T < 2; T++) { if constexpr (SSM) tp[T] = cat(RT(3, 0, T, 0), RT(3, 0, T, 1)); else tp[T] = RT(3, 0, T, 0); }
  step(I3{}, [&](int i) { if (i == 11) own(3); });
  waitv<0>(); WAITL(); BAR(); step(I0{}, [&](int i) { if (i == 11) own(4); });
  WAITL(); BAR(); step(I1{}, [&](int i) { if (i == 11) own(5); });
  WAITL(); BAR(); step(I2{}, nop);
  BAR(); step(I3{}, nop);
  BAR(); step(I0{}, nop);
  const int tok = rt * 128 + 64 * rg + 16 * g + jl, col = 192 * j + 48 * cg + 4 * q;
  u32x2 xv[3], bv[3], dv[3];
  if constexpr (!SSM) {
#pragma unroll
    for (int n = 0; n < 3; n++) {
      xv[n] = *(const GAS u32x2*)(p.x + tok * D + col + 16 * n); bv[n] = *(const GAS u32x2*)(p.b2 + col + 16 * n); dv[n] = *(const GAS u32x2*)(p.dskip + col + 16 * n);
    }
  }
  SB();
  for (int it = 0; it < 3; it++) {
    BAR(); step(I1{}, nop); BAR(); step(I2{}, nop); BAR(); step(I3{}, nop); BAR(); step(I0{}, nop);
  }
  STAMP(5);
  const rsrc_t rWS = mk(p.ws, WS_END);
  const u32 xb = WS_XB + (rt * 3 + j) * 32 * XBW + w * XBW;
  f32x4 mine[3];
#pragma unroll
  for (int T = 0; T < 4; T++) {
    if (SSM || T != g) {
      const u32 o = xb + T * 8 * XBW + g * 1536;
      const u32x4 a = {cvtpk(ob[0][T][0], ob[0][T][1]), cvtpk(ob[0][T][2], ob[0][T][3]), cvtpk(ob[1][T][0], ob[1][T][1]), cvtpk(ob[1][T][2], ob[1][T][3])};
      const u32 b0 = cvtpk(ob[2][T][0], ob[2][T][1]), b1 = cvtpk(ob[2][T][2], ob[2][T][3]);
      const auto s0 = __builtin_amdgcn_permlane32_swap(b0, b0, false, false), s1 = __builtin_amdgcn_permlane32_swap(b1, b1, false, false);
      __builtin_amdgcn_raw_buffer_store_b128(a, rWS, lane * 16, o, 16);
      if (lane < 32) __builtin_amdgcn_raw_buffer_store_b128((u32x4){b0, b1, s0[1], s1[1]}, rWS, lane * 16, o + 1024, 16);
    } else {
#pragma unroll
      for (int n = 0; n < 3; n++) mine[n] = ob[n][T];
    }
  }
  if constexpr (SSM) {
    waitv<0>(); BAR();
    if (threadIdx.x == 0) st_flag(p.ws, FL_O + (rt * 3 + j) * 5 + g, tag);
    STAMP(6); STAMP(7);
  } else {
    waitv<0>(); BAR();
    if (threadIdx.x == 0) st_flag(p.ws, FL_O + (rt * 3 + j) * 5 + g, tag);
    STAMP(6);
    spin_all(p.ws + WS_FLAGS + (FL_O + (rt * 3 + j) * 5 + (lane % 5)) * 128, tag);
    const u32 o = xb + g * 8 * XBW;
    u32x4 a[4]; u32x2 b[4];
#pragma unroll
    for (int k = 0; k < 4; k++) {
      const int src = k == 3 ? 4 : (k < g ? k : k + 1);
      a[k] = __builtin_amdgcn_raw_buffer_load_b128(rWS, lane * 16, o + src * 1536, 16);
      b[k] = __builtin_amdgcn_raw_buffer_load_b64(rWS, (lane & 31) * 16 + (lane >> 5) * 8, o + src * 1536 + 1024, 16);
    }
#pragma unroll
    for (int n = 0; n < 3; n++) {
      f32x4 r;
#pragma unroll
      for (int c = 0; c < 4; c++) {
        float G = mine[n][c] + ((c & 1) ? bfhi(bv[n][c >> 1]) : bflo(bv[n][c >> 1]));
#pragma unroll
        for (int k = 0; k < 3; k++) { const u32 v = n < 2 ? a[k][2 * n + (c >> 1)] : b[k][c >> 1]; G += (c & 1) ? bfhi(v) : bflo(v); }
        const u32 yv = n < 2 ? a[3][2 * n + (c >> 1)] : b[3][c >> 1];
        const float yy = (c & 1) ? bfhi(yv) : bflo(yv);
        const float xx = (c & 1) ? bfhi(xv[n][c >> 1]) : bflo(xv[n][c >> 1]);
        const float dd = (c & 1) ? bfhi(dv[n][c >> 1]) : bflo(dv[n][c >> 1]);
        const float sv = yy + (xx - mean) * rstd * dd;
        const float sig = __builtin_amdgcn_rcpf(1.f + __builtin_amdgcn_exp2f(-1.4426950408889634f * G));
        r[c] = xx + sv * (G * sig);
      }
      *(GAS f32x4*)(p.out + tok * D + col + 16 * n) = r;
    }
    STAMP(7);
  }
}

DI void ffn_compute(const Params& p, int rt, int g, int j, int w, int lane, u32 lds0, u32 tag) {
  const int rg = w >> 2, cg = w & 3, q = lane >> 4, e = (lane >> 2) & 3, pp = lane & 3, jl = lane & 15;
  const int z = 2 * (q & 1) + (e >> 1);
  u32 wc[4];
#pragma unroll
  for (int n = 0; n < 4; n++) { const int t = 6 * cg + n; wc[n] = lds0 + (t >> 2) * 4096 + (4 * q + e) * 128 + (((t & 3) ^ z) * 32) + pp * 8; }
  const u32 xbase = lds0 + TOFF + (64 * rg + jl) * 128 + ((q ^ (jl >> 1)) * 16);
  f32x4 acc[6][4];
#pragma unroll
  for (int n = 0; n < 6; n++)
#pragma unroll
    for (int T = 0; T < 4; T++) { acc[n][T] = (f32x4){0.f, 0.f, 0.f, 0.f}; asm volatile("" : "+v"(acc[n][T])); }
  bf16x8 wp[2], t[4];
  BAR();
  STAMP(1);
  wp[0] = rd_tr(wc[0], 0); wp[1] = rd_tr(wc[1], 0);
#pragma unroll
  for (int T = 0; T < 4; T++) t[T] = rd128(xbase, T * 2048);
  u32 xs = 0; int dw = 2 * WSLOT;
#pragma nounroll
  for (int it = 0; it < 9; it++) {
    BAR(); stepA<0>(acc, wp, t, wc, 0, 0, (xbase ^ 64) + xs); xs = (xs + TSLOT) & (4 * TSLOT - 1);
    const u32 n0 = wc[0] + dw, n1 = wc[1] + dw;
    BAR(); stepA<1>(acc, wp, t, wc, n0, n1, xbase + xs);
    wc[0] = n0; wc[1] = n1; wc[2] += dw; wc[3] += dw; dw = -dw;
  }
  WAITL(); BAR();
  STAMP(2);
  const u32 ax = lds0 + (18 & 3) * WSLOT;
  float k1[4], k2[4], sg[4];
#pragma unroll
  for (int T = 0; T < 4; T++) {
    u32x4 v = __builtin_bit_cast(u32x4, rd128(ax + AX_T + (64 * rg + jl) * 16, T * 256));
    if (q != 0) v = (u32x4){0u, 0u, 0u, 0u};
    t[T] = __builtin_bit_cast(bf16x8, v);
    const float r = rdf(ax + AX_S + 512 + (64 * rg + jl) * 4, T * 64);
    sg[T] = rdf(ax + AX_S + 1024 + (64 * rg + jl) * 4, T * 64);
    k1[T] = -2.3022081985f * r; k2[T] = -0.10294324f * r * r * r;
  }
#pragma unroll
  for (int n = 0; n < 6; n++) {
    const bf16x8 a = rd128(ax + AX_W + (96 * cg + jl) * 16, n * 256);
#pragma unroll
    for (int T = 0; T < 4; T++) acc[n][T] = MFMA(a, t[T], acc[n][T]);
  }
  const rsrc_t rWS = mk(p.ws, WS_END);
  const u32 pg = WS_P + (rt * 15 + 3 * g + j) * 98304 + (cg >> 1) * 16384 + (cg & 1) * 8192 + rg * 4096;
  const u32 pl = lds0 + TOFF + (cg & 1) * 8192 + rg * 4096 + lane * 16;
  u32x4 P[3][4];
#pragma unroll
  for (int b = 0; b < 3; b++)
#pragma unroll
    for (int T = 0; T < 4; T++) {
      P[b][T] = gelu_frag(acc[2 * b][T], acc[2 * b + 1][T], k1[T], k2[T], sg[T]);
      __builtin_amdgcn_raw_buffer_store_b128(P[b][T], rWS, lane * 16, pg + b * 32768 + T * 1024, 16);
      SB();
    }
  auto own = [&](int i) {
    if ((cg >> 1) == (i & 1)) {
#pragma unroll
      for (int T = 0; T < 4; T++) wr128(pl, ((19 + i) & 3) * TSLOT + T * 1024, P[i >> 1][T]);
    }
  };
  own(0); own(1); own(2);
  STAMP(3);
  phaseB<false>(p, rt, g, j, w, lane, lds0, tag, own);
}

DI void ssm_compute(const Params& p, int rt, int j, int w, int lane, u32 lds0, u32 tag) {
  const int rg = w >> 2, cg = w & 3; int q = lane >> 4, jl = lane & 15;
  u32 wb = lds0 + (16 * cg + jl) * 128 + ((q ^ (jl >> 1)) * 16);
  const u32 xb0 = lds0 + TOFF + (64 * rg + jl) * 128 + ((q ^ (jl >> 1)) * 16);
  f32x4 ar[3][4], ai[3][4];
#pragma unroll
  for (int n = 0; n < 3; n++)
#pragma unroll
    for (int T = 0; T < 4; T++) { ar[n][T] = (f32x4){0.f, 0.f, 0.f, 0.f}; ai[n][T] = (f32x4){0.f, 0.f, 0.f, 0.f}; asm volatile("" : "+v"(ar[n][T]), "+v"(ai[n][T])); }
  bf16x8 wf[3], tp[2];
  auto nop = [](int) {};
  BAR();
  STAMP(1);
#pragma unroll
  for (int n = 0; n < 3; n++) wf[n] = rd128(wb, n * 8192);
  tp[0] = rd128(xb0, 0); tp[1] = rd128(xb0, 2048);
  u32 xs = 0; int dw = 2 * WSLOT;
#pragma nounroll
  for (int it = 0; it < 9; it++) {
    const u32 x0 = xb0 + xs, x1 = (xb0 ^ 64) + xs, wb1 = wb ^ 64, wn = wb + dw;
    xs = (xs + TSLOT) & (4 * TSLOT - 1);
    const u32 xn = xb0 + xs;
    BAR();
    half<2>(ar, wf, tp, [&](int n, int) { return rd128(wb1, n * 8192); }, [&](int T, int) { return rd128(x0, T * 2048); }, [&](int T, int) { return rd128(x1, T * 2048); }, nop);
    half<2>(ar, wf, tp, [&](int n, int) { return rd128(wb, WSLOT + n * 8192); }, [&](int T, int) { return rd128(x1, T * 2048); }, [&](int T, int) { return rd128(x0, T * 2048); }, nop);
    BAR();
    half<2>(ai, wf, tp, [&](int n, int) { return rd128(wb1, WSLOT + n * 8192); }, [&](int T, int) { return rd128(x0, T * 2048); }, [&](int T, int) { return rd128(x1, T * 2048); }, nop);
    half<2>(ai, wf, tp, [&](int n, int) { return rd128(wn, n * 8192); }, [&](int T, int) { return rd128(x1, T * 2048); }, [&](int T, int) { return rd128(xn, T * 2048); }, nop);
    wb = wn; dw = -dw;
  }
  WAITL(); BAR();
  STAMP(2);
  asm volatile("" : "+v"(lane));
  q = lane >> 4; jl = lane & 15;
  const u32 ax = lds0 + (18 & 3) * WSLOT;
  {
    bf16x8 t[4];
#pragma unroll
    for (int T = 0; T < 4; T++) {
      u32x4 v = __builtin_bit_cast(u32x4, rd128(ax + AX_T + (64 * rg + jl) * 16, T * 256));
      if (q != 0) v = (u32x4){0u, 0u, 0u, 0u};
      t[T] = __builtin_bit_cast(bf16x8, v);
    }
#pragma unroll
    for (int b = 0; b < 3; b++) {
      const bf16x8 a0 = rd128(ax + AX_W + (16 * cg + jl) * 16, b * 1024), a1 = rd128(ax + AX_W + (16 * cg + jl) * 16, 3072 + b * 1024);
#pragma unroll
      for (int T = 0; T < 4; T++) { ar[b][T] = MFMA(t[T], a0, ar[b][T]); ai[b][T] = MFMA(t[T], a1, ai[b][T]); }
    }
  }
  f32x4 lc[3];
  const u32 scr = lds0 + TOFF + (22 & 3) * TSLOT + ((4 * rg + q) * 192 + 16 * cg + jl) * 8;
  f32x2 e0[3];
#pragma unroll
  for (int T = 0; T < 4; T++) {
    const f32x4 r = rdf4(ax + AX_S + 512 + (64 * rg + 4 * q) * 4, T * 64);
#pragma unroll
    for (int b = 0; b < 3; b++) { ar[b][T] *= r; ai[b][T] *= r; }
  }
  f32x2 l8[3];
#pragma unroll
  for (int b = 0; b < 3; b++) {
    lc[b] = rdf4(ax + AX_TAB + (16 * cg + jl) * 32, b * 2048);
    l8[b] = *(LAS f32x2*)(size_t)(ax + AX_TAB + (16 * cg + jl) * 32 + b * 2048 + 16);
    f32x2 hr = {ar[b][0][0], ar[b][0][1]}, hi = {ai[b][0][0], ai[b][0][1]};
#pragma unroll
    for (int k = 1; k < 8; k++) {
      const f32x2 ur = {ar[b][k >> 1][(k & 1) * 2], ar[b][k >> 1][(k & 1) * 2 + 1]}, ui = {ai[b][k >> 1][(k & 1) * 2], ai[b][k >> 1][(k & 1) * 2 + 1]};
      const f32x2 nr = lc[b][0] * hr + (ur - lc[b][1] * hi), ni = lc[b][0] * hi + (ui + lc[b][1] * hr);
      hr = nr; hi = ni;
    }
    e0[b] = (f32x2){hr.x, hi.x};
    *(LAS f32x2*)(size_t)(scr + b * 512) = (f32x2){l8[b].x * hr.x - l8[b].y * hi.x + hr.y, l8[b].x * hi.x + l8[b].y * hr.x + hi.y};
  }
  STAMP(8);
  WAITL(); BAR();
  STAMP(9);
#pragma unroll
  for (int b = 0; b < 3; b++) {
    const float c2r = 2.f * lc[b][2], c2i = 2.f * lc[b][3];
#pragma unroll
    for (int T = 0; T < 4; T++) {
      const f32x4 nr = c2r * ar[b][T] - c2i * ai[b][T], ni = -c2r * ai[b][T] - c2i * ar[b][T];
      ar[b][T] = nr; ai[b][T] = ni;
      asm volatile("" : "+v"(ar[b][T]), "+v"(ai[b][T]));
    }
  }
  BAR();
  STAMP(10);
  const rsrc_t rWS = mk(p.ws, WS_END);
  const u32 io0 = w * 2048 + lane * 16, io1 = w * 2048 + 1024 + ((lane * 16) ^ 64);
  const u32 pg = WS_P + (rt * 15 + 12 + j) * 98304;
  u32x4 H[6][2];
#pragma unroll
  for (int b = 0; b < 3; b++) {
    const f32x2 cin = *(LAS f32x2*)(size_t)(scr + b * 512);
    const f32x2 sr = {cin.x, l8[b].x * cin.x - l8[b].y * cin.y + e0[b].x}, si = {cin.y, l8[b].x * cin.y + l8[b].y * cin.x + e0[b].y};
    const float c2r = 2.f * lc[b][2], c2i = 2.f * lc[b][3];
    f32x2 hr = c2r * sr - c2i * si, hi = -c2r * si - c2i * sr;
#pragma unroll
    for (int T = 0; T < 4; T++) {
      f32x2 pr[2], pi[2];
#pragma unroll
      for (int k = 0; k < 2; k++) {
        const f32x2 ur = {ar[b][T][2 * k], ar[b][T][2 * k + 1]}, ui = {ai[b][T][2 * k], ai[b][T][2 * k + 1]};
        const f32x2 nr = lc[b][0] * hr + (ur + lc[b][1] * hi), ni = lc[b][0] * hi + (ui - lc[b][1] * hr);
        hr = nr; hi = ni; pr[k] = nr; pi[k] = ni;
      }
      H[2 * b][0][T] = cvtpk(pr[0].x, pr[1].x); H[2 * b][1][T] = cvtpk(pr[0].y, pr[1].y);
      H[2 * b + 1][0][T] = cvtpk(pi[0].x, pi[1].x); H[2 * b + 1][1][T] = cvtpk(pi[0].y, pi[1].y);
    }
#pragma unroll
    for (int part = 0; part < 2; part++)
#pragma unroll
      for (int hh = 0; hh < 2; hh++) __builtin_amdgcn_raw_buffer_store_b128(H[2 * b + part][hh], rWS, hh ? io1 : io0, pg + (2 * b + part) * 16384, 16);
    SB();
  }
  auto own = [&](int i) { wr128(lds0 + TOFF + io0, ((19 + i) & 3) * TSLOT, H[i][0]); wr128(lds0 + TOFF + io1, ((19 + i) & 3) * TSLOT, H[i][1]); };
  own(0); own(1); own(2);
  STAMP(3);
  phaseB<true>(p, rt, 4, j, w, lane, lds0, tag, own);
}

DI void spare(const Params& p, int sid, u32 lds0, u32 tag) {
  const int tid = threadIdx.x;
  const rsrc_t rWS = mk(p.ws, WS_END);
  if (sid < 12) {
    const int cgp = tid % 48, rgp = tid / 48;
    const GAS u32x4* src = (const GAS u32x4*)(p.w1 + 2 * rgp * F + sid * 384) + cgp;
    float a[8];
#pragma unroll
    for (int c = 0; c < 8; c++) a[c] = 0.f;
    u32x4 v[36];
#pragma unroll
    for (int i = 0; i < 24; i++) v[i] = src[((i >> 1) * 32 + (i & 1)) * (F / 8)];
#pragma unroll
    for (int i = 0; i < 18; i++) {
#pragma unroll
      for (int c = 0; c < 4; c++) {
        a[2 * c] = dot2(__builtin_amdgcn_perm(v[2 * i + 1][c], v[2 * i][c], 0x05040100), ONES, a[2 * c]);
        a[2 * c + 1] = dot2(__builtin_amdgcn_perm(v[2 * i + 1][c], v[2 * i][c], 0x07060302), ONES, a[2 * c + 1]);
      }
      SB();
      if (i < 6) { v[24 + 2 * i] = src[((12 + i) * 32) * (F / 8)]; v[25 + 2 * i] = src[((12 + i) * 32 + 1) * (F / 8)]; }
      SB();
    }
    *(LAS f32x4*)(size_t)(lds0 + (rgp * 384 + cgp * 8) * 4) = (f32x4){a[0], a[1], a[2], a[3]};
    *(LAS f32x4*)(size_t)(lds0 + (rgp * 384 + cgp * 8) * 4 + 16) = (f32x4){a[4], a[5], a[6], a[7]};
    WAITL(); BAR();
    if (tid < 384) {
      float s = 0.f;
#pragma unroll
      for (int r = 0; r < 16; r++) s += rdf(lds0 + tid * 4, r * 1536);
      float c1, c2, c3; split3(s, c1, c2, c3);
      const float b = bf2f(p.b1[sid * 384 + tid]);
      __builtin_amdgcn_raw_buffer_store_b128((u32x4){cvtpk(c1, c2), cvtpk(c1, c2), cvtpk(c3, c1), cvtpk(b, b)}, rWS, tid * 16, WS_AUXW + sid * 6144, 16);
    }
  } else if (sid < 15) {
    const int row = tid >> 2, part = tid & 3;
#pragma unroll
    for (int h = 0; h < 2; h++) {
      const GAS u32x4* src = (const GAS u32x4*)((h ? p.bim : p.bre) + ((sid - 12) * 192 + row) * D) + part;
      u32x4 v[18];
#pragma unroll
      for (int i = 0; i < 18; i++) v[i] = src[4 * i];
      float s = 0.f, s2 = 0.f;
#pragma unroll
      for (int i = 0; i < 18; i++) { s = dot2(v[i][0], ONES, s); s2 = dot2(v[i][1], ONES, s2); s = dot2(v[i][2], ONES, s); s2 = dot2(v[i][3], ONES, s2); }
      s += s2; s += __shfl_xor(s, 1); s += __shfl_xor(s, 2);
      float c1, c2, c3; split3(s, c1, c2, c3);
      *(LAS u32x4*)(size_t)(lds0 + (h * 192 + row) * 16) = (u32x4){cvtpk(c1, c2), cvtpk(c1, c2), cvtpk(c3, c1), 0u};
    }
    if (tid < 192) {
      const int c = (sid - 12) * 192 + tid;
      const float lr = bf2f(p.lre[c]), li = bf2f(p.lim[c]);
      const float dt = rbf(expf(bf2f(p.logdt[c])));
      const float ex = expf(dt * lr), an = dt * li;
      const float br = ex * cosf(an), bi = ex * sinf(an);
      const float nr = br - 1.f, den = 1.f / (lr * lr + li * li);
      const float cr = (nr * lr + bi * li) * den, ci = (bi * lr - nr * li) * den;
      float ar = br, ai = bi, r16 = 0.f, i16 = 0.f;
#pragma unroll
      for (int k = 0; k < 7; k++) { const float t = ar * ar - ai * ai; ai = 2.f * ar * ai; ar = t; if (k == 2) { r16 = ar; i16 = ai; } }
      *(LAS f32x4*)(size_t)(lds0 + 6144 + tid * 32) = (f32x4){br, bi, cr, ci};
      *(LAS f32x4*)(size_t)(lds0 + 6144 + tid * 32 + 16) = (f32x4){r16, i16, ar, ai};
    }
    WAITL(); BAR();
    const u32x4 v = *(LAS u32x4*)(size_t)(lds0 + tid * 16);
    __builtin_amdgcn_raw_buffer_store_b128(v, rWS, tid < 384 ? WS_AUXW + sid * 6144 + tid * 16 : WS_TAB + (sid - 12) * 6144 + (tid - 384) * 16, 0, 16);
  }
  waitv<0>(); BAR();
  if (tid == 0 && sid < 15) st_flag(p.ws, FL_C + sid, tag);
  {
    u32 acc = 0;
    const GAS u32* w2 = (const GAS u32*)p.w2;
    if (tid < 648) {
#pragma unroll
      for (int i = 0; i < 4; i++) {
        const int L = (i * 16 + sid) * 648 + tid;
        const int r = L / 9, kb = r / 1536, rr = r - kb * 1536;
        acc ^= w2[((rr >> 5) * 96 + kb * 32 + (rr & 31)) * (ROWB / 4) + (L - r * 9) * 32];
      }
      if (tid < 324) acc ^= ((const GAS u32*)p.cre)[(sid * 324 + tid) * 32]; else acc ^= ((const GAS u32*)p.cim)[(sid * 324 + tid - 324) * 32];
    }
    asm volatile("" :: "v"(acc));
  }
}

__global__ __launch_bounds__(768, 3) void fused_kernel(Params p) {
  extern __shared__ __attribute__((aligned(16))) char smem[];
  const u32 lds0 = (u32)(size_t)(LAS char*)smem;
  const int b = blockIdx.x, xcd = b & 7, idx = b >> 3;
  const u32 tag = 1u;
  STAMP(0);
  if (idx >= 30) { spare(p, xcd * 2 + idx - 30, lds0, tag); STAMP(7); return; }
  const int tri = xcd * 10 + idx / 3, j = idx % 3, g = tri >> 4, rt = tri & 15;
  const int lane = threadIdx.x & 63, w = __builtin_amdgcn_readfirstlane(threadIdx.x >> 6);
  if (g < 4) {
    if (w >= 8) loader<false>(p, rt, g, j, w - 8, lane, lds0, tag); else ffn_compute(p, rt, g, j, w, lane, lds0, tag);
  } else {
    if (w >= 8) loader<true>(p, rt, g, j, w - 8, lane, lds0, tag); else ssm_compute(p, rt, j, w, lane, lds0, tag);
  }
}

extern "C" void kernel_launch(void* const* d_in, const int* in_sizes, int n_in, void* d_out, int out_size, void* d_ws, size_t ws_size, hipStream_t stream) {
  Params p;
  p.x = (const u16*)d_in[0]; p.lre = (const u16*)d_in[1]; p.lim = (const u16*)d_in[2]; p.bre = (const u16*)d_in[3]; p.bim = (const u16*)d_in[4];
  p.cre = (const u16*)d_in[5]; p.cim = (const u16*)d_in[6]; p.logdt = (const u16*)d_in[7]; p.dskip = (const u16*)d_in[8];
  p.w1 = (const u16*)d_in[10]; p.b1 = (const u16*)d_in[11]; p.w2 = (const u16*)d_in[12]; p.b2 = (const u16*)d_in[13];
  p.out = (float*)d_out; p.ws = (char*)d_ws;
  (void)hipFuncSetAttribute((const void*)fused_kernel, hipFuncAttributeMaxDynamicSharedMemorySize, LDS_BYTES);
  (void)hipMemsetAsync(d_ws, 0, WS_ZERO_BYTES, stream);
  fused_kernel<<<256, 768, LDS_BYTES, stream>>>(p);
}
```

```cpp
#define STAMPS
#define K0 0
#define A0 0
#define B0 0
#define K1 0
#define A1 0
#define B1 0
#include <hip/hip_runtime.h>
#include <utility>

#define LAS __attribute__((address_space(3)))
#define GAS __attribute__((address_space(1)))
#define DI __device__ __forceinline__
typedef unsigned short u16;
typedef unsigned u32;
typedef unsigned long long u64;
typedef unsigned u32x4 __attribute__((ext_vector_type(4)));
typedef unsigned u32x2 __attribute__((ext_vector_type(2)));
typedef short s16x4 __attribute__((ext_vector_type(4)));
typedef short bf16x8 __attribute__((ext_vector_type(8)));
typedef float f32x4 __attribute__((ext_vector_type(4)));
typedef float f32x2 __attribute__((ext_vector_type(2)));
typedef __bf16 bf16x2 __attribute__((ext_vector_type(2)));
typedef __amdgpu_buffer_rsrc_t rsrc_t;

constexpr int S = 2048, D = 576, F = 4608;
constexpr int ROWB = D * 2;
constexpr int WSLOT = 24576, TOFF = 4 * WSLOT, TSLOT = 16384, LDS_BYTES = 163840;
constexpr int AX_W = 0, AX_T = 6144, AX_S = 8192, AX_TAB = 10240;
constexpr u32 WS_FLAGS = 0;
constexpr u32 FL_S = 0, FL_C = 16, FL_P = 32, FL_O = 32 + 240, NFLAG = 32 + 480;
constexpr u32 WS_EG = NFLAG * 128;
constexpr u32 WS_ZERO_BYTES = WS_EG + 16 * 576 * 16;
constexpr u32 WS_AUXW = 256 * 1024;
constexpr u32 WS_TAB = WS_AUXW + 15 * 6144;
constexpr u32 WS_P = 1u << 20;
constexpr u32 XBW = 5 * 1536;
constexpr u32 WS_XB = WS_P + 240 * 98304;
constexpr u32 WS_END = WS_XB + 48 * 32 * XBW;
constexpr u32 SPIN_CAP = 1u << 18;

struct Params {
  const u16 *x, *lre, *lim, *bre, *bim, *cre, *cim, *logdt, *dskip, *w1, *b1, *w2, *b2;
  float* out; char* ws;
  u64* stamps;
};

template <int N> DI void waitv() { asm volatile("s_waitcnt vmcnt(%0)" :: "n"(N) : "memory"); }
#define WAITL() asm volatile("s_waitcnt lgkmcnt(0)" ::: "memory")
#define BAR() do { asm volatile("" ::: "memory"); __builtin_amdgcn_s_barrier(); asm volatile("" ::: "memory"); } while (0)
#define SB() __builtin_amdgcn_sched_barrier(0)
#define MFMA(a, b, c) __builtin_amdgcn_mfma_f32_16x16x32_bf16((a), (b), (c), 0, 0, 0)
#define STAMP(i) do { SB(); if (threadIdx.x == 0) p.stamps[blockIdx.x * 16 + (i)] = __builtin_amdgcn_s_memrealtime(); SB(); } while (0)

DI void dma(rsrc_t r, u32 lds, u32 voff, u32 soff) {
  asm volatile("s_mov_b32 m0, %0\n\ts_nop 0\n\tbuffer_load_dwordx4 %1, %2, %3 offen lds" :: "s"(lds), "v"(voff), "s"(r), "s"(soff) : "memory");
}
DI void dma_sc1(rsrc_t r, u32 lds, u32 voff, u32 soff) {
  asm volatile("s_mov_b32 m0, %0\n\ts_nop 0\n\tbuffer_load_dwordx4 %1, %2, %3 offen sc1 lds" :: "s"(lds), "v"(voff), "s"(r), "s"(soff) : "memory");
}
DI rsrc_t mk(const void* ptr, u32 bytes) { return __builtin_amdgcn_make_buffer_rsrc((void*)ptr, 0, bytes, 0x00020000); }
DI s16x4 rd_tr1(u32 a, int off) { return __builtin_amdgcn_ds_read_tr16_b64_v4i16((LAS s16x4*)(size_t)(a + off)); }
DI bf16x8 cat(s16x4 lo, s16x4 hi) { return __builtin_shufflevector(lo, hi, 0, 1, 2, 3, 4, 5, 6, 7); }
DI bf16x8 rd_tr(u32 a, int off) { return cat(rd_tr1(a, off), rd_tr1(a, off + 2048)); }
DI bf16x8 rd128(u32 a, int off) { return *(LAS bf16x8*)(size_t)(a + off); }
DI f32x4 rdf4(u32 a, int off) { return *(LAS f32x4*)(size_t)(a + off); }
DI float rdf(u32 a, int off) { return *(LAS float*)(size_t)(a + off); }
DI void wr128(u32 a, int off, u32x4 v) { *(LAS u32x4*)(size_t)(a + off) = v; }
DI u32 cvtpk(float lo, float hi) { f32x2 v = {lo, hi}; bf16x2 b = __builtin_convertvector(v, bf16x2); return __builtin_bit_cast(u32, b); }
DI float bflo(u32 v) { return __uint_as_float(v << 16); }
DI float bfhi(u32 v) { return __uint_as_float(v & 0xffff0000u); }
DI float bf2f(u16 v) { return __uint_as_float((u32)v << 16); }
DI float dot2(u32 a, u32 b, float c) { return __builtin_amdgcn_fdot2_f32_bf16(__builtin_bit_cast(bf16x2, a), __builtin_bit_cast(bf16x2, b), c, false); }
constexpr u32 ONES = 0x3f803f80u;
DI float rbf(float v) { return bflo(cvtpk(v, 0.f)); }
DI void split3(float v, float& a, float& b, float& c) { a = rbf(v); const float r = v - a; b = rbf(r); c = rbf(r - b); }
template <int C> DI float dppx(float v) { return __uint_as_float(__builtin_amdgcn_update_dpp(0u, __float_as_uint(v), C, 0xf, 0xf, true)); }
template <int C> DI double dppd(double v) { u32x2 t = __builtin_bit_cast(u32x2, v); t[0] = __builtin_amdgcn_update_dpp(0u, t[0], C, 0xf, 0xf, true); t[1] = __builtin_amdgcn_update_dpp(0u, t[1], C, 0xf, 0xf, true); return __builtin_bit_cast(double, t); }
DI void st_flag(char* ws, u32 idx, u32 tag) { __hip_atomic_store((GAS u32*)(ws + WS_FLAGS + idx * 128), tag, __ATOMIC_RELAXED, __HIP_MEMORY_SCOPE_AGENT); }
DI u32 ld_flag(const char* a) { return __hip_atomic_load((GAS u32*)a, __ATOMIC_RELAXED, __HIP_MEMORY_SCOPE_AGENT); }
DI void spin_all(const char* a, u32 tag) {
  for (u32 i = 0; i < SPIN_CAP; i++) { if (__all(ld_flag(a) == tag)) break; __builtin_amdgcn_s_sleep(1); }
  asm volatile("" ::: "memory");
}
DI u32 poll_issue(const char* a) { u32 v; asm volatile("global_load_dword %0, %1, off sc1" : "=v"(v) : "v"(a) : "memory"); return v; }

DI f32x2 gelu2(f32x2 a, float k1, float k2, float sg) {
  f32x2 t = a * a; t = t * k2 + k1; t = t * a;
  t.x = __builtin_amdgcn_exp2f(t.x); t.y = __builtin_amdgcn_exp2f(t.y);
  t = t * sg + sg;
  t.x = __builtin_amdgcn_rcpf(t.x); t.y = __builtin_amdgcn_rcpf(t.y);
  return t * a;
}
DI u32x4 gelu_frag(f32x4 a, f32x4 b, float k1, float k2, float sg) {
  f32x2 g0 = gelu2((f32x2){a[0], a[1]}, k1, k2, sg), g1 = gelu2((f32x2){a[2], a[3]}, k1, k2, sg);
  f32x2 g2 = gelu2((f32x2){b[0], b[1]}, k1, k2, sg), g3 = gelu2((f32x2){b[2], b[3]}, k1, k2, sg);
  return (u32x4){cvtpk(g0.x, g0.y), cvtpk(g1.x, g1.y), cvtpk(g2.x, g2.y), cvtpk(g3.x, g3.y)};
}

template <int H>
DI void stepA(f32x4 (&acc)[6][4], bf16x8 (&wp)[2], bf16x8 (&t)[4], const u32 (&wc)[4], u32 n0, u32 n1, u32 xn) {
  bf16x8 w[8], nx[4];
  w[0] = wp[0]; w[1] = wp[1];
#pragma unroll
  for (int n = 0; n < 6; n++) {
    const int f = n + 2, ff = f < 6 ? f : f - 6;
    const u32 a = (f < 6 || H == 0) ? wc[ff & 3] : (ff ? n1 : n0); const int off = (f < 6 ? H : 1 - H) * WSLOT + (ff >> 2) * 4096;
    acc[n][0] = MFMA(w[n], t[0], acc[n][0]); SB();
    s16x4 lo = rd_tr1(a, off); SB();
    acc[n][1] = MFMA(w[n], t[1], acc[n][1]); SB();
    s16x4 hi = rd_tr1(a, off + 2048); SB();
    acc[n][2] = MFMA(w[n], t[2], acc[n][2]); SB();
    if (n >= 1 && n <= 4) { nx[n - 1] = rd128(xn, (n - 1) * 2048); SB(); }
    acc[n][3] = MFMA(w[n], t[3], acc[n][3]); SB();
    w[f] = cat(lo, hi);
  }
  wp[0] = w[6]; wp[1] = w[7];
#pragma unroll
  for (int i = 0; i < 4; i++) t[i] = nx[i];
}

template <int KIND, class FW, class FT, class FN, class MID>
DI void half(f32x4 (&acc)[3][4], bf16x8 (&w)[3], bf16x8 (&tp)[2], FW&& rw, FT&& rt_, FN&& rn, MID&& mid) {
  bf16x8 t[4], wn[3], tn[2];
  t[0] = tp[0]; t[1] = tp[1];
  s16x4 lo;
#pragma unroll
  for (int i = 0; i < 12; i++) {
    const int T = i / 3, n = i % 3;
    if constexpr (KIND == 2) acc[n][T] = MFMA(t[T], w[n], acc[n][T]); else acc[n][T] = MFMA(w[n], t[T], acc[n][T]);
    SB();
    if constexpr (KIND == 0) {
      if (i == 0) t[2] = rt_(2, 0); if (i == 1) t[3] = rt_(3, 0);
      if (i >= 2 && i <= 7) { if ((i & 1) == 0) lo = rw((i - 2) >> 1, 0); else wn[(i - 2) >> 1] = cat(lo, rw((i - 2) >> 1, 1)); }
      if (i == 8) tn[0] = rn(0, 0); if (i == 9) tn[1] = rn(1, 0);
    } else if constexpr (KIND == 1) {
      if (i <= 3) { if ((i & 1) == 0) lo = rt_(2 + (i >> 1), 0); else t[2 + (i >> 1)] = cat(lo, rt_(2 + (i >> 1), 1)); }
      if (i >= 4 && i <= 6) wn[i - 4] = rw(i - 4, 0);
      if (i >= 7 && i <= 10) { if ((i & 1) == 1) lo = rn((i - 7) >> 1, 0); else tn[(i - 7) >> 1] = cat(lo, rn((i - 7) >> 1, 1)); }
    } else {
      if (i == 0) t[2] = rt_(2, 0); if (i == 2) t[3] = rt_(3, 0);
      if (i == 3 || i == 5 || i == 6) wn[i == 3 ? 0 : i - 4] = rw(i == 3 ? 0 : i - 4, 0);
      if (i == 8) tn[0] = rn(0, 0); if (i == 9) tn[1] = rn(1, 0);
    }
    mid(i);
    SB();
  }
#pragma unroll
  for (int i = 0; i < 3; i++) w[i] = wn[i];
  tp[0] = tn[0]; tp[1] = tn[1];
}

template <int I, int N, class FN> DI void sfor(FN&& f) { if constexpr (I < N) { f(std::integral_constant<int, I>{}); sfor<I + 1, N>(f); } }

DI void ssm_carry(const Params& p, int rt, int j, int c, u32 lds0, u32 tag) {
  const u32 ax = lds0 + (18 & 3) * WSLOT, scr = lds0 + TOFF + (22 & 3) * TSLOT;
  {
    f32x4 pw = rdf4(ax + AX_TAB + c * 32, 16);
    { const float t = pw[0] * pw[0] - pw[1] * pw[1]; pw[1] = 2.f * pw[0] * pw[1]; pw[0] = t; }
    f32x2 es[8];
#pragma unroll
    for (int s = 0; s < 8; s++) es[s] = *(LAS f32x2*)(size_t)(scr + (s * 192 + c) * 8);
    float sr = 0.f, si = 0.f;
#pragma unroll
    for (int s = 0; s < 8; s++) { const float nr = pw[0] * sr - pw[1] * si + es[s].x, ni = pw[0] * si + pw[1] * sr + es[s].y; sr = nr; si = ni; }
    const rsrc_t rWS = mk(p.ws, WS_END);
    __builtin_amdgcn_raw_buffer_store_b128((u32x4){__float_as_uint(sr), tag, __float_as_uint(si), tag}, rWS, (192 * j + c) * 16, WS_EG + rt * 576 * 16, 16);
    float cr = 0.f, ci = 0.f;
    for (u32 spin = 0; spin < SPIN_CAP; spin++) {
      u32x4 v[15]; bool ok = true;
#pragma unroll
      for (int i = 0; i < 15; i++) v[i] = __builtin_amdgcn_raw_buffer_load_b128(rWS, (192 * j + c) * 16, WS_EG + (i < rt ? i : 0) * 576 * 16, 16);
      cr = 0.f; ci = 0.f;
#pragma unroll
      for (int i = 0; i < 15; i++) {
        if (i < rt) {
          ok = ok && v[i][1] == tag && v[i][3] == tag;
          const float nr = pw[2] * cr - pw[3] * ci + __uint_as_float(v[i][0]), ni = pw[2] * ci + pw[3] * cr + __uint_as_float(v[i][2]); cr = nr; ci = ni;
        }
      }
      if (__all(ok)) break;
      __builtin_amdgcn_s_sleep(1);
    }
#pragma unroll
    for (int s = 0; s < 8; s++) {
      *(LAS f32x2*)(size_t)(scr + (s * 192 + c) * 8) = (f32x2){cr, ci};
      const float nr = pw[0] * cr - pw[1] * ci + es[s].x, ni = pw[0] * ci + pw[1] * cr + es[s].y; cr = nr; ci = ni;
    }
  }
}

template <bool SSM>
DI void loader(const Params& p, int rt, int g, int j, int u, int lane, u32 lds0, u32 tag) {
  const rsrc_t rX = mk(p.x, S * ROWB), rWS = mk(p.ws, WS_END);
  const rsrc_t rA0 = SSM ? mk(p.bre, D * ROWB) : mk(p.w1, D * F * 2), rA1 = SSM ? mk(p.bim, D * ROWB) : rA0;
  const rsrc_t rB0 = SSM ? mk(p.cre, D * ROWB) : mk(p.w2, D * F * 2), rB1 = SSM ? mk(p.cim, D * ROWB) : rB0;
  const int r8 = lane >> 3, gp = lane & 7;
  const int cgr = ((((gp >> 1) ^ ((r8 >> 1) & 3)) << 1) | (gp & 1));
  const int rgr = gp ^ (4 * (u & 1) + (r8 >> 1));
  const int slice = 3 * g + j;
  const int kA = 16 * (u & 1) + 8 * (r8 >> 2) + 4 * (u >> 1) + (r8 & 3);
  const u32 voffA = SSM ? (192 * j + 8 * u + r8) * ROWB + rgr * 16 : kA * (F * 2) + slice * 768 + cgr * 16;
  const u32 voffB = SSM ? voffA : (8 * u + r8) * ROWB + j * 384 + cgr * 16;
  const int R = 8 * u + r8;
  const int tok = SSM ? 16 * ((R >> 2) & 3) + 8 * (R & 1) + 2 * (R >> 4) + ((R >> 1) & 1) : R;
  const u32 voffX = (rt * 128 + tok) * ROWB + rgr * 16;
  const u32 voffL = lane * 16;
  const u32 ldsW = lds0 + u * 1024, ldsT = lds0 + TOFF;
  const int rot = rt % 9;
  const int j1 = j == 2 ? 0 : j + 1, j2 = j == 0 ? 2 : j - 1;
  auto issueA = [&](int s, auto&& fill) {
    int kt = (s >> 1) + rot; if (kt >= 9) kt -= 9;
    const u32 l = ldsW + (s & 3) * WSLOT;
    const u32 so = SSM ? kt * 128 : (kt * 64 + (s & 1) * 32) * (F * 2);
#pragma unroll
    for (int i = 0; i < 6; i++) { dma((s & 1) ? rA1 : rA0, l + i * 4096, voffA, so + i * (SSM ? 32 * ROWB : 128)); SB(); fill(i); SB(); }
  };
  auto nofill = [](int) {};
  auto issueX = [&](int kap, int h) {
    int kt = kap + rot; if (kt >= 9) kt -= 9;
    const u32 l = ldsT + (kap & 3) * TSLOT + h * 8192 + u * 1024, so = h * 64 * ROWB + kt * 128;
    dma(rX, l, voffX, so); dma(rX, l + 4096, voffX, so + (SSM ? 4 : 32) * ROWB);
  };
  auto issueB = [&](int s) {
    const int i = s < 25 ? s - 19 : (s - 25) >> 1;
    const int jj = s < 25 ? j : (((s - 25) & 1) ? j2 : j1);
    const u32 l = ldsW + (s & 3) * WSLOT;
    if (SSM) {
      const u32 so = (192 * jj + 64 * (i >> 1)) * 2;
#pragma unroll
      for (int k = 0; k < 6; k++) dma((i & 1) ? rB1 : rB0, l + k * 4096, voffB, k * 32 * ROWB + so);
    } else {
      const u32 so = ((3 * g + jj) * 384 + 192 * (i & 1) + 32 * (i >> 1)) * ROWB;
#pragma unroll
      for (int k = 0; k < 6; k++) dma(rB0, l + k * 4096, voffB, so + (k / 3) * 96 * ROWB + (k % 3) * 128);
    }
    if (s >= 25) {
      const u32 sp = WS_P + (rt * 15 + 3 * g + jj) * 98304 + i * 16384 + u * 4096, lp = ldsT + (s & 3) * TSLOT + u * 4096;
#pragma unroll
      for (int k = 0; k < 4; k++) dma_sc1(rWS, lp + k * 1024, voffL, sp + k * 1024);
    }
  };
  auto issueAux = [&]() {
    const u32 l = lds0 + (18 & 3) * WSLOT;
#pragma unroll
    for (int k = 0; k < (SSM ? 3 : 2); k++) {
      int pc = SSM ? u * 3 + k : u * 2 + k; if (!SSM && pc >= 6) pc -= 6;
      const u32 so = pc < 6 ? WS_AUXW + slice * 6144 + pc * 1024 : WS_TAB + j * 6144 + (pc - 6) * 1024;
      dma_sc1(rWS, l + (pc < 6 ? AX_W + pc * 1024 : AX_TAB + (pc - 6) * 1024), voffL, so);
    }
  };
  float s1[4] = {0.f, 0.f, 0.f, 0.f}, s2[4] = {0.f, 0.f, 0.f, 0.f};
  u32x4 sv[2];
  auto statRead = [&](int hs) {
    sv[0] = __builtin_bit_cast(u32x4, rd128(ldsT + u * 1024 + voffL, ((hs >> 1) & 3) * TSLOT + (hs & 1) * 8192));
    sv[1] = __builtin_bit_cast(u32x4, rd128(ldsT + u * 1024 + voffL, ((hs >> 1) & 3) * TSLOT + (hs & 1) * 8192 + 4096));
  };
  auto statAcc = [&](int hs, int c) {
#pragma unroll
    for (int i = 0; i < 2; i++) { const int k = (hs & 1) * 2 + i; s1[k] = dot2(sv[i][c], ONES, s1[k]); s2[k] = dot2(sv[i][c], sv[i][c], s2[k]); }
  };
  auto statFinish = [&]() {
    const u32 ax = lds0 + (18 & 3) * WSLOT;
    const bool o1 = gp & 1, o2 = gp & 2;
    double a[2], b[2];
#pragma unroll
    for (int i = 0; i < 2; i++) {
      a[i] = (double)(o1 ? s1[2 * i + 1] : s1[2 * i]) + (double)dppx<0xb1>(o1 ? s1[2 * i] : s1[2 * i + 1]);
      b[i] = (double)(o1 ? s2[2 * i + 1] : s2[2 * i]) + (double)dppx<0xb1>(o1 ? s2[2 * i] : s2[2 * i + 1]);
    }
    double A = (o2 ? a[1] : a[0]) + dppd<0x4e>(o2 ? a[0] : a[1]), B = (o2 ? b[1] : b[0]) + dppd<0x4e>(o2 ? b[0] : b[1]);
    A += dppd<0x1b>(dppd<0x141>(A)); B += dppd<0x1b>(dppd<0x141>(B));
    const double m = A * (1.0 / D);
    const float mean = (float)m, ve = fmaxf((float)(B * (1.0 / D) - m * m), 0.f) + 1e-5f;
    const float rstd = __builtin_amdgcn_rsqf(ve), sig = ve * rstd;
    float m1, m2, m3; split3(-mean, m1, m2, m3);
    const float g1 = rbf(sig), g2 = sig - g1;
    const int tk = R + (gp & 3) * 32;
    if (gp < 4) wr128(ax + AX_T + tk * 16, 0, (u32x4){cvtpk(m1, m1), cvtpk(m2, m2), cvtpk(m1, m3), cvtpk(g1, g2)});
    else { LAS float* sp = (LAS float*)(size_t)(ax + AX_S + tk * 4); sp[0] = mean; sp[128] = rstd; sp[256] = sig; }
  };
  const char* fAux = p.ws + WS_FLAGS + (FL_C + slice) * 128;
  const char* fP = p.ws + WS_FLAGS + (FL_P + rt * 15 + 3 * g + ((lane & 1) ? j1 : j2)) * 128;
  __builtin_amdgcn_s_setprio(3);
  issueA(0, nofill); issueX(0, 0); issueX(0, 1); issueA(1, nofill); issueX(1, 0); issueX(1, 1); issueA(2, nofill);
  u32 pv = 0;
  waitv<16>(); BAR();
  sfor<0, 37>([&](auto sc) {
    constexpr int s = decltype(sc)::value;
    constexpr int prev = s == 0 ? 10 : (s <= 14 ? 8 : (s == 15 ? 6 : (s == 16 ? 0 : (s == 17 ? (SSM ? 9 : 8) : (s <= 22 ? 6 : (s <= 34 ? 10 : 0))))));
    waitv<prev>();
    if constexpr (SSM && s == 19) { BAR(); if (u < 3) ssm_carry(p, rt, j, u * 64 + lane, lds0, tag); WAITL(); BAR(); }
    BAR();
    if constexpr (s == 20) { if (u == 0 && lane == 0) st_flag(p.ws, FL_P + rt * 15 + slice, tag); }
    if constexpr (s == 16) { asm volatile("" : "+v"(pv)); if (!__all(pv == tag)) spin_all(fAux, tag); issueAux(); }
    if constexpr (s == 22) { asm volatile("" : "+v"(pv)); if (!__all(pv == tag)) spin_all(fP, tag); }
    if constexpr (s == 15) pv = poll_issue(fAux);
    if constexpr (s == 21) pv = poll_issue(fP);
    if constexpr (s + 3 < 18) { statRead(s); SB(); issueA(s + 3, [&](int i) { if (i >= 1 && i <= 4) statAcc(s, i - 1); }); }
    else if constexpr (s + 3 > 18 && s + 3 < 37) issueB(s + 3);
    if constexpr ((s >> 1) + 2 < 9) issueX((s >> 1) + 2, s & 1);
    if constexpr (s == 15) {
#pragma unroll
      for (int hs = 15; hs < 18; hs++) { statRead(hs);
#pragma unroll
        for (int c = 0; c < 4; c++) statAcc(hs, c); }
    }
    if constexpr (s == 16) { statFinish(); WAITL(); }
  });
  BAR();
}

template <bool SSM, class OWN>
DI void phaseB(const Params& p, int rt, int g, int j, int w, int lane, u32 lds0, u32 tag, OWN&& own) {
  asm volatile("" : "+v"(lane));
  const int rg = w >> 2, cg = w & 3, q = lane >> 4, e = (lane >> 2) & 3, pp = lane & 3, jl = lane & 15;
  const int z = 2 * (q & 1) + (e >> 1);
  u32 wlo[3], whi[3], tb[2];
#pragma unroll
  for (int n = 0; n < 3; n++) {
    const int t = 3 * cg + n;
    wlo[n] = SSM ? lds0 + (16 * t + jl) * 128 + ((q ^ (jl >> 1)) * 16) : lds0 + (t >> 2) * 4096 + (4 * q + e) * 128 + (((t & 3) ^ z) * 32) + pp * 8;
    whi[n] = SSM ? (wlo[n] ^ 64) : wlo[n] + 2 * WSLOT;
  }
  tb[0] = SSM ? lds0 + TOFF + rg * 8192 + (q >> 1) * 2048 + (pp >> 1) * 1024 + (((8 * (q & 1) + e) * 16) ^ ((pp >> 1) * 64)) + 8 * (pp & 1) : lds0 + TOFF + rg * 4096 + lane * 16;
  tb[1] = tb[0] ^ 64;
  float mean = 0.f, rstd = 0.f;
  if constexpr (!SSM) { const u32 sa = lds0 + (18 & 3) * WSLOT + AX_S + (64 * rg + 16 * g + jl) * 4; mean = rdf(sa, 0); rstd = rdf(sa, 512); }
  f32x4 ob[3][4];
#pragma unroll
  for (int n = 0; n < 3; n++)
#pragma unroll
    for (int T = 0; T < 4; T++) ob[n][T] = (f32x4){0.f, 0.f, 0.f, 0.f};
  auto RW = [&](int sl, int kb, int n, int part) {
    if constexpr (SSM) return rd128((kb ? whi[n] : wlo[n]) + (sl >> 1) * 2 * WSLOT, (sl & 1) * WSLOT);
    else return rd_tr1(sl < 2 ? wlo[n] : whi[n], (sl & 1) * WSLOT + kb * 12288 + part * 2048);
  };
  auto RT = [&](int sl, int kb, int T, int part) {
    if constexpr (SSM) return rd_tr1(tb[part], sl * TSLOT + kb * 4096 + T * 256);
    else return rd128(tb[0], sl * TSLOT + kb * 8192 + T * 1024);
  };
  bf16x8 wf[3], tp[2];
  auto step = [&](auto slc, auto&& mid) {
    constexpr int SL = decltype(slc)::value, NS = (SL + 1) & 3;
    half<SSM ? 1 : 0>(ob, wf, tp, [&](int n, int part) { return RW(SL, 1, n, part); }, [&](int T, int part) { return RT(SL, 0, T, part); }, [&](int T, int part) { return RT(SL, 1, T, part); }, mid);
    half<SSM ? 1 : 0>(ob, wf, tp, [&](int n, int part) { return RW(NS, 0, n, part); }, [&](int T, int part) { return RT(SL, 1, T, part); }, [&](int T, int part) { return RT(NS, 0, T, part); }, [](int) {});
  };
  auto nop = [](int) {};
  using I0 = std::integral_constant<int, 0>; using I1 = std::integral_constant<int, 1>; using I2 = std::integral_constant<int, 2>; using I3 = std::integral_constant<int, 3>;
  WAITL(); BAR();
  STAMP(4);
#pragma unroll
  for (int n = 0; n < 3; n++) { if constexpr (SSM) wf[n] = RW(3, 0, n, 0); else wf[n] = cat(RW(3, 0, n, 0), RW(3, 0, n, 1)); }
#pragma unroll
  for (int T = 0; T < 2; T++) { if constexpr (SSM) tp[T] = cat(RT(3, 0, T, 0), RT(3, 0, T, 1)); else tp[T] = RT(3, 0, T, 0); }
  step(I3{}, [&](int i) { if (i == 11) own(3); });
  waitv<0>(); WAITL(); BAR(); step(I0{}, [&](int i) { if (i == 11) own(4); });
  WAITL(); BAR(); step(I1{}, [&](int i) { if (i == 11) own(5); });
  WAITL(); BAR(); step(I2{}, nop);
  BAR(); step(I3{}, nop);
  BAR(); step(I0{}, nop);
  const int tok = rt * 128 + 64 * rg + 16 * g + jl, col = 192 * j + 48 * cg + 4 * q;
  u32x2 xv[3], bv[3], dv[3];
  if constexpr (!SSM) {
#pragma unroll
    for (int n = 0; n < 3; n++) {
      xv[n] = *(const GAS u32x2*)(p.x + tok * D + col + 16 * n); bv[n] = *(const GAS u32x2*)(p.b2 + col + 16 * n); dv[n] = *(const GAS u32x2*)(p.dskip + col + 16 * n);
    }
  }
  SB();
  for (int it = 0; it < 3; it++) {
    BAR(); step(I1{}, nop); BAR(); step(I2{}, nop); BAR(); step(I3{}, nop); BAR(); step(I0{}, nop);
  }
  STAMP(5);
  const rsrc_t rWS = mk(p.ws, WS_END);
  const u32 xb = WS_XB + (rt * 3 + j) * 32 * XBW + w * XBW;
  f32x4 mine[3];
#pragma unroll
  for (int T = 0; T < 4; T++) {
    if (SSM || T != g) {
      const u32 o = xb + T * 8 * XBW + g * 1536;
      const u32x4 a = {cvtpk(ob[0][T][0], ob[0][T][1]), cvtpk(ob[0][T][2], ob[0][T][3]), cvtpk(ob[1][T][0], ob[1][T][1]), cvtpk(ob[1][T][2], ob[1][T][3])};
      const u32 b0 = cvtpk(ob[2][T][0], ob[2][T][1]), b1 = cvtpk(ob[2][T][2], ob[2][T][3]);
      const auto s0 = __builtin_amdgcn_permlane32_swap(b0, b0, false, false), s1 = __builtin_amdgcn_permlane32_swap(b1, b1, false, false);
      __builtin_amdgcn_raw_buffer_store_b128(a, rWS, lane * 16, o, 16);
      if (lane < 32) __builtin_amdgcn_raw_buffer_store_b128((u32x4){b0, b1, s0[1], s1[1]}, rWS, lane * 16, o + 1024, 16);
    } else {
#pragma unroll
      for (int n = 0; n < 3; n++) mine[n] = ob[n][T];
    }
  }
  if constexpr (SSM) {
    waitv<0>(); BAR();
    if (threadIdx.x == 0) st_flag(p.ws, FL_O + (rt * 3 + j) * 5 + g, tag);
    STAMP(6); STAMP(7);
  } else {
    waitv<0>(); BAR();
    if (threadIdx.x == 0) st_flag(p.ws, FL_O + (rt * 3 + j) * 5 + g, tag);
    STAMP(6);
    spin_all(p.ws + WS_FLAGS + (FL_O + (rt * 3 + j) * 5 + (lane % 5)) * 128, tag);
    const u32 o = xb + g * 8 * XBW;
    u32x4 a[4]; u32x2 b[4];
#pragma unroll
    for (int k = 0; k < 4; k++) {
      const int src = k == 3 ? 4 : (k < g ? k : k + 1);
      a[k] = __builtin_amdgcn_raw_buffer_load_b128(rWS, lane * 16, o + src * 1536, 16);
      b[k] = __builtin_amdgcn_raw_buffer_load_b64(rWS, (lane & 31) * 16 + (lane >> 5) * 8, o + src * 1536 + 1024, 16);
    }
#pragma unroll
    for (int n = 0; n < 3; n++) {
      f32x4 r;
#pragma unroll
      for (int c = 0; c < 4; c++) {
        float G = mine[n][c] + ((c & 1) ? bfhi(bv[n][c >> 1]) : bflo(bv[n][c >> 1]));
#pragma unroll
        for (int k = 0; k < 3; k++) { const u32 v = n < 2 ? a[k][2 * n + (c >> 1)] : b[k][c >> 1]; G += (c & 1) ? bfhi(v) : bflo(v); }
        const u32 yv = n < 2 ? a[3][2 * n + (c >> 1)] : b[3][c >> 1];
        const float yy = (c & 1) ? bfhi(yv) : bflo(yv);
        const float xx = (c & 1) ? bfhi(xv[n][c >> 1]) : bflo(xv[n][c >> 1]);
        const float dd = (c & 1) ? bfhi(dv[n][c >> 1]) : bflo(dv[n][c >> 1]);
        const float sv = yy + (xx - mean) * rstd * dd;
        const float sig = __builtin_amdgcn_rcpf(1.f + __builtin_amdgcn_exp2f(-1.4426950408889634f * G));
        r[c] = xx + sv * (G * sig);
      }
      *(GAS f32x4*)(p.out + tok * D + col + 16 * n) = r;
    }
    STAMP(7);
  }
}

DI void ffn_compute(const Params& p, int rt, int g, int j, int w, int lane, u32 lds0, u32 tag) {
  const int rg = w >> 2, cg = w & 3, q = lane >> 4, e = (lane >> 2) & 3, pp = lane & 3, jl = lane & 15;
  const int z = 2 * (q & 1) + (e >> 1);
  u32 wc[4];
#pragma unroll
  for (int n = 0; n < 4; n++) { const int t = 6 * cg + n; wc[n] = lds0 + (t >> 2) * 4096 + (4 * q + e) * 128 + (((t & 3) ^ z) * 32) + pp * 8; }
  const u32 xbase = lds0 + TOFF + (64 * rg + jl) * 128 + ((q ^ (jl >> 1)) * 16);
  f32x4 acc[6][4];
#pragma unroll
  for (int n = 0; n < 6; n++)
#pragma unroll
    for (int T = 0; T < 4; T++) { acc[n][T] = (f32x4){0.f, 0.f, 0.f, 0.f}; asm volatile("" : "+v"(acc[n][T])); }
  bf16x8 wp[2], t[4];
  BAR();
  STAMP(1);
  wp[0] = rd_tr(wc[0], 0); wp[1] = rd_tr(wc[1], 0);
#pragma unroll
  for (int T = 0; T < 4; T++) t[T] = rd128(xbase, T * 2048);
  u32 xs = 0; int dw = 2 * WSLOT;
#pragma nounroll
  for (int it = 0; it < 9; it++) {
    if (it == 2) STAMP(8); if (it == 4) STAMP(9); if (it == 7) STAMP(10); if (it == 8) STAMP(11);
    BAR(); stepA<0>(acc, wp, t, wc, 0, 0, (xbase ^ 64) + xs); xs = (xs + TSLOT) & (4 * TSLOT - 1);
    const u32 n0 = wc[0] + dw, n1 = wc[1] + dw;
    BAR(); stepA<1>(acc, wp, t, wc, n0, n1, xbase + xs);
    wc[0] = n0; wc[1] = n1; wc[2] += dw; wc[3] += dw; dw = -dw;
  }
  WAITL(); BAR();
  STAMP(2);
  const u32 ax = lds0 + (18 & 3) * WSLOT;
  float k1[4], k2[4], sg[4];
#pragma unroll
  for (int T = 0; T < 4; T++) {
    u32x4 v = __builtin_bit_cast(u32x4, rd128(ax + AX_T + (64 * rg + jl) * 16, T * 256));
    if (q != 0) v = (u32x4){0u, 0u, 0u, 0u};
    t[T] = __builtin_bit_cast(bf16x8, v);
    const float r = rdf(ax + AX_S + 512 + (64 * rg + jl) * 4, T * 64);
    sg[T] = rdf(ax + AX_S + 1024 + (64 * rg + jl) * 4, T * 64);
    k1[T] = -2.3022081985f * r; k2[T] = -0.10294324f * r * r * r;
  }
#pragma unroll
  for (int n = 0; n < 6; n++) {
    const bf16x8 a = rd128(ax + AX_W + (96 * cg + jl) * 16, n * 256);
#pragma unroll
    for (int T = 0; T < 4; T++) acc[n][T] = MFMA(a, t[T], acc[n][T]);
  }
  const rsrc_t rWS = mk(p.ws, WS_END);
  const u32 pg = WS_P + (rt * 15 + 3 * g + j) * 98304 + (cg >> 1) * 16384 + (cg & 1) * 8192 + rg * 4096;
  const u32 pl = lds0 + TOFF + (cg & 1) * 8192 + rg * 4096 + lane * 16;
  u32x4 P[3][4];
#pragma unroll
  for (int b = 0; b < 3; b++)
#pragma unroll
    for (int T = 0; T < 4; T++) {
      P[b][T] = gelu_frag(acc[2 * b][T], acc[2 * b + 1][T], k1[T], k2[T], sg[T]);
      __builtin_amdgcn_raw_buffer_store_b128(P[b][T], rWS, lane * 16, pg + b * 32768 + T * 1024, 16);
      SB();
    }
  auto own = [&](int i) {
    if ((cg >> 1) == (i & 1)) {
#pragma unroll
      for (int T = 0; T < 4; T++) wr128(pl, ((19 + i) & 3) * TSLOT + T * 1024, P[i >> 1][T]);
    }
  };
  own(0); own(1); own(2);
  STAMP(3);
  phaseB<false>(p, rt, g, j, w, lane, lds0, tag, own);
}

DI void ssm_compute(const Params& p, int rt, int j, int w, int lane, u32 lds0, u32 tag) {
  const int rg = w >> 2, cg = w & 3; int q = lane >> 4, jl = lane & 15;
  u32 wb = lds0 + (16 * cg + jl) * 128 + ((q ^ (jl >> 1)) * 16);
  const u32 xb0 = lds0 + TOFF + (64 * rg + jl) * 128 + ((q ^ (jl >> 1)) * 16);
  f32x4 ar[3][4], ai[3][4];
#pragma unroll
  for (int n = 0; n < 3; n++)
#pragma unroll
    for (int T = 0; T < 4; T++) { ar[n][T] = (f32x4){0.f, 0.f, 0.f, 0.f}; ai[n][T] = (f32x4){0.f, 0.f, 0.f, 0.f}; asm volatile("" : "+v"(ar[n][T]), "+v"(ai[n][T])); }
  bf16x8 wf[3], tp[2];
  auto nop = [](int) {};
  BAR();
  STAMP(1);
#pragma unroll
  for (int n = 0; n < 3; n++) wf[n] = rd128(wb, n * 8192);
  tp[0] = rd128(xb0, 0); tp[1] = rd128(xb0, 2048);
  u32 xs = 0; int dw = 2 * WSLOT;
#pragma nounroll
  for (int it = 0; it < 9; it++) {
    const u32 x0 = xb0 + xs, x1 = (xb0 ^ 64) + xs, wb1 = wb ^ 64, wn = wb + dw;
    xs = (xs + TSLOT) & (4 * TSLOT - 1);
    const u32 xn = xb0 + xs;
    BAR();
    half<2>(ar, wf, tp, [&](int n, int) { return rd128(wb1, n * 8192); }, [&](int T, int) { return rd128(x0, T * 2048); }, [&](int T, int) { return rd128(x1, T * 2048); }, nop);
    half<2>(ar, wf, tp, [&](int n, int) { return rd128(wb, WSLOT + n * 8192); }, [&](int T, int) { return rd128(x1, T * 2048); }, [&](int T, int) { return rd128(x0, T * 2048); }, nop);
    BAR();
    half<2>(ai, wf, tp, [&](int n, int) { return rd128(wb1, WSLOT + n * 8192); }, [&](int T, int) { return rd128(x0, T * 2048); }, [&](int T, int) { return rd128(x1, T * 2048); }, nop);
    half<2>(ai, wf, tp, [&](int n, int) { return rd128(wn, n * 8192); }, [&](int T, int) { return rd128(x1, T * 2048); }, [&](int T, int) { return rd128(xn, T * 2048); }, nop);
    wb = wn; dw = -dw;
  }
  WAITL(); BAR();
  STAMP(2);
  asm volatile("" : "+v"(lane));
  q = lane >> 4; jl = lane & 15;
  const u32 ax = lds0 + (18 & 3) * WSLOT;
  {
    bf16x8 t[4];
#pragma unroll
    for (int T = 0; T < 4; T++) {
      u32x4 v = __builtin_bit_cast(u32x4, rd128(ax + AX_T + (64 * rg + jl) * 16, T * 256));
      if (q != 0) v = (u32x4){0u, 0u, 0u, 0u};
      t[T] = __builtin_bit_cast(bf16x8, v);
    }
#pragma unroll
    for (int b = 0; b < 3; b++) {
      const bf16x8 a0 = rd128(ax + AX_W + (16 * cg + jl) * 16, b * 1024), a1 = rd128(ax + AX_W + (16 * cg + jl) * 16, 3072 + b * 1024);
#pragma unroll
      for (int T = 0; T < 4; T++) { ar[b][T] = MFMA(t[T], a0, ar[b][T]); ai[b][T] = MFMA(t[T], a1, ai[b][T]); }
    }
  }
  f32x4 lc[3];
  const u32 scr = lds0 + TOFF + (22 & 3) * TSLOT + ((4 * rg + q) * 192 + 16 * cg + jl) * 8;
  f32x2 e0[3];
#pragma unroll
  for (int T = 0; T < 4; T++) {
    const f32x4 r = rdf4(ax + AX_S + 512 + (64 * rg + 4 * q) * 4, T * 64);
#pragma unroll
    for (int b = 0; b < 3; b++) { ar[b][T] *= r; ai[b][T] *= r; }
  }
  f32x2 l8[3];
#pragma unroll
  for (int b = 0; b < 3; b++) {
    lc[b] = rdf4(ax + AX_TAB + (16 * cg + jl) * 32, b * 2048);
    l8[b] = *(LAS f32x2*)(size_t)(ax + AX_TAB + (16 * cg + jl) * 32 + b * 2048 + 16);
    f32x2 hr = {ar[b][0][0], ar[b][0][1]}, hi = {ai[b][0][0], ai[b][0][1]};
#pragma unroll
    for (int k = 1; k < 8; k++) {
      const f32x2 ur = {ar[b][k >> 1][(k & 1) * 2], ar[b][k >> 1][(k & 1) * 2 + 1]}, ui = {ai[b][k >> 1][(k & 1) * 2], ai[b][k >> 1][(k & 1) * 2 + 1]};
      const f32x2 nr = lc[b][0] * hr + (ur - lc[b][1] * hi), ni = lc[b][0] * hi + (ui + lc[b][1] * hr);
      hr = nr; hi = ni;
    }
    e0[b] = (f32x2){hr.x, hi.x};
    *(LAS f32x2*)(size_t)(scr + b * 512) = (f32x2){l8[b].x * hr.x - l8[b].y * hi.x + hr.y, l8[b].x * hi.x + l8[b].y * hr.x + hi.y};
  }
  STAMP(8);
  WAITL(); BAR();
  STAMP(9);
#pragma unroll
  for (int b = 0; b < 3; b++) {
    const float c2r = 2.f * lc[b][2], c2i = 2.f * lc[b][3];
#pragma unroll
    for (int T = 0; T < 4; T++) {
      const f32x4 nr = c2r * ar[b][T] - c2i * ai[b][T], ni = -c2r * ai[b][T] - c2i * ar[b][T];
      ar[b][T] = nr; ai[b][T] = ni;
      asm volatile("" : "+v"(ar[b][T]), "+v"(ai[b][T]));
    }
  }
  BAR();
  STAMP(10);
  const rsrc_t rWS = mk(p.ws, WS_END);
  const u32 io0 = w * 2048 + lane * 16, io1 = w * 2048 + 1024 + ((lane * 16) ^ 64);
  const u32 pg = WS_P + (rt * 15 + 12 + j) * 98304;
  u32x4 H[6][2];
#pragma unroll
  for (int b = 0; b < 3; b++) {
    const f32x2 cin = *(LAS f32x2*)(size_t)(scr + b * 512);
    const f32x2 sr = {cin.x, l8[b].x * cin.x - l8[b].y * cin.y + e0[b].x}, si = {cin.y, l8[b].x * cin.y + l8[b].y * cin.x + e0[b].y};
    const float c2r = 2.f * lc[b][2], c2i = 2.f * lc[b][3];
    f32x2 hr = c2r * sr - c2i * si, hi = -c2r * si - c2i * sr;
#pragma unroll
    for (int T = 0; T < 4; T++) {
      f32x2 pr[2], pi[2];
#pragma unroll
      for (int k = 0; k < 2; k++) {
        const f32x2 ur = {ar[b][T][2 * k], ar[b][T][2 * k + 1]}, ui = {ai[b][T][2 * k], ai[b][T][2 * k + 1]};
        const f32x2 nr = lc[b][0] * hr + (ur + lc[b][1] * hi), ni = lc[b][0] * hi + (ui - lc[b][1] * hr);
        hr = nr; hi = ni; pr[k] = nr; pi[k] = ni;
      }
      H[2 * b][0][T] = cvtpk(pr[0].x, pr[1].x); H[2 * b][1][T] = cvtpk(pr[0].y, pr[1].y);
      H[2 * b + 1][0][T] = cvtpk(pi[0].x, pi[1].x); H[2 * b + 1][1][T] = cvtpk(pi[0].y, pi[1].y);
    }
#pragma unroll
    for (int part = 0; part < 2; part++)
#pragma unroll
      for (int hh = 0; hh < 2; hh++) __builtin_amdgcn_raw_buffer_store_b128(H[2 * b + part][hh], rWS, hh ? io1 : io0, pg + (2 * b + part) * 16384, 16);
    SB();
  }
  auto own = [&](int i) { wr128(lds0 + TOFF + io0, ((19 + i) & 3) * TSLOT, H[i][0]); wr128(lds0 + TOFF + io1, ((19 + i) & 3) * TSLOT, H[i][1]); };
  own(0); own(1); own(2);
  STAMP(3);
  phaseB<true>(p, rt, 4, j, w, lane, lds0, tag, own);
}

DI void spare(const Params& p, int sid, u32 lds0, u32 tag) {
  const int tid = threadIdx.x;
  const rsrc_t rWS = mk(p.ws, WS_END);
  if (sid < 12) {
    const int cgp = tid % 48, rgp = tid / 48;
    const GAS u32x4* src = (const GAS u32x4*)(p.w1 + 2 * rgp * F + sid * 384) + cgp;
    float a[8];
#pragma unroll
    for (int c = 0; c < 8; c++) a[c] = 0.f;
    u32x4 v[36];
#pragma unroll
    for (int i = 0; i < 24; i++) v[i] = src[((i >> 1) * 32 + (i & 1)) * (F / 8)];
#pragma unroll
    for (int i = 0; i < 18; i++) {
#pragma unroll
      for (int c = 0; c < 4; c++) {
        a[2 * c] = dot2(__builtin_amdgcn_perm(v[2 * i + 1][c], v[2 * i][c], 0x05040100), ONES, a[2 * c]);
        a[2 * c + 1] = dot2(__builtin_amdgcn_perm(v[2 * i + 1][c], v[2 * i][c], 0x07060302), ONES, a[2 * c + 1]);
      }
      SB();
      if (i < 6) { v[24 + 2 * i] = src[((12 + i) * 32) * (F / 8)]; v[25 + 2 * i] = src[((12 + i) * 32 + 1) * (F / 8)]; }
      SB();
    }
    *(LAS f32x4*)(size_t)(lds0 + (rgp * 384 + cgp * 8) * 4) = (f32x4){a[0], a[1], a[2], a[3]};
    *(LAS f32x4*)(size_t)(lds0 + (rgp * 384 + cgp * 8) * 4 + 16) = (f32x4){a[4], a[5], a[6], a[7]};
    WAITL(); BAR();
    if (tid < 384) {
      float s = 0.f;
#pragma unroll
      for (int r = 0; r < 16; r++) s += rdf(lds0 + tid * 4, r * 1536);
      float c1, c2, c3; split3(s, c1, c2, c3);
      const float b = bf2f(p.b1[sid * 384 + tid]);
      __builtin_amdgcn_raw_buffer_store_b128((u32x4){cvtpk(c1, c2), cvtpk(c1, c2), cvtpk(c3, c1), cvtpk(b, b)}, rWS, tid * 16, WS_AUXW + sid * 6144, 16);
    }
  } else if (sid < 15) {
    const int row = tid >> 2, part = tid & 3;
#pragma unroll
    for (int h = 0; h < 2; h++) {
      const GAS u32x4* src = (const GAS u32x4*)((h ? p.bim : p.bre) + ((sid - 12) * 192 + row) * D) + part;
      u32x4 v[18];
#pragma unroll
      for (int i = 0; i < 18; i++) v[i] = src[4 * i];
      float s = 0.f, s2 = 0.f;
#pragma unroll
      for (int i = 0; i < 18; i++) { s = dot2(v[i][0], ONES, s); s2 = dot2(v[i][1], ONES, s2); s = dot2(v[i][2], ONES, s); s2 = dot2(v[i][3], ONES, s2); }
      s += s2; s += __shfl_xor(s, 1); s += __shfl_xor(s, 2);
      float c1, c2, c3; split3(s, c1, c2, c3);
      *(LAS u32x4*)(size_t)(lds0 + (h * 192 + row) * 16) = (u32x4){cvtpk(c1, c2), cvtpk(c1, c2), cvtpk(c3, c1), 0u};
    }
    if (tid < 192) {
      const int c = (sid - 12) * 192 + tid;
      const float lr = bf2f(p.lre[c]), li = bf2f(p.lim[c]);
      const float dt = rbf(expf(bf2f(p.logdt[c])));
      const float ex = expf(dt * lr), an = dt * li;
      const float br = ex * cosf(an), bi = ex * sinf(an);
      const float nr = br - 1.f, den = 1.f / (lr * lr + li * li);
      const float cr = (nr * lr + bi * li) * den, ci = (bi * lr - nr * li) * den;
      float ar = br, ai = bi, r16 = 0.f, i16 = 0.f;
#pragma unroll
      for (int k = 0; k < 7; k++) { const float t = ar * ar - ai * ai; ai = 2.f * ar * ai; ar = t; if (k == 2) { r16 = ar; i16 = ai; } }
      *(LAS f32x4*)(size_t)(lds0 + 6144 + tid * 32) = (f32x4){br, bi, cr, ci};
      *(LAS f32x4*)(size_t)(lds0 + 6144 + tid * 32 + 16) = (f32x4){r16, i16, ar, ai};
    }
    WAITL(); BAR();
    const u32x4 v = *(LAS u32x4*)(size_t)(lds0 + tid * 16);
    __builtin_amdgcn_raw_buffer_store_b128(v, rWS, tid < 384 ? WS_AUXW + sid * 6144 + tid * 16 : WS_TAB + (sid - 12) * 6144 + (tid - 384) * 16, 0, 16);
  }
  waitv<0>(); BAR();
  if (tid == 0 && sid < 15) st_flag(p.ws, FL_C + sid, tag);
  {
    u32 acc = 0;
    const GAS u32* w2 = (const GAS u32*)p.w2;
    if (tid < 648) {
#pragma unroll
      for (int i = 0; i < 4; i++) {
        const int L = (i * 16 + sid) * 648 + tid;
        const int r = L / 9, kb = r / 1536, rr = r - kb * 1536;
        acc ^= w2[((rr >> 5) * 96 + kb * 32 + (rr & 31)) * (ROWB / 4) + (L - r * 9) * 32];
      }
      if (tid < 324) acc ^= ((const GAS u32*)p.cre)[(sid * 324 + tid) * 32]; else acc ^= ((const GAS u32*)p.cim)[(sid * 324 + tid - 324) * 32];
    }
    asm volatile("" :: "v"(acc));
  }
}

__global__ __launch_bounds__(768, 3) void fused_kernel(Params p) {
  extern __shared__ __attribute__((aligned(16))) char smem[];
  const u32 lds0 = (u32)(size_t)(LAS char*)smem;
  const int b = blockIdx.x, xcd = b & 7, idx = b >> 3;
  const u32 tag = 1u;
  STAMP(0);
  if (idx >= 30) { spare(p, xcd * 2 + idx - 30, lds0, tag); STAMP(7); return; }
  const int tri = xcd * 10 + idx / 3, j = idx % 3, g = tri >> 4, rt = tri & 15;
  const int lane = threadIdx.x & 63, w = __builtin_amdgcn_readfirstlane(threadIdx.x >> 6);
  if (g < 4) {
    if (w >= 8) loader<false>(p, rt, g, j, w - 8, lane, lds0, tag); else ffn_compute(p, rt, g, j, w, lane, lds0, tag);
  } else {
    if (w >= 8) loader<true>(p, rt, g, j, w - 8, lane, lds0, tag); else ssm_compute(p, rt, j, w, lane, lds0, tag);
  }
}

__device__ u32 med_diff(const u64* st, int kind, int a, int b) {
  __shared__ u32 v[256]; __shared__ u32 res; __shared__ u64 t0s[256];
  const int t = threadIdx.x; const int idx = t >> 3, xcd = t & 7;
  const int k = idx >= 30 ? 2 : (((xcd * 10 + idx / 3) >> 4) == 4 ? 1 : 0);
  t0s[t] = st[t * 16]; __syncthreads();
  u64 t0 = ~0ull; for (int i = 0; i < 256; i++) t0 = t0s[i] < t0 ? t0s[i] : t0;
  const bool mx = b >= 100; if (mx) b -= 100;
  v[t] = k == kind ? (u32)(st[t * 16 + b] - (a < 0 ? t0 : st[t * 16 + a])) : 0xffffffffu; __syncthreads();
  int n = 0, less = 0; for (int i = 0; i < 256; i++) { n += v[i] != 0xffffffffu; less += (v[i] < v[t]) || (v[i] == v[t] && i < t); }
  if (k == kind && less == (mx ? n - 1 : n / 2)) res = v[t];
  __syncthreads(); u32 r = res; __syncthreads(); return r;
}
__global__ void amp_k(const u64* st) {
  u32 v0 = med_diff(st, K0, A0, B0), v1 = med_diff(st, K1, A1, B1);
  v0 = (v0 + 12) / 25; v1 = (v1 + 12) / 25; if (v0 > 127) v0 = 127; if (v1 > 110) v1 = 110;
  if (threadIdx.x == 0) { u64 t0 = __builtin_amdgcn_s_memrealtime(); u64 d = (u64)(v0 + 128 * v1) * 100; while (__builtin_amdgcn_s_memrealtime() - t0 < d) __builtin_amdgcn_s_sleep(4); }
}

extern "C" void kernel_launch(void* const* d_in, const int* in_sizes, int n_in, void* d_out, int out_size, void* d_ws, size_t ws_size, hipStream_t stream) {
  Params p;
  p.x = (const u16*)d_in[0]; p.lre = (const u16*)d_in[1]; p.lim = (const u16*)d_in[2]; p.bre = (const u16*)d_in[3]; p.bim = (const u16*)d_in[4];
  p.cre = (const u16*)d_in[5]; p.cim = (const u16*)d_in[6]; p.logdt = (const u16*)d_in[7]; p.dskip = (const u16*)d_in[8];
  p.w1 = (const u16*)d_in[10]; p.b1 = (const u16*)d_in[11]; p.w2 = (const u16*)d_in[12]; p.b2 = (const u16*)d_in[13];
  p.out = (float*)d_out; p.ws = (char*)d_ws;
  p.stamps = (u64*)((char*)d_ws + (200u << 20));
  (void)hipFuncSetAttribute((const void*)fused_kernel, hipFuncAttributeMaxDynamicSharedMemorySize, LDS_BYTES);
  (void)hipMemsetAsync(d_ws, 0, WS_ZERO_BYTES, stream);
  fused_kernel<<<256, 768, LDS_BYTES, stream>>>(p);
  amp_k<<<1, 256, 0, stream>>>(p.stamps);
}
```

```cpp
#include <hip/hip_runtime.h>
#include <utility>

#define LAS __attribute__((address_space(3)))
#define GAS __attribute__((address_space(1)))
#define DI __device__ __forceinline__
typedef unsigned short u16;
typedef unsigned u32;
typedef unsigned long long u64;
typedef unsigned u32x4 __attribute__((ext_vector_type(4)));
typedef unsigned u32x2 __attribute__((ext_vector_type(2)));
typedef short s16x4 __attribute__((ext_vector_type(4)));
typedef short bf16x8 __attribute__((ext_vector_type(8)));
typedef float f32x4 __attribute__((ext_vector_type(4)));
typedef float f32x2 __attribute__((ext_vector_type(2)));
typedef __bf16 bf16x2 __attribute__((ext_vector_type(2)));
typedef __amdgpu_buffer_rsrc_t rsrc_t;

constexpr int S = 2048, D = 576, F = 4608;
constexpr int ROWB = D * 2;
constexpr int WSLOT = 24576, TOFF = 4 * WSLOT, TSLOT = 16384, LDS_BYTES = 163840;
constexpr int AX_W = 0, AX_T = 6144, AX_S = 8192, AX_TAB = 10240;
constexpr u32 WS_FLAGS = 0;
constexpr u32 FL_S = 0, FL_C = 16, FL_P = 32, FL_O = 32 + 240, NFLAG = 32 + 480;
constexpr u32 WS_EG = NFLAG * 128;
constexpr u32 WS_REQ = WS_EG + 16 * 576 * 16, WS_RSP = WS_REQ + 1024, WS_ACK = WS_RSP + 1024;
constexpr u32 WS_AUXW = 256 * 1024;
constexpr u32 WS_TAB = WS_AUXW + 15 * 6144;
constexpr u32 WS_P = 1u << 20;
constexpr u32 XBW = 5 * 1536;
constexpr u32 WS_XB = WS_P + 240 * 98304;
constexpr u32 WS_END = WS_XB + 48 * 32 * XBW;
constexpr u32 SPIN_CAP = 1u << 18;

struct Params {
  const u16 *x, *lre, *lim, *bre, *bim, *cre, *cim, *logdt, *dskip, *w1, *b1, *w2, *b2;
  float* out; char* ws;
};

template <int N> DI void waitv() { asm volatile("s_waitcnt vmcnt(%0)" :: "n"(N) : "memory"); }
#define WAITL() asm volatile("s_waitcnt lgkmcnt(0)" ::: "memory")
#define BAR() do { asm volatile("" ::: "memory"); __builtin_amdgcn_s_barrier(); asm volatile("" ::: "memory"); } while (0)
#define SB() __builtin_amdgcn_sched_barrier(0)
#define MFMA(a, b, c) __builtin_amdgcn_mfma_f32_16x16x32_bf16((a), (b), (c), 0, 0, 0)
#define STAMP(i)

DI void dma(rsrc_t r, u32 lds, u32 voff, u32 soff) {
  asm volatile("s_mov_b32 m0, %0\n\ts_nop 0\n\tbuffer_load_dwordx4 %1, %2, %3 offen lds" :: "s"(lds), "v"(voff), "s"(r), "s"(soff) : "memory");
}
DI void dma_sc1(rsrc_t r, u32 lds, u32 voff, u32 soff) {
  asm volatile("s_mov_b32 m0, %0\n\ts_nop 0\n\tbuffer_load_dwordx4 %1, %2, %3 offen sc1 lds" :: "s"(lds), "v"(voff), "s"(r), "s"(soff) : "memory");
}
DI rsrc_t mk(const void* ptr, u32 bytes) { return __builtin_amdgcn_make_buffer_rsrc((void*)ptr, 0, bytes, 0x00020000); }
DI s16x4 rd_tr1(u32 a, int off) { return __builtin_amdgcn_ds_read_tr16_b64_v4i16((LAS s16x4*)(size_t)(a + off)); }
DI bf16x8 cat(s16x4 lo, s16x4 hi) { return __builtin_shufflevector(lo, hi, 0, 1, 2, 3, 4, 5, 6, 7); }
DI bf16x8 rd_tr(u32 a, int off) { return cat(rd_tr1(a, off), rd_tr1(a, off + 2048)); }
DI bf16x8 rd128(u32 a, int off) { return *(LAS bf16x8*)(size_t)(a + off); }
DI f32x4 rdf4(u32 a, int off) { return *(LAS f32x4*)(size_t)(a + off); }
DI float rdf(u32 a, int off) { return *(LAS float*)(size_t)(a + off); }
DI void wr128(u32 a, int off, u32x4 v) { *(LAS u32x4*)(size_t)(a + off) = v; }
DI u32 cvtpk(float lo, float hi) { f32x2 v = {lo, hi}; bf16x2 b = __builtin_convertvector(v, bf16x2); return __builtin_bit_cast(u32, b); }
DI float bflo(u32 v) { return __uint_as_float(v << 16); }
DI float bfhi(u32 v) { return __uint_as_float(v & 0xffff0000u); }
DI float bf2f(u16 v) { return __uint_as_float((u32)v << 16); }
DI float dot2(u32 a, u32 b, float c) { return __builtin_amdgcn_fdot2_f32_bf16(__builtin_bit_cast(bf16x2, a), __builtin_bit_cast(bf16x2, b), c, false); }
constexpr u32 ONES = 0x3f803f80u;
DI float rbf(float v) { return bflo(cvtpk(v, 0.f)); }
DI void split3(float v, float& a, float& b, float& c) { a = rbf(v); const float r = v - a; b = rbf(r); c = rbf(r - b); }
template <int C> DI float dppx(float v) { return __uint_as_float(__builtin_amdgcn_update_dpp(0u, __float_as_uint(v), C, 0xf, 0xf, true)); }
template <int C> DI double dppd(double v) { u32x2 t = __builtin_bit_cast(u32x2, v); t[0] = __builtin_amdgcn_update_dpp(0u, t[0], C, 0xf, 0xf, true); t[1] = __builtin_amdgcn_update_dpp(0u, t[1], C, 0xf, 0xf, true); return __builtin_bit_cast(double, t); }
DI void st_flag(char* ws, u32 idx, u32 tag) { __hip_atomic_store((GAS u32*)(ws + WS_FLAGS + idx * 128), tag, __ATOMIC_RELAXED, __HIP_MEMORY_SCOPE_AGENT); }
DI u32 ld_flag(const char* a) { return __hip_atomic_load((GAS u32*)a, __ATOMIC_RELAXED, __HIP_MEMORY_SCOPE_AGENT); }
DI void spin_all(const char* a, u32 tag) {
  for (u32 i = 0; i < SPIN_CAP; i++) { if (__all(ld_flag(a) == tag)) break; __builtin_amdgcn_s_sleep(1); }
  asm volatile("" ::: "memory");
}
DI void st_word(char* a, u32 v) { __hip_atomic_store((GAS u32*)a, v, __ATOMIC_RELAXED, __HIP_MEMORY_SCOPE_AGENT); }
DI u32 hs_request(char* ws) { const u32 n = ld_flag(ws + WS_RSP + blockIdx.x * 4) + 1u; st_word(ws + WS_REQ + blockIdx.x * 4, n); return n; }
DI void hs_finish(char* ws, u32 n) { spin_all(ws + WS_RSP + blockIdx.x * 4, n); st_word(ws + WS_ACK + blockIdx.x * 4, 1u); }
DI void hs_leader(char* ws) {
  const int tid = threadIdx.x;
  const rsrc_t r = mk(ws, WS_END);
  if (tid < (int)NFLAG) st_word(ws + WS_FLAGS + tid * 128, 0u); else st_word(ws + WS_ACK + (tid - NFLAG) * 4, 0u);
#pragma unroll
  for (int i = 0; i < 12; i++) __builtin_amdgcn_raw_buffer_store_b128((u32x4){0u, 0u, 0u, 0u}, r, tid * 16, WS_EG + i * 12288, 16);
  waitv<0>(); BAR();
  if (tid < 256) {
    bool done = tid == 255; bool first = true; u32 last = 0;
    for (u32 i = 0; i < SPIN_CAP; i++) {
      const u32 rq = ld_flag(ws + WS_REQ + tid * 4), ak = ld_flag(ws + WS_ACK + tid * 4);
      if (first || rq != last) st_word(ws + WS_RSP + tid * 4, rq);
      first = false; last = rq; done = done || ak == 1u;
      if (__all(done)) break;
      __builtin_amdgcn_s_sleep(8);
    }
  }
}
DI u32 poll_issue(const char* a) { u32 v; asm volatile("global_load_dword %0, %1, off sc1" : "=v"(v) : "v"(a) : "memory"); return v; }

DI f32x2 gelu2(f32x2 a, float k1, float k2, float sg) {
  f32x2 t = a * a; t = t * k2 + k1; t = t * a;
  t.x = __builtin_amdgcn_exp2f(t.x); t.y = __builtin_amdgcn_exp2f(t.y);
  t = t * sg + sg;
  t.x = __builtin_amdgcn_rcpf(t.x); t.y = __builtin_amdgcn_rcpf(t.y);
  return t * a;
}
DI u32x4 gelu_frag(f32x4 a, f32x4 b, float k1, float k2, float sg) {
  f32x2 g0 = gelu2((f32x2){a[0], a[1]}, k1, k2, sg), g1 = gelu2((f32x2){a[2], a[3]}, k1, k2, sg);
  f32x2 g2 = gelu2((f32x2){b[0], b[1]}, k1, k2, sg), g3 = gelu2((f32x2){b[2], b[3]}, k1, k2, sg);
  return (u32x4){cvtpk(g0.x, g0.y), cvtpk(g1.x, g1.y), cvtpk(g2.x, g2.y), cvtpk(g3.x, g3.y)};
}

template <int H>
DI void stepA(f32x4 (&acc)[6][4], bf16x8 (&wp)[2], bf16x8 (&t)[4], const u32 (&wc)[4], u32 n0, u32 n1, u32 xn) {
  bf16x8 w[8], nx[4];
  w[0] = wp[0]; w[1] = wp[1];
#pragma unroll
  for (int n = 0; n < 6; n++) {
    const int f = n + 2, ff = f < 6 ? f : f - 6;
    const u32 a = (f < 6 || H == 0) ? wc[ff & 3] : (ff ? n1 : n0); const int off = (f < 6 ? H : 1 - H) * WSLOT + (ff >> 2) * 4096;
    acc[n][0] = MFMA(w[n], t[0], acc[n][0]); SB();
    s16x4 lo = rd_tr1(a, off); SB();
    acc[n][1] = MFMA(w[n], t[1], acc[n][1]); SB();
    s16x4 hi = rd_tr1(a, off + 2048); SB();
    acc[n][2] = MFMA(w[n], t[2], acc[n][2]); SB();
    if (n >= 1 && n <= 4) { nx[n - 1] = rd128(xn, (n - 1) * 2048); SB(); }
    acc[n][3] = MFMA(w[n], t[3], acc[n][3]); SB();
    w[f] = cat(lo, hi);
  }
  wp[0] = w[6]; wp[1] = w[7];
#pragma unroll
  for (int i = 0; i < 4; i++) t[i] = nx[i];
}

template <int KIND, class FW, class FT, class FN, class MID>
DI void half(f32x4 (&acc)[3][4], bf16x8 (&w)[3], bf16x8 (&tp)[2], FW&& rw, FT&& rt_, FN&& rn, MID&& mid) {
  bf16x8 t[4], wn[3], tn[2];
  t[0] = tp[0]; t[1] = tp[1];
  s16x4 lo;
#pragma unroll
  for (int i = 0; i < 12; i++) {
    const int T = i / 3, n = i % 3;
    if constexpr (KIND == 2) acc[n][T] = MFMA(t[T], w[n], acc[n][T]); else acc[n][T] = MFMA(w[n], t[T], acc[n][T]);
    SB();
    if constexpr (KIND == 0) {
      if (i == 0) t[2] = rt_(2, 0); if (i == 1) t[3] = rt_(3, 0);
      if (i >= 2 && i <= 7) { if ((i & 1) == 0) lo = rw((i - 2) >> 1, 0); else wn[(i - 2) >> 1] = cat(lo, rw((i - 2) >> 1, 1)); }
      if (i == 8) tn[0] = rn(0, 0); if (i == 9) tn[1] = rn(1, 0);
    } else if constexpr (KIND == 1) {
      if (i <= 3) { if ((i & 1) == 0) lo = rt_(2 + (i >> 1), 0); else t[2 + (i >> 1)] = cat(lo, rt_(2 + (i >> 1), 1)); }
      if (i >= 4 && i <= 6) wn[i - 4] = rw(i - 4, 0);
      if (i >= 7 && i <= 10) { if ((i & 1) == 1) lo = rn((i - 7) >> 1, 0); else tn[(i - 7) >> 1] = cat(lo, rn((i - 7) >> 1, 1)); }
    } else {
      if (i == 0) t[2] = rt_(2, 0); if (i == 2) t[3] = rt_(3, 0);
      if (i == 3 || i == 5 || i == 6) wn[i == 3 ? 0 : i - 4] = rw(i == 3 ? 0 : i - 4, 0);
      if (i == 8) tn[0] = rn(0, 0); if (i == 9) tn[1] = rn(1, 0);
    }
    mid(i);
    SB();
  }
#pragma unroll
  for (int i = 0; i < 3; i++) w[i] = wn[i];
  tp[0] = tn[0]; tp[1] = tn[1];
}

template <int I, int N, class FN> DI void sfor(FN&& f) { if constexpr (I < N) { f(std::integral_constant<int, I>{}); sfor<I + 1, N>(f); } }

DI void ssm_carry(const Params& p, int rt, int j, int c, u32 lds0, u32 tag) {
  const u32 ax = lds0 + (18 & 3) * WSLOT, scr = lds0 + TOFF + (22 & 3) * TSLOT;
  {
    f32x4 pw = rdf4(ax + AX_TAB + c * 32, 16);
    { const float t = pw[0] * pw[0] - pw[1] * pw[1]; pw[1] = 2.f * pw[0] * pw[1]; pw[0] = t; }
    f32x2 es[8];
#pragma unroll
    for (int s = 0; s < 8; s++) es[s] = *(LAS f32x2*)(size_t)(scr + (s * 192 + c) * 8);
    float sr = 0.f, si = 0.f;
#pragma unroll
    for (int s = 0; s < 8; s++) { const float nr = pw[0] * sr - pw[1] * si + es[s].x, ni = pw[0] * si + pw[1] * sr + es[s].y; sr = nr; si = ni; }
    const rsrc_t rWS = mk(p.ws, WS_END);
    __builtin_amdgcn_raw_buffer_store_b128((u32x4){__float_as_uint(sr), tag, __float_as_uint(si), tag}, rWS, (192 * j + c) * 16, WS_EG + rt * 576 * 16, 16);
    float cr = 0.f, ci = 0.f;
    for (u32 spin = 0; spin < SPIN_CAP; spin++) {
      u32x4 v[15]; bool ok = true;
#pragma unroll
      for (int i = 0; i < 15; i++) v[i] = __builtin_amdgcn_raw_buffer_load_b128(rWS, (192 * j + c) * 16, WS_EG + (i < rt ? i : 0) * 576 * 16, 16);
      cr = 0.f; ci = 0.f;
#pragma unroll
      for (int i = 0; i < 15; i++) {
        if (i < rt) {
          ok = ok && v[i][1] == tag && v[i][3] == tag;
          const float nr = pw[2] * cr - pw[3] * ci + __uint_as_float(v[i][0]), ni = pw[2] * ci + pw[3] * cr + __uint_as_float(v[i][2]); cr = nr; ci = ni;
        }
      }
      if (__all(ok)) break;
      __builtin_amdgcn_s_sleep(1);
    }
#pragma unroll
    for (int s = 0; s < 8; s++) {
      *(LAS f32x2*)(size_t)(scr + (s * 192 + c) * 8) = (f32x2){cr, ci};
      const float nr = pw[0] * cr - pw[1] * ci + es[s].x, ni = pw[0] * ci + pw[1] * cr + es[s].y; cr = nr; ci = ni;
    }
  }
}

template <bool SSM>
DI void loader(const Params& p, int rt, int g, int j, int u, int lane, u32 lds0, u32 tag) {
  const rsrc_t rX = mk(p.x, S * ROWB), rWS = mk(p.ws, WS_END);
  const rsrc_t rA0 = SSM ? mk(p.bre, D * ROWB) : mk(p.w1, D * F * 2), rA1 = SSM ? mk(p.bim, D * ROWB) : rA0;
  const rsrc_t rB0 = SSM ? mk(p.cre, D * ROWB) : mk(p.w2, D * F * 2), rB1 = SSM ? mk(p.cim, D * ROWB) : rB0;
  const int r8 = lane >> 3, gp = lane & 7;
  const int cgr = ((((gp >> 1) ^ ((r8 >> 1) & 3)) << 1) | (gp & 1));
  const int rgr = gp ^ (4 * (u & 1) + (r8 >> 1));
  const int slice = 3 * g + j;
  const int kA = 16 * (u & 1) + 8 * (r8 >> 2) + 4 * (u >> 1) + (r8 & 3);
  const u32 voffA = SSM ? (192 * j + 8 * u + r8) * ROWB + rgr * 16 : kA * (F * 2) + slice * 768 + cgr * 16;
  const u32 voffB = SSM ? voffA : (8 * u + r8) * ROWB + j * 384 + cgr * 16;
  const int R = 8 * u + r8;
  const int tok = SSM ? 16 * ((R >> 2) & 3) + 8 * (R & 1) + 2 * (R >> 4) + ((R >> 1) & 1) : R;
  const u32 voffX = (rt * 128 + tok) * ROWB + rgr * 16;
  const u32 voffL = lane * 16;
  const u32 ldsW = lds0 + u * 1024, ldsT = lds0 + TOFF;
  const int rot = rt % 9;
  const int j1 = j == 2 ? 0 : j + 1, j2 = j == 0 ? 2 : j - 1;
  auto issueA = [&](int s, auto&& fill) {
    int kt = (s >> 1) + rot; if (kt >= 9) kt -= 9;
    const u32 l = ldsW + (s & 3) * WSLOT;
    const u32 so = SSM ? kt * 128 : (kt * 64 + (s & 1) * 32) * (F * 2);
#pragma unroll
    for (int i = 0; i < 6; i++) { dma((s & 1) ? rA1 : rA0, l + i * 4096, voffA, so + i * (SSM ? 32 * ROWB : 128)); SB(); fill(i); SB(); }
  };
  auto nofill = [](int) {};
  auto issueX = [&](int kap, int h) {
    int kt = kap + rot; if (kt >= 9) kt -= 9;
    const u32 l = ldsT + (kap & 3) * TSLOT + h * 8192 + u * 1024, so = h * 64 * ROWB + kt * 128;
    dma(rX, l, voffX, so); dma(rX, l + 4096, voffX, so + (SSM ? 4 : 32) * ROWB);
  };
  auto issueB = [&](int s) {
    const int i = s < 25 ? s - 19 : (s - 25) >> 1;
    const int jj = s < 25 ? j : (((s - 25) & 1) ? j2 : j1);
    const u32 l = ldsW + (s & 3) * WSLOT;
    if (SSM) {
      const u32 so = (192 * jj + 64 * (i >> 1)) * 2;
#pragma unroll
      for (int k = 0; k < 6; k++) dma((i & 1) ? rB1 : rB0, l + k * 4096, voffB, k * 32 * ROWB + so);
    } else {
      const u32 so = ((3 * g + jj) * 384 + 192 * (i & 1) + 32 * (i >> 1)) * ROWB;
#pragma unroll
      for (int k = 0; k < 6; k++) dma(rB0, l + k * 4096, voffB, so + (k / 3) * 96 * ROWB + (k % 3) * 128);
    }
    if (s >= 25) {
      const u32 sp = WS_P + (rt * 15 + 3 * g + jj) * 98304 + i * 16384 + u * 4096, lp = ldsT + (s & 3) * TSLOT + u * 4096;
#pragma unroll
      for (int k = 0; k < 4; k++) dma_sc1(rWS, lp + k * 1024, voffL, sp + k * 1024);
    }
  };
  auto issueAux = [&]() {
    const u32 l = lds0 + (18 & 3) * WSLOT;
#pragma unroll
    for (int k = 0; k < (SSM ? 3 : 2); k++) {
      int pc = SSM ? u * 3 + k : u * 2 + k; if (!SSM && pc >= 6) pc -= 6;
      const u32 so = pc < 6 ? WS_AUXW + slice * 6144 + pc * 1024 : WS_TAB + j * 6144 + (pc - 6) * 1024;
      dma_sc1(rWS, l + (pc < 6 ? AX_W + pc * 1024 : AX_TAB + (pc - 6) * 1024), voffL, so);
    }
  };
  float s1[4] = {0.f, 0.f, 0.f, 0.f}, s2[4] = {0.f, 0.f, 0.f, 0.f};
  u32x4 sv[2];
  auto statRead = [&](int hs) {
    sv[0] = __builtin_bit_cast(u32x4, rd128(ldsT + u * 1024 + voffL, ((hs >> 1) & 3) * TSLOT + (hs & 1) * 8192));
    sv[1] = __builtin_bit_cast(u32x4, rd128(ldsT + u * 1024 + voffL, ((hs >> 1) & 3) * TSLOT + (hs & 1) * 8192 + 4096));
  };
  auto statAcc = [&](int hs, int c) {
#pragma unroll
    for (int i = 0; i < 2; i++) { const int k = (hs & 1) * 2 + i; s1[k] = dot2(sv[i][c], ONES, s1[k]); s2[k] = dot2(sv[i][c], sv[i][c], s2[k]); }
  };
  auto statFinish = [&]() {
    const u32 ax = lds0 + (18 & 3) * WSLOT;
    const bool o1 = gp & 1, o2 = gp & 2;
    double a[2], b[2];
#pragma unroll
    for (int i = 0; i < 2; i++) {
      a[i] = (double)(o1 ? s1[2 * i + 1] : s1[2 * i]) + (double)dppx<0xb1>(o1 ? s1[2 * i] : s1[2 * i + 1]);
      b[i] = (double)(o1 ? s2[2 * i + 1] : s2[2 * i]) + (double)dppx<0xb1>(o1 ? s2[2 * i] : s2[2 * i + 1]);
    }
    double A = (o2 ? a[1] : a[0]) + dppd<0x4e>(o2 ? a[0] : a[1]), B = (o2 ? b[1] : b[0]) + dppd<0x4e>(o2 ? b[0] : b[1]);
    A += dppd<0x1b>(dppd<0x141>(A)); B += dppd<0x1b>(dppd<0x141>(B));
    const double m = A * (1.0 / D);
    const float mean = (float)m, ve = fmaxf((float)(B * (1.0 / D) - m * m), 0.f) + 1e-5f;
    const float rstd = __builtin_amdgcn_rsqf(ve), sig = ve * rstd;
    float m1, m2, m3; split3(-mean, m1, m2, m3);
    const float g1 = rbf(sig), g2 = sig - g1;
    const int tk = R + (gp & 3) * 32;
    if (gp < 4) wr128(ax + AX_T + tk * 16, 0, (u32x4){cvtpk(m1, m1), cvtpk(m2, m2), cvtpk(m1, m3), cvtpk(g1, g2)});
    else { LAS float* sp = (LAS float*)(size_t)(ax + AX_S + tk * 4); sp[0] = mean; sp[128] = rstd; sp[256] = sig; }
  };
  const char* fAux = p.ws + WS_FLAGS + (FL_C + slice) * 128;
  const char* fP = p.ws + WS_FLAGS + (FL_P + rt * 15 + 3 * g + ((lane & 1) ? j1 : j2)) * 128;
  __builtin_amdgcn_s_setprio(3);
  issueA(0, nofill); issueX(0, 0); issueX(0, 1); issueA(1, nofill); issueX(1, 0); issueX(1, 1); issueA(2, nofill);
  u32 pv = 0;
  waitv<16>(); BAR();
  sfor<0, 37>([&](auto sc) {
    constexpr int s = decltype(sc)::value;
    constexpr int prev = s == 0 ? 10 : (s <= 14 ? 8 : (s == 15 ? 6 : (s == 16 ? 0 : (s == 17 ? (SSM ? 9 : 8) : (s <= 22 ? 6 : (s <= 34 ? 10 : 0))))));
    waitv<prev>();
    if constexpr (SSM && s == 19) { BAR(); if (u < 3) ssm_carry(p, rt, j, u * 64 + lane, lds0, tag); WAITL(); BAR(); }
    BAR();
    if constexpr (s == 20) { if (u == 0 && lane == 0) st_flag(p.ws, FL_P + rt * 15 + slice, tag); }
    if constexpr (s == 16) { asm volatile("" : "+v"(pv)); if (!__all(pv == tag)) spin_all(fAux, tag); issueAux(); }
    if constexpr (s == 22) { asm volatile("" : "+v"(pv)); if (!__all(pv == tag)) spin_all(fP, tag); }
    if constexpr (s == 15) pv = poll_issue(fAux);
    if constexpr (s == 21) pv = poll_issue(fP);
    if constexpr (s + 3 < 18) { statRead(s); SB(); issueA(s + 3, [&](int i) { if (i >= 1 && i <= 4) statAcc(s, i - 1); }); }
    else if constexpr (s + 3 > 18 && s + 3 < 37) issueB(s + 3);
    if constexpr ((s >> 1) + 2 < 9) issueX((s >> 1) + 2, s & 1);
    if constexpr (s == 15) {
#pragma unroll
      for (int hs = 15; hs < 18; hs++) { statRead(hs);
#pragma unroll
        for (int c = 0; c < 4; c++) statAcc(hs, c); }
    }
    if constexpr (s == 16) { statFinish(); WAITL(); }
  });
  BAR();
}

template <bool SSM, class OWN>
DI void phaseB(const Params& p, int rt, int g, int j, int w, int lane, u32 lds0, u32 tag, OWN&& own) {
  asm volatile("" : "+v"(lane));
  const int rg = w >> 2, cg = w & 3, q = lane >> 4, e = (lane >> 2) & 3, pp = lane & 3, jl = lane & 15;
  const int z = 2 * (q & 1) + (e >> 1);
  u32 wlo[3], whi[3], tb[2];
#pragma unroll
  for (int n = 0; n < 3; n++) {
    const int t = 3 * cg + n;
    wlo[n] = SSM ? lds0 + (16 * t + jl) * 128 + ((q ^ (jl >> 1)) * 16) : lds0 + (t >> 2) * 4096 + (4 * q + e) * 128 + (((t & 3) ^ z) * 32) + pp * 8;
    whi[n] = SSM ? (wlo[n] ^ 64) : wlo[n] + 2 * WSLOT;
  }
  tb[0] = SSM ? lds0 + TOFF + rg * 8192 + (q >> 1) * 2048 + (pp >> 1) * 1024 + (((8 * (q & 1) + e) * 16) ^ ((pp >> 1) * 64)) + 8 * (pp & 1) : lds0 + TOFF + rg * 4096 + lane * 16;
  tb[1] = tb[0] ^ 64;
  float mean = 0.f, rstd = 0.f;
  if constexpr (!SSM) { const u32 sa = lds0 + (18 & 3) * WSLOT + AX_S + (64 * rg + 16 * g + jl) * 4; mean = rdf(sa, 0); rstd = rdf(sa, 512); }
  f32x4 ob[3][4];
#pragma unroll
  for (int n = 0; n < 3; n++)
#pragma unroll
    for (int T = 0; T < 4; T++) ob[n][T] = (f32x4){0.f, 0.f, 0.f, 0.f};
  auto RW = [&](int sl, int kb, int n, int part) {
    if constexpr (SSM) return rd128((kb ? whi[n] : wlo[n]) + (sl >> 1) * 2 * WSLOT, (sl & 1) * WSLOT);
    else return rd_tr1(sl < 2 ? wlo[n] : whi[n], (sl & 1) * WSLOT + kb * 12288 + part * 2048);
  };
  auto RT = [&](int sl, int kb, int T, int part) {
    if constexpr (SSM) return rd_tr1(tb[part], sl * TSLOT + kb * 4096 + T * 256);
    else return rd128(tb[0], sl * TSLOT + kb * 8192 + T * 1024);
  };
  bf16x8 wf[3], tp[2];
  auto step = [&](auto slc, auto&& mid) {
    constexpr int SL = decltype(slc)::value, NS = (SL + 1) & 3;
    half<SSM ? 1 : 0>(ob, wf, tp, [&](int n, int part) { return RW(SL, 1, n, part); }, [&](int T, int part) { return RT(SL, 0, T, part); }, [&](int T, int part) { return RT(SL, 1, T, part); }, mid);
    half<SSM ? 1 : 0>(ob, wf, tp, [&](int n, int part) { return RW(NS, 0, n, part); }, [&](int T, int part) { return RT(SL, 1, T, part); }, [&](int T, int part) { return RT(NS, 0, T, part); }, [](int) {});
  };
  auto nop = [](int) {};
  using I0 = std::integral_constant<int, 0>; using I1 = std::integral_constant<int, 1>; using I2 = std::integral_constant<int, 2>; using I3 = std::integral_constant<int, 3>;
  WAITL(); BAR();
  STAMP(4);
#pragma unroll
  for (int n = 0; n < 3; n++) { if constexpr (SSM) wf[n] = RW(3, 0, n, 0); else wf[n] = cat(RW(3, 0, n, 0), RW(3, 0, n, 1)); }
#pragma unroll
  for (int T = 0; T < 2; T++) { if constexpr (SSM) tp[T] = cat(RT(3, 0, T, 0), RT(3, 0, T, 1)); else tp[T] = RT(3, 0, T, 0); }
  step(I3{}, [&](int i) { if (i == 11) own(3); });
  waitv<0>(); WAITL(); BAR(); step(I0{}, [&](int i) { if (i == 11) own(4); });
  WAITL(); BAR(); step(I1{}, [&](int i) { if (i == 11) own(5); });
  WAITL(); BAR(); step(I2{}, nop);
  BAR(); step(I3{}, nop);
  BAR(); step(I0{}, nop);
  const int tok = rt * 128 + 64 * rg + 16 * g + jl, col = 192 * j + 48 * cg + 4 * q;
  u32x2 xv[3], bv[3], dv[3];
  if constexpr (!SSM) {
#pragma unroll
    for (int n = 0; n < 3; n++) {
      xv[n] = *(const GAS u32x2*)(p.x + tok * D + col + 16 * n); bv[n] = *(const GAS u32x2*)(p.b2 + col + 16 * n); dv[n] = *(const GAS u32x2*)(p.dskip + col + 16 * n);
    }
  }
  SB();
  for (int it = 0; it < 3; it++) {
    BAR(); step(I1{}, nop); BAR(); step(I2{}, nop); BAR(); step(I3{}, nop); BAR(); step(I0{}, nop);
  }
  STAMP(5);
  const rsrc_t rWS = mk(p.ws, WS_END);
  const u32 xb = WS_XB + (rt * 3 + j) * 32 * XBW + w * XBW;
  f32x4 mine[3];
#pragma unroll
  for (int T = 0; T < 4; T++) {
    if (SSM || T != g) {
      const u32 o = xb + T * 8 * XBW + g * 1536;
      const u32x4 a = {cvtpk(ob[0][T][0], ob[0][T][1]), cvtpk(ob[0][T][2], ob[0][T][3]), cvtpk(ob[1][T][0], ob[1][T][1]), cvtpk(ob[1][T][2], ob[1][T][3])};
      const u32 b0 = cvtpk(ob[2][T][0], ob[2][T][1]), b1 = cvtpk(ob[2][T][2], ob[2][T][3]);
      const auto s0 = __builtin_amdgcn_permlane32_swap(b0, b0, false, false), s1 = __builtin_amdgcn_permlane32_swap(b1, b1, false, false);
      __builtin_amdgcn_raw_buffer_store_b128(a, rWS, lane * 16, o, 16);
      if (lane < 32) __builtin_amdgcn_raw_buffer_store_b128((u32x4){b0, b1, s0[1], s1[1]}, rWS, lane * 16, o + 1024, 16);
    } else {
#pragma unroll
      for (int n = 0; n < 3; n++) mine[n] = ob[n][T];
    }
  }
  if constexpr (SSM) {
    waitv<0>(); BAR();
    if (threadIdx.x == 0) st_flag(p.ws, FL_O + (rt * 3 + j) * 5 + g, tag);
    STAMP(6); STAMP(7);
  } else {
    waitv<0>(); BAR();
    if (threadIdx.x == 0) st_flag(p.ws, FL_O + (rt * 3 + j) * 5 + g, tag);
    STAMP(6);
    spin_all(p.ws + WS_FLAGS + (FL_O + (rt * 3 + j) * 5 + (lane % 5)) * 128, tag);
    const u32 o = xb + g * 8 * XBW;
    u32x4 a[4]; u32x2 b[4];
#pragma unroll
    for (int k = 0; k < 4; k++) {
      const int src = k == 3 ? 4 : (k < g ? k : k + 1);
      a[k] = __builtin_amdgcn_raw_buffer_load_b128(rWS, lane * 16, o + src * 1536, 16);
      b[k] = __builtin_amdgcn_raw_buffer_load_b64(rWS, (lane & 31) * 16 + (lane >> 5) * 8, o + src * 1536 + 1024, 16);
    }
#pragma unroll
    for (int n = 0; n < 3; n++) {
      f32x4 r;
#pragma unroll
      for (int c = 0; c < 4; c++) {
        float G = mine[n][c] + ((c & 1) ? bfhi(bv[n][c >> 1]) : bflo(bv[n][c >> 1]));
#pragma unroll
        for (int k = 0; k < 3; k++) { const u32 v = n < 2 ? a[k][2 * n + (c >> 1)] : b[k][c >> 1]; G += (c & 1) ? bfhi(v) : bflo(v); }
        const u32 yv = n < 2 ? a[3][2 * n + (c >> 1)] : b[3][c >> 1];
        const float yy = (c & 1) ? bfhi(yv) : bflo(yv);
        const float xx = (c & 1) ? bfhi(xv[n][c >> 1]) : bflo(xv[n][c >> 1]);
        const float dd = (c & 1) ? bfhi(dv[n][c >> 1]) : bflo(dv[n][c >> 1]);
        const float sv = yy + (xx - mean) * rstd * dd;
        const float sig = __builtin_amdgcn_rcpf(1.f + __builtin_amdgcn_exp2f(-1.4426950408889634f * G));
        r[c] = xx + sv * (G * sig);
      }
      *(GAS f32x4*)(p.out + tok * D + col + 16 * n) = r;
    }
    STAMP(7);
  }
}

DI void ffn_compute(const Params& p, int rt, int g, int j, int w, int lane, u32 lds0, u32 tag) {
  const int rg = w >> 2, cg = w & 3, q = lane >> 4, e = (lane >> 2) & 3, pp = lane & 3, jl = lane & 15;
  const int z = 2 * (q & 1) + (e >> 1);
  u32 wc[4];
#pragma unroll
  for (int n = 0; n < 4; n++) { const int t = 6 * cg + n; wc[n] = lds0 + (t >> 2) * 4096 + (4 * q + e) * 128 + (((t & 3) ^ z) * 32) + pp * 8; }
  const u32 xbase = lds0 + TOFF + (64 * rg + jl) * 128 + ((q ^ (jl >> 1)) * 16);
  f32x4 acc[6][4];
#pragma unroll
  for (int n = 0; n < 6; n++)
#pragma unroll
    for (int T = 0; T < 4; T++) { acc[n][T] = (f32x4){0.f, 0.f, 0.f, 0.f}; asm volatile("" : "+v"(acc[n][T])); }
  bf16x8 wp[2], t[4];
  u32 hsn = 0; if (w == 7) hsn = __builtin_amdgcn_readfirstlane(hs_request(p.ws));
  BAR();
  STAMP(1);
  wp[0] = rd_tr(wc[0], 0); wp[1] = rd_tr(wc[1], 0);
#pragma unroll
  for (int T = 0; T < 4; T++) t[T] = rd128(xbase, T * 2048);
  u32 xs = 0; int dw = 2 * WSLOT;
#pragma nounroll
  for (int it = 0; it < 9; it++) {
    if (it == 7 && w == 7) hs_finish(p.ws, hsn);
    BAR(); stepA<0>(acc, wp, t, wc, 0, 0, (xbase ^ 64) + xs); xs = (xs + TSLOT) & (4 * TSLOT - 1);
    const u32 n0 = wc[0] + dw, n1 = wc[1] + dw;
    BAR(); stepA<1>(acc, wp, t, wc, n0, n1, xbase + xs);
    wc[0] = n0; wc[1] = n1; wc[2] += dw; wc[3] += dw; dw = -dw;
  }
  WAITL(); BAR();
  STAMP(2);
  const u32 ax = lds0 + (18 & 3) * WSLOT;
  float k1[4], k2[4], sg[4];
#pragma unroll
  for (int T = 0; T < 4; T++) {
    u32x4 v = __builtin_bit_cast(u32x4, rd128(ax + AX_T + (64 * rg + jl) * 16, T * 256));
    if (q != 0) v = (u32x4){0u, 0u, 0u, 0u};
    t[T] = __builtin_bit_cast(bf16x8, v);
    const float r = rdf(ax + AX_S + 512 + (64 * rg + jl) * 4, T * 64);
    sg[T] = rdf(ax + AX_S + 1024 + (64 * rg + jl) * 4, T * 64);
    k1[T] = -2.3022081985f * r; k2[T] = -0.10294324f * r * r * r;
  }
#pragma unroll
  for (int n = 0; n < 6; n++) {
    const bf16x8 a = rd128(ax + AX_W + (96 * cg + jl) * 16, n * 256);
#pragma unroll
    for (int T = 0; T < 4; T++) acc[n][T] = MFMA(a, t[T], acc[n][T]);
  }
  const rsrc_t rWS = mk(p.ws, WS_END);
  const u32 pg = WS_P + (rt * 15 + 3 * g + j) * 98304 + (cg >> 1) * 16384 + (cg & 1) * 8192 + rg * 4096;
  const u32 pl = lds0 + TOFF + (cg & 1) * 8192 + rg * 4096 + lane * 16;
  u32x4 P[3][4];
#pragma unroll
  for (int b = 0; b < 3; b++)
#pragma unroll
    for (int T = 0; T < 4; T++) {
      P[b][T] = gelu_frag(acc[2 * b][T], acc[2 * b + 1][T], k1[T], k2[T], sg[T]);
      __builtin_amdgcn_raw_buffer_store_b128(P[b][T], rWS, lane * 16, pg + b * 32768 + T * 1024, 16);
      SB();
    }
  auto own = [&](int i) {
    if ((cg >> 1) == (i & 1)) {
#pragma unroll
      for (int T = 0; T < 4; T++) wr128(pl, ((19 + i) & 3) * TSLOT + T * 1024, P[i >> 1][T]);
    }
  };
  own(0); own(1); own(2);
  STAMP(3);
  phaseB<false>(p, rt, g, j, w, lane, lds0, tag, own);
}

DI void ssm_compute(const Params& p, int rt, int j, int w, int lane, u32 lds0, u32 tag) {
  const int rg = w >> 2, cg = w & 3; int q = lane >> 4, jl = lane & 15;
  u32 wb = lds0 + (16 * cg + jl) * 128 + ((q ^ (jl >> 1)) * 16);
  const u32 xb0 = lds0 + TOFF + (64 * rg + jl) * 128 + ((q ^ (jl >> 1)) * 16);
  f32x4 ar[3][4], ai[3][4];
#pragma unroll
  for (int n = 0; n < 3; n++)
#pragma unroll
    for (int T = 0; T < 4; T++) { ar[n][T] = (f32x4){0.f, 0.f, 0.f, 0.f}; ai[n][T] = (f32x4){0.f, 0.f, 0.f, 0.f}; asm volatile("" : "+v"(ar[n][T]), "+v"(ai[n][T])); }
  bf16x8 wf[3], tp[2];
  auto nop = [](int) {};
  u32 hsn = 0; if (w == 7) hsn = __builtin_amdgcn_readfirstlane(hs_request(p.ws));
  BAR();
  STAMP(1);
#pragma unroll
  for (int n = 0; n < 3; n++) wf[n] = rd128(wb, n * 8192);
  tp[0] = rd128(xb0, 0); tp[1] = rd128(xb0, 2048);
  u32 xs = 0; int dw = 2 * WSLOT;
#pragma nounroll
  for (int it = 0; it < 9; it++) {
    const u32 x0 = xb0 + xs, x1 = (xb0 ^ 64) + xs, wb1 = wb ^ 64, wn = wb + dw;
    xs = (xs + TSLOT) & (4 * TSLOT - 1);
    const u32 xn = xb0 + xs;
    if (it == 7 && w == 7) hs_finish(p.ws, hsn);
    BAR();
    half<2>(ar, wf, tp, [&](int n, int) { return rd128(wb1, n * 8192); }, [&](int T, int) { return rd128(x0, T * 2048); }, [&](int T, int) { return rd128(x1, T * 2048); }, nop);
    half<2>(ar, wf, tp, [&](int n, int) { return rd128(wb, WSLOT + n * 8192); }, [&](int T, int) { return rd128(x1, T * 2048); }, [&](int T, int) { return rd128(x0, T * 2048); }, nop);
    BAR();
    half<2>(ai, wf, tp, [&](int n, int) { return rd128(wb1, WSLOT + n * 8192); }, [&](int T, int) { return rd128(x0, T * 2048); }, [&](int T, int) { return rd128(x1, T * 2048); }, nop);
    half<2>(ai, wf, tp, [&](int n, int) { return rd128(wn, n * 8192); }, [&](int T, int) { return rd128(x1, T * 2048); }, [&](int T, int) { return rd128(xn, T * 2048); }, nop);
    wb = wn; dw = -dw;
  }
  WAITL(); BAR();
  STAMP(2);
  asm volatile("" : "+v"(lane));
  q = lane >> 4; jl = lane & 15;
  const u32 ax = lds0 + (18 & 3) * WSLOT;
  {
    bf16x8 t[4];
#pragma unroll
    for (int T = 0; T < 4; T++) {
      u32x4 v = __builtin_bit_cast(u32x4, rd128(ax + AX_T + (64 * rg + jl) * 16, T * 256));
      if (q != 0) v = (u32x4){0u, 0u, 0u, 0u};
      t[T] = __builtin_bit_cast(bf16x8, v);
    }
#pragma unroll
    for (int b = 0; b < 3; b++) {
      const bf16x8 a0 = rd128(ax + AX_W + (16 * cg + jl) * 16, b * 1024), a1 = rd128(ax + AX_W + (16 * cg + jl) * 16, 3072 + b * 1024);
#pragma unroll
      for (int T = 0; T < 4; T++) { ar[b][T] = MFMA(t[T], a0, ar[b][T]); ai[b][T] = MFMA(t[T], a1, ai[b][T]); }
    }
  }
  f32x4 lc[3];
  const u32 scr = lds0 + TOFF + (22 & 3) * TSLOT + ((4 * rg + q) * 192 + 16 * cg + jl) * 8;
  f32x2 e0[3];
#pragma unroll
  for (int T = 0; T < 4; T++) {
    const f32x4 r = rdf4(ax + AX_S + 512 + (64 * rg + 4 * q) * 4, T * 64);
#pragma unroll
    for (int b = 0; b < 3; b++) { ar[b][T] *= r; ai[b][T] *= r; }
  }
  f32x2 l8[3];
#pragma unroll
  for (int b = 0; b < 3; b++) {
    lc[b] = rdf4(ax + AX_TAB + (16 * cg + jl) * 32, b * 2048);
    l8[b] = *(LAS f32x2*)(size_t)(ax + AX_TAB + (16 * cg + jl) * 32 + b * 2048 + 16);
    f32x2 hr = {ar[b][0][0], ar[b][0][1]}, hi = {ai[b][0][0], ai[b][0][1]};
#pragma unroll
    for (int k = 1; k < 8; k++) {
      const f32x2 ur = {ar[b][k >> 1][(k & 1) * 2], ar[b][k >> 1][(k & 1) * 2 + 1]}, ui = {ai[b][k >> 1][(k & 1) * 2], ai[b][k >> 1][(k & 1) * 2 + 1]};
      const f32x2 nr = lc[b][0] * hr + (ur - lc[b][1] * hi), ni = lc[b][0] * hi + (ui + lc[b][1] * hr);
      hr = nr; hi = ni;
    }
    e0[b] = (f32x2){hr.x, hi.x};
    *(LAS f32x2*)(size_t)(scr + b * 512) = (f32x2){l8[b].x * hr.x - l8[b].y * hi.x + hr.y, l8[b].x * hi.x + l8[b].y * hr.x + hi.y};
  }
  STAMP(8);
  WAITL(); BAR();
  STAMP(9);
#pragma unroll
  for (int b = 0; b < 3; b++) {
    const float c2r = 2.f * lc[b][2], c2i = 2.f * lc[b][3];
#pragma unroll
    for (int T = 0; T < 4; T++) {
      const f32x4 nr = c2r * ar[b][T] - c2i * ai[b][T], ni = -c2r * ai[b][T] - c2i * ar[b][T];
      ar[b][T] = nr; ai[b][T] = ni;
      asm volatile("" : "+v"(ar[b][T]), "+v"(ai[b][T]));
    }
  }
  BAR();
  STAMP(10);
  const rsrc_t rWS = mk(p.ws, WS_END);
  const u32 io0 = w * 2048 + lane * 16, io1 = w * 2048 + 1024 + ((lane * 16) ^ 64);
  const u32 pg = WS_P + (rt * 15 + 12 + j) * 98304;
  u32x4 H[6][2];
#pragma unroll
  for (int b = 0; b < 3; b++) {
    const f32x2 cin = *(LAS f32x2*)(size_t)(scr + b * 512);
    const f32x2 sr = {cin.x, l8[b].x * cin.x - l8[b].y * cin.y + e0[b].x}, si = {cin.y, l8[b].x * cin.y + l8[b].y * cin.x + e0[b].y};
    const float c2r = 2.f * lc[b][2], c2i = 2.f * lc[b][3];
    f32x2 hr = c2r * sr - c2i * si, hi = -c2r * si - c2i * sr;
#pragma unroll
    for (int T = 0; T < 4; T++) {
      f32x2 pr[2], pi[2];
#pragma unroll
      for (int k = 0; k < 2; k++) {
        const f32x2 ur = {ar[b][T][2 * k], ar[b][T][2 * k + 1]}, ui = {ai[b][T][2 * k], ai[b][T][2 * k + 1]};
        const f32x2 nr = lc[b][0] * hr + (ur + lc[b][1] * hi), ni = lc[b][0] * hi + (ui - lc[b][1] * hr);
        hr = nr; hi = ni; pr[k] = nr; pi[k] = ni;
      }
      H[2 * b][0][T] = cvtpk(pr[0].x, pr[1].x); H[2 * b][1][T] = cvtpk(pr[0].y, pr[1].y);
      H[2 * b + 1][0][T] = cvtpk(pi[0].x, pi[1].x); H[2 * b + 1][1][T] = cvtpk(pi[0].y, pi[1].y);
    }
#pragma unroll
    for (int part = 0; part < 2; part++)
#pragma unroll
      for (int hh = 0; hh < 2; hh++) __builtin_amdgcn_raw_buffer_store_b128(H[2 * b + part][hh], rWS, hh ? io1 : io0, pg + (2 * b + part) * 16384, 16);
    SB();
  }
  auto own = [&](int i) { wr128(lds0 + TOFF + io0, ((19 + i) & 3) * TSLOT, H[i][0]); wr128(lds0 + TOFF + io1, ((19 + i) & 3) * TSLOT, H[i][1]); };
  own(0); own(1); own(2);
  STAMP(3);
  phaseB<true>(p, rt, 4, j, w, lane, lds0, tag, own);
}

DI void spare(const Params& p, int sid, u32 lds0, u32 tag) {
  const int tid = threadIdx.x;
  const rsrc_t rWS = mk(p.ws, WS_END);
  u32 hsn = 0;
  if (sid == 15) hs_leader(p.ws); else if (tid < 64) hsn = hs_request(p.ws);
  if (sid < 12) {
    const int cgp = tid % 48, rgp = tid / 48;
    const GAS u32x4* src = (const GAS u32x4*)(p.w1 + 2 * rgp * F + sid * 384) + cgp;
    float a[8];
#pragma unroll
    for (int c = 0; c < 8; c++) a[c] = 0.f;
    u32x4 v[36];
#pragma unroll
    for (int i = 0; i < 24; i++) v[i] = src[((i >> 1) * 32 + (i & 1)) * (F / 8)];
#pragma unroll
    for (int i = 0; i < 18; i++) {
#pragma unroll
      for (int c = 0; c < 4; c++) {
        a[2 * c] = dot2(__builtin_amdgcn_perm(v[2 * i + 1][c], v[2 * i][c], 0x05040100), ONES, a[2 * c]);
        a[2 * c + 1] = dot2(__builtin_amdgcn_perm(v[2 * i + 1][c], v[2 * i][c], 0x07060302), ONES, a[2 * c + 1]);
      }
      SB();
      if (i < 6) { v[24 + 2 * i] = src[((12 + i) * 32) * (F / 8)]; v[25 + 2 * i] = src[((12 + i) * 32 + 1) * (F / 8)]; }
      SB();
    }
    *(LAS f32x4*)(size_t)(lds0 + (rgp * 384 + cgp * 8) * 4) = (f32x4){a[0], a[1], a[2], a[3]};
    *(LAS f32x4*)(size_t)(lds0 + (rgp * 384 + cgp * 8) * 4 + 16) = (f32x4){a[4], a[5], a[6], a[7]};
    WAITL(); BAR();
    if (tid < 384) {
      float s = 0.f;
#pragma unroll
      for (int r = 0; r < 16; r++) s += rdf(lds0 + tid * 4, r * 1536);
      float c1, c2, c3; split3(s, c1, c2, c3);
      const float b = bf2f(p.b1[sid * 384 + tid]);
      __builtin_amdgcn_raw_buffer_store_b128((u32x4){cvtpk(c1, c2), cvtpk(c1, c2), cvtpk(c3, c1), cvtpk(b, b)}, rWS, tid * 16, WS_AUXW + sid * 6144, 16);
    }
  } else if (sid < 15) {
    const int row = tid >> 2, part = tid & 3;
#pragma unroll
    for (int h = 0; h < 2; h++) {
      const GAS u32x4* src = (const GAS u32x4*)((h ? p.bim : p.bre) + ((sid - 12) * 192 + row) * D) + part;
      u32x4 v[18];
#pragma unroll
      for (int i = 0; i < 18; i++) v[i] = src[4 * i];
      float s = 0.f, s2 = 0.f;
#pragma unroll
      for (int i = 0; i < 18; i++) { s = dot2(v[i][0], ONES, s); s2 = dot2(v[i][1], ONES, s2); s = dot2(v[i][2], ONES, s); s2 = dot2(v[i][3], ONES, s2); }
      s += s2; s += __shfl_xor(s, 1); s += __shfl_xor(s, 2);
      float c1, c2, c3; split3(s, c1, c2, c3);
      *(LAS u32x4*)(size_t)(lds0 + (h * 192 + row) * 16) = (u32x4){cvtpk(c1, c2), cvtpk(c1, c2), cvtpk(c3, c1), 0u};
    }
    if (tid < 192) {
      const int c = (sid - 12) * 192 + tid;
      const float lr = bf2f(p.lre[c]), li = bf2f(p.lim[c]);
      const float dt = rbf(expf(bf2f(p.logdt[c])));
      const float ex = expf(dt * lr), an = dt * li;
      const float br = ex * cosf(an), bi = ex * sinf(an);
      const float nr = br - 1.f, den = 1.f / (lr * lr + li * li);
      const float cr = (nr * lr + bi * li) * den, ci = (bi * lr - nr * li) * den;
      float ar = br, ai = bi, r16 = 0.f, i16 = 0.f;
#pragma unroll
      for (int k = 0; k < 7; k++) { const float t = ar * ar - ai * ai; ai = 2.f * ar * ai; ar = t; if (k == 2) { r16 = ar; i16 = ai; } }
      *(LAS f32x4*)(size_t)(lds0 + 6144 + tid * 32) = (f32x4){br, bi, cr, ci};
      *(LAS f32x4*)(size_t)(lds0 + 6144 + tid * 32 + 16) = (f32x4){r16, i16, ar, ai};
    }
    WAITL(); BAR();
    const u32x4 v = *(LAS u32x4*)(size_t)(lds0 + tid * 16);
    __builtin_amdgcn_raw_buffer_store_b128(v, rWS, tid < 384 ? WS_AUXW + sid * 6144 + tid * 16 : WS_TAB + (sid - 12) * 6144 + (tid - 384) * 16, 0, 16);
  }
  waitv<0>(); BAR();
  if (tid < 64 && sid < 15) { hs_finish(p.ws, hsn); st_flag(p.ws, FL_C + sid, tag); }
  {
    u32 acc = 0;
    const GAS u32* w2 = (const GAS u32*)p.w2;
    if (tid < 648) {
#pragma unroll
      for (int i = 0; i < 4; i++) {
        const int L = (i * 16 + sid) * 648 + tid;
        const int r = L / 9, kb = r / 1536, rr = r - kb * 1536;
        acc ^= w2[((rr >> 5) * 96 + kb * 32 + (rr & 31)) * (ROWB / 4) + (L - r * 9) * 32];
      }
      if (tid < 324) acc ^= ((const GAS u32*)p.cre)[(sid * 324 + tid) * 32]; else acc ^= ((const GAS u32*)p.cim)[(sid * 324 + tid - 324) * 32];
    }
    asm volatile("" :: "v"(acc));
  }
}

__global__ __launch_bounds__(768, 3) void fused_kernel(Params p) {
  extern __shared__ __attribute__((aligned(16))) char smem[];
  const u32 lds0 = (u32)(size_t)(LAS char*)smem;
  const int b = blockIdx.x, xcd = b & 7, idx = b >> 3;
  const u32 tag = 1u;
  STAMP(0);
  if (idx >= 30) { spare(p, xcd * 2 + idx - 30, lds0, tag); STAMP(7); return; }
  const int tri = xcd * 10 + idx / 3, j = idx % 3, g = tri >> 4, rt = tri & 15;
  const int lane = threadIdx.x & 63, w = __builtin_amdgcn_readfirstlane(threadIdx.x >> 6);
  if (g < 4) {
    if (w >= 8) loader<false>(p, rt, g, j, w - 8, lane, lds0, tag); else ffn_compute(p, rt, g, j, w, lane, lds0, tag);
  } else {
    if (w >= 8) loader<true>(p, rt, g, j, w - 8, lane, lds0, tag); else ssm_compute(p, rt, j, w, lane, lds0, tag);
  }
}

extern "C" void kernel_launch(void* const* d_in, const int* in_sizes, int n_in, void* d_out, int out_size, void* d_ws, size_t ws_size, hipStream_t stream) {
  Params p;
  p.x = (const u16*)d_in[0]; p.lre = (const u16*)d_in[1]; p.lim = (const u16*)d_in[2]; p.bre = (const u16*)d_in[3]; p.bim = (const u16*)d_in[4];
  p.cre = (const u16*)d_in[5]; p.cim = (const u16*)d_in[6]; p.logdt = (const u16*)d_in[7]; p.dskip = (const u16*)d_in[8];
  p.w1 = (const u16*)d_in[10]; p.b1 = (const u16*)d_in[11]; p.w2 = (const u16*)d_in[12]; p.b2 = (const u16*)d_in[13];
  p.out = (float*)d_out; p.ws = (char*)d_ws;
  (void)hipFuncSetAttribute((const void*)fused_kernel, hipFuncAttributeMaxDynamicSharedMemorySize, LDS_BYTES);
  fused_kernel<<<256, 768, LDS_BYTES, stream>>>(p);
}
```

```cpp
#define STAMPS
#include <hip/hip_runtime.h>
#include <utility>

#define LAS __attribute__((address_space(3)))
#define GAS __attribute__((address_space(1)))
#define DI __device__ __forceinline__
typedef unsigned short u16;
typedef unsigned u32;
typedef unsigned long long u64;
typedef unsigned u32x4 __attribute__((ext_vector_type(4)));
typedef unsigned u32x2 __attribute__((ext_vector_type(2)));
typedef short s16x4 __attribute__((ext_vector_type(4)));
typedef short bf16x8 __attribute__((ext_vector_type(8)));
typedef float f32x4 __attribute__((ext_vector_type(4)));
typedef float f32x2 __attribute__((ext_vector_type(2)));
typedef __bf16 bf16x2 __attribute__((ext_vector_type(2)));
typedef __amdgpu_buffer_rsrc_t rsrc_t;

constexpr int S = 2048, D = 576, F = 4608;
constexpr int ROWB = D * 2;
constexpr int WSLOT = 24576, TOFF = 4 * WSLOT, TSLOT = 16384, LDS_BYTES = 163840;
constexpr int AX_W = 0, AX_T = 6144, AX_S = 8192, AX_TAB = 10240;
constexpr u32 WS_FLAGS = 0;
constexpr u32 FL_S = 0, FL_C = 16, FL_P = 32, FL_O = 32 + 240, NFLAG = 32 + 480;
constexpr u32 WS_EG = NFLAG * 128;
constexpr u32 WS_REQ = WS_EG + 16 * 576 * 16, WS_RSP = WS_REQ + 1024, WS_ACK = WS_RSP + 1024;
constexpr u32 WS_AUXW = 256 * 1024;
constexpr u32 WS_TAB = WS_AUXW + 15 * 6144;
constexpr u32 WS_P = 1u << 20;
constexpr u32 XBW = 5 * 1536;
constexpr u32 WS_XB = WS_P + 240 * 98304;
constexpr u32 WS_END = WS_XB + 48 * 32 * XBW;
constexpr u32 SPIN_CAP = 1u << 18;

struct Params {
  const u16 *x, *lre, *lim, *bre, *bim, *cre, *cim, *logdt, *dskip, *w1, *b1, *w2, *b2;
  float* out; char* ws;
  u64* stamps;
};

template <int N> DI void waitv() { asm volatile("s_waitcnt vmcnt(%0)" :: "n"(N) : "memory"); }
#define WAITL() asm volatile("s_waitcnt lgkmcnt(0)" ::: "memory")
#define BAR() do { asm volatile("" ::: "memory"); __builtin_amdgcn_s_barrier(); asm volatile("" ::: "memory"); } while (0)
#define SB() __builtin_amdgcn_sched_barrier(0)
#define MFMA(a, b, c) __builtin_amdgcn_mfma_f32_16x16x32_bf16((a), (b), (c), 0, 0, 0)
#define STAMP(i) do { SB(); if (threadIdx.x == 0) p.stamps[blockIdx.x * 16 + (i)] = __builtin_amdgcn_s_memrealtime(); SB(); } while (0)

DI void dma(rsrc_t r, u32 lds, u32 voff, u32 soff) {
  asm volatile("s_mov_b32 m0, %0\n\ts_nop 0\n\tbuffer_load_dwordx4 %1, %2, %3 offen lds" :: "s"(lds), "v"(voff), "s"(r), "s"(soff) : "memory");
}
DI void dma_sc1(rsrc_t r, u32 lds, u32 voff, u32 soff) {
  asm volatile("s_mov_b32 m0, %0\n\ts_nop 0\n\tbuffer_load_dwordx4 %1, %2, %3 offen sc1 lds" :: "s"(lds), "v"(voff), "s"(r), "s"(soff) : "memory");
}
DI rsrc_t mk(const void* ptr, u32 bytes) { return __builtin_amdgcn_make_buffer_rsrc((void*)ptr, 0, bytes, 0x00020000); }
DI s16x4 rd_tr1(u32 a, int off) { return __builtin_amdgcn_ds_read_tr16_b64_v4i16((LAS s16x4*)(size_t)(a + off)); }
DI bf16x8 cat(s16x4 lo, s16x4 hi) { return __builtin_shufflevector(lo, hi, 0, 1, 2, 3, 4, 5, 6, 7); }
DI bf16x8 rd_tr(u32 a, int off) { return cat(rd_tr1(a, off), rd_tr1(a, off + 2048)); }
DI bf16x8 rd128(u32 a, int off) { return *(LAS bf16x8*)(size_t)(a + off); }
DI f32x4 rdf4(u32 a, int off) { return *(LAS f32x4*)(size_t)(a + off); }
DI float rdf(u32 a, int off) { return *(LAS float*)(size_t)(a + off); }
DI void wr128(u32 a, int off, u32x4 v) { *(LAS u32x4*)(size_t)(a + off) = v; }
DI u32 cvtpk(float lo, float hi) { f32x2 v = {lo, hi}; bf16x2 b = __builtin_convertvector(v, bf16x2); return __builtin_bit_cast(u32, b); }
DI float bflo(u32 v) { return __uint_as_float(v << 16); }
DI float bfhi(u32 v) { return __uint_as_float(v & 0xffff0000u); }
DI float bf2f(u16 v) { return __uint_as_float((u32)v << 16); }
DI float dot2(u32 a, u32 b, float c) { return __builtin_amdgcn_fdot2_f32_bf16(__builtin_bit_cast(bf16x2, a), __builtin_bit_cast(bf16x2, b), c, false); }
constexpr u32 ONES = 0x3f803f80u;
DI float rbf(float v) { return bflo(cvtpk(v, 0.f)); }
DI void split3(float v, float& a, float& b, float& c) { a = rbf(v); const float r = v - a; b = rbf(r); c = rbf(r - b); }
template <int C> DI float dppx(float v) { return __uint_as_float(__builtin_amdgcn_update_dpp(0u, __float_as_uint(v), C, 0xf, 0xf, true)); }
template <int C> DI double dppd(double v) { u32x2 t = __builtin_bit_cast(u32x2, v); t[0] = __builtin_amdgcn_update_dpp(0u, t[0], C, 0xf, 0xf, true); t[1] = __builtin_amdgcn_update_dpp(0u, t[1], C, 0xf, 0xf, true); return __builtin_bit_cast(double, t); }
DI void st_flag(char* ws, u32 idx, u32 tag) { __hip_atomic_store((GAS u32*)(ws + WS_FLAGS + idx * 128), tag, __ATOMIC_RELAXED, __HIP_MEMORY_SCOPE_AGENT); }
DI u32 ld_flag(const char* a) { return __hip_atomic_load((GAS u32*)a, __ATOMIC_RELAXED, __HIP_MEMORY_SCOPE_AGENT); }
DI void spin_all(const char* a, u32 tag) {
  for (u32 i = 0; i < SPIN_CAP; i++) { if (__all(ld_flag(a) == tag)) break; __builtin_amdgcn_s_sleep(1); }
  asm volatile("" ::: "memory");
}
DI void st_word(char* a, u32 v) { __hip_atomic_store((GAS u32*)a, v, __ATOMIC_RELAXED, __HIP_MEMORY_SCOPE_AGENT); }
DI u32 hs_request(char* ws) { const u32 n = ld_flag(ws + WS_RSP + blockIdx.x * 4) + 1u; st_word(ws + WS_REQ + blockIdx.x * 4, n); return n; }
DI void hs_finish(char* ws, u32 n) { spin_all(ws + WS_RSP + blockIdx.x * 4, n); st_word(ws + WS_ACK + blockIdx.x * 4, 1u); }
DI void hs_leader(char* ws) {
  const int tid = threadIdx.x;
  const rsrc_t r = mk(ws, WS_END);
  if (tid < (int)NFLAG) st_word(ws + WS_FLAGS + tid * 128, 0u); else st_word(ws + WS_ACK + (tid - NFLAG) * 4, 0u);
#pragma unroll
  for (int i = 0; i < 12; i++) __builtin_amdgcn_raw_buffer_store_b128((u32x4){0u, 0u, 0u, 0u}, r, tid * 16, WS_EG + i * 12288, 16);
  waitv<0>(); BAR();
  if (tid < 256) {
    bool done = tid == 255; bool first = true; u32 last = 0;
    for (u32 i = 0; i < SPIN_CAP; i++) {
      const u32 rq = ld_flag(ws + WS_REQ + tid * 4), ak = ld_flag(ws + WS_ACK + tid * 4);
      if (first || rq != last) st_word(ws + WS_RSP + tid * 4, rq);
      first = false; last = rq; done = done || ak == 1u;
      if (__all(done)) break;
      __builtin_amdgcn_s_sleep(8);
    }
  }
}
DI u32 poll_issue(const char* a) { u32 v; asm volatile("global_load_dword %0, %1, off sc1" : "=v"(v) : "v"(a) : "memory"); return v; }

DI f32x2 gelu2(f32x2 a, float k1, float k2, float sg) {
  f32x2 t = a * a; t = t * k2 + k1; t = t * a;
  t.x = __builtin_amdgcn_exp2f(t.x); t.y = __builtin_amdgcn_exp2f(t.y);
  t = t * sg + sg;
  t.x = __builtin_amdgcn_rcpf(t.x); t.y = __builtin_amdgcn_rcpf(t.y);
  return t * a;
}
DI u32x4 gelu_frag(f32x4 a, f32x4 b, float k1, float k2, float sg) {
  f32x2 g0 = gelu2((f32x2){a[0], a[1]}, k1, k2, sg), g1 = gelu2((f32x2){a[2], a[3]}, k1, k2, sg);
  f32x2 g2 = gelu2((f32x2){b[0], b[1]}, k1, k2, sg), g3 = gelu2((f32x2){b[2], b[3]}, k1, k2, sg);
  return (u32x4){cvtpk(g0.x, g0.y), cvtpk(g1.x, g1.y), cvtpk(g2.x, g2.y), cvtpk(g3.x, g3.y)};
}

template <int H>
DI void stepA(f32x4 (&acc)[6][4], bf16x8 (&wp)[2], bf16x8 (&t)[4], const u32 (&wc)[4], u32 n0, u32 n1, u32 xn) {
  bf16x8 w[8], nx[4];
  w[0] = wp[0]; w[1] = wp[1];
#pragma unroll
  for (int n = 0; n < 6; n++) {
    const int f = n + 2, ff = f < 6 ? f : f - 6;
    const u32 a = (f < 6 || H == 0) ? wc[ff & 3] : (ff ? n1 : n0); const int off = (f < 6 ? H : 1 - H) * WSLOT + (ff >> 2) * 4096;
    acc[n][0] = MFMA(w[n], t[0], acc[n][0]); SB();
    s16x4 lo = rd_tr1(a, off); SB();
    acc[n][1] = MFMA(w[n], t[1], acc[n][1]); SB();
    s16x4 hi = rd_tr1(a, off + 2048); SB();
    acc[n][2] = MFMA(w[n], t[2], acc[n][2]); SB();
    if (n >= 1 && n <= 4) { nx[n - 1] = rd128(xn, (n - 1) * 2048); SB(); }
    acc[n][3] = MFMA(w[n], t[3], acc[n][3]); SB();
    w[f] = cat(lo, hi);
  }
  wp[0] = w[6]; wp[1] = w[7];
#pragma unroll
  for (int i = 0; i < 4; i++) t[i] = nx[i];
}

template <int KIND, class FW, class FT, class FN, class MID>
DI void half(f32x4 (&acc)[3][4], bf16x8 (&w)[3], bf16x8 (&tp)[2], FW&& rw, FT&& rt_, FN&& rn, MID&& mid) {
  bf16x8 t[4], wn[3], tn[2];
  t[0] = tp[0]; t[1] = tp[1];
  s16x4 lo;
#pragma unroll
  for (int i = 0; i < 12; i++) {
    const int T = i / 3, n = i % 3;
    if constexpr (KIND == 2) acc[n][T] = MFMA(t[T], w[n], acc[n][T]); else acc[n][T] = MFMA(w[n], t[T], acc[n][T]);
    SB();
    if constexpr (KIND == 0) {
      if (i == 0) t[2] = rt_(2, 0); if (i == 1) t[3] = rt_(3, 0);
      if (i >= 2 && i <= 7) { if ((i & 1) == 0) lo = rw((i - 2) >> 1, 0); else wn[(i - 2) >> 1] = cat(lo, rw((i - 2) >> 1, 1)); }
      if (i == 8) tn[0] = rn(0, 0); if (i == 9) tn[1] = rn(1, 0);
    } else if constexpr (KIND == 1) {
      if (i <= 3) { if ((i & 1) == 0) lo = rt_(2 + (i >> 1), 0); else t[2 + (i >> 1)] = cat(lo, rt_(2 + (i >> 1), 1)); }
      if (i >= 4 && i <= 6) wn[i - 4] = rw(i - 4, 0);
      if (i >= 7 && i <= 10) { if ((i & 1) == 1) lo = rn((i - 7) >> 1, 0); else tn[(i - 7) >> 1] = cat(lo, rn((i - 7) >> 1, 1)); }
    } else {
      if (i == 0) t[2] = rt_(2, 0); if (i == 2) t[3] = rt_(3, 0);
      if (i == 3 || i == 5 || i == 6) wn[i == 3 ? 0 : i - 4] = rw(i == 3 ? 0 : i - 4, 0);
      if (i == 8) tn[0] = rn(0, 0); if (i == 9) tn[1] = rn(1, 0);
    }
    mid(i);
    SB();
  }
#pragma unroll
  for (int i = 0; i < 3; i++) w[i] = wn[i];
  tp[0] = tn[0]; tp[1] = tn[1];
}

template <int I, int N, class FN> DI void sfor(FN&& f) { if constexpr (I < N) { f(std::integral_constant<int, I>{}); sfor<I + 1, N>(f); } }

DI void ssm_carry(const Params& p, int rt, int j, int c, u32 lds0, u32 tag) {
  const u32 ax = lds0 + (18 & 3) * WSLOT, scr = lds0 + TOFF + (22 & 3) * TSLOT;
  {
    f32x4 pw = rdf4(ax + AX_TAB + c * 32, 16);
    { const float t = pw[0] * pw[0] - pw[1] * pw[1]; pw[1] = 2.f * pw[0] * pw[1]; pw[0] = t; }
    f32x2 es[8];
#pragma unroll
    for (int s = 0; s < 8; s++) es[s] = *(LAS f32x2*)(size_t)(scr + (s * 192 + c) * 8);
    float sr = 0.f, si = 0.f;
#pragma unroll
    for (int s = 0; s < 8; s++) { const float nr = pw[0] * sr - pw[1] * si + es[s].x, ni = pw[0] * si + pw[1] * sr + es[s].y; sr = nr; si = ni; }
    const rsrc_t rWS = mk(p.ws, WS_END);
    __builtin_amdgcn_raw_buffer_store_b128((u32x4){__float_as_uint(sr), tag, __float_as_uint(si), tag}, rWS, (192 * j + c) * 16, WS_EG + rt * 576 * 16, 16);
    float cr = 0.f, ci = 0.f;
    for (u32 spin = 0; spin < SPIN_CAP; spin++) {
      u32x4 v[15]; bool ok = true;
#pragma unroll
      for (int i = 0; i < 15; i++) v[i] = __builtin_amdgcn_raw_buffer_load_b128(rWS, (192 * j + c) * 16, WS_EG + (i < rt ? i : 0) * 576 * 16, 16);
      cr = 0.f; ci = 0.f;
#pragma unroll
      for (int i = 0; i < 15; i++) {
        if (i < rt) {
          ok = ok && v[i][1] == tag && v[i][3] == tag;
          const float nr = pw[2] * cr - pw[3] * ci + __uint_as_float(v[i][0]), ni = pw[2] * ci + pw[3] * cr + __uint_as_float(v[i][2]); cr = nr; ci = ni;
        }
      }
      if (__all(ok)) break;
      __builtin_amdgcn_s_sleep(1);
    }
#pragma unroll
    for (int s = 0; s < 8; s++) {
      *(LAS f32x2*)(size_t)(scr + (s * 192 + c) * 8) = (f32x2){cr, ci};
      const float nr = pw[0] * cr - pw[1] * ci + es[s].x, ni = pw[0] * ci + pw[1] * cr + es[s].y; cr = nr; ci = ni;
    }
  }
}

template <bool SSM>
DI void loader(const Params& p, int rt, int g, int j, int u, int lane, u32 lds0, u32 tag) {
  const rsrc_t rX = mk(p.x, S * ROWB), rWS = mk(p.ws, WS_END);
  const rsrc_t rA0 = SSM ? mk(p.bre, D * ROWB) : mk(p.w1, D * F * 2), rA1 = SSM ? mk(p.bim, D * ROWB) : rA0;
  const rsrc_t rB0 = SSM ? mk(p.cre, D * ROWB) : mk(p.w2, D * F * 2), rB1 = SSM ? mk(p.cim, D * ROWB) : rB0;
  const int r8 = lane >> 3, gp = lane & 7;
  const int cgr = ((((gp >> 1) ^ ((r8 >> 1) & 3)) << 1) | (gp & 1));
  const int rgr = gp ^ (4 * (u & 1) + (r8 >> 1));
  const int slice = 3 * g + j;
  const int kA = 16 * (u & 1) + 8 * (r8 >> 2) + 4 * (u >> 1) + (r8 & 3);
  const u32 voffA = SSM ? (192 * j + 8 * u + r8) * ROWB + rgr * 16 : kA * (F * 2) + slice * 768 + cgr * 16;
  const u32 voffB = SSM ? voffA : (8 * u + r8) * ROWB + j * 384 + cgr * 16;
  const int R = 8 * u + r8;
  const int tok = SSM ? 16 * ((R >> 2) & 3) + 8 * (R & 1) + 2 * (R >> 4) + ((R >> 1) & 1) : R;
  const u32 voffX = (rt * 128 + tok) * ROWB + rgr * 16;
  const u32 voffL = lane * 16;
  const u32 ldsW = lds0 + u * 1024, ldsT = lds0 + TOFF;
  const int rot = rt % 9;
  const int j1 = j == 2 ? 0 : j + 1, j2 = j == 0 ? 2 : j - 1;
  auto issueA = [&](int s, auto&& fill) {
    int kt = (s >> 1) + rot; if (kt >= 9) kt -= 9;
    const u32 l = ldsW + (s & 3) * WSLOT;
    const u32 so = SSM ? kt * 128 : (kt * 64 + (s & 1) * 32) * (F * 2);
#pragma unroll
    for (int i = 0; i < 6; i++) { dma((s & 1) ? rA1 : rA0, l + i * 4096, voffA, so + i * (SSM ? 32 * ROWB : 128)); SB(); fill(i); SB(); }
  };
  auto nofill = [](int) {};
  auto issueX = [&](int kap, int h) {
    int kt = kap + rot; if (kt >= 9) kt -= 9;
    const u32 l = ldsT + (kap & 3) * TSLOT + h * 8192 + u * 1024, so = h * 64 * ROWB + kt * 128;
    dma(rX, l, voffX, so); dma(rX, l + 4096, voffX, so + (SSM ? 4 : 32) * ROWB);
  };
  auto issueB = [&](int s) {
    const int i = s < 25 ? s - 19 : (s - 25) >> 1;
    const int jj = s < 25 ? j : (((s - 25) & 1) ? j2 : j1);
    const u32 l = ldsW + (s & 3) * WSLOT;
    if (SSM) {
      const u32 so = (192 * jj + 64 * (i >> 1)) * 2;
#pragma unroll
      for (int k = 0; k < 6; k++) dma((i & 1) ? rB1 : rB0, l + k * 4096, voffB, k * 32 * ROWB + so);
    } else {
      const u32 so = ((3 * g + jj) * 384 + 192 * (i & 1) + 32 * (i >> 1)) * ROWB;
#pragma unroll
      for (int k = 0; k < 6; k++) dma(rB0, l + k * 4096, voffB, so + (k / 3) * 96 * ROWB + (k % 3) * 128);
    }
    if (s >= 25) {
      const u32 sp = WS_P + (rt * 15 + 3 * g + jj) * 98304 + i * 16384 + u * 4096, lp = ldsT + (s & 3) * TSLOT + u * 4096;
#pragma unroll
      for (int k = 0; k < 4; k++) dma_sc1(rWS, lp + k * 1024, voffL, sp + k * 1024);
    }
  };
  auto issueAux = [&]() {
    const u32 l = lds0 + (18 & 3) * WSLOT;
#pragma unroll
    for (int k = 0; k < (SSM ? 3 : 2); k++) {
      int pc = SSM ? u * 3 + k : u * 2 + k; if (!SSM && pc >= 6) pc -= 6;
      const u32 so = pc < 6 ? WS_AUXW + slice * 6144 + pc * 1024 : WS_TAB + j * 6144 + (pc - 6) * 1024;
      dma_sc1(rWS, l + (pc < 6 ? AX_W + pc * 1024 : AX_TAB + (pc - 6) * 1024), voffL, so);
    }
  };
  float s1[4] = {0.f, 0.f, 0.f, 0.f}, s2[4] = {0.f, 0.f, 0.f, 0.f};
  u32x4 sv[2];
  auto statRead = [&](int hs) {
    sv[0] = __builtin_bit_cast(u32x4, rd128(ldsT + u * 1024 + voffL, ((hs >> 1) & 3) * TSLOT + (hs & 1) * 8192));
    sv[1] = __builtin_bit_cast(u32x4, rd128(ldsT + u * 1024 + voffL, ((hs >> 1) & 3) * TSLOT + (hs & 1) * 8192 + 4096));
  };
  auto statAcc = [&](int hs, int c) {
#pragma unroll
    for (int i = 0; i < 2; i++) { const int k = (hs & 1) * 2 + i; s1[k] = dot2(sv[i][c], ONES, s1[k]); s2[k] = dot2(sv[i][c], sv[i][c], s2[k]); }
  };
  auto statFinish = [&]() {
    const u32 ax = lds0 + (18 & 3) * WSLOT;
    const bool o1 = gp & 1, o2 = gp & 2;
    double a[2], b[2];
#pragma unroll
    for (int i = 0; i < 2; i++) {
      a[i] = (double)(o1 ? s1[2 * i + 1] : s1[2 * i]) + (double)dppx<0xb1>(o1 ? s1[2 * i] : s1[2 * i + 1]);
      b[i] = (double)(o1 ? s2[2 * i + 1] : s2[2 * i]) + (double)dppx<0xb1>(o1 ? s2[2 * i] : s2[2 * i + 1]);
    }
    double A = (o2 ? a[1] : a[0]) + dppd<0x4e>(o2 ? a[0] : a[1]), B = (o2 ? b[1] : b[0]) + dppd<0x4e>(o2 ? b[0] : b[1]);
    A += dppd<0x1b>(dppd<0x141>(A)); B += dppd<0x1b>(dppd<0x141>(B));
    const double m = A * (1.0 / D);
    const float mean = (float)m, ve = fmaxf((float)(B * (1.0 / D) - m * m), 0.f) + 1e-5f;
    const float rstd = __builtin_amdgcn_rsqf(ve), sig = ve * rstd;
    float m1, m2, m3; split3(-mean, m1, m2, m3);
    const float g1 = rbf(sig), g2 = sig - g1;
    const int tk = R + (gp & 3) * 32;
    if (gp < 4) wr128(ax + AX_T + tk * 16, 0, (u32x4){cvtpk(m1, m1), cvtpk(m2, m2), cvtpk(m1, m3), cvtpk(g1, g2)});
    else { LAS float* sp = (LAS float*)(size_t)(ax + AX_S + tk * 4); sp[0] = mean; sp[128] = rstd; sp[256] = sig; }
  };
  const char* fAux = p.ws + WS_FLAGS + (FL_C + slice) * 128;
  const char* fP = p.ws + WS_FLAGS + (FL_P + rt * 15 + 3 * g + ((lane & 1) ? j1 : j2)) * 128;
  __builtin_amdgcn_s_setprio(3);
  issueA(0, nofill); issueX(0, 0); issueX(0, 1); issueA(1, nofill); issueX(1, 0); issueX(1, 1); issueA(2, nofill);
  u32 pv = 0;
  waitv<16>(); BAR();
  sfor<0, 37>([&](auto sc) {
    constexpr int s = decltype(sc)::value;
    constexpr int prev = s == 0 ? 10 : (s <= 14 ? 8 : (s == 15 ? 6 : (s == 16 ? 0 : (s == 17 ? (SSM ? 9 : 8) : (s <= 22 ? 6 : (s <= 34 ? 10 : 0))))));
    waitv<prev>();
    if constexpr (SSM && s == 19) { BAR(); if (u < 3) ssm_carry(p, rt, j, u * 64 + lane, lds0, tag); WAITL(); BAR(); }
    BAR();
    if constexpr (s == 20) { if (u == 0 && lane == 0) st_flag(p.ws, FL_P + rt * 15 + slice, tag); }
    if constexpr (s == 16) { asm volatile("" : "+v"(pv)); if (!__all(pv == tag)) spin_all(fAux, tag); issueAux(); }
    if constexpr (s == 22) { asm volatile("" : "+v"(pv)); if (!__all(pv == tag)) spin_all(fP, tag); }
    if constexpr (s == 15) pv = poll_issue(fAux);
    if constexpr (s == 21) pv = poll_issue(fP);
    if constexpr (s + 3 < 18) { statRead(s); SB(); issueA(s + 3, [&](int i) { if (i >= 1 && i <= 4) statAcc(s, i - 1); }); }
    else if constexpr (s + 3 > 18 && s + 3 < 37) issueB(s + 3);
    if constexpr ((s >> 1) + 2 < 9) issueX((s >> 1) + 2, s & 1);
    if constexpr (s == 15) {
#pragma unroll
      for (int hs = 15; hs < 18; hs++) { statRead(hs);
#pragma unroll
        for (int c = 0; c < 4; c++) statAcc(hs, c); }
    }
    if constexpr (s == 16) { statFinish(); WAITL(); }
  });
  BAR();
}

template <bool SSM, class OWN>
DI void phaseB(const Params& p, int rt, int g, int j, int w, int lane, u32 lds0, u32 tag, OWN&& own) {
  asm volatile("" : "+v"(lane));
  const int rg = w >> 2, cg = w & 3, q = lane >> 4, e = (lane >> 2) & 3, pp = lane & 3, jl = lane & 15;
  const int z = 2 * (q & 1) + (e >> 1);
  u32 wlo[3], whi[3], tb[2];
#pragma unroll
  for (int n = 0; n < 3; n++) {
    const int t = 3 * cg + n;
    wlo[n] = SSM ? lds0 + (16 * t + jl) * 128 + ((q ^ (jl >> 1)) * 16) : lds0 + (t >> 2) * 4096 + (4 * q + e) * 128 + (((t & 3) ^ z) * 32) + pp * 8;
    whi[n] = SSM ? (wlo[n] ^ 64) : wlo[n] + 2 * WSLOT;
  }
  tb[0] = SSM ? lds0 + TOFF + rg * 8192 + (q >> 1) * 2048 + (pp >> 1) * 1024 + (((8 * (q & 1) + e) * 16) ^ ((pp >> 1) * 64)) + 8 * (pp & 1) : lds0 + TOFF + rg * 4096 + lane * 16;
  tb[1] = tb[0] ^ 64;
  float mean = 0.f, rstd = 0.f;
  if constexpr (!SSM) { const u32 sa = lds0 + (18 & 3) * WSLOT + AX_S + (64 * rg + 16 * g + jl) * 4; mean = rdf(sa, 0); rstd = rdf(sa, 512); }
  f32x4 ob[3][4];
#pragma unroll
  for (int n = 0; n < 3; n++)
#pragma unroll
    for (int T = 0; T < 4; T++) ob[n][T] = (f32x4){0.f, 0.f, 0.f, 0.f};
  auto RW = [&](int sl, int kb, int n, int part) {
    if constexpr (SSM) return rd128((kb ? whi[n] : wlo[n]) + (sl >> 1) * 2 * WSLOT, (sl & 1) * WSLOT);
    else return rd_tr1(sl < 2 ? wlo[n] : whi[n], (sl & 1) * WSLOT + kb * 12288 + part * 2048);
  };
  auto RT = [&](int sl, int kb, int T, int part) {
    if constexpr (SSM) return rd_tr1(tb[part], sl * TSLOT + kb * 4096 + T * 256);
    else return rd128(tb[0], sl * TSLOT + kb * 8192 + T * 1024);
  };
  bf16x8 wf[3], tp[2];
  auto step = [&](auto slc, auto&& mid) {
    constexpr int SL = decltype(slc)::value, NS = (SL + 1) & 3;
    half<SSM ? 1 : 0>(ob, wf, tp, [&](int n, int part) { return RW(SL, 1, n, part); }, [&](int T, int part) { return RT(SL, 0, T, part); }, [&](int T, int part) { return RT(SL, 1, T, part); }, mid);
    half<SSM ? 1 : 0>(ob, wf, tp, [&](int n, int part) { return RW(NS, 0, n, part); }, [&](int T, int part) { return RT(SL, 1, T, part); }, [&](int T, int part) { return RT(NS, 0, T, part); }, [](int) {});
  };
  auto nop = [](int) {};
  using I0 = std::integral_constant<int, 0>; using I1 = std::integral_constant<int, 1>; using I2 = std::integral_constant<int, 2>; using I3 = std::integral_constant<int, 3>;
  WAITL(); BAR();
  STAMP(4);
#pragma unroll
  for (int n = 0; n < 3; n++) { if constexpr (SSM) wf[n] = RW(3, 0, n, 0); else wf[n] = cat(RW(3, 0, n, 0), RW(3, 0, n, 1)); }
#pragma unroll
  for (int T = 0; T < 2; T++) { if constexpr (SSM) tp[T] = cat(RT(3, 0, T, 0), RT(3, 0, T, 1)); else tp[T] = RT(3, 0, T, 0); }
  step(I3{}, [&](int i) { if (i == 11) own(3); });
  waitv<0>(); WAITL(); BAR(); step(I0{}, [&](int i) { if (i == 11) own(4); });
  WAITL(); BAR(); step(I1{}, [&](int i) { if (i == 11) own(5); });
  WAITL(); BAR(); step(I2{}, nop);
  BAR(); step(I3{}, nop);
  BAR(); step(I0{}, nop);
  const int tok = rt * 128 + 64 * rg + 16 * g + jl, col = 192 * j + 48 * cg + 4 * q;
  u32x2 xv[3], bv[3], dv[3];
  if constexpr (!SSM) {
#pragma unroll
    for (int n = 0; n < 3; n++) {
      xv[n] = *(const GAS u32x2*)(p.x + tok * D + col + 16 * n); bv[n] = *(const GAS u32x2*)(p.b2 + col + 16 * n); dv[n] = *(const GAS u32x2*)(p.dskip + col + 16 * n);
    }
  }
  SB();
  for (int it = 0; it < 3; it++) {
    BAR(); step(I1{}, nop); BAR(); step(I2{}, nop); BAR(); step(I3{}, nop); BAR(); step(I0{}, nop);
  }
  STAMP(5);
  const rsrc_t rWS = mk(p.ws, WS_END);
  const u32 xb = WS_XB + (rt * 3 + j) * 32 * XBW + w * XBW;
  f32x4 mine[3];
#pragma unroll
  for (int T = 0; T < 4; T++) {
    if (SSM || T != g) {
      const u32 o = xb + T * 8 * XBW + g * 1536;
      const u32x4 a = {cvtpk(ob[0][T][0], ob[0][T][1]), cvtpk(ob[0][T][2], ob[0][T][3]), cvtpk(ob[1][T][0], ob[1][T][1]), cvtpk(ob[1][T][2], ob[1][T][3])};
      const u32 b0 = cvtpk(ob[2][T][0], ob[2][T][1]), b1 = cvtpk(ob[2][T][2], ob[2][T][3]);
      const auto s0 = __builtin_amdgcn_permlane32_swap(b0, b0, false, false), s1 = __builtin_amdgcn_permlane32_swap(b1, b1, false, false);
      __builtin_amdgcn_raw_buffer_store_b128(a, rWS, lane * 16, o, 16);
      if (lane < 32) __builtin_amdgcn_raw_buffer_store_b128((u32x4){b0, b1, s0[1], s1[1]}, rWS, lane * 16, o + 1024, 16);
    } else {
#pragma unroll
      for (int n = 0; n < 3; n++) mine[n] = ob[n][T];
    }
  }
  if constexpr (SSM) {
    waitv<0>(); BAR();
    if (threadIdx.x == 0) st_flag(p.ws, FL_O + (rt * 3 + j) * 5 + g, tag);
    STAMP(6); STAMP(7);
  } else {
    waitv<0>(); BAR();
    if (threadIdx.x == 0) st_flag(p.ws, FL_O + (rt * 3 + j) * 5 + g, tag);
    STAMP(6);
    spin_all(p.ws + WS_FLAGS + (FL_O + (rt * 3 + j) * 5 + (lane % 5)) * 128, tag);
    const u32 o = xb + g * 8 * XBW;
    u32x4 a[4]; u32x2 b[4];
#pragma unroll
    for (int k = 0; k < 4; k++) {
      const int src = k == 3 ? 4 : (k < g ? k : k + 1);
      a[k] = __builtin_amdgcn_raw_buffer_load_b128(rWS, lane * 16, o + src * 1536, 16);
      b[k] = __builtin_amdgcn_raw_buffer_load_b64(rWS, (lane & 31) * 16 + (lane >> 5) * 8, o + src * 1536 + 1024, 16);
    }
#pragma unroll
    for (int n = 0; n < 3; n++) {
      f32x4 r;
#pragma unroll
      for (int c = 0; c < 4; c++) {
        float G = mine[n][c] + ((c & 1) ? bfhi(bv[n][c >> 1]) : bflo(bv[n][c >> 1]));
#pragma unroll
        for (int k = 0; k < 3; k++) { const u32 v = n < 2 ? a[k][2 * n + (c >> 1)] : b[k][c >> 1]; G += (c & 1) ? bfhi(v) : bflo(v); }
        const u32 yv = n < 2 ? a[3][2 * n + (c >> 1)] : b[3][c >> 1];
        const float yy = (c & 1) ? bfhi(yv) : bflo(yv);
        const float xx = (c & 1) ? bfhi(xv[n][c >> 1]) : bflo(xv[n][c >> 1]);
        const float dd = (c & 1) ? bfhi(dv[n][c >> 1]) : bflo(dv[n][c >> 1]);
        const float sv = yy + (xx - mean) * rstd * dd;
        const float sig = __builtin_amdgcn_rcpf(1.f + __builtin_amdgcn_exp2f(-1.4426950408889634f * G));
        r[c] = xx + sv * (G * sig);
      }
      *(GAS f32x4*)(p.out + tok * D + col + 16 * n) = r;
    }
    STAMP(7);
  }
}

DI void ffn_compute(const Params& p, int rt, int g, int j, int w, int lane, u32 lds0, u32 tag) {
  const int rg = w >> 2, cg = w & 3, q = lane >> 4, e = (lane >> 2) & 3, pp = lane & 3, jl = lane & 15;
  const int z = 2 * (q & 1) + (e >> 1);
  u32 wc[4];
#pragma unroll
  for (int n = 0; n < 4; n++) { const int t = 6 * cg + n; wc[n] = lds0 + (t >> 2) * 4096 + (4 * q + e) * 128 + (((t & 3) ^ z) * 32) + pp * 8; }
  const u32 xbase = lds0 + TOFF + (64 * rg + jl) * 128 + ((q ^ (jl >> 1)) * 16);
  f32x4 acc[6][4];
#pragma unroll
  for (int n = 0; n < 6; n++)
#pragma unroll
    for (int T = 0; T < 4; T++) { acc[n][T] = (f32x4){0.f, 0.f, 0.f, 0.f}; asm volatile("" : "+v"(acc[n][T])); }
  bf16x8 wp[2], t[4];
  u32 hsn = 0; if (w == 7) hsn = __builtin_amdgcn_readfirstlane(hs_request(p.ws));
  BAR();
  STAMP(1);
  wp[0] = rd_tr(wc[0], 0); wp[1] = rd_tr(wc[1], 0);
#pragma unroll
  for (int T = 0; T < 4; T++) t[T] = rd128(xbase, T * 2048);
  u32 xs = 0; int dw = 2 * WSLOT;
#pragma nounroll
  for (int it = 0; it < 9; it++) {
    if (it == 2) STAMP(8); if (it == 4) STAMP(9); if (it == 7) STAMP(10); if (it == 8) STAMP(11);
    if (it == 7 && w == 7) hs_finish(p.ws, hsn);
    BAR(); stepA<0>(acc, wp, t, wc, 0, 0, (xbase ^ 64) + xs); xs = (xs + TSLOT) & (4 * TSLOT - 1);
    const u32 n0 = wc[0] + dw, n1 = wc[1] + dw;
    BAR(); stepA<1>(acc, wp, t, wc, n0, n1, xbase + xs);
    wc[0] = n0; wc[1] = n1; wc[2] += dw; wc[3] += dw; dw = -dw;
  }
  WAITL(); BAR();
  STAMP(2);
  const u32 ax = lds0 + (18 & 3) * WSLOT;
  float k1[4], k2[4], sg[4];
#pragma unroll
  for (int T = 0; T < 4; T++) {
    u32x4 v = __builtin_bit_cast(u32x4, rd128(ax + AX_T + (64 * rg + jl) * 16, T * 256));
    if (q != 0) v = (u32x4){0u, 0u, 0u, 0u};
    t[T] = __builtin_bit_cast(bf16x8, v);
    const float r = rdf(ax + AX_S + 512 + (64 * rg + jl) * 4, T * 64);
    sg[T] = rdf(ax + AX_S + 1024 + (64 * rg + jl) * 4, T * 64);
    k1[T] = -2.3022081985f * r; k2[T] = -0.10294324f * r * r * r;
  }
#pragma unroll
  for (int n = 0; n < 6; n++) {
    const bf16x8 a = rd128(ax + AX_W + (96 * cg + jl) * 16, n * 256);
#pragma unroll
    for (int T = 0; T < 4; T++) acc[n][T] = MFMA(a, t[T], acc[n][T]);
  }
  const rsrc_t rWS = mk(p.ws, WS_END);
  const u32 pg = WS_P + (rt * 15 + 3 * g + j) * 98304 + (cg >> 1) * 16384 + (cg & 1) * 8192 + rg * 4096;
  const u32 pl = lds0 + TOFF + (cg & 1) * 8192 + rg * 4096 + lane * 16;
  u32x4 P[3][4];
#pragma unroll
  for (int b = 0; b < 3; b++)
#pragma unroll
    for (int T = 0; T < 4; T++) {
      P[b][T] = gelu_frag(acc[2 * b][T], acc[2 * b + 1][T], k1[T], k2[T], sg[T]);
      __builtin_amdgcn_raw_buffer_store_b128(P[b][T], rWS, lane * 16, pg + b * 32768 + T * 1024, 16);
      SB();
    }
  auto own = [&](int i) {
    if ((cg >> 1) == (i & 1)) {
#pragma unroll
      for (int T = 0; T < 4; T++) wr128(pl, ((19 + i) & 3) * TSLOT + T * 1024, P[i >> 1][T]);
    }
  };
  own(0); own(1); own(2);
  STAMP(3);
  phaseB<false>(p, rt, g, j, w, lane, lds0, tag, own);
}

DI void ssm_compute(const Params& p, int rt, int j, int w, int lane, u32 lds0, u32 tag) {
  const int rg = w >> 2, cg = w & 3; int q = lane >> 4, jl = lane & 15;
  u32 wb = lds0 + (16 * cg + jl) * 128 + ((q ^ (jl >> 1)) * 16);
  const u32 xb0 = lds0 + TOFF + (64 * rg + jl) * 128 + ((q ^ (jl >> 1)) * 16);
  f32x4 ar[3][4], ai[3][4];
#pragma unroll
  for (int n = 0; n < 3; n++)
#pragma unroll
    for (int T = 0; T < 4; T++) { ar[n][T] = (f32x4){0.f, 0.f, 0.f, 0.f}; ai[n][T] = (f32x4){0.f, 0.f, 0.f, 0.f}; asm volatile("" : "+v"(ar[n][T]), "+v"(ai[n][T])); }
  bf16x8 wf[3], tp[2];
  auto nop = [](int) {};
  u32 hsn = 0; if (w == 7) hsn = __builtin_amdgcn_readfirstlane(hs_request(p.ws));
  BAR();
  STAMP(1);
#pragma unroll
  for (int n = 0; n < 3; n++) wf[n] = rd128(wb, n * 8192);
  tp[0] = rd128(xb0, 0); tp[1] = rd128(xb0, 2048);
  u32 xs = 0; int dw = 2 * WSLOT;
#pragma nounroll
  for (int it = 0; it < 9; it++) {
    const u32 x0 = xb0 + xs, x1 = (xb0 ^ 64) + xs, wb1 = wb ^ 64, wn = wb + dw;
    xs = (xs + TSLOT) & (4 * TSLOT - 1);
    const u32 xn = xb0 + xs;
    if (it == 7 && w == 7) hs_finish(p.ws, hsn);
    BAR();
    half<2>(ar, wf, tp, [&](int n, int) { return rd128(wb1, n * 8192); }, [&](int T, int) { return rd128(x0, T * 2048); }, [&](int T, int) { return rd128(x1, T * 2048); }, nop);
    half<2>(ar, wf, tp, [&](int n, int) { return rd128(wb, WSLOT + n * 8192); }, [&](int T, int) { return rd128(x1, T * 2048); }, [&](int T, int) { return rd128(x0, T * 2048); }, nop);
    BAR();
    half<2>(ai, wf, tp, [&](int n, int) { return rd128(wb1, WSLOT + n * 8192); }, [&](int T, int) { return rd128(x0, T * 2048); }, [&](int T, int) { return rd128(x1, T * 2048); }, nop);
    half<2>(ai, wf, tp, [&](int n, int) { return rd128(wn, n * 8192); }, [&](int T, int) { return rd128(x1, T * 2048); }, [&](int T, int) { return rd128(xn, T * 2048); }, nop);
    wb = wn; dw = -dw;
  }
  WAITL(); BAR();
  STAMP(2);
  asm volatile("" : "+v"(lane));
  q = lane >> 4; jl = lane & 15;
  const u32 ax = lds0 + (18 & 3) * WSLOT;
  {
    bf16x8 t[4];
#pragma unroll
    for (int T = 0; T < 4; T++) {
      u32x4 v = __builtin_bit_cast(u32x4, rd128(ax + AX_T + (64 * rg + jl) * 16, T * 256));
      if (q != 0) v = (u32x4){0u, 0u, 0u, 0u};
      t[T] = __builtin_bit_cast(bf16x8, v);
    }
#pragma unroll
    for (int b = 0; b < 3; b++) {
      const bf16x8 a0 = rd128(ax + AX_W + (16 * cg + jl) * 16, b * 1024), a1 = rd128(ax + AX_W + (16 * cg + jl) * 16, 3072 + b * 1024);
#pragma unroll
      for (int T = 0; T < 4; T++) { ar[b][T] = MFMA(t[T], a0, ar[b][T]); ai[b][T] = MFMA(t[T], a1, ai[b][T]); }
    }
  }
  f32x4 lc[3];
  const u32 scr = lds0 + TOFF + (22 & 3) * TSLOT + ((4 * rg + q) * 192 + 16 * cg + jl) * 8;
  f32x2 e0[3];
#pragma unroll
  for (int T = 0; T < 4; T++) {
    const f32x4 r = rdf4(ax + AX_S + 512 + (64 * rg + 4 * q) * 4, T * 64);
#pragma unroll
    for (int b = 0; b < 3; b++) { ar[b][T] *= r; ai[b][T] *= r; }
  }
  f32x2 l8[3];
#pragma unroll
  for (int b = 0; b < 3; b++) {
    lc[b] = rdf4(ax + AX_TAB + (16 * cg + jl) * 32, b * 2048);
    l8[b] = *(LAS f32x2*)(size_t)(ax + AX_TAB + (16 * cg + jl) * 32 + b * 2048 + 16);
    f32x2 hr = {ar[b][0][0], ar[b][0][1]}, hi = {ai[b][0][0], ai[b][0][1]};
#pragma unroll
    for (int k = 1; k < 8; k++) {
      const f32x2 ur = {ar[b][k >> 1][(k & 1) * 2], ar[b][k >> 1][(k & 1) * 2 + 1]}, ui = {ai[b][k >> 1][(k & 1) * 2], ai[b][k >> 1][(k & 1) * 2 + 1]};
      const f32x2 nr = lc[b][0] * hr + (ur - lc[b][1] * hi), ni = lc[b][0] * hi + (ui + lc[b][1] * hr);
      hr = nr; hi = ni;
    }
    e0[b] = (f32x2){hr.x, hi.x};
    *(LAS f32x2*)(size_t)(scr + b * 512) = (f32x2){l8[b].x * hr.x - l8[b].y * hi.x + hr.y, l8[b].x * hi.x + l8[b].y * hr.x + hi.y};
  }
  STAMP(8);
  WAITL(); BAR();
  STAMP(9);
#pragma unroll
  for (int b = 0; b < 3; b++) {
    const float c2r = 2.f * lc[b][2], c2i = 2.f * lc[b][3];
#pragma unroll
    for (int T = 0; T < 4; T++) {
      const f32x4 nr = c2r * ar[b][T] - c2i * ai[b][T], ni = -c2r * ai[b][T] - c2i * ar[b][T];
      ar[b][T] = nr; ai[b][T] = ni;
      asm volatile("" : "+v"(ar[b][T]), "+v"(ai[b][T]));
    }
  }
  BAR();
  STAMP(10);
  const rsrc_t rWS = mk(p.ws, WS_END);
  const u32 io0 = w * 2048 + lane * 16, io1 = w * 2048 + 1024 + ((lane * 16) ^ 64);
  const u32 pg = WS_P + (rt * 15 + 12 + j) * 98304;
  u32x4 H[6][2];
#pragma unroll
  for (int b = 0; b < 3; b++) {
    const f32x2 cin = *(LAS f32x2*)(size_t)(scr + b * 512);
    const f32x2 sr = {cin.x, l8[b].x * cin.x - l8[b].y * cin.y + e0[b].x}, si = {cin.y, l8[b].x * cin.y + l8[b].y * cin.x + e0[b].y};
    const float c2r = 2.f * lc[b][2], c2i = 2.f * lc[b][3];
    f32x2 hr = c2r * sr - c2i * si, hi = -c2r * si - c2i * sr;
#pragma unroll
    for (int T = 0; T < 4; T++) {
      f32x2 pr[2], pi[2];
#pragma unroll
      for (int k = 0; k < 2; k++) {
        const f32x2 ur = {ar[b][T][2 * k], ar[b][T][2 * k + 1]}, ui = {ai[b][T][2 * k], ai[b][T][2 * k + 1]};
        const f32x2 nr = lc[b][0] * hr + (ur + lc[b][1] * hi), ni = lc[b][0] * hi + (ui - lc[b][1] * hr);
        hr = nr; hi = ni; pr[k] = nr; pi[k] = ni;
      }
      H[2 * b][0][T] = cvtpk(pr[0].x, pr[1].x); H[2 * b][1][T] = cvtpk(pr[0].y, pr[1].y);
      H[2 * b + 1][0][T] = cvtpk(pi[0].x, pi[1].x); H[2 * b + 1][1][T] = cvtpk(pi[0].y, pi[1].y);
    }
#pragma unroll
    for (int part = 0; part < 2; part++)
#pragma unroll
      for (int hh = 0; hh < 2; hh++) __builtin_amdgcn_raw_buffer_store_b128(H[2 * b + part][hh], rWS, hh ? io1 : io0, pg + (2 * b + part) * 16384, 16);
    SB();
  }
  auto own = [&](int i) { wr128(lds0 + TOFF + io0, ((19 + i) & 3) * TSLOT, H[i][0]); wr128(lds0 + TOFF + io1, ((19 + i) & 3) * TSLOT, H[i][1]); };
  own(0); own(1); own(2);
  STAMP(3);
  phaseB<true>(p, rt, 4, j, w, lane, lds0, tag, own);
}

DI void spare(const Params& p, int sid, u32 lds0, u32 tag) {
  const int tid = threadIdx.x;
  const rsrc_t rWS = mk(p.ws, WS_END);
  u32 hsn = 0;
  if (sid == 15) hs_leader(p.ws); else if (tid < 64) hsn = hs_request(p.ws);
  if (sid < 12) {
    const int cgp = tid % 48, rgp = tid / 48;
    const GAS u32x4* src = (const GAS u32x4*)(p.w1 + 2 * rgp * F + sid * 384) + cgp;
    float a[8];
#pragma unroll
    for (int c = 0; c < 8; c++) a[c] = 0.f;
    u32x4 v[36];
#pragma unroll
    for (int i = 0; i < 24; i++) v[i] = src[((i >> 1) * 32 + (i & 1)) * (F / 8)];
#pragma unroll
    for (int i = 0; i < 18; i++) {
#pragma unroll
      for (int c = 0; c < 4; c++) {
        a[2 * c] = dot2(__builtin_amdgcn_perm(v[2 * i + 1][c], v[2 * i][c], 0x05040100), ONES, a[2 * c]);
        a[2 * c + 1] = dot2(__builtin_amdgcn_perm(v[2 * i + 1][c], v[2 * i][c], 0x07060302), ONES, a[2 * c + 1]);
      }
      SB();
      if (i < 6) { v[24 + 2 * i] = src[((12 + i) * 32) * (F / 8)]; v[25 + 2 * i] = src[((12 + i) * 32 + 1) * (F / 8)]; }
      SB();
    }
    *(LAS f32x4*)(size_t)(lds0 + (rgp * 384 + cgp * 8) * 4) = (f32x4){a[0], a[1], a[2], a[3]};
    *(LAS f32x4*)(size_t)(lds0 + (rgp * 384 + cgp * 8) * 4 + 16) = (f32x4){a[4], a[5], a[6], a[7]};
    WAITL(); BAR();
    if (tid < 384) {
      float s = 0.f;
#pragma unroll
      for (int r = 0; r < 16; r++) s += rdf(lds0 + tid * 4, r * 1536);
      float c1, c2, c3; split3(s, c1, c2, c3);
      const float b = bf2f(p.b1[sid * 384 + tid]);
      __builtin_amdgcn_raw_buffer_store_b128((u32x4){cvtpk(c1, c2), cvtpk(c1, c2), cvtpk(c3, c1), cvtpk(b, b)}, rWS, tid * 16, WS_AUXW + sid * 6144, 16);
    }
  } else if (sid < 15) {
    const int row = tid >> 2, part = tid & 3;
#pragma unroll
    for (int h = 0; h < 2; h++) {
      const GAS u32x4* src = (const GAS u32x4*)((h ? p.bim : p.bre) + ((sid - 12) * 192 + row) * D) + part;
      u32x4 v[18];
#pragma unroll
      for (int i = 0; i < 18; i++) v[i] = src[4 * i];
      float s = 0.f, s2 = 0.f;
#pragma unroll
      for (int i = 0; i < 18; i++) { s = dot2(v[i][0], ONES, s); s2 = dot2(v[i][1], ONES, s2); s = dot2(v[i][2], ONES, s); s2 = dot2(v[i][3], ONES, s2); }
      s += s2; s += __shfl_xor(s, 1); s += __shfl_xor(s, 2);
      float c1, c2, c3; split3(s, c1, c2, c3);
      *(LAS u32x4*)(size_t)(lds0 + (h * 192 + row) * 16) = (u32x4){cvtpk(c1, c2), cvtpk(c1, c2), cvtpk(c3, c1), 0u};
    }
    if (tid < 192) {
      const int c = (sid - 12) * 192 + tid;
      const float lr = bf2f(p.lre[c]), li = bf2f(p.lim[c]);
      const float dt = rbf(expf(bf2f(p.logdt[c])));
      const float ex = expf(dt * lr), an = dt * li;
      const float br = ex * cosf(an), bi = ex * sinf(an);
      const float nr = br - 1.f, den = 1.f / (lr * lr + li * li);
      const float cr = (nr * lr + bi * li) * den, ci = (bi * lr - nr * li) * den;
      float ar = br, ai = bi, r16 = 0.f, i16 = 0.f;
#pragma unroll
      for (int k = 0; k < 7; k++) { const float t = ar * ar - ai * ai; ai = 2.f * ar * ai; ar = t; if (k == 2) { r16 = ar; i16 = ai; } }
      *(LAS f32x4*)(size_t)(lds0 + 6144 + tid * 32) = (f32x4){br, bi, cr, ci};
      *(LAS f32x4*)(size_t)(lds0 + 6144 + tid * 32 + 16) = (f32x4){r16, i16, ar, ai};
    }
    WAITL(); BAR();
    const u32x4 v = *(LAS u32x4*)(size_t)(lds0 + tid * 16);
    __builtin_amdgcn_raw_buffer_store_b128(v, rWS, tid < 384 ? WS_AUXW + sid * 6144 + tid * 16 : WS_TAB + (sid - 12) * 6144 + (tid - 384) * 16, 0, 16);
  }
  waitv<0>(); BAR();
  if (tid < 64 && sid < 15) { hs_finish(p.ws, hsn); st_flag(p.ws, FL_C + sid, tag); }
  {
    u32 acc = 0;
    const GAS u32* w2 = (const GAS u32*)p.w2;
    if (tid < 648) {
#pragma unroll
      for (int i = 0; i < 4; i++) {
        const int L = (i * 16 + sid) * 648 + tid;
        const int r = L / 9, kb = r / 1536, rr = r - kb * 1536;
        acc ^= w2[((rr >> 5) * 96 + kb * 32 + (rr & 31)) * (ROWB / 4) + (L - r * 9) * 32];
      }
      if (tid < 324) acc ^= ((const GAS u32*)p.cre)[(sid * 324 + tid) * 32]; else acc ^= ((const GAS u32*)p.cim)[(sid * 324 + tid - 324) * 32];
    }
    asm volatile("" :: "v"(acc));
  }
}

__global__ __launch_bounds__(768, 3) void fused_kernel(Params p) {
  extern __shared__ __attribute__((aligned(16))) char smem[];
  const u32 lds0 = (u32)(size_t)(LAS char*)smem;
  const int b = blockIdx.x, xcd = b & 7, idx = b >> 3;
  const u32 tag = 1u;
  STAMP(0);
  if (idx >= 30) { spare(p, xcd * 2 + idx - 30, lds0, tag); STAMP(7); return; }
  const int tri = xcd * 10 + idx / 3, j = idx % 3, g = tri >> 4, rt = tri & 15;
  const int lane = threadIdx.x & 63, w = __builtin_amdgcn_readfirstlane(threadIdx.x >> 6);
  if (g < 4) {
    if (w >= 8) loader<false>(p, rt, g, j, w - 8, lane, lds0, tag); else ffn_compute(p, rt, g, j, w, lane, lds0, tag);
  } else {
    if (w >= 8) loader<true>(p, rt, g, j, w - 8, lane, lds0, tag); else ssm_compute(p, rt, j, w, lane, lds0, tag);
  }
}

extern "C" void kernel_launch(void* const* d_in, const int* in_sizes, int n_in, void* d_out, int out_size, void* d_ws, size_t ws_size, hipStream_t stream) {
  Params p;
  p.x = (const u16*)d_in[0]; p.lre = (const u16*)d_in[1]; p.lim = (const u16*)d_in[2]; p.bre = (const u16*)d_in[3]; p.bim = (const u16*)d_in[4];
  p.cre = (const u16*)d_in[5]; p.cim = (const u16*)d_in[6]; p.logdt = (const u16*)d_in[7]; p.dskip = (const u16*)d_in[8];
  p.w1 = (const u16*)d_in[10]; p.b1 = (const u16*)d_in[11]; p.w2 = (const u16*)d_in[12]; p.b2 = (const u16*)d_in[13];
  p.out = (float*)d_out; p.ws = (char*)d_ws;
  p.stamps = (u64*)((char*)d_ws + (200u << 20));
  (void)hipFuncSetAttribute((const void*)fused_kernel, hipFuncAttributeMaxDynamicSharedMemorySize, LDS_BYTES);
  fused_kernel<<<256, 768, LDS_BYTES, stream>>>(p);
}
```

```cpp
#define STAMPS
#define K0 0
#define A0 0
#define B0 0
#define K1 0
#define A1 0
#define B1 0
#include <hip/hip_runtime.h>
#include <utility>

#define LAS __attribute__((address_space(3)))
#define GAS __attribute__((address_space(1)))
#define DI __device__ __forceinline__
typedef unsigned short u16;
typedef unsigned u32;
typedef unsigned long long u64;
typedef unsigned u32x4 __attribute__((ext_vector_type(4)));
typedef unsigned u32x2 __attribute__((ext_vector_type(2)));
typedef short s16x4 __attribute__((ext_vector_type(4)));
typedef short bf16x8 __attribute__((ext_vector_type(8)));
typedef float f32x4 __attribute__((ext_vector_type(4)));
typedef float f32x2 __attribute__((ext_vector_type(2)));
typedef __bf16 bf16x2 __attribute__((ext_vector_type(2)));
typedef __amdgpu_buffer_rsrc_t rsrc_t;

constexpr int S = 2048, D = 576, F = 4608;
constexpr int ROWB = D * 2;
constexpr int WSLOT = 24576, TOFF = 4 * WSLOT, TSLOT = 16384, LDS_BYTES = 163840;
constexpr int AX_W = 0, AX_T = 6144, AX_S = 8192, AX_TAB = 10240;
constexpr u32 WS_FLAGS = 0;
constexpr u32 FL_S = 0, FL_C = 16, FL_P = 32, FL_O = 32 + 240, NFLAG = 32 + 480;
constexpr u32 WS_EG = NFLAG * 128;
constexpr u32 WS_REQ = WS_EG + 16 * 576 * 16, WS_RSP = WS_REQ + 1024, WS_ACK = WS_RSP + 1024;
constexpr u32 WS_AUXW = 256 * 1024;
constexpr u32 WS_TAB = WS_AUXW + 15 * 6144;
constexpr u32 WS_P = 1u << 20;
constexpr u32 XBW = 5 * 1536;
constexpr u32 WS_XB = WS_P + 240 * 98304;
constexpr u32 WS_END = WS_XB + 48 * 32 * XBW;
constexpr u32 SPIN_CAP = 1u << 18;

struct Params {
  const u16 *x, *lre, *lim, *bre, *bim, *cre, *cim, *logdt, *dskip, *w1, *b1, *w2, *b2;
  float* out; char* ws;
  u64* stamps;
};

template <int N> DI void waitv() { asm volatile("s_waitcnt vmcnt(%0)" :: "n"(N) : "memory"); }
#define WAITL() asm volatile("s_waitcnt lgkmcnt(0)" ::: "memory")
#define BAR() do { asm volatile("" ::: "memory"); __builtin_amdgcn_s_barrier(); asm volatile("" ::: "memory"); } while (0)
#define SB() __builtin_amdgcn_sched_barrier(0)
#define MFMA(a, b, c) __builtin_amdgcn_mfma_f32_16x16x32_bf16((a), (b), (c), 0, 0, 0)
#define STAMP(i) do { SB(); if (threadIdx.x == 0) p.stamps[blockIdx.x * 16 + (i)] = __builtin_amdgcn_s_memrealtime(); SB(); } while (0)

DI void dma(rsrc_t r, u32 lds, u32 voff, u32 soff) {
  asm volatile("s_mov_b32 m0, %0\n\ts_nop 0\n\tbuffer_load_dwordx4 %1, %2, %3 offen lds" :: "s"(lds), "v"(voff), "s"(r), "s"(soff) : "memory");
}
DI void dma_sc1(rsrc_t r, u32 lds, u32 voff, u32 soff) {
  asm volatile("s_mov_b32 m0, %0\n\ts_nop 0\n\tbuffer_load_dwordx4 %1, %2, %3 offen sc1 lds" :: "s"(lds), "v"(voff), "s"(r), "s"(soff) : "memory");
}
DI rsrc_t mk(const void* ptr, u32 bytes) { return __builtin_amdgcn_make_buffer_rsrc((void*)ptr, 0, bytes, 0x00020000); }
DI s16x4 rd_tr1(u32 a, int off) { return __builtin_amdgcn_ds_read_tr16_b64_v4i16((LAS s16x4*)(size_t)(a + off)); }
DI bf16x8 cat(s16x4 lo, s16x4 hi) { return __builtin_shufflevector(lo, hi, 0, 1, 2, 3, 4, 5, 6, 7); }
DI bf16x8 rd_tr(u32 a, int off) { return cat(rd_tr1(a, off), rd_tr1(a, off + 2048)); }
DI bf16x8 rd128(u32 a, int off) { return *(LAS bf16x8*)(size_t)(a + off); }
DI f32x4 rdf4(u32 a, int off) { return *(LAS f32x4*)(size_t)(a + off); }
DI float rdf(u32 a, int off) { return *(LAS float*)(size_t)(a + off); }
DI void wr128(u32 a, int off, u32x4 v) { *(LAS u32x4*)(size_t)(a + off) = v; }
DI u32 cvtpk(float lo, float hi) { f32x2 v = {lo, hi}; bf16x2 b = __builtin_convertvector(v, bf16x2); return __builtin_bit_cast(u32, b); }
DI float bflo(u32 v) { return __uint_as_float(v << 16); }
DI float bfhi(u32 v) { return __uint_as_float(v & 0xffff0000u); }
DI float bf2f(u16 v) { return __uint_as_float((u32)v << 16); }
DI float dot2(u32 a, u32 b, float c) { return __builtin_amdgcn_fdot2_f32_bf16(__builtin_bit_cast(bf16x2, a), __builtin_bit_cast(bf16x2, b), c, false); }
constexpr u32 ONES = 0x3f803f80u;
DI float rbf(float v) { return bflo(cvtpk(v, 0.f)); }
DI void split3(float v, float& a, float& b, float& c) { a = rbf(v); const float r = v - a; b = rbf(r); c = rbf(r - b); }
template <int C> DI float dppx(float v) { return __uint_as_float(__builtin_amdgcn_update_dpp(0u, __float_as_uint(v), C, 0xf, 0xf, true)); }
template <int C> DI double dppd(double v) { u32x2 t = __builtin_bit_cast(u32x2, v); t[0] = __builtin_amdgcn_update_dpp(0u, t[0], C, 0xf, 0xf, true); t[1] = __builtin_amdgcn_update_dpp(0u, t[1], C, 0xf, 0xf, true); return __builtin_bit_cast(double, t); }
DI void st_flag(char* ws, u32 idx, u32 tag) { __hip_atomic_store((GAS u32*)(ws + WS_FLAGS + idx * 128), tag, __ATOMIC_RELAXED, __HIP_MEMORY_SCOPE_AGENT); }
DI u32 ld_flag(const char* a) { return __hip_atomic_load((GAS u32*)a, __ATOMIC_RELAXED, __HIP_MEMORY_SCOPE_AGENT); }
DI void spin_all(const char* a, u32 tag) {
  for (u32 i = 0; i < SPIN_CAP; i++) { if (__all(ld_flag(a) == tag)) break; __builtin_amdgcn_s_sleep(1); }
  asm volatile("" ::: "memory");
}
DI void st_word(char* a, u32 v) { __hip_atomic_store((GAS u32*)a, v, __ATOMIC_RELAXED, __HIP_MEMORY_SCOPE_AGENT); }
DI u32 hs_request(char* ws) { const u32 n = ld_flag(ws + WS_RSP + blockIdx.x * 4) + 1u; st_word(ws + WS_REQ + blockIdx.x * 4, n); return n; }
DI void hs_finish(char* ws, u32 n) { spin_all(ws + WS_RSP + blockIdx.x * 4, n); st_word(ws + WS_ACK + blockIdx.x * 4, 1u); }
DI void hs_leader(char* ws) {
  const int tid = threadIdx.x;
  const rsrc_t r = mk(ws, WS_END);
  if (tid < (int)NFLAG) st_word(ws + WS_FLAGS + tid * 128, 0u); else st_word(ws + WS_ACK + (tid - NFLAG) * 4, 0u);
#pragma unroll
  for (int i = 0; i < 12; i++) __builtin_amdgcn_raw_buffer_store_b128((u32x4){0u, 0u, 0u, 0u}, r, tid * 16, WS_EG + i * 12288, 16);
  waitv<0>(); BAR();
  if (tid < 256) {
    bool done = tid == 255; bool first = true; u32 last = 0;
    for (u32 i = 0; i < SPIN_CAP; i++) {
      const u32 rq = ld_flag(ws + WS_REQ + tid * 4), ak = ld_flag(ws + WS_ACK + tid * 4);
      if (first || rq != last) st_word(ws + WS_RSP + tid * 4, rq);
      first = false; last = rq; done = done || ak == 1u;
      if (__all(done)) break;
      __builtin_amdgcn_s_sleep(8);
    }
  }
}
DI u32 poll_issue(const char* a) { u32 v; asm volatile("global_load_dword %0, %1, off sc1" : "=v"(v) : "v"(a) : "memory"); return v; }

DI f32x2 gelu2(f32x2 a, float k1, float k2, float sg) {
  f32x2 t = a * a; t = t * k2 + k1; t = t * a;
  t.x = __builtin_amdgcn_exp2f(t.x); t.y = __builtin_amdgcn_exp2f(t.y);
  t = t * sg + sg;
  t.x = __builtin_amdgcn_rcpf(t.x); t.y = __builtin_amdgcn_rcpf(t.y);
  return t * a;
}
DI u32x4 gelu_frag(f32x4 a, f32x4 b, float k1, float k2, float sg) {
  f32x2 g0 = gelu2((f32x2){a[0], a[1]}, k1, k2, sg), g1 = gelu2((f32x2){a[2], a[3]}, k1, k2, sg);
  f32x2 g2 = gelu2((f32x2){b[0], b[1]}, k1, k2, sg), g3 = gelu2((f32x2){b[2], b[3]}, k1, k2, sg);
  return (u32x4){cvtpk(g0.x, g0.y), cvtpk(g1.x, g1.y), cvtpk(g2.x, g2.y), cvtpk(g3.x, g3.y)};
}

template <int H>
DI void stepA(f32x4 (&acc)[6][4], bf16x8 (&wp)[2], bf16x8 (&t)[4], const u32 (&wc)[4], u32 n0, u32 n1, u32 xn) {
  bf16x8 w[8], nx[4];
  w[0] = wp[0]; w[1] = wp[1];
#pragma unroll
  for (int n = 0; n < 6; n++) {
    const int f = n + 2, ff = f < 6 ? f : f - 6;
    const u32 a = (f < 6 || H == 0) ? wc[ff & 3] : (ff ? n1 : n0); const int off = (f < 6 ? H : 1 - H) * WSLOT + (ff >> 2) * 4096;
    acc[n][0] = MFMA(w[n], t[0], acc[n][0]); SB();
    s16x4 lo = rd_tr1(a, off); SB();
    acc[n][1] = MFMA(w[n], t[1], acc[n][1]); SB();
    s16x4 hi = rd_tr1(a, off + 2048); SB();
    acc[n][2] = MFMA(w[n], t[2], acc[n][2]); SB();
    if (n >= 1 && n <= 4) { nx[n - 1] = rd128(xn, (n - 1) * 2048); SB(); }
    acc[n][3] = MFMA(w[n], t[3], acc[n][3]); SB();
    w[f] = cat(lo, hi);
  }
  wp[0] = w[6]; wp[1] = w[7];
#pragma unroll
  for (int i = 0; i < 4; i++) t[i] = nx[i];
}

template <int KIND, class FW, class FT, class FN, class MID>
DI void half(f32x4 (&acc)[3][4], bf16x8 (&w)[3], bf16x8 (&tp)[2], FW&& rw, FT&& rt_, FN&& rn, MID&& mid) {
  bf16x8 t[4], wn[3], tn[2];
  t[0] = tp[0]; t[1] = tp[1];
  s16x4 lo;
#pragma unroll
  for (int i = 0; i < 12; i++) {
    const int T = i / 3, n = i % 3;
    if constexpr (KIND == 2) acc[n][T] = MFMA(t[T], w[n], acc[n][T]); else acc[n][T] = MFMA(w[n], t[T], acc[n][T]);
    SB();
    if constexpr (KIND == 0) {
      if (i == 0) t[2] = rt_(2, 0); if (i == 1) t[3] = rt_(3, 0);
      if (i >= 2 && i <= 7) { if ((i & 1) == 0) lo = rw((i - 2) >> 1, 0); else wn[(i - 2) >> 1] = cat(lo, rw((i - 2) >> 1, 1)); }
      if (i == 8) tn[0] = rn(0, 0); if (i == 9) tn[1] = rn(1, 0);
    } else if constexpr (KIND == 1) {
      if (i <= 3) { if ((i & 1) == 0) lo = rt_(2 + (i >> 1), 0); else t[2 + (i >> 1)] = cat(lo, rt_(2 + (i >> 1), 1)); }
      if (i >= 4 && i <= 6) wn[i - 4] = rw(i - 4, 0);
      if (i >= 7 && i <= 10) { if ((i & 1) == 1) lo = rn((i - 7) >> 1, 0); else tn[(i - 7) >> 1] = cat(lo, rn((i - 7) >> 1, 1)); }
    } else {
      if (i == 0) t[2] = rt_(2, 0); if (i == 2) t[3] = rt_(3, 0);
      if (i == 3 || i == 5 || i == 6) wn[i == 3 ? 0 : i - 4] = rw(i == 3 ? 0 : i - 4, 0);
      if (i == 8) tn[0] = rn(0, 0); if (i == 9) tn[1] = rn(1, 0);
    }
    mid(i);
    SB();
  }
#pragma unroll
  for (int i = 0; i < 3; i++) w[i] = wn[i];
  tp[0] = tn[0]; tp[1] = tn[1];
}

template <int I, int N, class FN> DI void sfor(FN&& f) { if constexpr (I < N) { f(std::integral_constant<int, I>{}); sfor<I + 1, N>(f); } }

DI void ssm_carry(const Params& p, int rt, int j, int c, u32 lds0, u32 tag) {
  const u32 ax = lds0 + (18 & 3) * WSLOT, scr = lds0 + TOFF + (22 & 3) * TSLOT;
  {
    f32x4 pw = rdf4(ax + AX_TAB + c * 32, 16);
    { const float t = pw[0] * pw[0] - pw[1] * pw[1]; pw[1] = 2.f * pw[0] * pw[1]; pw[0] = t; }
    f32x2 es[8];
#pragma unroll
    for (int s = 0; s < 8; s++) es[s] = *(LAS f32x2*)(size_t)(scr + (s * 192 + c) * 8);
    float sr = 0.f, si = 0.f;
#pragma unroll
    for (int s = 0; s < 8; s++) { const float nr = pw[0] * sr - pw[1] * si + es[s].x, ni = pw[0] * si + pw[1] * sr + es[s].y; sr = nr; si = ni; }
    const rsrc_t rWS = mk(p.ws, WS_END);
    __builtin_amdgcn_raw_buffer_store_b128((u32x4){__float_as_uint(sr), tag, __float_as_uint(si), tag}, rWS, (192 * j + c) * 16, WS_EG + rt * 576 * 16, 16);
    float cr = 0.f, ci = 0.f;
    for (u32 spin = 0; spin < SPIN_CAP; spin++) {
      u32x4 v[15]; bool ok = true;
#pragma unroll
      for (int i = 0; i < 15; i++) v[i] = __builtin_amdgcn_raw_buffer_load_b128(rWS, (192 * j + c) * 16, WS_EG + (i < rt ? i : 0) * 576 * 16, 16);
      cr = 0.f; ci = 0.f;
#pragma unroll
      for (int i = 0; i < 15; i++) {
        if (i < rt) {
          ok = ok && v[i][1] == tag && v[i][3] == tag;
          const float nr = pw[2] * cr - pw[3] * ci + __uint_as_float(v[i][0]), ni = pw[2] * ci + pw[3] * cr + __uint_as_float(v[i][2]); cr = nr; ci = ni;
        }
      }
      if (__all(ok)) break;
      __builtin_amdgcn_s_sleep(1);
    }
#pragma unroll
    for (int s = 0; s < 8; s++) {
      *(LAS f32x2*)(size_t)(scr + (s * 192 + c) * 8) = (f32x2){cr, ci};
      const float nr = pw[0] * cr - pw[1] * ci + es[s].x, ni = pw[0] * ci + pw[1] * cr + es[s].y; cr = nr; ci = ni;
    }
  }
}

template <bool SSM>
DI void loader(const Params& p, int rt, int g, int j, int u, int lane, u32 lds0, u32 tag) {
  const rsrc_t rX = mk(p.x, S * ROWB), rWS = mk(p.ws, WS_END);
  const rsrc_t rA0 = SSM ? mk(p.bre, D * ROWB) : mk(p.w1, D * F * 2), rA1 = SSM ? mk(p.bim, D * ROWB) : rA0;
  const rsrc_t rB0 = SSM ? mk(p.cre, D * ROWB) : mk(p.w2, D * F * 2), rB1 = SSM ? mk(p.cim, D * ROWB) : rB0;
  const int r8 = lane >> 3, gp = lane & 7;
  const int cgr = ((((gp >> 1) ^ ((r8 >> 1) & 3)) << 1) | (gp & 1));
  const int rgr = gp ^ (4 * (u & 1) + (r8 >> 1));
  const int slice = 3 * g + j;
  const int kA = 16 * (u & 1) + 8 * (r8 >> 2) + 4 * (u >> 1) + (r8 & 3);
  const u32 voffA = SSM ? (192 * j + 8 * u + r8) * ROWB + rgr * 16 : kA * (F * 2) + slice * 768 + cgr * 16;
  const u32 voffB = SSM ? voffA : (8 * u + r8) * ROWB + j * 384 + cgr * 16;
  const int R = 8 * u + r8;
  const int tok = SSM ? 16 * ((R >> 2) & 3) + 8 * (R & 1) + 2 * (R >> 4) + ((R >> 1) & 1) : R;
  const u32 voffX = (rt * 128 + tok) * ROWB + rgr * 16;
  const u32 voffL = lane * 16;
  const u32 ldsW = lds0 + u * 1024, ldsT = lds0 + TOFF;
  const int rot = rt % 9;
  const int j1 = j == 2 ? 0 : j + 1, j2 = j == 0 ? 2 : j - 1;
  auto issueA = [&](int s, auto&& fill) {
    int kt = (s >> 1) + rot; if (kt >= 9) kt -= 9;
    const u32 l = ldsW + (s & 3) * WSLOT;
    const u32 so = SSM ? kt * 128 : (kt * 64 + (s & 1) * 32) * (F * 2);
#pragma unroll
    for (int i = 0; i < 6; i++) { dma((s & 1) ? rA1 : rA0, l + i * 4096, voffA, so + i * (SSM ? 32 * ROWB : 128)); SB(); fill(i); SB(); }
  };
  auto nofill = [](int) {};
  auto issueX = [&](int kap, int h) {
    int kt = kap + rot; if (kt >= 9) kt -= 9;
    const u32 l = ldsT + (kap & 3) * TSLOT + h * 8192 + u * 1024, so = h * 64 * ROWB + kt * 128;
    dma(rX, l, voffX, so); dma(rX, l + 4096, voffX, so + (SSM ? 4 : 32) * ROWB);
  };
  auto issueB = [&](int s) {
    const int i = s < 25 ? s - 19 : (s - 25) >> 1;
    const int jj = s < 25 ? j : (((s - 25) & 1) ? j2 : j1);
    const u32 l = ldsW + (s & 3) * WSLOT;
    if (SSM) {
      const u32 so = (192 * jj + 64 * (i >> 1)) * 2;
#pragma unroll
      for (int k = 0; k < 6; k++) dma((i & 1) ? rB1 : rB0, l + k * 4096, voffB, k * 32 * ROWB + so);
    } else {
      const u32 so = ((3 * g + jj) * 384 + 192 * (i & 1) + 32 * (i >> 1)) * ROWB;
#pragma unroll
      for (int k = 0; k < 6; k++) dma(rB0, l + k * 4096, voffB, so + (k / 3) * 96 * ROWB + (k % 3) * 128);
    }
    if (s >= 25) {
      const u32 sp = WS_P + (rt * 15 + 3 * g + jj) * 98304 + i * 16384 + u * 4096, lp = ldsT + (s & 3) * TSLOT + u * 4096;
#pragma unroll
      for (int k = 0; k < 4; k++) dma_sc1(rWS, lp + k * 1024, voffL, sp + k * 1024);
    }
  };
  auto issueAux = [&]() {
    const u32 l = lds0 + (18 & 3) * WSLOT;
#pragma unroll
    for (int k = 0; k < (SSM ? 3 : 2); k++) {
      int pc = SSM ? u * 3 + k : u * 2 + k; if (!SSM && pc >= 6) pc -= 6;
      const u32 so = pc < 6 ? WS_AUXW + slice * 6144 + pc * 1024 : WS_TAB + j * 6144 + (pc - 6) * 1024;
      dma_sc1(rWS, l + (pc < 6 ? AX_W + pc * 1024 : AX_TAB + (pc - 6) * 1024), voffL, so);
    }
  };
  float s1[4] = {0.f, 0.f, 0.f, 0.f}, s2[4] = {0.f, 0.f, 0.f, 0.f};
  u32x4 sv[2];
  auto statRead = [&](int hs) {
    sv[0] = __builtin_bit_cast(u32x4, rd128(ldsT + u * 1024 + voffL, ((hs >> 1) & 3) * TSLOT + (hs & 1) * 8192));
    sv[1] = __builtin_bit_cast(u32x4, rd128(ldsT + u * 1024 + voffL, ((hs >> 1) & 3) * TSLOT + (hs & 1) * 8192 + 4096));
  };
  auto statAcc = [&](int hs, int c) {
#pragma unroll
    for (int i = 0; i < 2; i++) { const int k = (hs & 1) * 2 + i; s1[k] = dot2(sv[i][c], ONES, s1[k]); s2[k] = dot2(sv[i][c], sv[i][c], s2[k]); }
  };
  auto statFinish = [&]() {
    const u32 ax = lds0 + (18 & 3) * WSLOT;
    const bool o1 = gp & 1, o2 = gp & 2;
    double a[2], b[2];
#pragma unroll
    for (int i = 0; i < 2; i++) {
      a[i] = (double)(o1 ? s1[2 * i + 1] : s1[2 * i]) + (double)dppx<0xb1>(o1 ? s1[2 * i] : s1[2 * i + 1]);
      b[i] = (double)(o1 ? s2[2 * i + 1] : s2[2 * i]) + (double)dppx<0xb1>(o1 ? s2[2 * i] : s2[2 * i + 1]);
    }
    double A = (o2 ? a[1] : a[0]) + dppd<0x4e>(o2 ? a[0] : a[1]), B = (o2 ? b[1] : b[0]) + dppd<0x4e>(o2 ? b[0] : b[1]);
    A += dppd<0x1b>(dppd<0x141>(A)); B += dppd<0x1b>(dppd<0x141>(B));
    const double m = A * (1.0 / D);
    const float mean = (float)m, ve = fmaxf((float)(B * (1.0 / D) - m * m), 0.f) + 1e-5f;
    const float rstd = __builtin_amdgcn_rsqf(ve), sig = ve * rstd;
    float m1, m2, m3; split3(-mean, m1, m2, m3);
    const float g1 = rbf(sig), g2 = sig - g1;
    const int tk = R + (gp & 3) * 32;
    if (gp < 4) wr128(ax + AX_T + tk * 16, 0, (u32x4){cvtpk(m1, m1), cvtpk(m2, m2), cvtpk(m1, m3), cvtpk(g1, g2)});
    else { LAS float* sp = (LAS float*)(size_t)(ax + AX_S + tk * 4); sp[0] = mean; sp[128] = rstd; sp[256] = sig; }
  };
  const char* fAux = p.ws + WS_FLAGS + (FL_C + slice) * 128;
  const char* fP = p.ws + WS_FLAGS + (FL_P + rt * 15 + 3 * g + ((lane & 1) ? j1 : j2)) * 128;
  __builtin_amdgcn_s_setprio(3);
  SB(); const u64 L0 = __builtin_amdgcn_s_memrealtime(); SB();
  issueA(0, nofill); issueX(0, 0); issueX(0, 1); issueA(1, nofill); issueX(1, 0); issueX(1, 1); issueA(2, nofill);
  SB(); const u64 L1 = __builtin_amdgcn_s_memrealtime(); SB();
  u32 pv = 0;
  waitv<25>(); SB(); const u64 L2 = __builtin_amdgcn_s_memrealtime(); SB();
  waitv<16>(); SB(); const u64 L3 = __builtin_amdgcn_s_memrealtime(); SB(); BAR();
  sfor<0, 37>([&](auto sc) {
    constexpr int s = decltype(sc)::value;
    constexpr int prev = s == 0 ? 10 : (s <= 14 ? 8 : (s == 15 ? 6 : (s == 16 ? 0 : (s == 17 ? (SSM ? 9 : 8) : (s <= 22 ? 6 : (s <= 34 ? 10 : 0))))));
    waitv<prev>();
    if constexpr (SSM && s == 19) { BAR(); if (u < 3) ssm_carry(p, rt, j, u * 64 + lane, lds0, tag); WAITL(); BAR(); }
    BAR();
    if constexpr (s == 20) { if (u == 0 && lane == 0) st_flag(p.ws, FL_P + rt * 15 + slice, tag); }
    if constexpr (s == 16) { asm volatile("" : "+v"(pv)); if (!__all(pv == tag)) spin_all(fAux, tag); issueAux(); }
    if constexpr (s == 22) { asm volatile("" : "+v"(pv)); if (!__all(pv == tag)) spin_all(fP, tag); }
    if constexpr (s == 15) pv = poll_issue(fAux);
    if constexpr (s == 21) pv = poll_issue(fP);
    if constexpr (s + 3 < 18) { statRead(s); SB(); issueA(s + 3, [&](int i) { if (i >= 1 && i <= 4) statAcc(s, i - 1); }); }
    else if constexpr (s + 3 > 18 && s + 3 < 37) issueB(s + 3);
    if constexpr ((s >> 1) + 2 < 9) issueX((s >> 1) + 2, s & 1);
    if constexpr (s == 15) {
#pragma unroll
      for (int hs = 15; hs < 18; hs++) { statRead(hs);
#pragma unroll
        for (int c = 0; c < 4; c++) statAcc(hs, c); }
    }
    if constexpr (s == 16) { statFinish(); WAITL(); }
  });
  BAR();
  if (threadIdx.x == 512) { p.stamps[blockIdx.x * 16 + 12] = L0; p.stamps[blockIdx.x * 16 + 13] = L1; p.stamps[blockIdx.x * 16 + 14] = L2; p.stamps[blockIdx.x * 16 + 15] = L3; }
}

template <bool SSM, class OWN>
DI void phaseB(const Params& p, int rt, int g, int j, int w, int lane, u32 lds0, u32 tag, OWN&& own) {
  asm volatile("" : "+v"(lane));
  const int rg = w >> 2, cg = w & 3, q = lane >> 4, e = (lane >> 2) & 3, pp = lane & 3, jl = lane & 15;
  const int z = 2 * (q & 1) + (e >> 1);
  u32 wlo[3], whi[3], tb[2];
#pragma unroll
  for (int n = 0; n < 3; n++) {
    const int t = 3 * cg + n;
    wlo[n] = SSM ? lds0 + (16 * t + jl) * 128 + ((q ^ (jl >> 1)) * 16) : lds0 + (t >> 2) * 4096 + (4 * q + e) * 128 + (((t & 3) ^ z) * 32) + pp * 8;
    whi[n] = SSM ? (wlo[n] ^ 64) : wlo[n] + 2 * WSLOT;
  }
  tb[0] = SSM ? lds0 + TOFF + rg * 8192 + (q >> 1) * 2048 + (pp >> 1) * 1024 + (((8 * (q & 1) + e) * 16) ^ ((pp >> 1) * 64)) + 8 * (pp & 1) : lds0 + TOFF + rg * 4096 + lane * 16;
  tb[1] = tb[0] ^ 64;
  float mean = 0.f, rstd = 0.f;
  if constexpr (!SSM) { const u32 sa = lds0 + (18 & 3) * WSLOT + AX_S + (64 * rg + 16 * g + jl) * 4; mean = rdf(sa, 0); rstd = rdf(sa, 512); }
  f32x4 ob[3][4];
#pragma unroll
  for (int n = 0; n < 3; n++)
#pragma unroll
    for (int T = 0; T < 4; T++) ob[n][T] = (f32x4){0.f, 0.f, 0.f, 0.f};
  auto RW = [&](int sl, int kb, int n, int part) {
    if constexpr (SSM) return rd128((kb ? whi[n] : wlo[n]) + (sl >> 1) * 2 * WSLOT, (sl & 1) * WSLOT);
    else return rd_tr1(sl < 2 ? wlo[n] : whi[n], (sl & 1) * WSLOT + kb * 12288 + part * 2048);
  };
  auto RT = [&](int sl, int kb, int T, int part) {
    if constexpr (SSM) return rd_tr1(tb[part], sl * TSLOT + kb * 4096 + T * 256);
    else return rd128(tb[0], sl * TSLOT + kb * 8192 + T * 1024);
  };
  bf16x8 wf[3], tp[2];
  auto step = [&](auto slc, auto&& mid) {
    constexpr int SL = decltype(slc)::value, NS = (SL + 1) & 3;
    half<SSM ? 1 : 0>(ob, wf, tp, [&](int n, int part) { return RW(SL, 1, n, part); }, [&](int T, int part) { return RT(SL, 0, T, part); }, [&](int T, int part) { return RT(SL, 1, T, part); }, mid);
    half<SSM ? 1 : 0>(ob, wf, tp, [&](int n, int part) { return RW(NS, 0, n, part); }, [&](int T, int part) { return RT(SL, 1, T, part); }, [&](int T, int part) { return RT(NS, 0, T, part); }, [](int) {});
  };
  auto nop = [](int) {};
  using I0 = std::integral_constant<int, 0>; using I1 = std::integral_constant<int, 1>; using I2 = std::integral_constant<int, 2>; using I3 = std::integral_constant<int, 3>;
  WAITL(); BAR();
  STAMP(4);
#pragma unroll
  for (int n = 0; n < 3; n++) { if constexpr (SSM) wf[n] = RW(3, 0, n, 0); else wf[n] = cat(RW(3, 0, n, 0), RW(3, 0, n, 1)); }
#pragma unroll
  for (int T = 0; T < 2; T++) { if constexpr (SSM) tp[T] = cat(RT(3, 0, T, 0), RT(3, 0, T, 1)); else tp[T] = RT(3, 0, T, 0); }
  step(I3{}, [&](int i) { if (i == 11) own(3); });
  waitv<0>(); WAITL(); BAR(); step(I0{}, [&](int i) { if (i == 11) own(4); });
  WAITL(); BAR(); step(I1{}, [&](int i) { if (i == 11) own(5); });
  WAITL(); BAR(); step(I2{}, nop);
  BAR(); step(I3{}, nop);
  BAR(); step(I0{}, nop);
  const int tok = rt * 128 + 64 * rg + 16 * g + jl, col = 192 * j + 48 * cg + 4 * q;
  u32x2 xv[3], bv[3], dv[3];
  if constexpr (!SSM) {
#pragma unroll
    for (int n = 0; n < 3; n++) {
      xv[n] = *(const GAS u32x2*)(p.x + tok * D + col + 16 * n); bv[n] = *(const GAS u32x2*)(p.b2 + col + 16 * n); dv[n] = *(const GAS u32x2*)(p.dskip + col + 16 * n);
    }
  }
  SB();
  for (int it = 0; it < 3; it++) {
    BAR(); step(I1{}, nop); BAR(); step(I2{}, nop); BAR(); step(I3{}, nop); BAR(); step(I0{}, nop);
  }
  STAMP(5);
  const rsrc_t rWS = mk(p.ws, WS_END);
  const u32 xb = WS_XB + (rt * 3 + j) * 32 * XBW + w * XBW;
  f32x4 mine[3];
#pragma unroll
  for (int T = 0; T < 4; T++) {
    if (SSM || T != g) {
      const u32 o = xb + T * 8 * XBW + g * 1536;
      const u32x4 a = {cvtpk(ob[0][T][0], ob[0][T][1]), cvtpk(ob[0][T][2], ob[0][T][3]), cvtpk(ob[1][T][0], ob[1][T][1]), cvtpk(ob[1][T][2], ob[1][T][3])};
      const u32 b0 = cvtpk(ob[2][T][0], ob[2][T][1]), b1 = cvtpk(ob[2][T][2], ob[2][T][3]);
      const auto s0 = __builtin_amdgcn_permlane32_swap(b0, b0, false, false), s1 = __builtin_amdgcn_permlane32_swap(b1, b1, false, false);
      __builtin_amdgcn_raw_buffer_store_b128(a, rWS, lane * 16, o, 16);
      if (lane < 32) __builtin_amdgcn_raw_buffer_store_b128((u32x4){b0, b1, s0[1], s1[1]}, rWS, lane * 16, o + 1024, 16);
    } else {
#pragma unroll
      for (int n = 0; n < 3; n++) mine[n] = ob[n][T];
    }
  }
  if constexpr (SSM) {
    waitv<0>(); BAR();
    if (threadIdx.x == 0) st_flag(p.ws, FL_O + (rt * 3 + j) * 5 + g, tag);
    STAMP(6); STAMP(7);
  } else {
    waitv<0>(); BAR();
    if (threadIdx.x == 0) st_flag(p.ws, FL_O + (rt * 3 + j) * 5 + g, tag);
    STAMP(6);
    spin_all(p.ws + WS_FLAGS + (FL_O + (rt * 3 + j) * 5 + (lane % 5)) * 128, tag);
    const u32 o = xb + g * 8 * XBW;
    u32x4 a[4]; u32x2 b[4];
#pragma unroll
    for (int k = 0; k < 4; k++) {
      const int src = k == 3 ? 4 : (k < g ? k : k + 1);
      a[k] = __builtin_amdgcn_raw_buffer_load_b128(rWS, lane * 16, o + src * 1536, 16);
      b[k] = __builtin_amdgcn_raw_buffer_load_b64(rWS, (lane & 31) * 16 + (lane >> 5) * 8, o + src * 1536 + 1024, 16);
    }
#pragma unroll
    for (int n = 0; n < 3; n++) {
      f32x4 r;
#pragma unroll
      for (int c = 0; c < 4; c++) {
        float G = mine[n][c] + ((c & 1) ? bfhi(bv[n][c >> 1]) : bflo(bv[n][c >> 1]));
#pragma unroll
        for (int k = 0; k < 3; k++) { const u32 v = n < 2 ? a[k][2 * n + (c >> 1)] : b[k][c >> 1]; G += (c & 1) ? bfhi(v) : bflo(v); }
        const u32 yv = n < 2 ? a[3][2 * n + (c >> 1)] : b[3][c >> 1];
        const float yy = (c & 1) ? bfhi(yv) : bflo(yv);
        const float xx = (c & 1) ? bfhi(xv[n][c >> 1]) : bflo(xv[n][c >> 1]);
        const float dd = (c & 1) ? bfhi(dv[n][c >> 1]) : bflo(dv[n][c >> 1]);
        const float sv = yy + (xx - mean) * rstd * dd;
        const float sig = __builtin_amdgcn_rcpf(1.f + __builtin_amdgcn_exp2f(-1.4426950408889634f * G));
        r[c] = xx + sv * (G * sig);
      }
      *(GAS f32x4*)(p.out + tok * D + col + 16 * n) = r;
    }
    STAMP(7);
  }
}

DI void ffn_compute(const Params& p, int rt, int g, int j, int w, int lane, u32 lds0, u32 tag) {
  const int rg = w >> 2, cg = w & 3, q = lane >> 4, e = (lane >> 2) & 3, pp = lane & 3, jl = lane & 15;
  const int z = 2 * (q & 1) + (e >> 1);
  u32 wc[4];
#pragma unroll
  for (int n = 0; n < 4; n++) { const int t = 6 * cg + n; wc[n] = lds0 + (t >> 2) * 4096 + (4 * q + e) * 128 + (((t & 3) ^ z) * 32) + pp * 8; }
  const u32 xbase = lds0 + TOFF + (64 * rg + jl) * 128 + ((q ^ (jl >> 1)) * 16);
  f32x4 acc[6][4];
#pragma unroll
  for (int n = 0; n < 6; n++)
#pragma unroll
    for (int T = 0; T < 4; T++) { acc[n][T] = (f32x4){0.f, 0.f, 0.f, 0.f}; asm volatile("" : "+v"(acc[n][T])); }
  bf16x8 wp[2], t[4];
  u32 hsn = 0; if (w == 7) hsn = __builtin_amdgcn_readfirstlane(hs_request(p.ws));
  BAR();
  STAMP(1);
  wp[0] = rd_tr(wc[0], 0); wp[1] = rd_tr(wc[1], 0);
#pragma unroll
  for (int T = 0; T < 4; T++) t[T] = rd128(xbase, T * 2048);
  u32 xs = 0; int dw = 2 * WSLOT;
#pragma nounroll
  for (int it = 0; it < 9; it++) {
    if (it == 2) STAMP(8); if (it == 4) STAMP(9); if (it == 7) STAMP(10); if (it == 8) STAMP(11);
    if (it == 7 && w == 7) hs_finish(p.ws, hsn);
    BAR(); stepA<0>(acc, wp, t, wc, 0, 0, (xbase ^ 64) + xs); xs = (xs + TSLOT) & (4 * TSLOT - 1);
    const u32 n0 = wc[0] + dw, n1 = wc[1] + dw;
    BAR(); stepA<1>(acc, wp, t, wc, n0, n1, xbase + xs);
    wc[0] = n0; wc[1] = n1; wc[2] += dw; wc[3] += dw; dw = -dw;
  }
  WAITL(); BAR();
  STAMP(2);
  const u32 ax = lds0 + (18 & 3) * WSLOT;
  float k1[4], k2[4], sg[4];
#pragma unroll
  for (int T = 0; T < 4; T++) {
    u32x4 v = __builtin_bit_cast(u32x4, rd128(ax + AX_T + (64 * rg + jl) * 16, T * 256));
    if (q != 0) v = (u32x4){0u, 0u, 0u, 0u};
    t[T] = __builtin_bit_cast(bf16x8, v);
    const float r = rdf(ax + AX_S + 512 + (64 * rg + jl) * 4, T * 64);
    sg[T] = rdf(ax + AX_S + 1024 + (64 * rg + jl) * 4, T * 64);
    k1[T] = -2.3022081985f * r; k2[T] = -0.10294324f * r * r * r;
  }
#pragma unroll
  for (int n = 0; n < 6; n++) {
    const bf16x8 a = rd128(ax + AX_W + (96 * cg + jl) * 16, n * 256);
#pragma unroll
    for (int T = 0; T < 4; T++) acc[n][T] = MFMA(a, t[T], acc[n][T]);
  }
  const rsrc_t rWS = mk(p.ws, WS_END);
  const u32 pg = WS_P + (rt * 15 + 3 * g + j) * 98304 + (cg >> 1) * 16384 + (cg & 1) * 8192 + rg * 4096;
  const u32 pl = lds0 + TOFF + (cg & 1) * 8192 + rg * 4096 + lane * 16;
  u32x4 P[3][4];
#pragma unroll
  for (int b = 0; b < 3; b++)
#pragma unroll
    for (int T = 0; T < 4; T++) {
      P[b][T] = gelu_frag(acc[2 * b][T], acc[2 * b + 1][T], k1[T], k2[T], sg[T]);
      __builtin_amdgcn_raw_buffer_store_b128(P[b][T], rWS, lane * 16, pg + b * 32768 + T * 1024, 16);
      SB();
    }
  auto own = [&](int i) {
    if ((cg >> 1) == (i & 1)) {
#pragma unroll
      for (int T = 0; T < 4; T++) wr128(pl, ((19 + i) & 3) * TSLOT + T * 1024, P[i >> 1][T]);
    }
  };
  own(0); own(1); own(2);
  STAMP(3);
  phaseB<false>(p, rt, g, j, w, lane, lds0, tag, own);
}

DI void ssm_compute(const Params& p, int rt, int j, int w, int lane, u32 lds0, u32 tag) {
  const int rg = w >> 2, cg = w & 3; int q = lane >> 4, jl = lane & 15;
  u32 wb = lds0 + (16 * cg + jl) * 128 + ((q ^ (jl >> 1)) * 16);
  const u32 xb0 = lds0 + TOFF + (64 * rg + jl) * 128 + ((q ^ (jl >> 1)) * 16);
  f32x4 ar[3][4], ai[3][4];
#pragma unroll
  for (int n = 0; n < 3; n++)
#pragma unroll
    for (int T = 0; T < 4; T++) { ar[n][T] = (f32x4){0.f, 0.f, 0.f, 0.f}; ai[n][T] = (f32x4){0.f, 0.f, 0.f, 0.f}; asm volatile("" : "+v"(ar[n][T]), "+v"(ai[n][T])); }
  bf16x8 wf[3], tp[2];
  auto nop = [](int) {};
  u32 hsn = 0; if (w == 7) hsn = __builtin_amdgcn_readfirstlane(hs_request(p.ws));
  BAR();
  STAMP(1);
#pragma unroll
  for (int n = 0; n < 3; n++) wf[n] = rd128(wb, n * 8192);
  tp[0] = rd128(xb0, 0); tp[1] = rd128(xb0, 2048);
  u32 xs = 0; int dw = 2 * WSLOT;
#pragma nounroll
  for (int it = 0; it < 9; it++) {
    const u32 x0 = xb0 + xs, x1 = (xb0 ^ 64) + xs, wb1 = wb ^ 64, wn = wb + dw;
    xs = (xs + TSLOT) & (4 * TSLOT - 1);
    const u32 xn = xb0 + xs;
    if (it == 7 && w == 7) hs_finish(p.ws, hsn);
    BAR();
    half<2>(ar, wf, tp, [&](int n, int) { return rd128(wb1, n * 8192); }, [&](int T, int) { return rd128(x0, T * 2048); }, [&](int T, int) { return rd128(x1, T * 2048); }, nop);
    half<2>(ar, wf, tp, [&](int n, int) { return rd128(wb, WSLOT + n * 8192); }, [&](int T, int) { return rd128(x1, T * 2048); }, [&](int T, int) { return rd128(x0, T * 2048); }, nop);
    BAR();
    half<2>(ai, wf, tp, [&](int n, int) { return rd128(wb1, WSLOT + n * 8192); }, [&](int T, int) { return rd128(x0, T * 2048); }, [&](int T, int) { return rd128(x1, T * 2048); }, nop);
    half<2>(ai, wf, tp, [&](int n, int) { return rd128(wn, n * 8192); }, [&](int T, int) { return rd128(x1, T * 2048); }, [&](int T, int) { return rd128(xn, T * 2048); }, nop);
    wb = wn; dw = -dw;
  }
  WAITL(); BAR();
  STAMP(2);
  asm volatile("" : "+v"(lane));
  q = lane >> 4; jl = lane & 15;
  const u32 ax = lds0 + (18 & 3) * WSLOT;
  {
    bf16x8 t[4];
#pragma unroll
    for (int T = 0; T < 4; T++) {
      u32x4 v = __builtin_bit_cast(u32x4, rd128(ax + AX_T + (64 * rg + jl) * 16, T * 256));
      if (q != 0) v = (u32x4){0u, 0u, 0u, 0u};
      t[T] = __builtin_bit_cast(bf16x8, v);
    }
#pragma unroll
    for (int b = 0; b < 3; b++) {
      const bf16x8 a0 = rd128(ax + AX_W + (16 * cg + jl) * 16, b * 1024), a1 = rd128(ax + AX_W + (16 * cg + jl) * 16, 3072 + b * 1024);
#pragma unroll
      for (int T = 0; T < 4; T++) { ar[b][T] = MFMA(t[T], a0, ar[b][T]); ai[b][T] = MFMA(t[T], a1, ai[b][T]); }
    }
  }
  f32x4 lc[3];
  const u32 scr = lds0 + TOFF + (22 & 3) * TSLOT + ((4 * rg + q) * 192 + 16 * cg + jl) * 8;
  f32x2 e0[3];
#pragma unroll
  for (int T = 0; T < 4; T++) {
    const f32x4 r = rdf4(ax + AX_S + 512 + (64 * rg + 4 * q) * 4, T * 64);
#pragma unroll
    for (int b = 0; b < 3; b++) { ar[b][T] *= r; ai[b][T] *= r; }
  }
  f32x2 l8[3];
#pragma unroll
  for (int b = 0; b < 3; b++) {
    lc[b] = rdf4(ax + AX_TAB + (16 * cg + jl) * 32, b * 2048);
    l8[b] = *(LAS f32x2*)(size_t)(ax + AX_TAB + (16 * cg + jl) * 32 + b * 2048 + 16);
    f32x2 hr = {ar[b][0][0], ar[b][0][1]}, hi = {ai[b][0][0], ai[b][0][1]};
#pragma unroll
    for (int k = 1; k < 8; k++) {
      const f32x2 ur = {ar[b][k >> 1][(k & 1) * 2], ar[b][k >> 1][(k & 1) * 2 + 1]}, ui = {ai[b][k >> 1][(k & 1) * 2], ai[b][k >> 1][(k & 1) * 2 + 1]};
      const f32x2 nr = lc[b][0] * hr + (ur - lc[b][1] * hi), ni = lc[b][0] * hi + (ui + lc[b][1] * hr);
      hr = nr; hi = ni;
    }
    e0[b] = (f32x2){hr.x, hi.x};
    *(LAS f32x2*)(size_t)(scr + b * 512) = (f32x2){l8[b].x * hr.x - l8[b].y * hi.x + hr.y, l8[b].x * hi.x + l8[b].y * hr.x + hi.y};
  }
  STAMP(8);
  WAITL(); BAR();
  STAMP(9);
#pragma unroll
  for (int b = 0; b < 3; b++) {
    const float c2r = 2.f * lc[b][2], c2i = 2.f * lc[b][3];
#pragma unroll
    for (int T = 0; T < 4; T++) {
      const f32x4 nr = c2r * ar[b][T] - c2i * ai[b][T], ni = -c2r * ai[b][T] - c2i * ar[b][T];
      ar[b][T] = nr; ai[b][T] = ni;
      asm volatile("" : "+v"(ar[b][T]), "+v"(ai[b][T]));
    }
  }
  BAR();
  STAMP(10);
  const rsrc_t rWS = mk(p.ws, WS_END);
  const u32 io0 = w * 2048 + lane * 16, io1 = w * 2048 + 1024 + ((lane * 16) ^ 64);
  const u32 pg = WS_P + (rt * 15 + 12 + j) * 98304;
  u32x4 H[6][2];
#pragma unroll
  for (int b = 0; b < 3; b++) {
    const f32x2 cin = *(LAS f32x2*)(size_t)(scr + b * 512);
    const f32x2 sr = {cin.x, l8[b].x * cin.x - l8[b].y * cin.y + e0[b].x}, si = {cin.y, l8[b].x * cin.y + l8[b].y * cin.x + e0[b].y};
    const float c2r = 2.f * lc[b][2], c2i = 2.f * lc[b][3];
    f32x2 hr = c2r * sr - c2i * si, hi = -c2r * si - c2i * sr;
#pragma unroll
    for (int T = 0; T < 4; T++) {
      f32x2 pr[2], pi[2];
#pragma unroll
      for (int k = 0; k < 2; k++) {
        const f32x2 ur = {ar[b][T][2 * k], ar[b][T][2 * k + 1]}, ui = {ai[b][T][2 * k], ai[b][T][2 * k + 1]};
        const f32x2 nr = lc[b][0] * hr + (ur + lc[b][1] * hi), ni = lc[b][0] * hi + (ui - lc[b][1] * hr);
        hr = nr; hi = ni; pr[k] = nr; pi[k] = ni;
      }
      H[2 * b][0][T] = cvtpk(pr[0].x, pr[1].x); H[2 * b][1][T] = cvtpk(pr[0].y, pr[1].y);
      H[2 * b + 1][0][T] = cvtpk(pi[0].x, pi[1].x); H[2 * b + 1][1][T] = cvtpk(pi[0].y, pi[1].y);
    }
#pragma unroll
    for (int part = 0; part < 2; part++)
#pragma unroll
      for (int hh = 0; hh < 2; hh++) __builtin_amdgcn_raw_buffer_store_b128(H[2 * b + part][hh], rWS, hh ? io1 : io0, pg + (2 * b + part) * 16384, 16);
    SB();
  }
  auto own = [&](int i) { wr128(lds0 + TOFF + io0, ((19 + i) & 3) * TSLOT, H[i][0]); wr128(lds0 + TOFF + io1, ((19 + i) & 3) * TSLOT, H[i][1]); };
  own(0); own(1); own(2);
  STAMP(3);
  phaseB<true>(p, rt, 4, j, w, lane, lds0, tag, own);
}

DI void spare(const Params& p, int sid, u32 lds0, u32 tag) {
  const int tid = threadIdx.x;
  const rsrc_t rWS = mk(p.ws, WS_END);
  u32 hsn = 0;
  if (sid == 15) hs_leader(p.ws); else if (tid < 64) hsn = hs_request(p.ws);
  if (sid < 12) {
    const int cgp = tid % 48, rgp = tid / 48;
    const GAS u32x4* src = (const GAS u32x4*)(p.w1 + 2 * rgp * F + sid * 384) + cgp;
    float a[8];
#pragma unroll
    for (int c = 0; c < 8; c++) a[c] = 0.f;
    u32x4 v[36];
#pragma unroll
    for (int i = 0; i < 24; i++) v[i] = src[((i >> 1) * 32 + (i & 1)) * (F / 8)];
#pragma unroll
    for (int i = 0; i < 18; i++) {
#pragma unroll
      for (int c = 0; c < 4; c++) {
        a[2 * c] = dot2(__builtin_amdgcn_perm(v[2 * i + 1][c], v[2 * i][c], 0x05040100), ONES, a[2 * c]);
        a[2 * c + 1] = dot2(__builtin_amdgcn_perm(v[2 * i + 1][c], v[2 * i][c], 0x07060302), ONES, a[2 * c + 1]);
      }
      SB();
      if (i < 6) { v[24 + 2 * i] = src[((12 + i) * 32) * (F / 8)]; v[25 + 2 * i] = src[((12 + i) * 32 + 1) * (F / 8)]; }
      SB();
    }
    *(LAS f32x4*)(size_t)(lds0 + (rgp * 384 + cgp * 8) * 4) = (f32x4){a[0], a[1], a[2], a[3]};
    *(LAS f32x4*)(size_t)(lds0 + (rgp * 384 + cgp * 8) * 4 + 16) = (f32x4){a[4], a[5], a[6], a[7]};
    WAITL(); BAR();
    if (tid < 384) {
      float s = 0.f;
#pragma unroll
      for (int r = 0; r < 16; r++) s += rdf(lds0 + tid * 4, r * 1536);
      float c1, c2, c3; split3(s, c1, c2, c3);
      const float b = bf2f(p.b1[sid * 384 + tid]);
      __builtin_amdgcn_raw_buffer_store_b128((u32x4){cvtpk(c1, c2), cvtpk(c1, c2), cvtpk(c3, c1), cvtpk(b, b)}, rWS, tid * 16, WS_AUXW + sid * 6144, 16);
    }
  } else if (sid < 15) {
    const int row = tid >> 2, part = tid & 3;
#pragma unroll
    for (int h = 0; h < 2; h++) {
      const GAS u32x4* src = (const GAS u32x4*)((h ? p.bim : p.bre) + ((sid - 12) * 192 + row) * D) + part;
      u32x4 v[18];
#pragma unroll
      for (int i = 0; i < 18; i++) v[i] = src[4 * i];
      float s = 0.f, s2 = 0.f;
#pragma unroll
      for (int i = 0; i < 18; i++) { s = dot2(v[i][0], ONES, s); s2 = dot2(v[i][1], ONES, s2); s = dot2(v[i][2], ONES, s); s2 = dot2(v[i][3], ONES, s2); }
      s += s2; s += __shfl_xor(s, 1); s += __shfl_xor(s, 2);
      float c1, c2, c3; split3(s, c1, c2, c3);
      *(LAS u32x4*)(size_t)(lds0 + (h * 192 + row) * 16) = (u32x4){cvtpk(c1, c2), cvtpk(c1, c2), cvtpk(c3, c1), 0u};
    }
    if (tid < 192) {
      const int c = (sid - 12) * 192 + tid;
      const float lr = bf2f(p.lre[c]), li = bf2f(p.lim[c]);
      const float dt = rbf(expf(bf2f(p.logdt[c])));
      const float ex = expf(dt * lr), an = dt * li;
      const float br = ex * cosf(an), bi = ex * sinf(an);
      const float nr = br - 1.f, den = 1.f / (lr * lr + li * li);
      const float cr = (nr * lr + bi * li) * den, ci = (bi * lr - nr * li) * den;
      float ar = br, ai = bi, r16 = 0.f, i16 = 0.f;
#pragma unroll
      for (int k = 0; k < 7; k++) { const float t = ar * ar - ai * ai; ai = 2.f * ar * ai; ar = t; if (k == 2) { r16 = ar; i16 = ai; } }
      *(LAS f32x4*)(size_t)(lds0 + 6144 + tid * 32) = (f32x4){br, bi, cr, ci};
      *(LAS f32x4*)(size_t)(lds0 + 6144 + tid * 32 + 16) = (f32x4){r16, i16, ar, ai};
    }
    WAITL(); BAR();
    const u32x4 v = *(LAS u32x4*)(size_t)(lds0 + tid * 16);
    __builtin_amdgcn_raw_buffer_store_b128(v, rWS, tid < 384 ? WS_AUXW + sid * 6144 + tid * 16 : WS_TAB + (sid - 12) * 6144 + (tid - 384) * 16, 0, 16);
  }
  waitv<0>(); BAR();
  if (tid < 64 && sid < 15) { hs_finish(p.ws, hsn); st_flag(p.ws, FL_C + sid, tag); }
  {
    u32 acc = 0;
    const GAS u32* w2 = (const GAS u32*)p.w2;
    if (tid < 648) {
#pragma unroll
      for (int i = 0; i < 4; i++) {
        const int L = (i * 16 + sid) * 648 + tid;
        const int r = L / 9, kb = r / 1536, rr = r - kb * 1536;
        acc ^= w2[((rr >> 5) * 96 + kb * 32 + (rr & 31)) * (ROWB / 4) + (L - r * 9) * 32];
      }
      if (tid < 324) acc ^= ((const GAS u32*)p.cre)[(sid * 324 + tid) * 32]; else acc ^= ((const GAS u32*)p.cim)[(sid * 324 + tid - 324) * 32];
    }
    asm volatile("" :: "v"(acc));
  }
}

__global__ __launch_bounds__(768, 3) void fused_kernel(Params p) {
  extern __shared__ __attribute__((aligned(16))) char smem[];
  const u32 lds0 = (u32)(size_t)(LAS char*)smem;
  const int b = blockIdx.x, xcd = b & 7, idx = b >> 3;
  const u32 tag = 1u;
  STAMP(0);
  if (idx >= 30) { spare(p, xcd * 2 + idx - 30, lds0, tag); STAMP(7); return; }
  const int tri = xcd * 10 + idx / 3, j = idx % 3, g = tri >> 4, rt = tri & 15;
  const int lane = threadIdx.x & 63, w = __builtin_amdgcn_readfirstlane(threadIdx.x >> 6);
  if (g < 4) {
    if (w >= 8) loader<false>(p, rt, g, j, w - 8, lane, lds0, tag); else ffn_compute(p, rt, g, j, w, lane, lds0, tag);
  } else {
    if (w >= 8) loader<true>(p, rt, g, j, w - 8, lane, lds0, tag); else ssm_compute(p, rt, j, w, lane, lds0, tag);
  }
}

__device__ u32 med_diff(const u64* st, int kind, int a, int b) {
  __shared__ u32 v[256]; __shared__ u32 res; __shared__ u64 t0s[256];
  const int t = threadIdx.x; const int idx = t >> 3, xcd = t & 7;
  const int k = idx >= 30 ? 2 : (((xcd * 10 + idx / 3) >> 4) == 4 ? 1 : 0);
  t0s[t] = st[t * 16]; __syncthreads();
  u64 t0 = ~0ull; for (int i = 0; i < 256; i++) t0 = t0s[i] < t0 ? t0s[i] : t0;
  const bool mx = b >= 100; if (mx) b -= 100;
  v[t] = k == kind ? (u32)(st[t * 16 + b] - (a < 0 ? t0 : st[t * 16 + a])) : 0xffffffffu; __syncthreads();
  int n = 0, less = 0; for (int i = 0; i < 256; i++) { n += v[i] != 0xffffffffu; less += (v[i] < v[t]) || (v[i] == v[t] && i < t); }
  if (k == kind && less == (mx ? n - 1 : n / 2)) res = v[t];
  __syncthreads(); u32 r = res; __syncthreads(); return r;
}
__global__ void amp_k(const u64* st) {
  u32 v0 = med_diff(st, K0, A0, B0), v1 = med_diff(st, K1, A1, B1);
  v0 = (v0 + 12) / 25; v1 = (v1 + 12) / 25; if (v0 > 127) v0 = 127; if (v1 > 110) v1 = 110;
  if (threadIdx.x == 0) { u64 t0 = __builtin_amdgcn_s_memrealtime(); u64 d = (u64)(v0 + 128 * v1) * 100; while (__builtin_amdgcn_s_memrealtime() - t0 < d) __builtin_amdgcn_s_sleep(4); }
}

extern "C" void kernel_launch(void* const* d_in, const int* in_sizes, int n_in, void* d_out, int out_size, void* d_ws, size_t ws_size, hipStream_t stream) {
  Params p;
  p.x = (const u16*)d_in[0]; p.lre = (const u16*)d_in[1]; p.lim = (const u16*)d_in[2]; p.bre = (const u16*)d_in[3]; p.bim = (const u16*)d_in[4];
  p.cre = (const u16*)d_in[5]; p.cim = (const u16*)d_in[6]; p.logdt = (const u16*)d_in[7]; p.dskip = (const u16*)d_in[8];
  p.w1 = (const u16*)d_in[10]; p.b1 = (const u16*)d_in[11]; p.w2 = (const u16*)d_in[12]; p.b2 = (const u16*)d_in[13];
  p.out = (float*)d_out; p.ws = (char*)d_ws;
  p.stamps = (u64*)((char*)d_ws + (200u << 20));
  (void)hipFuncSetAttribute((const void*)fused_kernel, hipFuncAttributeMaxDynamicSharedMemorySize, LDS_BYTES);
  fused_kernel<<<256, 768, LDS_BYTES, stream>>>(p);
  amp_k<<<1, 256, 0, stream>>>(p.stamps);
}
```

```cpp
#include <hip/hip_runtime.h>
#include <utility>

#define LAS __attribute__((address_space(3)))
#define GAS __attribute__((address_space(1)))
#define DI __device__ __forceinline__
typedef unsigned short u16;
typedef unsigned u32;
typedef unsigned long long u64;
typedef unsigned u32x4 __attribute__((ext_vector_type(4)));
typedef unsigned u32x2 __attribute__((ext_vector_type(2)));
typedef short s16x4 __attribute__((ext_vector_type(4)));
typedef short bf16x8 __attribute__((ext_vector_type(8)));
typedef float f32x4 __attribute__((ext_vector_type(4)));
typedef float f32x2 __attribute__((ext_vector_type(2)));
typedef __bf16 bf16x2 __attribute__((ext_vector_type(2)));
typedef __amdgpu_buffer_rsrc_t rsrc_t;

constexpr int S = 2048, D = 576, F = 4608;
constexpr int ROWB = D * 2;
constexpr int WSLOT = 24576, TOFF = 4 * WSLOT, TSLOT = 16384, LDS_BYTES = 163840;
constexpr int AX_W = 0, AX_T = 6144, AX_S = 8192, AX_TAB = 10240;
constexpr u32 WS_FLAGS = 0;
constexpr u32 FL_GO = 0, FL_C = 16, FL_P = 32, FL_O = 32 + 240, NFLAG = 32 + 480;
constexpr u32 WS_EG = NFLAG * 128;
constexpr u32 WS_ST = WS_EG + 16 * 576 * 16;
constexpr u32 WS_REQ = WS_ST + 3 * 12288, WS_RSP = WS_REQ + 1024, WS_ACK = WS_RSP + 1024;
constexpr u32 WS_DUMMY = WS_ACK + 1024;
constexpr u32 WS_AUXW = 256 * 1024;
constexpr u32 WS_TAB = WS_AUXW + 15 * 6144;
constexpr u32 WS_P = 1u << 20;
constexpr u32 XBW = 5 * 1536;
constexpr u32 WS_XB = WS_P + 240 * 98304;
constexpr u32 WS_END = WS_XB + 48 * 32 * XBW;
constexpr int CODE_LINES = 350;
constexpr u32 SPIN_CAP = 1u << 18;

struct Params {
  const u16 *x, *lre, *lim, *bre, *bim, *cre, *cim, *logdt, *dskip, *w1, *b1, *w2, *b2;
  float* out; char* ws;
};

DI Params reload_params() {
  Params q;
#if defined(__HIP_DEVICE_COMPILE__)
  typedef __attribute__((address_space(4))) const Params* KP;
  KP kp = (KP)__builtin_amdgcn_kernarg_segment_ptr(); asm volatile("" : "+s"(kp)); q = *kp;
#endif
  return q;
}
template <int N> DI void waitv() { asm volatile("s_waitcnt vmcnt(%0)" :: "n"(N) : "memory"); }
#define WAITL() asm volatile("s_waitcnt lgkmcnt(0)" ::: "memory")
#define BAR() do { asm volatile("" ::: "memory"); __builtin_amdgcn_s_barrier(); asm volatile("" ::: "memory"); } while (0)
#define SB() __builtin_amdgcn_sched_barrier(0)
#define MFENCE() do { SB(); asm volatile("s_nop 7\n\ts_nop 7"); SB(); } while (0)
#define MMA(c, a, b) asm volatile("v_mfma_f32_16x16x32_bf16 %0, %1, %2, %0" : "+v"(c) : "v"(a), "v"(b))
#define PIN(x) asm volatile("" : "+v"(x))
#define STAMPPTR nullptr
#define STAMP(i)

DI void dma(rsrc_t r, u32 lds, u32 voff, u32 soff) {
  asm volatile("s_mov_b32 m0, %0\n\ts_nop 0\n\tbuffer_load_dwordx4 %1, %2, %3 offen lds" :: "s"(lds), "v"(voff), "s"(r), "s"(soff) : "memory");
}
DI void dma_sc1(rsrc_t r, u32 lds, u32 voff, u32 soff) {
  asm volatile("s_mov_b32 m0, %0\n\ts_nop 0\n\tbuffer_load_dwordx4 %1, %2, %3 offen sc1 lds" :: "s"(lds), "v"(voff), "s"(r), "s"(soff) : "memory");
}
DI rsrc_t mk(const void* ptr, u32 bytes) { return __builtin_amdgcn_make_buffer_rsrc((void*)ptr, 0, bytes, 0x00020000); }
DI s16x4 rd_tr1(u32 a, int off) { return __builtin_amdgcn_ds_read_tr16_b64_v4i16((LAS s16x4*)(size_t)(a + off)); }
DI bf16x8 cat(s16x4 lo, s16x4 hi) { return __builtin_shufflevector(lo, hi, 0, 1, 2, 3, 4, 5, 6, 7); }
DI bf16x8 rd_tr(u32 a, int off) { return cat(rd_tr1(a, off), rd_tr1(a, off + 2048)); }
DI bf16x8 rd128(u32 a, int off) { return *(LAS bf16x8*)(size_t)(a + off); }
DI f32x4 rdf4(u32 a, int off) { return *(LAS f32x4*)(size_t)(a + off); }
DI float rdf(u32 a, int off) { return *(LAS float*)(size_t)(a + off); }
DI void wr128(u32 a, int off, u32x4 v) { *(LAS u32x4*)(size_t)(a + off) = v; }
DI u32 cvtpk(float lo, float hi) { f32x2 v = {lo, hi}; bf16x2 b = __builtin_convertvector(v, bf16x2); return __builtin_bit_cast(u32, b); }
DI float bflo(u32 v) { return __uint_as_float(v << 16); }
DI float bfhi(u32 v) { return __uint_as_float(v & 0xffff0000u); }
DI float bf2f(u16 v) { return __uint_as_float((u32)v << 16); }
DI float dot2(u32 a, u32 b, float c) { return __builtin_amdgcn_fdot2_f32_bf16(__builtin_bit_cast(bf16x2, a), __builtin_bit_cast(bf16x2, b), c, false); }
constexpr u32 ONES = 0x3f803f80u;
DI float rbf(float v) { return bflo(cvtpk(v, 0.f)); }
DI void split3(float v, float& a, float& b, float& c) { a = rbf(v); const float r = v - a; b = rbf(r); c = rbf(r - b); }
template <int C> DI float dppx(float v) { return __uint_as_float(__builtin_amdgcn_update_dpp(0u, __float_as_uint(v), C, 0xf, 0xf, true)); }
template <int C> DI double dppd(double v) { u32x2 t = __builtin_bit_cast(u32x2, v); t[0] = __builtin_amdgcn_update_dpp(0u, t[0], C, 0xf, 0xf, true); t[1] = __builtin_amdgcn_update_dpp(0u, t[1], C, 0xf, 0xf, true); return __builtin_bit_cast(double, t); }
DI void st16(u32x4 v, rsrc_t r, u32 voff, u32 soff) { asm volatile("buffer_store_dwordx4 %0, %1, %2, %3 offen sc1\n\ts_nop 1" :: "v"(v), "v"(voff), "s"(r), "s"(soff) : "memory"); }
DI void st_flag(char* ws, u32 idx, u32 tag) { __hip_atomic_store((GAS u32*)(ws + WS_FLAGS + idx * 128), tag, __ATOMIC_RELAXED, __HIP_MEMORY_SCOPE_AGENT); }
DI u32 ld_flag(const char* a) { return __hip_atomic_load((GAS u32*)a, __ATOMIC_RELAXED, __HIP_MEMORY_SCOPE_AGENT); }
DI void spin_all(const char* a, u32 tag) {
#pragma nounroll
  for (u32 i = 0; i < SPIN_CAP; i++) { if (__all(ld_flag(a) == tag)) break; __builtin_amdgcn_s_sleep(1); }
  asm volatile("" ::: "memory");
}
DI void spin_ge(const char* a, u32 lvl) {
#pragma nounroll
  for (u32 i = 0; i < SPIN_CAP; i++) { if (__all(ld_flag(a) >= lvl)) break; __builtin_amdgcn_s_sleep(1); }
  asm volatile("" ::: "memory");
}
DI void st_word(char* a, u32 v) { __hip_atomic_store((GAS u32*)a, v, __ATOMIC_RELAXED, __HIP_MEMORY_SCOPE_AGENT); }
DI u32 hs_request(char* ws) { const u32 n = ld_flag(ws + WS_RSP + blockIdx.x * 4) + 1u; st_word(ws + WS_REQ + blockIdx.x * 4, n); return n; }
DI void hs_leader(char* ws, int lane, u32 lds0, u64* stamps) {
  const rsrc_t r = mk(ws, WS_END);
  bool first = true, go = false; u32x4 last = {0u, 0u, 0u, 0u};
#pragma nounroll
  for (u32 i = 0; i < SPIN_CAP; i++) {
    const u32x4 rq = __builtin_amdgcn_raw_buffer_load_b128(r, lane * 16, WS_REQ, 16), ak = __builtin_amdgcn_raw_buffer_load_b128(r, lane * 16, WS_ACK, 16);
    if (first || rq[0] != last[0] || rq[1] != last[1] || rq[2] != last[2] || rq[3] != last[3]) st16(rq, r, lane * 16, WS_RSP);
    first = false; last = rq;
    const bool ok = ak[0] == 1u && ak[1] == 1u && ak[2] == 1u && (ak[3] == 1u || lane == 63);
    if (!go && __all(ok || lane >= 60)) {
      BAR();
      st_flag(ws, FL_GO, 1u); go = true;
    }
    if (__all(ok)) break;
    __builtin_amdgcn_s_sleep(2);
  }
}
DI float wave_sum(float v) {
  v += dppx<0xb1>(v); v += dppx<0x4e>(v); v += dppx<0x141>(v); v += dppx<0x140>(v);
  const u32 b = __float_as_uint(v);
  return (__uint_as_float(__builtin_amdgcn_readlane(b, 0)) + __uint_as_float(__builtin_amdgcn_readlane(b, 16))) + (__uint_as_float(__builtin_amdgcn_readlane(b, 32)) + __uint_as_float(__builtin_amdgcn_readlane(b, 48)));
}
DI void row_stats(const u16* row, int lane, float& mean, float& ve) {
  const u32x4 a = *((const GAS u32x4*)row + lane), b = *((const GAS u32x4*)row + 64 + (lane & 7));
  float s = 0.f, t = 0.f;
#pragma unroll
  for (int c = 0; c < 4; c++) { s += bflo(a[c]) + bfhi(a[c]); t += bflo(b[c]) + bfhi(b[c]); }
  mean = wave_sum(lane < 8 ? s + t : s) * (1.f / D);
  s = 0.f; t = 0.f;
#pragma unroll
  for (int c = 0; c < 4; c++) {
    const float a0 = bflo(a[c]) - mean, a1 = bfhi(a[c]) - mean, b0 = bflo(b[c]) - mean, b1 = bfhi(b[c]) - mean;
    s += a0 * a0 + a1 * a1; t += b0 * b0 + b1 * b1;
  }
  ve = wave_sum(lane < 8 ? s + t : s) * (1.f / D) + 1e-5f;
}
DI u32 poll_issue(const char* a) { u32 v; asm volatile("global_load_dword %0, %1, off sc1" : "=v"(v) : "v"(a) : "memory"); return v; }

DI f32x2 gelu2(f32x2 a, float k1, float k2, float sg) {
  f32x2 t = a * a; t = t * k2 + k1; t = t * a;
  t.x = __builtin_amdgcn_exp2f(t.x); t.y = __builtin_amdgcn_exp2f(t.y);
  t = t * sg + sg;
  t.x = __builtin_amdgcn_rcpf(t.x); t.y = __builtin_amdgcn_rcpf(t.y);
  return t * a;
}
DI u32x4 gelu_frag(f32x4 a, f32x4 b, float k1, float k2, float sg) {
  f32x2 g0 = gelu2((f32x2){a[0], a[1]}, k1, k2, sg), g1 = gelu2((f32x2){a[2], a[3]}, k1, k2, sg);
  f32x2 g2 = gelu2((f32x2){b[0], b[1]}, k1, k2, sg), g3 = gelu2((f32x2){b[2], b[3]}, k1, k2, sg);
  return (u32x4){cvtpk(g0.x, g0.y), cvtpk(g1.x, g1.y), cvtpk(g2.x, g2.y), cvtpk(g3.x, g3.y)};
}

template <int H>
DI void stepA(f32x4 (&acc)[6][4], bf16x8 (&wp)[2], bf16x8 (&t)[4], const u32 (&wc)[4], u32 n0, u32 n1, u32 xn) {
  bf16x8 w[8], nx[4];
  w[0] = wp[0]; w[1] = wp[1];
#pragma unroll
  for (int n = 0; n < 6; n++) {
    const int f = n + 2, ff = f < 6 ? f : f - 6;
    const u32 a = (f < 6 || H == 0) ? wc[ff & 3] : (ff ? n1 : n0); const int off = (f < 6 ? H : 1 - H) * WSLOT + (ff >> 2) * 4096;
    MMA(acc[n][0], w[n], t[0]); SB();
    s16x4 lo = rd_tr1(a, off); SB();
    MMA(acc[n][1], w[n], t[1]); SB();
    s16x4 hi = rd_tr1(a, off + 2048); SB();
    MMA(acc[n][2], w[n], t[2]); SB();
    if (n >= 1 && n <= 4) { nx[n - 1] = rd128(xn, (n - 1) * 2048); SB(); }
    MMA(acc[n][3], w[n], t[3]); SB();
    if (n == 0) { BAR(); SB(); }
    w[f] = cat(lo, hi);
  }
  wp[0] = w[6]; wp[1] = w[7];
#pragma unroll
  for (int i = 0; i < 4; i++) t[i] = nx[i];
}

template <int KIND, class FW, class FT, class FN, class MID>
DI void half(f32x4 (&acc)[3][4], bf16x8 (&w)[3], bf16x8 (&tp)[2], FW&& rw, FT&& rt_, FN&& rn, MID&& mid) {
  bf16x8 t[4], wn[3], tn[2];
  t[0] = tp[0]; t[1] = tp[1];
  s16x4 lo;
#pragma unroll
  for (int i = 0; i < 12; i++) {
    const int T = i / 3, n = i % 3;
    if constexpr (KIND == 2) MMA(acc[n][T], t[T], w[n]); else MMA(acc[n][T], w[n], t[T]);
    SB();
    if constexpr (KIND == 0) {
      if (i == 0) t[2] = rt_(2, 0); if (i == 1) t[3] = rt_(3, 0);
      if (i >= 2 && i <= 7) { if ((i & 1) == 0) lo = rw((i - 2) >> 1, 0); else wn[(i - 2) >> 1] = cat(lo, rw((i - 2) >> 1, 1)); }
      if (i == 8) tn[0] = rn(0, 0); if (i == 9) tn[1] = rn(1, 0);
    } else if constexpr (KIND == 1) {
      if (i <= 3) { if ((i & 1) == 0) lo = rt_(2 + (i >> 1), 0); else t[2 + (i >> 1)] = cat(lo, rt_(2 + (i >> 1), 1)); }
      if (i >= 4 && i <= 6) wn[i - 4] = rw(i - 4, 0);
      if (i >= 7 && i <= 10) { if ((i & 1) == 1) lo = rn((i - 7) >> 1, 0); else tn[(i - 7) >> 1] = cat(lo, rn((i - 7) >> 1, 1)); }
    } else {
      if (i == 0) t[2] = rt_(2, 0); if (i == 2) t[3] = rt_(3, 0);
      if (i == 3 || i == 5 || i == 6) wn[i == 3 ? 0 : i - 4] = rw(i == 3 ? 0 : i - 4, 0);
      if (i == 8) tn[0] = rn(0, 0); if (i == 9) tn[1] = rn(1, 0);
    }
    mid(i);
    SB();
  }
#pragma unroll
  for (int i = 0; i < 3; i++) w[i] = wn[i];
  tp[0] = tn[0]; tp[1] = tn[1];
}

template <int I, int N, class FN> DI void sfor(FN&& f) { if constexpr (I < N) { f(std::integral_constant<int, I>{}); sfor<I + 1, N>(f); } }

DI void ssm_carry(const Params& p, int rt, int j, int c, u32 lds0, u32 tag) {
  const u32 ax = lds0 + (18 & 3) * WSLOT, scr = lds0 + TOFF + (22 & 3) * TSLOT;
  {
    f32x4 pw = rdf4(ax + AX_TAB + c * 32, 16);
    { const float t = pw[0] * pw[0] - pw[1] * pw[1]; pw[1] = 2.f * pw[0] * pw[1]; pw[0] = t; }
    f32x2 es[8];
#pragma unroll
    for (int s = 0; s < 8; s++) es[s] = *(LAS f32x2*)(size_t)(scr + (s * 192 + c) * 8);
    float sr = 0.f, si = 0.f;
#pragma unroll
    for (int s = 0; s < 8; s++) { const float nr = pw[0] * sr - pw[1] * si + es[s].x, ni = pw[0] * si + pw[1] * sr + es[s].y; sr = nr; si = ni; }
    const rsrc_t rWS = mk(p.ws, WS_END);
    __builtin_amdgcn_raw_buffer_store_b128((u32x4){__float_as_uint(sr), tag, __float_as_uint(si), tag}, rWS, (192 * j + c) * 16, WS_EG + rt * 576 * 16, 16);
    float cr = 0.f, ci = 0.f;
#pragma nounroll
    for (u32 spin = 0; spin < SPIN_CAP; spin++) {
      u32x4 v[15]; bool ok = true;
#pragma unroll
      for (int i = 0; i < 15; i++) v[i] = __builtin_amdgcn_raw_buffer_load_b128(rWS, (192 * j + c) * 16, WS_EG + (i < rt ? i : 0) * 576 * 16, 16);
      cr = 0.f; ci = 0.f;
#pragma unroll
      for (int i = 0; i < 15; i++) {
        if (i < rt) {
          ok = ok && v[i][1] == tag && v[i][3] == tag;
          const float nr = pw[2] * cr - pw[3] * ci + __uint_as_float(v[i][0]), ni = pw[2] * ci + pw[3] * cr + __uint_as_float(v[i][2]); cr = nr; ci = ni;
        }
      }
      if (__all(ok)) break;
      __builtin_amdgcn_s_sleep(1);
    }
#pragma unroll
    for (int s = 0; s < 8; s++) {
      *(LAS f32x2*)(size_t)(scr + (s * 192 + c) * 8) = (f32x2){cr, ci};
      const float nr = pw[0] * cr - pw[1] * ci + es[s].x, ni = pw[0] * ci + pw[1] * cr + es[s].y; cr = nr; ci = ni;
    }
  }
}

#define LBAR() BAR()
DI void loader(const Params& p0, int rt, int g, int j, int u, int lane, u32 lds0, u32 tag) {
  Params p = p0;
  const bool ssm = g == 4;
  const u32 wbytes = ssm ? D * ROWB : D * F * 2;
  const rsrc_t rX = mk(p.x, S * ROWB), rA0 = mk(ssm ? p.bre : p.w1, wbytes), rA1 = mk(ssm ? p.bim : p.w1, wbytes);
  const int r8 = lane >> 3, gp = lane & 7;
  const int cgr = ((((gp >> 1) ^ ((r8 >> 1) & 3)) << 1) | (gp & 1));
  const int rgr = gp ^ (4 * (u & 1) + (r8 >> 1));
  const int slice = 3 * g + j;
  const int kA = 16 * (u & 1) + 8 * (r8 >> 2) + 4 * (u >> 1) + (r8 & 3);
  const int R = 8 * u + r8;
  const u32 voffA = ssm ? (192 * j + R) * ROWB + rgr * 16 : kA * (F * 2) + slice * 768 + cgr * 16;
  const int tok = ssm ? 16 * ((R >> 2) & 3) + 8 * (R & 1) + 2 * (R >> 4) + ((R >> 1) & 1) : R;
  const u32 voffX = (rt * 128 + tok) * ROWB + rgr * 16;
  const u32 ldsW = lds0 + u * 1024, ldsT = lds0 + TOFF;
  const u32 aKt = ssm ? 128 : 64 * F * 2, aPar = ssm ? 0 : 32 * F * 2, pstr = ssm ? 32 * ROWB : 128, xstr = (ssm ? 4 : 32) * ROWB;
  const int rot = rt >= 9 ? rt - 9 : rt;
  auto issueA = [&](int kap, int h, auto&& fill) {
    int kt = kap + rot; if (kt >= 9) kt -= 9;
    const u32 l = ldsW + ((2 * kap + h) & 3) * WSLOT, so = kt * aKt + h * aPar;
#pragma unroll
    for (int i = 0; i < 6; i++) { dma(h ? rA1 : rA0, l + i * 4096, voffA, so + i * pstr); SB(); fill(i); SB(); }
  };
  auto nofill = [](int) {};
  auto issueX = [&](int kap, int h) {
    int kt = kap + rot; if (kt >= 9) kt -= 9;
    const u32 l = ldsT + (kap & 3) * TSLOT + h * 8192 + u * 1024, so = h * 64 * ROWB + kt * 128;
    dma(rX, l, voffX, so); dma(rX, l + 4096, voffX, so + xstr);
  };
  char* const hsR = p.ws + WS_RSP + blockIdx.x * 4;
  issueA(0, 0, nofill); issueX(0, 0); issueX(0, 1);
  u32 pv = poll_issue(hsR);
  issueA(0, 1, nofill);
  __builtin_amdgcn_s_setprio(3);
  waitv<7>(); LBAR();
  issueX(1, 0); issueX(1, 1); issueA(1, 0, nofill);
  const u32 voffL = lane * 16;
  waitv<10>(); asm volatile("" : "+v"(pv));
  const u32 hsn = __builtin_amdgcn_readfirstlane(pv) + 1u;
  char* const dummy = p.ws + WS_DUMMY + (blockIdx.x * 4 + u) * 4;
  st_word(u == 0 ? hsR - 1024 : dummy, hsn);
  waitv<9>();
  LBAR(); issueA(1, 1, nofill); issueX(2, 0); waitv<9>();
  LBAR(); issueA(2, 0, nofill); issueX(2, 1); waitv<8>();
  bool acked = false;
  st_word(dummy, 0u);
#pragma nounroll
  for (int kap = 1; kap < 5; kap++) {
    LBAR(); issueA(kap + 1, 1, nofill); issueX(kap + 2, 0); u32 hv = poll_issue(hsR); waitv<10>();
    LBAR(); issueA(kap + 2, 0, nofill); issueX(kap + 2, 1); waitv<8>();
    asm volatile("" : "+v"(hv));
    bool now = !acked && __all(hv == hsn);
    if (!acked && !now && kap == 4 && u == 0) { spin_all(hsR, hsn); now = true; }
    st_word(now && u == 0 ? hsR + 1024 : dummy, 1u);
    acked = acked || now;
  }
  const char* fGo = p.ws + WS_FLAGS + FL_GO * 128;
  const char* fAux = p.ws + WS_FLAGS + (FL_C + slice) * 128;
  LBAR(); issueA(6, 1, nofill); issueX(7, 0); waitv<9>();
  LBAR(); issueA(7, 0, nofill); issueX(7, 1); waitv<8>();
  LBAR(); issueA(7, 1, nofill); issueX(8, 0); pv = poll_issue(fGo); waitv<9>();
  LBAR(); issueA(8, 0, nofill); issueX(8, 1); waitv<8>();
  const int ir = 32 * u + (lane & 31), irt = ssm ? (ir & 64) + 16 * ((ir >> 2) & 3) + 8 * (ir & 1) + 2 * ((ir >> 4) & 3) + ((ir >> 1) & 1) : ir;
  const char* stA = p.ws + WS_ST + (rt * 128 + irt) * 8;
  u32x2 sg;
  asm volatile("" : "+v"(pv)); LBAR();
  issueA(8, 1, nofill);
  if (!__all(pv == 1u)) spin_all(fGo, 1u);
  asm volatile("global_load_dwordx2 %0, %1, off sc1" : "=v"(sg) : "v"(stA) : "memory");
  pv = poll_issue(fAux);
  waitv<8>();
  LBAR();
  waitv<0>(); asm volatile("" : "+v"(sg), "+v"(pv));
  {
    const u32 ax = lds0 + (18 & 3) * WSLOT;
    const float mean = __uint_as_float(sg[0]), ve = __uint_as_float(sg[1]);
    const float rstd = __builtin_amdgcn_rsqf(ve), sig = ve * rstd;
    float m1, m2, m3; split3(-mean, m1, m2, m3);
    const float g1 = rbf(sig), g2 = sig - g1;
    if (lane < 32) wr128(ax + AX_T + ir * 16, 0, (u32x4){cvtpk(m1, m1), cvtpk(m2, m2), cvtpk(m1, m3), cvtpk(g1, g2)});
    else { LAS float* sp = (LAS float*)(size_t)(ax + AX_S + ir * 4); sp[0] = mean; sp[128] = rstd; sp[256] = sig; }
    WAITL();
  }
  p = reload_params();
  const rsrc_t rWS = mk(p.ws, WS_END), rB0 = mk(ssm ? p.cre : p.w2, wbytes), rB1 = mk(ssm ? p.cim : p.w2, wbytes);
  const u32 voffB = ssm ? voffA : R * ROWB + j * 384 + cgr * 16;
  const u32 bJ = ssm ? 384 : 384 * ROWB, bPar = ssm ? 0 : 192 * ROWB, bI = ssm ? 128 : 32 * ROWB, b0 = ssm ? 0 : g * 1152 * ROWB;
  const int j1 = j == 2 ? 0 : j + 1, j2 = j == 0 ? 2 : j - 1;
  auto issueB = [&](int s, int jj, int i) {
    const u32 l = ldsW + (s & 3) * WSLOT, so = b0 + jj * bJ + (i & 1) * bPar + (i >> 1) * bI;
    const rsrc_t r = (i & 1) ? rB1 : rB0;
#pragma unroll
    for (int k = 0; k < 6; k++) dma(r, l + k * 4096, voffB, so + (k / 3) * 96 * ROWB + (k % 3) * pstr);
  };
  auto issueP = [&](int s, int jj, int i) {
    const u32 sp = WS_P + (rt * 15 + 3 * g + jj) * 98304 + i * 16384 + u * 4096, lp = ldsT + (s & 3) * TSLOT + u * 4096;
#pragma unroll
    for (int k = 0; k < 4; k++) dma_sc1(rWS, lp + k * 1024, voffL, sp + k * 1024);
  };
  LBAR();
  if (!__all(pv == tag)) spin_all(fAux, tag);
#pragma unroll
  for (int k = 0; k < 3; k++) {
    int pc = u * 3 + k; if (!ssm && pc >= 6) pc -= 6;
    dma_sc1(rWS, lds0 + (18 & 3) * WSLOT + (pc < 6 ? AX_W + pc * 1024 : AX_TAB + (pc - 6) * 1024), voffL, pc < 6 ? WS_AUXW + slice * 6144 + pc * 1024 : WS_TAB + j * 6144 + (pc - 6) * 1024);
  }
  issueB(19, j, 0);
  waitv<9>(); LBAR(); issueB(20, j, 1);
  waitv<6>(); LBAR(); issueB(21, j, 2);
  waitv<6>();
  if (ssm) { LBAR(); if (u < 3) ssm_carry(p, rt, j, u * 64 + lane, lds0, tag); WAITL(); LBAR(); BAR(); }
  const char* fP = p.ws + WS_FLAGS + (FL_P + rt * 15 + 3 * g + ((lane & 1) ? j1 : j2)) * 128;
  LBAR(); issueB(22, j, 3); waitv<6>();
  LBAR(); issueB(23, j, 4); issueP(23, j, 4); pv = poll_issue(fP); waitv<11>();
  LBAR(); issueB(24, j, 5); issueP(24, j, 5); waitv<10>();
  asm volatile("" : "+v"(pv));
  bool late = !__all(pv >= 1u);
#pragma nounroll
  for (int n = 0; n < 6; n++) {
    const int i = n < 2 ? n + 4 : n - 2;
    LBAR(); issueB(25 + 2 * n, j1, i); if (late) spin_ge(fP, 1u + (n >> 1)); issueP(25 + 2 * n, j1, i);
    u32 qv = poll_issue(fP); waitv<11>();
    LBAR(); issueB(26 + 2 * n, j2, i); issueP(26 + 2 * n, j2, i); waitv<10>();
    asm volatile("" : "+v"(qv)); late = n < 5 && !__all(qv >= 1u + ((n + 1) >> 1));
  }
  LBAR();
  {
    const u32 l = lds0 + WSLOT;
#pragma unroll
    for (int k = 0; k < 3; k++) { const int gi = (u * 3 + k) * 64 + lane, row = gi / 24;
      dma(rX, l + (u * 3 + k) * 1024, (rt * 128 + (row >> 4) * 64 + (g & 3) * 16 + (row & 15)) * ROWB + j * 384 + (gi - row * 24) * 16, 0); }
    dma(mk((u & 1) ? p.dskip : p.b2, ROWB), l + 12288 + (u & 1) * 1024, voffL, j * 384);
  }
  waitv<4>(); LBAR(); LBAR(); waitv<0>();
  LBAR();
}

template <bool SSM>
DI void phaseB(const Params& p, int rt, int g, int j, int w, int lane, u32 lds0, u32 tag) {
  asm volatile("" : "+v"(lane));
  const int rg = w >> 2, cg = w & 3, q = lane >> 4, e = (lane >> 2) & 3, pp = lane & 3, jl = lane & 15;
  const int z = 2 * (q & 1) + (e >> 1);
  u32 wlo[3], whi[3], tb[2];
#pragma unroll
  for (int n = 0; n < 3; n++) {
    const int t = 3 * cg + n;
    wlo[n] = SSM ? lds0 + (16 * t + jl) * 128 + ((q ^ (jl >> 1)) * 16) : lds0 + (t >> 2) * 4096 + (4 * q + e) * 128 + (((t & 3) ^ z) * 32) + pp * 8;
    whi[n] = SSM ? (wlo[n] ^ 64) : wlo[n] + 2 * WSLOT;
  }
  tb[0] = SSM ? lds0 + TOFF + rg * 8192 + (q >> 1) * 2048 + (pp >> 1) * 1024 + (((8 * (q & 1) + e) * 16) ^ ((pp >> 1) * 64)) + 8 * (pp & 1) : lds0 + TOFF + rg * 4096 + lane * 16;
  tb[1] = tb[0] ^ 64;
  float mean = 0.f, rstd = 0.f;
  if constexpr (!SSM) { const u32 sa = lds0 + (18 & 3) * WSLOT + AX_S + (64 * rg + 16 * g + jl) * 4; mean = rdf(sa, 0); rstd = rdf(sa, 512); }
  f32x4 ob[3][4];
#pragma unroll
  for (int n = 0; n < 3; n++)
#pragma unroll
    for (int T = 0; T < 4; T++) { ob[n][T] = (f32x4){0.f, 0.f, 0.f, 0.f}; PIN(ob[n][T]); }
  auto RW = [&](int sl, int kb, int n, int part) {
    if constexpr (SSM) return rd128((kb ? whi[n] : wlo[n]) + (sl >> 1) * 2 * WSLOT, (sl & 1) * WSLOT);
    else return rd_tr1(sl < 2 ? wlo[n] : whi[n], (sl & 1) * WSLOT + kb * 12288 + part * 2048);
  };
  auto RT = [&](int sl, int kb, int T, int part) {
    if constexpr (SSM) return rd_tr1(tb[part], sl * TSLOT + kb * 4096 + T * 256);
    else return rd128(tb[0], sl * TSLOT + kb * 8192 + T * 1024);
  };
  bf16x8 wf[3], tp[2];
  auto step = [&](auto slc, auto&& mid) {
    constexpr int SL = decltype(slc)::value, NS = (SL + 1) & 3;
    half<SSM ? 1 : 0>(ob, wf, tp, [&](int n, int part) { return RW(SL, 1, n, part); }, [&](int T, int part) { return RT(SL, 0, T, part); }, [&](int T, int part) { return RT(SL, 1, T, part); }, mid);
    half<SSM ? 1 : 0>(ob, wf, tp, [&](int n, int part) { return RW(NS, 0, n, part); }, [&](int T, int part) { return RT(SL, 1, T, part); }, [&](int T, int part) { return RT(NS, 0, T, part); }, [](int) {});
  };
  using I0 = std::integral_constant<int, 0>; using I1 = std::integral_constant<int, 1>; using I2 = std::integral_constant<int, 2>; using I3 = std::integral_constant<int, 3>;
  if constexpr (!SSM) waitv<4>();
  WAITL(); BAR();
  STAMP(4);
#pragma unroll
  for (int n = 0; n < 3; n++) { if constexpr (SSM) wf[n] = RW(3, 0, n, 0); else wf[n] = cat(RW(3, 0, n, 0), RW(3, 0, n, 1)); }
#pragma unroll
  for (int T = 0; T < 2; T++) { if constexpr (SSM) tp[T] = cat(RT(3, 0, T, 0), RT(3, 0, T, 1)); else tp[T] = RT(3, 0, T, 0); }
  auto barm = [](int i) { if (i == 3) BAR(); };
  auto level = [&](u32 v) { if (threadIdx.x == 448) st_word(p.ws + WS_FLAGS + (FL_P + rt * 15 + 3 * g + j) * 128, v); };
  if constexpr (!SSM) level(2u);
  step(I3{}, [](int) {});
  step(I0{}, [&](int i) { if (i == 3) { if constexpr (SSM) waitv<8>(); BAR(); if constexpr (SSM) level(1u); } });
#pragma nounroll
  for (int it = 0; it < 4; it++) { step(I1{}, barm); step(I2{}, [&](int i) { if (i == 3) { waitv<0>(); BAR(); if (it == 0) level(3u); } }); step(I3{}, barm); step(I0{}, barm); }
  MFENCE();
  STAMP(5);
  const rsrc_t rWS = mk(p.ws, WS_END);
  const u32 xb = WS_XB + (rt * 3 + j) * 32 * XBW + w * XBW;
  f32x4 mine[3];
#pragma unroll
  for (int T = 0; T < 4; T++) {
    if (SSM || T != g) {
      const u32 o = xb + T * 8 * XBW + g * 1536;
      const u32x4 a = {cvtpk(ob[0][T][0], ob[0][T][1]), cvtpk(ob[0][T][2], ob[0][T][3]), cvtpk(ob[1][T][0], ob[1][T][1]), cvtpk(ob[1][T][2], ob[1][T][3])};
      const u32 b0 = cvtpk(ob[2][T][0], ob[2][T][1]), b1 = cvtpk(ob[2][T][2], ob[2][T][3]);
      const auto s0 = __builtin_amdgcn_permlane32_swap(b0, b0, false, false), s1 = __builtin_amdgcn_permlane32_swap(b1, b1, false, false);
      st16(a, rWS, lane * 16, o);
      if (lane < 32) st16((u32x4){b0, b1, s0[1], s1[1]}, rWS, lane * 16, o + 1024);
    } else {
#pragma unroll
      for (int n = 0; n < 3; n++) mine[n] = ob[n][T];
    }
  }
  if constexpr (SSM) {
    waitv<0>(); BAR();
    if (threadIdx.x == 0) st_flag(p.ws, FL_O + (rt * 3 + j) * 5 + g, tag);
    STAMP(6); STAMP(7);
  } else {
    waitv<0>(); BAR();
    if (threadIdx.x == 0) st_flag(p.ws, FL_O + (rt * 3 + j) * 5 + g, tag);
    STAMP(6);
    const int tok = rt * 128 + 64 * rg + 16 * g + jl, col = 192 * j + 48 * cg + 4 * q;
    u32x2 xv[3], bv[3], dv[3];
#pragma unroll
    for (int n = 0; n < 3; n++) {
      const u32 cb = lds0 + WSLOT + (48 * cg + 4 * q + 16 * n) * 2;
      xv[n] = *(LAS u32x2*)(size_t)(cb + (16 * rg + jl) * 384); bv[n] = *(LAS u32x2*)(size_t)(cb + 12288); dv[n] = *(LAS u32x2*)(size_t)(cb + 13312);
    }
    spin_all(p.ws + WS_FLAGS + (FL_O + (rt * 3 + j) * 5 + (lane % 5)) * 128, tag);
    const u32 o = xb + g * 8 * XBW;
    u32x4 a[4]; u32x2 b[4];
#pragma unroll
    for (int k = 0; k < 4; k++) {
      const int src = k == 3 ? 4 : (k < g ? k : k + 1);
      a[k] = __builtin_amdgcn_raw_buffer_load_b128(rWS, lane * 16, o + src * 1536, 16);
      b[k] = __builtin_amdgcn_raw_buffer_load_b64(rWS, (lane & 31) * 16 + (lane >> 5) * 8, o + src * 1536 + 1024, 16);
    }
#pragma unroll
    for (int n = 0; n < 3; n++) {
      f32x4 r;
#pragma unroll
      for (int c = 0; c < 4; c++) {
        float G = mine[n][c] + ((c & 1) ? bfhi(bv[n][c >> 1]) : bflo(bv[n][c >> 1]));
#pragma unroll
        for (int k = 0; k < 3; k++) { const u32 v = n < 2 ? a[k][2 * n + (c >> 1)] : b[k][c >> 1]; G += (c & 1) ? bfhi(v) : bflo(v); }
        const u32 yv = n < 2 ? a[3][2 * n + (c >> 1)] : b[3][c >> 1];
        const float yy = (c & 1) ? bfhi(yv) : bflo(yv);
        const float xx = (c & 1) ? bfhi(xv[n][c >> 1]) : bflo(xv[n][c >> 1]);
        const float dd = (c & 1) ? bfhi(dv[n][c >> 1]) : bflo(dv[n][c >> 1]);
        const float sv = yy + (xx - mean) * rstd * dd;
        const float sig = __builtin_amdgcn_rcpf(1.f + __builtin_amdgcn_exp2f(-1.4426950408889634f * G));
        r[c] = xx + sv * (G * sig);
      }
      *(GAS f32x4*)(p.out + tok * D + col + 16 * n) = r;
    }
    STAMP(7);
  }
}

DI void ffn_compute(const Params& p, int rt, int g, int j, int w, int lane, u32 lds0, u32 tag) {
  const int rg = w >> 2, cg = w & 3, q = lane >> 4, e = (lane >> 2) & 3, pp = lane & 3, jl = lane & 15;
  const int z = 2 * (q & 1) + (e >> 1);
  u32 wc[4];
#pragma unroll
  for (int n = 0; n < 4; n++) { const int t = 6 * cg + n; wc[n] = lds0 + (t >> 2) * 4096 + (4 * q + e) * 128 + (((t & 3) ^ z) * 32) + pp * 8; }
  const u32 xbase = lds0 + TOFF + (64 * rg + jl) * 128 + ((q ^ (jl >> 1)) * 16);
  f32x4 acc[6][4];
#pragma unroll
  for (int n = 0; n < 6; n++)
#pragma unroll
    for (int T = 0; T < 4; T++) { acc[n][T] = (f32x4){0.f, 0.f, 0.f, 0.f}; asm volatile("" : "+v"(acc[n][T])); }
  bf16x8 wp[2], t[4];
  BAR();
  STAMP(1);
  wp[0] = rd_tr(wc[0], 0); wp[1] = rd_tr(wc[1], 0);
#pragma unroll
  for (int T = 0; T < 4; T++) t[T] = rd128(xbase, T * 2048);
  u32 xs = 0; int dw = 2 * WSLOT;
#pragma nounroll
  for (int it = 0; it < 9; it++) {
    stepA<0>(acc, wp, t, wc, 0, 0, (xbase ^ 64) + xs); xs = (xs + TSLOT) & (4 * TSLOT - 1);
    const u32 n0 = wc[0] + dw, n1 = wc[1] + dw;
    stepA<1>(acc, wp, t, wc, n0, n1, xbase + xs);
    waitv<0>();
    wc[0] = n0; wc[1] = n1; wc[2] += dw; wc[3] += dw; dw = -dw;
  }
  WAITL(); BAR();
  STAMP(2);
  const u32 ax = lds0 + (18 & 3) * WSLOT;
  float k1[4], k2[4], sg[4];
#pragma unroll
  for (int T = 0; T < 4; T++) {
    u32x4 v = __builtin_bit_cast(u32x4, rd128(ax + AX_T + (64 * rg + jl) * 16, T * 256));
    if (q != 0) v = (u32x4){0u, 0u, 0u, 0u};
    t[T] = __builtin_bit_cast(bf16x8, v);
    const float r = rdf(ax + AX_S + 512 + (64 * rg + jl) * 4, T * 64);
    sg[T] = rdf(ax + AX_S + 1024 + (64 * rg + jl) * 4, T * 64);
    k1[T] = -2.3022081985f * r; k2[T] = -0.10294324f * r * r * r;
  }
  {
    bf16x8 a[6];
#pragma unroll
    for (int n = 0; n < 6; n++) a[n] = rd128(ax + AX_W + (96 * cg + jl) * 16, n * 256);
    asm volatile("s_nop 1" : "+v"(t[0]), "+v"(t[1]), "+v"(t[2]), "+v"(t[3]));
#pragma unroll
    for (int n = 0; n < 6; n++)
#pragma unroll
      for (int T = 0; T < 4; T++) MMA(acc[n][T], a[n], t[T]);
    MFENCE();
  }
  const rsrc_t rWS = mk(p.ws, WS_END);
  const u32 pg = WS_P + (rt * 15 + 3 * g + j) * 98304 + (cg >> 1) * 16384 + (cg & 1) * 8192 + rg * 4096;
  const u32 pl = lds0 + TOFF + (cg & 1) * 8192 + rg * 4096 + lane * 16, pl0 = pl + ((19 + (cg >> 1)) & 3) * TSLOT, pl1 = pl + ((21 + (cg >> 1)) & 3) * TSLOT;
#pragma unroll
  for (int bb = 0; bb < 3; bb++) {
    const int b = (bb + 2) % 3;
#pragma unroll
    for (int T = 0; T < 4; T++) {
      const u32x4 P = gelu_frag(acc[2 * b][T], acc[2 * b + 1][T], k1[T], k2[T], sg[T]);
      st16(P, rWS, lane * 16, pg + b * 32768 + T * 1024);
      if (b < 2) wr128(b ? pl1 : pl0, T * 1024, P);
      SB();
    }
  }
  STAMP(3);
  phaseB<false>(p, rt, g, j, w, lane, lds0, tag);
}

DI void ssm_compute(const Params& p, int rt, int j, int w, int lane, u32 lds0, u32 tag) {
  const int rg = w >> 2, cg = w & 3; int q = lane >> 4, jl = lane & 15;
  u32 wb = lds0 + (16 * cg + jl) * 128 + ((q ^ (jl >> 1)) * 16);
  const u32 xb0 = lds0 + TOFF + (64 * rg + jl) * 128 + ((q ^ (jl >> 1)) * 16);
  f32x4 ar[3][4], ai[3][4];
#pragma unroll
  for (int n = 0; n < 3; n++)
#pragma unroll
    for (int T = 0; T < 4; T++) { ar[n][T] = (f32x4){0.f, 0.f, 0.f, 0.f}; ai[n][T] = (f32x4){0.f, 0.f, 0.f, 0.f}; asm volatile("" : "+v"(ar[n][T]), "+v"(ai[n][T])); }
  bf16x8 wf[3], tp[2];
  auto nop = [](int) {};
  auto barm = [](int i) { if (i == 3) BAR(); };
  BAR();
  STAMP(1);
#pragma unroll
  for (int n = 0; n < 3; n++) wf[n] = rd128(wb, n * 8192);
  tp[0] = rd128(xb0, 0); tp[1] = rd128(xb0, 2048);
  u32 xs = 0; int dw = 2 * WSLOT;
#pragma nounroll
  for (int it = 0; it < 9; it++) {
    const u32 x0 = xb0 + xs, x1 = (xb0 ^ 64) + xs, wb1 = wb ^ 64, wn = wb + dw;
    xs = (xs + TSLOT) & (4 * TSLOT - 1);
    const u32 xn = xb0 + xs;
    half<2>(ar, wf, tp, [&](int n, int) { return rd128(wb1, n * 8192); }, [&](int T, int) { return rd128(x0, T * 2048); }, [&](int T, int) { return rd128(x1, T * 2048); }, barm);
    half<2>(ar, wf, tp, [&](int n, int) { return rd128(wb, WSLOT + n * 8192); }, [&](int T, int) { return rd128(x1, T * 2048); }, [&](int T, int) { return rd128(x0, T * 2048); }, nop);
    half<2>(ai, wf, tp, [&](int n, int) { return rd128(wb1, WSLOT + n * 8192); }, [&](int T, int) { return rd128(x0, T * 2048); }, [&](int T, int) { return rd128(x1, T * 2048); }, barm);
    half<2>(ai, wf, tp, [&](int n, int) { return rd128(wn, n * 8192); }, [&](int T, int) { return rd128(x1, T * 2048); }, [&](int T, int) { return rd128(xn, T * 2048); }, nop);
    wb = wn; dw = -dw;
    waitv<0>();
  }
  WAITL(); BAR();
  STAMP(2);
  asm volatile("" : "+v"(lane));
  q = lane >> 4; jl = lane & 15;
  const u32 ax = lds0 + (18 & 3) * WSLOT;
  {
    bf16x8 t[4];
#pragma unroll
    for (int T = 0; T < 4; T++) {
      u32x4 v = __builtin_bit_cast(u32x4, rd128(ax + AX_T + (64 * rg + jl) * 16, T * 256));
      if (q != 0) v = (u32x4){0u, 0u, 0u, 0u};
      t[T] = __builtin_bit_cast(bf16x8, v);
    }
    bf16x8 a[6];
#pragma unroll
    for (int b = 0; b < 6; b++) a[b] = rd128(ax + AX_W + (16 * cg + jl) * 16, b * 1024);
    asm volatile("s_nop 1" : "+v"(t[0]), "+v"(t[1]), "+v"(t[2]), "+v"(t[3]));
#pragma unroll
    for (int b = 0; b < 3; b++)
#pragma unroll
      for (int T = 0; T < 4; T++) { MMA(ar[b][T], t[T], a[b]); MMA(ai[b][T], t[T], a[3 + b]); }
    MFENCE();
  }
  f32x4 lc[3];
  const u32 scr = lds0 + TOFF + (22 & 3) * TSLOT + ((4 * rg + q) * 192 + 16 * cg + jl) * 8;
  f32x2 e0[3];
#pragma unroll
  for (int T = 0; T < 4; T++) {
    const f32x4 r = rdf4(ax + AX_S + 512 + (64 * rg + 4 * q) * 4, T * 64);
#pragma unroll
    for (int b = 0; b < 3; b++) { ar[b][T] *= r; ai[b][T] *= r; }
  }
  f32x2 l8[3];
#pragma unroll
  for (int b = 0; b < 3; b++) {
    lc[b] = rdf4(ax + AX_TAB + (16 * cg + jl) * 32, b * 2048);
    l8[b] = *(LAS f32x2*)(size_t)(ax + AX_TAB + (16 * cg + jl) * 32 + b * 2048 + 16);
    f32x2 hr = {ar[b][0][0], ar[b][0][1]}, hi = {ai[b][0][0], ai[b][0][1]};
#pragma unroll
    for (int k = 1; k < 8; k++) {
      const f32x2 ur = {ar[b][k >> 1][(k & 1) * 2], ar[b][k >> 1][(k & 1) * 2 + 1]}, ui = {ai[b][k >> 1][(k & 1) * 2], ai[b][k >> 1][(k & 1) * 2 + 1]};
      const f32x2 nr = lc[b][0] * hr + (ur - lc[b][1] * hi), ni = lc[b][0] * hi + (ui + lc[b][1] * hr);
      hr = nr; hi = ni;
    }
    e0[b] = (f32x2){hr.x, hi.x};
    *(LAS f32x2*)(size_t)(scr + b * 512) = (f32x2){l8[b].x * hr.x - l8[b].y * hi.x + hr.y, l8[b].x * hi.x + l8[b].y * hr.x + hi.y};
  }
  STAMP(8);
  WAITL(); BAR();
  STAMP(9);
#pragma unroll
  for (int b = 0; b < 3; b++) {
    const float c2r = 2.f * lc[b][2], c2i = 2.f * lc[b][3];
#pragma unroll
    for (int T = 0; T < 4; T++) {
      const f32x4 nr = c2r * ar[b][T] - c2i * ai[b][T], ni = -c2r * ai[b][T] - c2i * ar[b][T];
      ar[b][T] = nr; ai[b][T] = ni;
      asm volatile("" : "+v"(ar[b][T]), "+v"(ai[b][T]));
    }
  }
  BAR();
  STAMP(10);
  const rsrc_t rWS = mk(p.ws, WS_END);
  const u32 io0 = w * 2048 + lane * 16, io1 = w * 2048 + 1024 + ((lane * 16) ^ 64);
  const u32 pg = WS_P + (rt * 15 + 12 + j) * 98304;
  f32x2 cins[3];
#pragma unroll
  for (int b = 0; b < 3; b++) cins[b] = *(LAS f32x2*)(size_t)(scr + b * 512);
#pragma unroll
  for (int bb = 0; bb < 3; bb++) {
    const int b = (bb + 2) % 3;
    u32x4 H[2][2];
    const f32x2 cin = cins[b];
    const f32x2 sr = {cin.x, l8[b].x * cin.x - l8[b].y * cin.y + e0[b].x}, si = {cin.y, l8[b].x * cin.y + l8[b].y * cin.x + e0[b].y};
    const float c2r = 2.f * lc[b][2], c2i = 2.f * lc[b][3];
    f32x2 hr = c2r * sr - c2i * si, hi = -c2r * si - c2i * sr;
#pragma unroll
    for (int T = 0; T < 4; T++) {
      f32x2 pr[2], pi[2];
#pragma unroll
      for (int k = 0; k < 2; k++) {
        const f32x2 ur = {ar[b][T][2 * k], ar[b][T][2 * k + 1]}, ui = {ai[b][T][2 * k], ai[b][T][2 * k + 1]};
        const f32x2 nr = lc[b][0] * hr + (ur + lc[b][1] * hi), ni = lc[b][0] * hi + (ui - lc[b][1] * hr);
        hr = nr; hi = ni; pr[k] = nr; pi[k] = ni;
      }
      H[0][0][T] = cvtpk(pr[0].x, pr[1].x); H[0][1][T] = cvtpk(pr[0].y, pr[1].y);
      H[1][0][T] = cvtpk(pi[0].x, pi[1].x); H[1][1][T] = cvtpk(pi[0].y, pi[1].y);
    }
    if (bb == 1) BAR();
#pragma unroll
    for (int k = 0; k < 4; k++) {
      st16(H[k >> 1][k & 1], rWS, (k & 1) ? io1 : io0, pg + (2 * b + (k >> 1)) * 16384);
      if (b < 2) wr128(lds0 + TOFF + ((k & 1) ? io1 : io0), ((19 + 2 * b + (k >> 1)) & 3) * TSLOT, H[k >> 1][k & 1]);
    }
    SB();
  }
  STAMP(3);
  phaseB<true>(p, rt, 4, j, w, lane, lds0, tag);
}

DI void spare(const Params& p, int sid, u32 lds0, u32 tag, u64 pc0) {
  const int tid = threadIdx.x;
  u32 cpf = 0;
  if (tid < CODE_LINES) cpf = *(const GAS u32*)(pc0 + tid * 128);
  const rsrc_t rWS = mk(p.ws, WS_END);
  const int w = __builtin_amdgcn_readfirstlane(tid >> 6), lane = tid & 63;
  u32 hsn = 0;
  if (sid == 15) {
    if (w == 11) { waitv<0>(); hs_leader(p.ws, lane, lds0, STAMPPTR); return; }
    if (w < 8) {
      u32x4 v[2][9];
#pragma unroll
      for (int h = 0; h < 2; h++)
#pragma unroll
        for (int i = 0; i < 9; i++) v[h][i] = ((const GAS u32x4*)(p.x + ((2 * w + h) * 128 + 120 + (lane >> 3)) * D) + (lane & 7))[8 * i];
#pragma unroll
      for (int h = 0; h < 2; h++) {
        float s = 0.f;
#pragma unroll
        for (int i = 0; i < 9; i++)
#pragma unroll
          for (int c = 0; c < 4; c++) s += bflo(v[h][i][c]) + bfhi(v[h][i][c]);
        s += dppx<0xb1>(s); s += dppx<0x4e>(s); s += dppx<0x141>(s);
        const float mean = s * (1.f / D); s = 0.f;
#pragma unroll
        for (int i = 0; i < 9; i++)
#pragma unroll
          for (int c = 0; c < 4; c++) { const float a = bflo(v[h][i][c]) - mean, b = bfhi(v[h][i][c]) - mean; s += a * a + b * b; }
        s += dppx<0xb1>(s); s += dppx<0x4e>(s); s += dppx<0x141>(s);
        if ((lane & 7) == 0) __hip_atomic_store((GAS u64*)(p.ws + WS_ST + ((2 * w + h) * 128 + 120 + (lane >> 3)) * 8), ((u64)__float_as_uint(s * (1.f / D) + 1e-5f) << 32) | __float_as_uint(mean), __ATOMIC_RELAXED, __HIP_MEMORY_SCOPE_AGENT);
      }
    }
    waitv<0>(); BAR();
  } else if (w == 11) {
    hsn = hs_request(p.ws); spin_all(p.ws + WS_RSP + blockIdx.x * 4, hsn); st_word(p.ws + WS_ACK + blockIdx.x * 4, 1u);
  } else if (sid < 12) {
    if (w < 9) {
      const int cgp = tid % 48, rgp = tid / 48;
      const GAS u32x4* src = (const GAS u32x4*)(p.w1 + 2 * rgp * F + sid * 384) + cgp;
      float a[8];
#pragma unroll
      for (int c = 0; c < 8; c++) a[c] = 0.f;
      u32x4 v[48];
#pragma unroll
      for (int i = 0; i < 24; i++) v[i] = src[((i >> 1) * 24 + (i & 1)) * (F / 8)];
#pragma unroll
      for (int i = 0; i < 24; i++) {
#pragma unroll
        for (int c = 0; c < 4; c++) {
          a[2 * c] = dot2(__builtin_amdgcn_perm(v[2 * i + 1][c], v[2 * i][c], 0x05040100), ONES, a[2 * c]);
          a[2 * c + 1] = dot2(__builtin_amdgcn_perm(v[2 * i + 1][c], v[2 * i][c], 0x07060302), ONES, a[2 * c + 1]);
        }
        SB();
        if (i < 12) { v[24 + 2 * i] = src[((12 + i) * 24) * (F / 8)]; v[25 + 2 * i] = src[((12 + i) * 24 + 1) * (F / 8)]; }
        SB();
      }
      *(LAS f32x4*)(size_t)(lds0 + (rgp * 384 + cgp * 8) * 4) = (f32x4){a[0], a[1], a[2], a[3]};
      *(LAS f32x4*)(size_t)(lds0 + (rgp * 384 + cgp * 8) * 4 + 16) = (f32x4){a[4], a[5], a[6], a[7]};
    }
  } else if (sid < 15) {
    if (w < 8) {
#pragma unroll
      for (int h = 0; h < 2; h++) {
        const GAS u32x4* src = (const GAS u32x4*)((h ? p.bim : p.bre) + ((sid - 12) * 192 + (tid >> 3)) * D) + (tid & 7);
        u32x4 v[27];
#pragma unroll
        for (int i = 0; i < 27; i++) v[i] = src[(i / 9) * 64 * (D / 8) + (i % 9) * 8];
#pragma unroll
        for (int k = 0; k < 3; k++) {
          float s = 0.f, s2 = 0.f;
#pragma unroll
          for (int i = 9 * k; i < 9 * k + 9; i++) { s = dot2(v[i][0], ONES, s); s2 = dot2(v[i][1], ONES, s2); s = dot2(v[i][2], ONES, s); s2 = dot2(v[i][3], ONES, s2); }
          s += s2; s += dppx<0xb1>(s); s += dppx<0x4e>(s); s += dppx<0x141>(s);
          float c1, c2, c3; split3(s, c1, c2, c3);
          *(LAS u32x4*)(size_t)(lds0 + (h * 192 + k * 64 + (tid >> 3)) * 16) = (u32x4){cvtpk(c1, c2), cvtpk(c1, c2), cvtpk(c3, c1), 0u};
        }
      }
    } else {
      const int cl = tid - 512, c = (sid - 12) * 192 + cl;
      const float lr = bf2f(p.lre[c]), li = bf2f(p.lim[c]);
      const float dt = rbf(expf(bf2f(p.logdt[c])));
      const float ex = expf(dt * lr), an = dt * li;
      const float br = ex * cosf(an), bi = ex * sinf(an);
      const float nr = br - 1.f, den = 1.f / (lr * lr + li * li);
      const float cr = (nr * lr + bi * li) * den, ci = (bi * lr - nr * li) * den;
      float ar = br, ai = bi, r8 = 0.f, i8 = 0.f;
#pragma unroll
      for (int k = 0; k < 7; k++) { const float t = ar * ar - ai * ai; ai = 2.f * ar * ai; ar = t; if (k == 2) { r8 = ar; i8 = ai; } }
      *(LAS f32x4*)(size_t)(lds0 + 6144 + cl * 32) = (f32x4){br, bi, cr, ci};
      *(LAS f32x4*)(size_t)(lds0 + 6144 + cl * 32 + 16) = (f32x4){r8, i8, ar, ai};
    }
  }
  if (sid < 15) {
    WAITL(); BAR();
    if (sid < 12) {
      if (tid < 384) {
        float s = 0.f;
#pragma unroll
        for (int r = 0; r < 12; r++) s += rdf(lds0 + tid * 4, r * 1536);
        float c1, c2, c3; split3(s, c1, c2, c3);
        const float b = bf2f(p.b1[sid * 384 + tid]);
        st16((u32x4){cvtpk(c1, c2), cvtpk(c1, c2), cvtpk(c3, c1), cvtpk(b, b)}, rWS, tid * 16, WS_AUXW + sid * 6144);
      }
    } else {
      const u32x4 v = *(LAS u32x4*)(size_t)(lds0 + tid * 16);
      st16(v, rWS, tid < 384 ? WS_AUXW + sid * 6144 + tid * 16 : WS_TAB + (sid - 12) * 6144 + (tid - 384) * 16, 0);
    }
    waitv<0>(); BAR();
    if (tid == 0) st_flag(p.ws, FL_C + sid, tag);
  }
  STAMP(1);
  {
    u32 acc = cpf;
    const GAS u32* w2 = (const GAS u32*)p.w2;
    if (tid < 648) {
#pragma unroll
      for (int i = 0; i < 4; i++) {
        const int L = (i * 16 + sid) * 648 + tid;
        const int r = L / 9, kb = r / 1536, rr = r - kb * 1536;
        acc ^= w2[((rr >> 5) * 96 + kb * 32 + (rr & 31)) * (ROWB / 4) + (L - r * 9) * 32];
      }
      if (tid < 324) acc ^= ((const GAS u32*)p.cre)[(sid * 324 + tid) * 32]; else acc ^= ((const GAS u32*)p.cim)[(sid * 324 + tid - 324) * 32];
    }
    asm volatile("" :: "v"(acc));
  }
}

__global__ __launch_bounds__(768, 3) void fused_kernel(Params p) {
  extern __shared__ __attribute__((aligned(16))) char smem[];
  const u32 lds0 = (u32)(size_t)(LAS char*)smem;
  const int b = blockIdx.x, xcd = b & 7, idx = b >> 3;
  const u32 tag = 1u;
  u64 pc0; asm volatile("s_getpc_b64 %0" : "=s"(pc0));
  STAMP(0);
  const int tri = xcd * 10 + idx / 3, j = idx % 3, g = tri >> 4, rt = tri & 15;
  const int lane = threadIdx.x & 63, w = __builtin_amdgcn_readfirstlane(threadIdx.x >> 6);
  const int sid = xcd * 2 + idx - 30;
  const int srow = rt * 128 + (3 * g + j) * 8 + w;
  int isld = idx < 30 ? w >> 3 : 0;
  if (!isld) {
    if (idx < 30) {
      if (w == 0 && lane < 2) st_flag(p.ws, lane ? FL_P + rt * 15 + 3 * g + j : FL_O + (rt * 3 + j) * 5 + g, 0u);
      if (g == 4 && w >= 1 && w < 4) st16((u32x4){0u, 0u, 0u, 0u}, mk(p.ws, WS_END), (192 * j + threadIdx.x - 64) * 16, WS_EG + rt * 576 * 16);
    } else if (w == 11 && sid == 15) {
      if (lane < 16) st_flag(p.ws, lane == 15 ? FL_GO : FL_C + lane, 0u);
      st16((u32x4){0u, 0u, 0u, 0u}, mk(p.ws, WS_END), lane * 16, WS_ACK);
    }
    if (idx < 30) {
      float mean, ve; row_stats(p.x + srow * D, lane, mean, ve);
      if (lane == 0) __hip_atomic_store((GAS u64*)(p.ws + WS_ST + srow * 8), ((u64)__float_as_uint(ve) << 32) | __float_as_uint(mean), __ATOMIC_RELAXED, __HIP_MEMORY_SCOPE_AGENT);
    }
  }
  asm volatile("" : "+s"(isld));
  if (__builtin_expect(isld, 1)) { loader(p, rt, g, j, w - 8, lane, lds0, tag); return; }
  if (idx >= 30) { const Params q = reload_params(); spare(q, sid, lds0, tag, pc0); STAMP(7); return; }
  if (g < 4) { const Params q = reload_params(); ffn_compute(q, rt, g, j, w, lane, lds0, tag); }
  else { const Params q = reload_params(); ssm_compute(q, rt, j, w, lane, lds0, tag); }
}

extern "C" void kernel_launch(void* const* d_in, const int* in_sizes, int n_in, void* d_out, int out_size, void* d_ws, size_t ws_size, hipStream_t stream) {
  Params p;
  p.x = (const u16*)d_in[0]; p.lre = (const u16*)d_in[1]; p.lim = (const u16*)d_in[2]; p.bre = (const u16*)d_in[3]; p.bim = (const u16*)d_in[4];
  p.cre = (const u16*)d_in[5]; p.cim = (const u16*)d_in[6]; p.logdt = (const u16*)d_in[7]; p.dskip = (const u16*)d_in[8];
  p.w1 = (const u16*)d_in[10]; p.b1 = (const u16*)d_in[11]; p.w2 = (const u16*)d_in[12]; p.b2 = (const u16*)d_in[13];
  p.out = (float*)d_out; p.ws = (char*)d_ws;
  (void)hipFuncSetAttribute((const void*)fused_kernel, hipFuncAttributeMaxDynamicSharedMemorySize, LDS_BYTES);
  fused_kernel<<<256, 768, LDS_BYTES, stream>>>(p);
}
```

```cpp
#define STAMPS
#define K0 0
#define A0 38
#define B0 71
#define K1 1
#define A1 19
#define B1 20
#include <hip/hip_runtime.h>
#include <utility>

#define LAS __attribute__((address_space(3)))
#define GAS __attribute__((address_space(1)))
#define DI __device__ __forceinline__
typedef unsigned short u16;
typedef unsigned u32;
typedef unsigned long long u64;
typedef unsigned u32x4 __attribute__((ext_vector_type(4)));
typedef unsigned u32x2 __attribute__((ext_vector_type(2)));
typedef short s16x4 __attribute__((ext_vector_type(4)));
typedef short bf16x8 __attribute__((ext_vector_type(8)));
typedef float f32x4 __attribute__((ext_vector_type(4)));
typedef float f32x2 __attribute__((ext_vector_type(2)));
typedef __bf16 bf16x2 __attribute__((ext_vector_type(2)));
typedef __amdgpu_buffer_rsrc_t rsrc_t;

constexpr int S = 2048, D = 576, F = 4608;
constexpr int ROWB = D * 2;
constexpr int WSLOT = 24576, TOFF = 4 * WSLOT, TSLOT = 16384, LDS_BYTES = 163840;
constexpr int AX_W = 0, AX_T = 6144, AX_S = 8192, AX_TAB = 10240;
constexpr u32 WS_FLAGS = 0;
constexpr u32 FL_GO = 0, FL_C = 16, FL_P = 32, FL_O = 32 + 240, NFLAG = 32 + 480;
constexpr u32 WS_EG = NFLAG * 128;
constexpr u32 WS_ST = WS_EG + 16 * 576 * 16;
constexpr u32 WS_REQ = WS_ST + 3 * 12288, WS_RSP = WS_REQ + 1024, WS_ACK = WS_RSP + 1024;
constexpr u32 WS_DUMMY = WS_ACK + 1024;
constexpr u32 WS_AUXW = 256 * 1024;
constexpr u32 WS_TAB = WS_AUXW + 15 * 6144;
constexpr u32 WS_P = 1u << 20;
constexpr u32 XBW = 5 * 1536;
constexpr u32 WS_XB = WS_P + 240 * 98304;
constexpr u32 WS_END = WS_XB + 48 * 32 * XBW;
constexpr int CODE_LINES = 350;
constexpr u32 SPIN_CAP = 1u << 18;

struct Params {
  const u16 *x, *lre, *lim, *bre, *bim, *cre, *cim, *logdt, *dskip, *w1, *b1, *w2, *b2;
  float* out; char* ws;
  u64* stamps;
};

DI Params reload_params() {
  Params q;
#if defined(__HIP_DEVICE_COMPILE__)
  typedef __attribute__((address_space(4))) const Params* KP;
  KP kp = (KP)__builtin_amdgcn_kernarg_segment_ptr(); asm volatile("" : "+s"(kp)); q = *kp;
#endif
  return q;
}
template <int N> DI void waitv() { asm volatile("s_waitcnt vmcnt(%0)" :: "n"(N) : "memory"); }
#define WAITL() asm volatile("s_waitcnt lgkmcnt(0)" ::: "memory")
#define BAR() do { asm volatile("" ::: "memory"); __builtin_amdgcn_s_barrier(); asm volatile("" ::: "memory"); } while (0)
#define SB() __builtin_amdgcn_sched_barrier(0)
#define MFENCE() do { SB(); asm volatile("s_nop 7\n\ts_nop 7"); SB(); } while (0)
#define MMA(c, a, b) asm volatile("v_mfma_f32_16x16x32_bf16 %0, %1, %2, %0" : "+v"(c) : "v"(a), "v"(b))
#define PIN(x) asm volatile("" : "+v"(x))
#define STAMPPTR p.stamps
#define STAMP(i) do { SB(); if (threadIdx.x == 0) p.stamps[blockIdx.x * 16 + (i)] = __builtin_amdgcn_s_memrealtime(); SB(); } while (0)

DI void dma(rsrc_t r, u32 lds, u32 voff, u32 soff) {
  asm volatile("s_mov_b32 m0, %0\n\ts_nop 0\n\tbuffer_load_dwordx4 %1, %2, %3 offen lds" :: "s"(lds), "v"(voff), "s"(r), "s"(soff) : "memory");
}
DI void dma_sc1(rsrc_t r, u32 lds, u32 voff, u32 soff) {
  asm volatile("s_mov_b32 m0, %0\n\ts_nop 0\n\tbuffer_load_dwordx4 %1, %2, %3 offen sc1 lds" :: "s"(lds), "v"(voff), "s"(r), "s"(soff) : "memory");
}
DI rsrc_t mk(const void* ptr, u32 bytes) { return __builtin_amdgcn_make_buffer_rsrc((void*)ptr, 0, bytes, 0x00020000); }
DI s16x4 rd_tr1(u32 a, int off) { return __builtin_amdgcn_ds_read_tr16_b64_v4i16((LAS s16x4*)(size_t)(a + off)); }
DI bf16x8 cat(s16x4 lo, s16x4 hi) { return __builtin_shufflevector(lo, hi, 0, 1, 2, 3, 4, 5, 6, 7); }
DI bf16x8 rd_tr(u32 a, int off) { return cat(rd_tr1(a, off), rd_tr1(a, off + 2048)); }
DI bf16x8 rd128(u32 a, int off) { return *(LAS bf16x8*)(size_t)(a + off); }
DI f32x4 rdf4(u32 a, int off) { return *(LAS f32x4*)(size_t)(a + off); }
DI float rdf(u32 a, int off) { return *(LAS float*)(size_t)(a + off); }
DI void wr128(u32 a, int off, u32x4 v) { *(LAS u32x4*)(size_t)(a + off) = v; }
DI u32 cvtpk(float lo, float hi) { f32x2 v = {lo, hi}; bf16x2 b = __builtin_convertvector(v, bf16x2); return __builtin_bit_cast(u32, b); }
DI float bflo(u32 v) { return __uint_as_float(v << 16); }
DI float bfhi(u32 v) { return __uint_as_float(v & 0xffff0000u); }
DI float bf2f(u16 v) { return __uint_as_float((u32)v << 16); }
DI float dot2(u32 a, u32 b, float c) { return __builtin_amdgcn_fdot2_f32_bf16(__builtin_bit_cast(bf16x2, a), __builtin_bit_cast(bf16x2, b), c, false); }
constexpr u32 ONES = 0x3f803f80u;
DI float rbf(float v) { return bflo(cvtpk(v, 0.f)); }
DI void split3(float v, float& a, float& b, float& c) { a = rbf(v); const float r = v - a; b = rbf(r); c = rbf(r - b); }
template <int C> DI float dppx(float v) { return __uint_as_float(__builtin_amdgcn_update_dpp(0u, __float_as_uint(v), C, 0xf, 0xf, true)); }
template <int C> DI double dppd(double v) { u32x2 t = __builtin_bit_cast(u32x2, v); t[0] = __builtin_amdgcn_update_dpp(0u, t[0], C, 0xf, 0xf, true); t[1] = __builtin_amdgcn_update_dpp(0u, t[1], C, 0xf, 0xf, true); return __builtin_bit_cast(double, t); }
DI void st16(u32x4 v, rsrc_t r, u32 voff, u32 soff) { asm volatile("buffer_store_dwordx4 %0, %1, %2, %3 offen sc1\n\ts_nop 1" :: "v"(v), "v"(voff), "s"(r), "s"(soff) : "memory"); }
DI void st_flag(char* ws, u32 idx, u32 tag) { __hip_atomic_store((GAS u32*)(ws + WS_FLAGS + idx * 128), tag, __ATOMIC_RELAXED, __HIP_MEMORY_SCOPE_AGENT); }
DI u32 ld_flag(const char* a) { return __hip_atomic_load((GAS u32*)a, __ATOMIC_RELAXED, __HIP_MEMORY_SCOPE_AGENT); }
DI void spin_all(const char* a, u32 tag) {
#pragma nounroll
  for (u32 i = 0; i < SPIN_CAP; i++) { if (__all(ld_flag(a) == tag)) break; __builtin_amdgcn_s_sleep(1); }
  asm volatile("" ::: "memory");
}
DI void spin_ge(const char* a, u32 lvl) {
#pragma nounroll
  for (u32 i = 0; i < SPIN_CAP; i++) { if (__all(ld_flag(a) >= lvl)) break; __builtin_amdgcn_s_sleep(1); }
  asm volatile("" ::: "memory");
}
DI void st_word(char* a, u32 v) { __hip_atomic_store((GAS u32*)a, v, __ATOMIC_RELAXED, __HIP_MEMORY_SCOPE_AGENT); }
DI u32 hs_request(char* ws) { const u32 n = ld_flag(ws + WS_RSP + blockIdx.x * 4) + 1u; st_word(ws + WS_REQ + blockIdx.x * 4, n); return n; }
DI void hs_leader(char* ws, int lane, u32 lds0, u64* stamps) {
  const rsrc_t r = mk(ws, WS_END);
  bool first = true, go = false; u32x4 last = {0u, 0u, 0u, 0u};
#pragma nounroll
  for (u32 i = 0; i < SPIN_CAP; i++) {
    const u32x4 rq = __builtin_amdgcn_raw_buffer_load_b128(r, lane * 16, WS_REQ, 16), ak = __builtin_amdgcn_raw_buffer_load_b128(r, lane * 16, WS_ACK, 16);
    if (first || rq[0] != last[0] || rq[1] != last[1] || rq[2] != last[2] || rq[3] != last[3]) st16(rq, r, lane * 16, WS_RSP);
    first = false; last = rq;
    const bool ok = ak[0] == 1u && ak[1] == 1u && ak[2] == 1u && (ak[3] == 1u || lane == 63);
    if (!go && __all(ok || lane >= 60)) {
      BAR();
      st_flag(ws, FL_GO, 1u); go = true;
      if (lane == 0) stamps[255 * 16 + 2] = __builtin_amdgcn_s_memrealtime();
    }
    if (__all(ok)) break;
    __builtin_amdgcn_s_sleep(2);
  }
}
DI float wave_sum(float v) {
  v += dppx<0xb1>(v); v += dppx<0x4e>(v); v += dppx<0x141>(v); v += dppx<0x140>(v);
  const u32 b = __float_as_uint(v);
  return (__uint_as_float(__builtin_amdgcn_readlane(b, 0)) + __uint_as_float(__builtin_amdgcn_readlane(b, 16))) + (__uint_as_float(__builtin_amdgcn_readlane(b, 32)) + __uint_as_float(__builtin_amdgcn_readlane(b, 48)));
}
DI void row_stats(const u16* row, int lane, float& mean, float& ve) {
  const u32x4 a = *((const GAS u32x4*)row + lane), b = *((const GAS u32x4*)row + 64 + (lane & 7));
  float s = 0.f, t = 0.f;
#pragma unroll
  for (int c = 0; c < 4; c++) { s += bflo(a[c]) + bfhi(a[c]); t += bflo(b[c]) + bfhi(b[c]); }
  mean = wave_sum(lane < 8 ? s + t : s) * (1.f / D);
  s = 0.f; t = 0.f;
#pragma unroll
  for (int c = 0; c < 4; c++) {
    const float a0 = bflo(a[c]) - mean, a1 = bfhi(a[c]) - mean, b0 = bflo(b[c]) - mean, b1 = bfhi(b[c]) - mean;
    s += a0 * a0 + a1 * a1; t += b0 * b0 + b1 * b1;
  }
  ve = wave_sum(lane < 8 ? s + t : s) * (1.f / D) + 1e-5f;
}
DI u32 poll_issue(const char* a) { u32 v; asm volatile("global_load_dword %0, %1, off sc1" : "=v"(v) : "v"(a) : "memory"); return v; }

DI f32x2 gelu2(f32x2 a, float k1, float k2, float sg) {
  f32x2 t = a * a; t = t * k2 + k1; t = t * a;
  t.x = __builtin_amdgcn_exp2f(t.x); t.y = __builtin_amdgcn_exp2f(t.y);
  t = t * sg + sg;
  t.x = __builtin_amdgcn_rcpf(t.x); t.y = __builtin_amdgcn_rcpf(t.y);
  return t * a;
}
DI u32x4 gelu_frag(f32x4 a, f32x4 b, float k1, float k2, float sg) {
  f32x2 g0 = gelu2((f32x2){a[0], a[1]}, k1, k2, sg), g1 = gelu2((f32x2){a[2], a[3]}, k1, k2, sg);
  f32x2 g2 = gelu2((f32x2){b[0], b[1]}, k1, k2, sg), g3 = gelu2((f32x2){b[2], b[3]}, k1, k2, sg);
  return (u32x4){cvtpk(g0.x, g0.y), cvtpk(g1.x, g1.y), cvtpk(g2.x, g2.y), cvtpk(g3.x, g3.y)};
}

template <int H>
DI void stepA(f32x4 (&acc)[6][4], bf16x8 (&wp)[2], bf16x8 (&t)[4], const u32 (&wc)[4], u32 n0, u32 n1, u32 xn) {
  bf16x8 w[8], nx[4];
  w[0] = wp[0]; w[1] = wp[1];
#pragma unroll
  for (int n = 0; n < 6; n++) {
    const int f = n + 2, ff = f < 6 ? f : f - 6;
    const u32 a = (f < 6 || H == 0) ? wc[ff & 3] : (ff ? n1 : n0); const int off = (f < 6 ? H : 1 - H) * WSLOT + (ff >> 2) * 4096;
    MMA(acc[n][0], w[n], t[0]); SB();
    s16x4 lo = rd_tr1(a, off); SB();
    MMA(acc[n][1], w[n], t[1]); SB();
    s16x4 hi = rd_tr1(a, off + 2048); SB();
    MMA(acc[n][2], w[n], t[2]); SB();
    if (n >= 1 && n <= 4) { nx[n - 1] = rd128(xn, (n - 1) * 2048); SB(); }
    MMA(acc[n][3], w[n], t[3]); SB();
    if (n == 0) { BAR(); SB(); }
    w[f] = cat(lo, hi);
  }
  wp[0] = w[6]; wp[1] = w[7];
#pragma unroll
  for (int i = 0; i < 4; i++) t[i] = nx[i];
}

template <int KIND, class FW, class FT, class FN, class MID>
DI void half(f32x4 (&acc)[3][4], bf16x8 (&w)[3], bf16x8 (&tp)[2], FW&& rw, FT&& rt_, FN&& rn, MID&& mid) {
  bf16x8 t[4], wn[3], tn[2];
  t[0] = tp[0]; t[1] = tp[1];
  s16x4 lo;
#pragma unroll
  for (int i = 0; i < 12; i++) {
    const int T = i / 3, n = i % 3;
    if constexpr (KIND == 2) MMA(acc[n][T], t[T], w[n]); else MMA(acc[n][T], w[n], t[T]);
    SB();
    if constexpr (KIND == 0) {
      if (i == 0) t[2] = rt_(2, 0); if (i == 1) t[3] = rt_(3, 0);
      if (i >= 2 && i <= 7) { if ((i & 1) == 0) lo = rw((i - 2) >> 1, 0); else wn[(i - 2) >> 1] = cat(lo, rw((i - 2) >> 1, 1)); }
      if (i == 8) tn[0] = rn(0, 0); if (i == 9) tn[1] = rn(1, 0);
    } else if constexpr (KIND == 1) {
      if (i <= 3) { if ((i & 1) == 0) lo = rt_(2 + (i >> 1), 0); else t[2 + (i >> 1)] = cat(lo, rt_(2 + (i >> 1), 1)); }
      if (i >= 4 && i <= 6) wn[i - 4] = rw(i - 4, 0);
      if (i >= 7 && i <= 10) { if ((i & 1) == 1) lo = rn((i - 7) >> 1, 0); else tn[(i - 7) >> 1] = cat(lo, rn((i - 7) >> 1, 1)); }
    } else {
      if (i == 0) t[2] = rt_(2, 0); if (i == 2) t[3] = rt_(3, 0);
      if (i == 3 || i == 5 || i == 6) wn[i == 3 ? 0 : i - 4] = rw(i == 3 ? 0 : i - 4, 0);
      if (i == 8) tn[0] = rn(0, 0); if (i == 9) tn[1] = rn(1, 0);
    }
    mid(i);
    SB();
  }
#pragma unroll
  for (int i = 0; i < 3; i++) w[i] = wn[i];
  tp[0] = tn[0]; tp[1] = tn[1];
}

template <int I, int N, class FN> DI void sfor(FN&& f) { if constexpr (I < N) { f(std::integral_constant<int, I>{}); sfor<I + 1, N>(f); } }

DI void ssm_carry(const Params& p, int rt, int j, int c, u32 lds0, u32 tag) {
  const u32 ax = lds0 + (18 & 3) * WSLOT, scr = lds0 + TOFF + (22 & 3) * TSLOT;
  {
    f32x4 pw = rdf4(ax + AX_TAB + c * 32, 16);
    { const float t = pw[0] * pw[0] - pw[1] * pw[1]; pw[1] = 2.f * pw[0] * pw[1]; pw[0] = t; }
    f32x2 es[8];
#pragma unroll
    for (int s = 0; s < 8; s++) es[s] = *(LAS f32x2*)(size_t)(scr + (s * 192 + c) * 8);
    float sr = 0.f, si = 0.f;
#pragma unroll
    for (int s = 0; s < 8; s++) { const float nr = pw[0] * sr - pw[1] * si + es[s].x, ni = pw[0] * si + pw[1] * sr + es[s].y; sr = nr; si = ni; }
    const rsrc_t rWS = mk(p.ws, WS_END);
    __builtin_amdgcn_raw_buffer_store_b128((u32x4){__float_as_uint(sr), tag, __float_as_uint(si), tag}, rWS, (192 * j + c) * 16, WS_EG + rt * 576 * 16, 16);
    float cr = 0.f, ci = 0.f;
#pragma nounroll
    for (u32 spin = 0; spin < SPIN_CAP; spin++) {
      u32x4 v[15]; bool ok = true;
#pragma unroll
      for (int i = 0; i < 15; i++) v[i] = __builtin_amdgcn_raw_buffer_load_b128(rWS, (192 * j + c) * 16, WS_EG + (i < rt ? i : 0) * 576 * 16, 16);
      cr = 0.f; ci = 0.f;
#pragma unroll
      for (int i = 0; i < 15; i++) {
        if (i < rt) {
          ok = ok && v[i][1] == tag && v[i][3] == tag;
          const float nr = pw[2] * cr - pw[3] * ci + __uint_as_float(v[i][0]), ni = pw[2] * ci + pw[3] * cr + __uint_as_float(v[i][2]); cr = nr; ci = ni;
        }
      }
      if (__all(ok)) break;
      __builtin_amdgcn_s_sleep(1);
    }
#pragma unroll
    for (int s = 0; s < 8; s++) {
      *(LAS f32x2*)(size_t)(scr + (s * 192 + c) * 8) = (f32x2){cr, ci};
      const float nr = pw[0] * cr - pw[1] * ci + es[s].x, ni = pw[0] * ci + pw[1] * cr + es[s].y; cr = nr; ci = ni;
    }
  }
}

#define LBAR() do { BAR(); const u32 t_ = (u32)__builtin_amdgcn_s_memtime(), r_ = (u32)__builtin_amdgcn_s_memrealtime(); tsv = lane == tsn ? t_ : tsv; tsr = lane == tsn ? r_ : tsr; tsn++; } while (0)
DI void loader(const Params& p0, int rt, int g, int j, int u, int lane, u32 lds0, u32 tag) {
  u32 tsv = 0, tsr = 0; int tsn = 0;
  Params p = p0;
  const bool ssm = g == 4;
  const u32 wbytes = ssm ? D * ROWB : D * F * 2;
  const rsrc_t rX = mk(p.x, S * ROWB), rA0 = mk(ssm ? p.bre : p.w1, wbytes), rA1 = mk(ssm ? p.bim : p.w1, wbytes);
  const int r8 = lane >> 3, gp = lane & 7;
  const int cgr = ((((gp >> 1) ^ ((r8 >> 1) & 3)) << 1) | (gp & 1));
  const int rgr = gp ^ (4 * (u & 1) + (r8 >> 1));
  const int slice = 3 * g + j;
  const int kA = 16 * (u & 1) + 8 * (r8 >> 2) + 4 * (u >> 1) + (r8 & 3);
  const int R = 8 * u + r8;
  const u32 voffA = ssm ? (192 * j + R) * ROWB + rgr * 16 : kA * (F * 2) + slice * 768 + cgr * 16;
  const int tok = ssm ? 16 * ((R >> 2) & 3) + 8 * (R & 1) + 2 * (R >> 4) + ((R >> 1) & 1) : R;
  const u32 voffX = (rt * 128 + tok) * ROWB + rgr * 16;
  const u32 ldsW = lds0 + u * 1024, ldsT = lds0 + TOFF;
  const u32 aKt = ssm ? 128 : 64 * F * 2, aPar = ssm ? 0 : 32 * F * 2, pstr = ssm ? 32 * ROWB : 128, xstr = (ssm ? 4 : 32) * ROWB;
  const int rot = rt >= 9 ? rt - 9 : rt;
  auto issueA = [&](int kap, int h, auto&& fill) {
    int kt = kap + rot; if (kt >= 9) kt -= 9;
    const u32 l = ldsW + ((2 * kap + h) & 3) * WSLOT, so = kt * aKt + h * aPar;
#pragma unroll
    for (int i = 0; i < 6; i++) { dma(h ? rA1 : rA0, l + i * 4096, voffA, so + i * pstr); SB(); fill(i); SB(); }
  };
  auto nofill = [](int) {};
  auto issueX = [&](int kap, int h) {
    int kt = kap + rot; if (kt >= 9) kt -= 9;
    const u32 l = ldsT + (kap & 3) * TSLOT + h * 8192 + u * 1024, so = h * 64 * ROWB + kt * 128;
    dma(rX, l, voffX, so); dma(rX, l + 4096, voffX, so + xstr);
  };
  char* const hsR = p.ws + WS_RSP + blockIdx.x * 4;
  issueA(0, 0, nofill); issueX(0, 0); issueX(0, 1);
  u32 pv = poll_issue(hsR);
  issueA(0, 1, nofill);
  __builtin_amdgcn_s_setprio(3);
  waitv<7>(); LBAR();
  issueX(1, 0); issueX(1, 1); issueA(1, 0, nofill);
  const u32 voffL = lane * 16;
  waitv<10>(); asm volatile("" : "+v"(pv));
  const u32 hsn = __builtin_amdgcn_readfirstlane(pv) + 1u;
  char* const dummy = p.ws + WS_DUMMY + (blockIdx.x * 4 + u) * 4;
  st_word(u == 0 ? hsR - 1024 : dummy, hsn);
  waitv<9>();
  LBAR(); issueA(1, 1, nofill); issueX(2, 0); waitv<9>();
  LBAR(); issueA(2, 0, nofill); issueX(2, 1); waitv<8>();
  bool acked = false;
  st_word(dummy, 0u);
#pragma nounroll
  for (int kap = 1; kap < 5; kap++) {
    LBAR(); issueA(kap + 1, 1, nofill); issueX(kap + 2, 0); u32 hv = poll_issue(hsR); waitv<10>();
    LBAR(); issueA(kap + 2, 0, nofill); issueX(kap + 2, 1); waitv<8>();
    asm volatile("" : "+v"(hv));
    bool now = !acked && __all(hv == hsn);
    if (!acked && !now && kap == 4 && u == 0) { spin_all(hsR, hsn); now = true; }
    st_word(now && u == 0 ? hsR + 1024 : dummy, 1u);
    acked = acked || now;
  }
  const char* fGo = p.ws + WS_FLAGS + FL_GO * 128;
  const char* fAux = p.ws + WS_FLAGS + (FL_C + slice) * 128;
  LBAR(); issueA(6, 1, nofill); issueX(7, 0); waitv<9>();
  LBAR(); issueA(7, 0, nofill); issueX(7, 1); waitv<8>();
  LBAR(); issueA(7, 1, nofill); issueX(8, 0); pv = poll_issue(fGo); waitv<9>();
  LBAR(); issueA(8, 0, nofill); issueX(8, 1); waitv<8>();
  const int ir = 32 * u + (lane & 31), irt = ssm ? (ir & 64) + 16 * ((ir >> 2) & 3) + 8 * (ir & 1) + 2 * ((ir >> 4) & 3) + ((ir >> 1) & 1) : ir;
  const char* stA = p.ws + WS_ST + (rt * 128 + irt) * 8;
  u32x2 sg;
  asm volatile("" : "+v"(pv)); LBAR();
  issueA(8, 1, nofill);
  if (!__all(pv == 1u)) spin_all(fGo, 1u);
  asm volatile("global_load_dwordx2 %0, %1, off sc1" : "=v"(sg) : "v"(stA) : "memory");
  pv = poll_issue(fAux);
  waitv<8>();
  LBAR();
  waitv<0>(); asm volatile("" : "+v"(sg), "+v"(pv));
  {
    const u32 ax = lds0 + (18 & 3) * WSLOT;
    const float mean = __uint_as_float(sg[0]), ve = __uint_as_float(sg[1]);
    const float rstd = __builtin_amdgcn_rsqf(ve), sig = ve * rstd;
    float m1, m2, m3; split3(-mean, m1, m2, m3);
    const float g1 = rbf(sig), g2 = sig - g1;
    if (lane < 32) wr128(ax + AX_T + ir * 16, 0, (u32x4){cvtpk(m1, m1), cvtpk(m2, m2), cvtpk(m1, m3), cvtpk(g1, g2)});
    else { LAS float* sp = (LAS float*)(size_t)(ax + AX_S + ir * 4); sp[0] = mean; sp[128] = rstd; sp[256] = sig; }
    WAITL();
  }
  p = reload_params();
  const rsrc_t rWS = mk(p.ws, WS_END), rB0 = mk(ssm ? p.cre : p.w2, wbytes), rB1 = mk(ssm ? p.cim : p.w2, wbytes);
  const u32 voffB = ssm ? voffA : R * ROWB + j * 384 + cgr * 16;
  const u32 bJ = ssm ? 384 : 384 * ROWB, bPar = ssm ? 0 : 192 * ROWB, bI = ssm ? 128 : 32 * ROWB, b0 = ssm ? 0 : g * 1152 * ROWB;
  const int j1 = j == 2 ? 0 : j + 1, j2 = j == 0 ? 2 : j - 1;
  auto issueB = [&](int s, int jj, int i) {
    const u32 l = ldsW + (s & 3) * WSLOT, so = b0 + jj * bJ + (i & 1) * bPar + (i >> 1) * bI;
    const rsrc_t r = (i & 1) ? rB1 : rB0;
#pragma unroll
    for (int k = 0; k < 6; k++) dma(r, l + k * 4096, voffB, so + (k / 3) * 96 * ROWB + (k % 3) * pstr);
  };
  auto issueP = [&](int s, int jj, int i) {
    const u32 sp = WS_P + (rt * 15 + 3 * g + jj) * 98304 + i * 16384 + u * 4096, lp = ldsT + (s & 3) * TSLOT + u * 4096;
#pragma unroll
    for (int k = 0; k < 4; k++) dma_sc1(rWS, lp + k * 1024, voffL, sp + k * 1024);
  };
  LBAR();
  if (!__all(pv == tag)) spin_all(fAux, tag);
#pragma unroll
  for (int k = 0; k < 3; k++) {
    int pc = u * 3 + k; if (!ssm && pc >= 6) pc -= 6;
    dma_sc1(rWS, lds0 + (18 & 3) * WSLOT + (pc < 6 ? AX_W + pc * 1024 : AX_TAB + (pc - 6) * 1024), voffL, pc < 6 ? WS_AUXW + slice * 6144 + pc * 1024 : WS_TAB + j * 6144 + (pc - 6) * 1024);
  }
  issueB(19, j, 0);
  waitv<9>(); LBAR(); issueB(20, j, 1);
  waitv<6>(); LBAR(); issueB(21, j, 2);
  waitv<6>();
  if (ssm) { LBAR(); if (u < 3) ssm_carry(p, rt, j, u * 64 + lane, lds0, tag); WAITL(); LBAR(); BAR(); }
  const char* fP = p.ws + WS_FLAGS + (FL_P + rt * 15 + 3 * g + ((lane & 1) ? j1 : j2)) * 128;
  LBAR(); issueB(22, j, 3); waitv<6>();
  LBAR(); issueB(23, j, 4); issueP(23, j, 4); pv = poll_issue(fP); waitv<11>();
  LBAR(); issueB(24, j, 5); issueP(24, j, 5); waitv<10>();
  asm volatile("" : "+v"(pv));
  bool late = !__all(pv >= 1u);
#pragma nounroll
  for (int n = 0; n < 6; n++) {
    const int i = n < 2 ? n + 4 : n - 2;
    LBAR(); issueB(25 + 2 * n, j1, i); if (late) spin_ge(fP, 1u + (n >> 1)); issueP(25 + 2 * n, j1, i);
    u32 qv = poll_issue(fP); waitv<11>();
    LBAR(); issueB(26 + 2 * n, j2, i); issueP(26 + 2 * n, j2, i); waitv<10>();
    asm volatile("" : "+v"(qv)); late = n < 5 && !__all(qv >= 1u + ((n + 1) >> 1));
  }
  LBAR();
  {
    const u32 l = lds0 + WSLOT;
#pragma unroll
    for (int k = 0; k < 3; k++) { const int gi = (u * 3 + k) * 64 + lane, row = gi / 24;
      dma(rX, l + (u * 3 + k) * 1024, (rt * 128 + (row >> 4) * 64 + (g & 3) * 16 + (row & 15)) * ROWB + j * 384 + (gi - row * 24) * 16, 0); }
    dma(mk((u & 1) ? p.dskip : p.b2, ROWB), l + 12288 + (u & 1) * 1024, voffL, j * 384);
  }
  waitv<4>(); LBAR(); LBAR(); waitv<0>();
  LBAR();
  if (u == 0) { ((u32*)(p.stamps + 4096))[blockIdx.x * 64 + lane] = tsv; ((u32*)(p.stamps + 4096))[16384 + blockIdx.x * 64 + lane] = tsr; }
}

template <bool SSM>
DI void phaseB(const Params& p, int rt, int g, int j, int w, int lane, u32 lds0, u32 tag) {
  asm volatile("" : "+v"(lane));
  const int rg = w >> 2, cg = w & 3, q = lane >> 4, e = (lane >> 2) & 3, pp = lane & 3, jl = lane & 15;
  const int z = 2 * (q & 1) + (e >> 1);
  u32 wlo[3], whi[3], tb[2];
#pragma unroll
  for (int n = 0; n < 3; n++) {
    const int t = 3 * cg + n;
    wlo[n] = SSM ? lds0 + (16 * t + jl) * 128 + ((q ^ (jl >> 1)) * 16) : lds0 + (t >> 2) * 4096 + (4 * q + e) * 128 + (((t & 3) ^ z) * 32) + pp * 8;
    whi[n] = SSM ? (wlo[n] ^ 64) : wlo[n] + 2 * WSLOT;
  }
  tb[0] = SSM ? lds0 + TOFF + rg * 8192 + (q >> 1) * 2048 + (pp >> 1) * 1024 + (((8 * (q & 1) + e) * 16) ^ ((pp >> 1) * 64)) + 8 * (pp & 1) : lds0 + TOFF + rg * 4096 + lane * 16;
  tb[1] = tb[0] ^ 64;
  float mean = 0.f, rstd = 0.f;
  if constexpr (!SSM) { const u32 sa = lds0 + (18 & 3) * WSLOT + AX_S + (64 * rg + 16 * g + jl) * 4; mean = rdf(sa, 0); rstd = rdf(sa, 512); }
  f32x4 ob[3][4];
#pragma unroll
  for (int n = 0; n < 3; n++)
#pragma unroll
    for (int T = 0; T < 4; T++) { ob[n][T] = (f32x4){0.f, 0.f, 0.f, 0.f}; PIN(ob[n][T]); }
  auto RW = [&](int sl, int kb, int n, int part) {
    if constexpr (SSM) return rd128((kb ? whi[n] : wlo[n]) + (sl >> 1) * 2 * WSLOT, (sl & 1) * WSLOT);
    else return rd_tr1(sl < 2 ? wlo[n] : whi[n], (sl & 1) * WSLOT + kb * 12288 + part * 2048);
  };
  auto RT = [&](int sl, int kb, int T, int part) {
    if constexpr (SSM) return rd_tr1(tb[part], sl * TSLOT + kb * 4096 + T * 256);
    else return rd128(tb[0], sl * TSLOT + kb * 8192 + T * 1024);
  };
  bf16x8 wf[3], tp[2];
  auto step = [&](auto slc, auto&& mid) {
    constexpr int SL = decltype(slc)::value, NS = (SL + 1) & 3;
    half<SSM ? 1 : 0>(ob, wf, tp, [&](int n, int part) { return RW(SL, 1, n, part); }, [&](int T, int part) { return RT(SL, 0, T, part); }, [&](int T, int part) { return RT(SL, 1, T, part); }, mid);
    half<SSM ? 1 : 0>(ob, wf, tp, [&](int n, int part) { return RW(NS, 0, n, part); }, [&](int T, int part) { return RT(SL, 1, T, part); }, [&](int T, int part) { return RT(NS, 0, T, part); }, [](int) {});
  };
  using I0 = std::integral_constant<int, 0>; using I1 = std::integral_constant<int, 1>; using I2 = std::integral_constant<int, 2>; using I3 = std::integral_constant<int, 3>;
  if constexpr (!SSM) waitv<4>();
  WAITL(); BAR();
#pragma unroll
  for (int n = 0; n < 3; n++) { if constexpr (SSM) wf[n] = RW(3, 0, n, 0); else wf[n] = cat(RW(3, 0, n, 0), RW(3, 0, n, 1)); }
#pragma unroll
  for (int T = 0; T < 2; T++) { if constexpr (SSM) tp[T] = cat(RT(3, 0, T, 0), RT(3, 0, T, 1)); else tp[T] = RT(3, 0, T, 0); }
  auto barm = [](int i) { if (i == 3) BAR(); };
  auto level = [&](u32 v) { if (threadIdx.x == 448) st_word(p.ws + WS_FLAGS + (FL_P + rt * 15 + 3 * g + j) * 128, v); };
  if constexpr (!SSM) level(2u);
  step(I3{}, [](int) {});
  step(I0{}, [&](int i) { if (i == 3) { if constexpr (SSM) waitv<8>(); BAR(); if constexpr (SSM) level(1u); } });
#pragma nounroll
  for (int it = 0; it < 4; it++) { step(I1{}, barm); step(I2{}, [&](int i) { if (i == 3) { waitv<0>(); BAR(); if (it == 0) level(3u); } }); step(I3{}, barm); step(I0{}, barm); }
  MFENCE();
  const rsrc_t rWS = mk(p.ws, WS_END);
  const u32 xb = WS_XB + (rt * 3 + j) * 32 * XBW + w * XBW;
  f32x4 mine[3];
#pragma unroll
  for (int T = 0; T < 4; T++) {
    if (SSM || T != g) {
      const u32 o = xb + T * 8 * XBW + g * 1536;
      const u32x4 a = {cvtpk(ob[0][T][0], ob[0][T][1]), cvtpk(ob[0][T][2], ob[0][T][3]), cvtpk(ob[1][T][0], ob[1][T][1]), cvtpk(ob[1][T][2], ob[1][T][3])};
      const u32 b0 = cvtpk(ob[2][T][0], ob[2][T][1]), b1 = cvtpk(ob[2][T][2], ob[2][T][3]);
      const auto s0 = __builtin_amdgcn_permlane32_swap(b0, b0, false, false), s1 = __builtin_amdgcn_permlane32_swap(b1, b1, false, false);
      st16(a, rWS, lane * 16, o);
      if (lane < 32) st16((u32x4){b0, b1, s0[1], s1[1]}, rWS, lane * 16, o + 1024);
    } else {
#pragma unroll
      for (int n = 0; n < 3; n++) mine[n] = ob[n][T];
    }
  }
  if constexpr (SSM) {
    waitv<0>(); BAR();
    if (threadIdx.x == 0) st_flag(p.ws, FL_O + (rt * 3 + j) * 5 + g, tag);
    STAMP(7);
  } else {
    waitv<0>(); BAR();
    if (threadIdx.x == 0) st_flag(p.ws, FL_O + (rt * 3 + j) * 5 + g, tag);
    const int tok = rt * 128 + 64 * rg + 16 * g + jl, col = 192 * j + 48 * cg + 4 * q;
    u32x2 xv[3], bv[3], dv[3];
#pragma unroll
    for (int n = 0; n < 3; n++) {
      const u32 cb = lds0 + WSLOT + (48 * cg + 4 * q + 16 * n) * 2;
      xv[n] = *(LAS u32x2*)(size_t)(cb + (16 * rg + jl) * 384); bv[n] = *(LAS u32x2*)(size_t)(cb + 12288); dv[n] = *(LAS u32x2*)(size_t)(cb + 13312);
    }
    spin_all(p.ws + WS_FLAGS + (FL_O + (rt * 3 + j) * 5 + (lane % 5)) * 128, tag);
    const u32 o = xb + g * 8 * XBW;
    u32x4 a[4]; u32x2 b[4];
#pragma unroll
    for (int k = 0; k < 4; k++) {
      const int src = k == 3 ? 4 : (k < g ? k : k + 1);
      a[k] = __builtin_amdgcn_raw_buffer_load_b128(rWS, lane * 16, o + src * 1536, 16);
      b[k] = __builtin_amdgcn_raw_buffer_load_b64(rWS, (lane & 31) * 16 + (lane >> 5) * 8, o + src * 1536 + 1024, 16);
    }
#pragma unroll
    for (int n = 0; n < 3; n++) {
      f32x4 r;
#pragma unroll
      for (int c = 0; c < 4; c++) {
        float G = mine[n][c] + ((c & 1) ? bfhi(bv[n][c >> 1]) : bflo(bv[n][c >> 1]));
#pragma unroll
        for (int k = 0; k < 3; k++) { const u32 v = n < 2 ? a[k][2 * n + (c >> 1)] : b[k][c >> 1]; G += (c & 1) ? bfhi(v) : bflo(v); }
        const u32 yv = n < 2 ? a[3][2 * n + (c >> 1)] : b[3][c >> 1];
        const float yy = (c & 1) ? bfhi(yv) : bflo(yv);
        const float xx = (c & 1) ? bfhi(xv[n][c >> 1]) : bflo(xv[n][c >> 1]);
        const float dd = (c & 1) ? bfhi(dv[n][c >> 1]) : bflo(dv[n][c >> 1]);
        const float sv = yy + (xx - mean) * rstd * dd;
        const float sig = __builtin_amdgcn_rcpf(1.f + __builtin_amdgcn_exp2f(-1.4426950408889634f * G));
        r[c] = xx + sv * (G * sig);
      }
      *(GAS f32x4*)(p.out + tok * D + col + 16 * n) = r;
    }
    STAMP(7);
  }
}

DI void ffn_compute(const Params& p, int rt, int g, int j, int w, int lane, u32 lds0, u32 tag) {
  const int rg = w >> 2, cg = w & 3, q = lane >> 4, e = (lane >> 2) & 3, pp = lane & 3, jl = lane & 15;
  const int z = 2 * (q & 1) + (e >> 1);
  u32 wc[4];
#pragma unroll
  for (int n = 0; n < 4; n++) { const int t = 6 * cg + n; wc[n] = lds0 + (t >> 2) * 4096 + (4 * q + e) * 128 + (((t & 3) ^ z) * 32) + pp * 8; }
  const u32 xbase = lds0 + TOFF + (64 * rg + jl) * 128 + ((q ^ (jl >> 1)) * 16);
  f32x4 acc[6][4];
#pragma unroll
  for (int n = 0; n < 6; n++)
#pragma unroll
    for (int T = 0; T < 4; T++) { acc[n][T] = (f32x4){0.f, 0.f, 0.f, 0.f}; asm volatile("" : "+v"(acc[n][T])); }
  bf16x8 wp[2], t[4];
  BAR();
  wp[0] = rd_tr(wc[0], 0); wp[1] = rd_tr(wc[1], 0);
#pragma unroll
  for (int T = 0; T < 4; T++) t[T] = rd128(xbase, T * 2048);
  u32 xs = 0; int dw = 2 * WSLOT;
#pragma nounroll
  for (int it = 0; it < 9; it++) {
    stepA<0>(acc, wp, t, wc, 0, 0, (xbase ^ 64) + xs); xs = (xs + TSLOT) & (4 * TSLOT - 1);
    const u32 n0 = wc[0] + dw, n1 = wc[1] + dw;
    stepA<1>(acc, wp, t, wc, n0, n1, xbase + xs);
    waitv<0>();
    wc[0] = n0; wc[1] = n1; wc[2] += dw; wc[3] += dw; dw = -dw;
  }
  WAITL(); BAR();
  const u32 ax = lds0 + (18 & 3) * WSLOT;
  float k1[4], k2[4], sg[4];
#pragma unroll
  for (int T = 0; T < 4; T++) {
    u32x4 v = __builtin_bit_cast(u32x4, rd128(ax + AX_T + (64 * rg + jl) * 16, T * 256));
    if (q != 0) v = (u32x4){0u, 0u, 0u, 0u};
    t[T] = __builtin_bit_cast(bf16x8, v);
    const float r = rdf(ax + AX_S + 512 + (64 * rg + jl) * 4, T * 64);
    sg[T] = rdf(ax + AX_S + 1024 + (64 * rg + jl) * 4, T * 64);
    k1[T] = -2.3022081985f * r; k2[T] = -0.10294324f * r * r * r;
  }
  {
    bf16x8 a[6];
#pragma unroll
    for (int n = 0; n < 6; n++) a[n] = rd128(ax + AX_W + (96 * cg + jl) * 16, n * 256);
    asm volatile("s_nop 1" : "+v"(t[0]), "+v"(t[1]), "+v"(t[2]), "+v"(t[3]));
#pragma unroll
    for (int n = 0; n < 6; n++)
#pragma unroll
      for (int T = 0; T < 4; T++) MMA(acc[n][T], a[n], t[T]);
    MFENCE();
  }
  const rsrc_t rWS = mk(p.ws, WS_END);
  const u32 pg = WS_P + (rt * 15 + 3 * g + j) * 98304 + (cg >> 1) * 16384 + (cg & 1) * 8192 + rg * 4096;
  const u32 pl = lds0 + TOFF + (cg & 1) * 8192 + rg * 4096 + lane * 16, pl0 = pl + ((19 + (cg >> 1)) & 3) * TSLOT, pl1 = pl + ((21 + (cg >> 1)) & 3) * TSLOT;
#pragma unroll
  for (int bb = 0; bb < 3; bb++) {
    const int b = (bb + 2) % 3;
#pragma unroll
    for (int T = 0; T < 4; T++) {
      const u32x4 P = gelu_frag(acc[2 * b][T], acc[2 * b + 1][T], k1[T], k2[T], sg[T]);
      st16(P, rWS, lane * 16, pg + b * 32768 + T * 1024);
      if (b < 2) wr128(b ? pl1 : pl0, T * 1024, P);
      SB();
    }
  }
  phaseB<false>(p, rt, g, j, w, lane, lds0, tag);
}

DI void ssm_compute(const Params& p, int rt, int j, int w, int lane, u32 lds0, u32 tag) {
  const int rg = w >> 2, cg = w & 3; int q = lane >> 4, jl = lane & 15;
  u32 wb = lds0 + (16 * cg + jl) * 128 + ((q ^ (jl >> 1)) * 16);
  const u32 xb0 = lds0 + TOFF + (64 * rg + jl) * 128 + ((q ^ (jl >> 1)) * 16);
  f32x4 ar[3][4], ai[3][4];
#pragma unroll
  for (int n = 0; n < 3; n++)
#pragma unroll
    for (int T = 0; T < 4; T++) { ar[n][T] = (f32x4){0.f, 0.f, 0.f, 0.f}; ai[n][T] = (f32x4){0.f, 0.f, 0.f, 0.f}; asm volatile("" : "+v"(ar[n][T]), "+v"(ai[n][T])); }
  bf16x8 wf[3], tp[2];
  auto nop = [](int) {};
  auto barm = [](int i) { if (i == 3) BAR(); };
  BAR();
#pragma unroll
  for (int n = 0; n < 3; n++) wf[n] = rd128(wb, n * 8192);
  tp[0] = rd128(xb0, 0); tp[1] = rd128(xb0, 2048);
  u32 xs = 0; int dw = 2 * WSLOT;
#pragma nounroll
  for (int it = 0; it < 9; it++) {
    const u32 x0 = xb0 + xs, x1 = (xb0 ^ 64) + xs, wb1 = wb ^ 64, wn = wb + dw;
    xs = (xs + TSLOT) & (4 * TSLOT - 1);
    const u32 xn = xb0 + xs;
    half<2>(ar, wf, tp, [&](int n, int) { return rd128(wb1, n * 8192); }, [&](int T, int) { return rd128(x0, T * 2048); }, [&](int T, int) { return rd128(x1, T * 2048); }, barm);
    half<2>(ar, wf, tp, [&](int n, int) { return rd128(wb, WSLOT + n * 8192); }, [&](int T, int) { return rd128(x1, T * 2048); }, [&](int T, int) { return rd128(x0, T * 2048); }, nop);
    half<2>(ai, wf, tp, [&](int n, int) { return rd128(wb1, WSLOT + n * 8192); }, [&](int T, int) { return rd128(x0, T * 2048); }, [&](int T, int) { return rd128(x1, T * 2048); }, barm);
    half<2>(ai, wf, tp, [&](int n, int) { return rd128(wn, n * 8192); }, [&](int T, int) { return rd128(x1, T * 2048); }, [&](int T, int) { return rd128(xn, T * 2048); }, nop);
    wb = wn; dw = -dw;
    waitv<0>();
  }
  WAITL(); BAR();
  asm volatile("" : "+v"(lane));
  q = lane >> 4; jl = lane & 15;
  const u32 ax = lds0 + (18 & 3) * WSLOT;
  {
    bf16x8 t[4];
#pragma unroll
    for (int T = 0; T < 4; T++) {
      u32x4 v = __builtin_bit_cast(u32x4, rd128(ax + AX_T + (64 * rg + jl) * 16, T * 256));
      if (q != 0) v = (u32x4){0u, 0u, 0u, 0u};
      t[T] = __builtin_bit_cast(bf16x8, v);
    }
    bf16x8 a[6];
#pragma unroll
    for (int b = 0; b < 6; b++) a[b] = rd128(ax + AX_W + (16 * cg + jl) * 16, b * 1024);
    asm volatile("s_nop 1" : "+v"(t[0]), "+v"(t[1]), "+v"(t[2]), "+v"(t[3]));
#pragma unroll
    for (int b = 0; b < 3; b++)
#pragma unroll
      for (int T = 0; T < 4; T++) { MMA(ar[b][T], t[T], a[b]); MMA(ai[b][T], t[T], a[3 + b]); }
    MFENCE();
  }
  f32x4 lc[3];
  const u32 scr = lds0 + TOFF + (22 & 3) * TSLOT + ((4 * rg + q) * 192 + 16 * cg + jl) * 8;
  f32x2 e0[3];
#pragma unroll
  for (int T = 0; T < 4; T++) {
    const f32x4 r = rdf4(ax + AX_S + 512 + (64 * rg + 4 * q) * 4, T * 64);
#pragma unroll
    for (int b = 0; b < 3; b++) { ar[b][T] *= r; ai[b][T] *= r; }
  }
  f32x2 l8[3];
#pragma unroll
  for (int b = 0; b < 3; b++) {
    lc[b] = rdf4(ax + AX_TAB + (16 * cg + jl) * 32, b * 2048);
    l8[b] = *(LAS f32x2*)(size_t)(ax + AX_TAB + (16 * cg + jl) * 32 + b * 2048 + 16);
    f32x2 hr = {ar[b][0][0], ar[b][0][1]}, hi = {ai[b][0][0], ai[b][0][1]};
#pragma unroll
    for (int k = 1; k < 8; k++) {
      const f32x2 ur = {ar[b][k >> 1][(k & 1) * 2], ar[b][k >> 1][(k & 1) * 2 + 1]}, ui = {ai[b][k >> 1][(k & 1) * 2], ai[b][k >> 1][(k & 1) * 2 + 1]};
      const f32x2 nr = lc[b][0] * hr + (ur - lc[b][1] * hi), ni = lc[b][0] * hi + (ui + lc[b][1] * hr);
      hr = nr; hi = ni;
    }
    e0[b] = (f32x2){hr.x, hi.x};
    *(LAS f32x2*)(size_t)(scr + b * 512) = (f32x2){l8[b].x * hr.x - l8[b].y * hi.x + hr.y, l8[b].x * hi.x + l8[b].y * hr.x + hi.y};
  }
  WAITL(); BAR();
#pragma unroll
  for (int b = 0; b < 3; b++) {
    const float c2r = 2.f * lc[b][2], c2i = 2.f * lc[b][3];
#pragma unroll
    for (int T = 0; T < 4; T++) {
      const f32x4 nr = c2r * ar[b][T] - c2i * ai[b][T], ni = -c2r * ai[b][T] - c2i * ar[b][T];
      ar[b][T] = nr; ai[b][T] = ni;
      asm volatile("" : "+v"(ar[b][T]), "+v"(ai[b][T]));
    }
  }
  BAR();
  const rsrc_t rWS = mk(p.ws, WS_END);
  const u32 io0 = w * 2048 + lane * 16, io1 = w * 2048 + 1024 + ((lane * 16) ^ 64);
  const u32 pg = WS_P + (rt * 15 + 12 + j) * 98304;
  f32x2 cins[3];
#pragma unroll
  for (int b = 0; b < 3; b++) cins[b] = *(LAS f32x2*)(size_t)(scr + b * 512);
#pragma unroll
  for (int bb = 0; bb < 3; bb++) {
    const int b = (bb + 2) % 3;
    u32x4 H[2][2];
    const f32x2 cin = cins[b];
    const f32x2 sr = {cin.x, l8[b].x * cin.x - l8[b].y * cin.y + e0[b].x}, si = {cin.y, l8[b].x * cin.y + l8[b].y * cin.x + e0[b].y};
    const float c2r = 2.f * lc[b][2], c2i = 2.f * lc[b][3];
    f32x2 hr = c2r * sr - c2i * si, hi = -c2r * si - c2i * sr;
#pragma unroll
    for (int T = 0; T < 4; T++) {
      f32x2 pr[2], pi[2];
#pragma unroll
      for (int k = 0; k < 2; k++) {
        const f32x2 ur = {ar[b][T][2 * k], ar[b][T][2 * k + 1]}, ui = {ai[b][T][2 * k], ai[b][T][2 * k + 1]};
        const f32x2 nr = lc[b][0] * hr + (ur + lc[b][1] * hi), ni = lc[b][0] * hi + (ui - lc[b][1] * hr);
        hr = nr; hi = ni; pr[k] = nr; pi[k] = ni;
      }
      H[0][0][T] = cvtpk(pr[0].x, pr[1].x); H[0][1][T] = cvtpk(pr[0].y, pr[1].y);
      H[1][0][T] = cvtpk(pi[0].x, pi[1].x); H[1][1][T] = cvtpk(pi[0].y, pi[1].y);
    }
    if (bb == 1) BAR();
#pragma unroll
    for (int k = 0; k < 4; k++) {
      st16(H[k >> 1][k & 1], rWS, (k & 1) ? io1 : io0, pg + (2 * b + (k >> 1)) * 16384);
      if (b < 2) wr128(lds0 + TOFF + ((k & 1) ? io1 : io0), ((19 + 2 * b + (k >> 1)) & 3) * TSLOT, H[k >> 1][k & 1]);
    }
    SB();
  }
  phaseB<true>(p, rt, 4, j, w, lane, lds0, tag);
}

DI void spare(const Params& p, int sid, u32 lds0, u32 tag, u64 pc0) {
  const int tid = threadIdx.x;
  u32 cpf = 0;
  if (tid < CODE_LINES) cpf = *(const GAS u32*)(pc0 + tid * 128);
  const rsrc_t rWS = mk(p.ws, WS_END);
  const int w = __builtin_amdgcn_readfirstlane(tid >> 6), lane = tid & 63;
  u32 hsn = 0;
  if (sid == 15) {
    if (w == 11) { waitv<0>(); hs_leader(p.ws, lane, lds0, STAMPPTR); return; }
    if (w < 8) {
      u32x4 v[2][9];
#pragma unroll
      for (int h = 0; h < 2; h++)
#pragma unroll
        for (int i = 0; i < 9; i++) v[h][i] = ((const GAS u32x4*)(p.x + ((2 * w + h) * 128 + 120 + (lane >> 3)) * D) + (lane & 7))[8 * i];
#pragma unroll
      for (int h = 0; h < 2; h++) {
        float s = 0.f;
#pragma unroll
        for (int i = 0; i < 9; i++)
#pragma unroll
          for (int c = 0; c < 4; c++) s += bflo(v[h][i][c]) + bfhi(v[h][i][c]);
        s += dppx<0xb1>(s); s += dppx<0x4e>(s); s += dppx<0x141>(s);
        const float mean = s * (1.f / D); s = 0.f;
#pragma unroll
        for (int i = 0; i < 9; i++)
#pragma unroll
          for (int c = 0; c < 4; c++) { const float a = bflo(v[h][i][c]) - mean, b = bfhi(v[h][i][c]) - mean; s += a * a + b * b; }
        s += dppx<0xb1>(s); s += dppx<0x4e>(s); s += dppx<0x141>(s);
        if ((lane & 7) == 0) __hip_atomic_store((GAS u64*)(p.ws + WS_ST + ((2 * w + h) * 128 + 120 + (lane >> 3)) * 8), ((u64)__float_as_uint(s * (1.f / D) + 1e-5f) << 32) | __float_as_uint(mean), __ATOMIC_RELAXED, __HIP_MEMORY_SCOPE_AGENT);
      }
    }
    waitv<0>(); BAR();
  } else if (w == 11) {
    hsn = hs_request(p.ws); spin_all(p.ws + WS_RSP + blockIdx.x * 4, hsn); st_word(p.ws + WS_ACK + blockIdx.x * 4, 1u);
  } else if (sid < 12) {
    if (w < 9) {
      const int cgp = tid % 48, rgp = tid / 48;
      const GAS u32x4* src = (const GAS u32x4*)(p.w1 + 2 * rgp * F + sid * 384) + cgp;
      float a[8];
#pragma unroll
      for (int c = 0; c < 8; c++) a[c] = 0.f;
      u32x4 v[48];
#pragma unroll
      for (int i = 0; i < 24; i++) v[i] = src[((i >> 1) * 24 + (i & 1)) * (F / 8)];
#pragma unroll
      for (int i = 0; i < 24; i++) {
#pragma unroll
        for (int c = 0; c < 4; c++) {
          a[2 * c] = dot2(__builtin_amdgcn_perm(v[2 * i + 1][c], v[2 * i][c], 0x05040100), ONES, a[2 * c]);
          a[2 * c + 1] = dot2(__builtin_amdgcn_perm(v[2 * i + 1][c], v[2 * i][c], 0x07060302), ONES, a[2 * c + 1]);
        }
        SB();
        if (i < 12) { v[24 + 2 * i] = src[((12 + i) * 24) * (F / 8)]; v[25 + 2 * i] = src[((12 + i) * 24 + 1) * (F / 8)]; }
        SB();
      }
      *(LAS f32x4*)(size_t)(lds0 + (rgp * 384 + cgp * 8) * 4) = (f32x4){a[0], a[1], a[2], a[3]};
      *(LAS f32x4*)(size_t)(lds0 + (rgp * 384 + cgp * 8) * 4 + 16) = (f32x4){a[4], a[5], a[6], a[7]};
    }
  } else if (sid < 15) {
    if (w < 8) {
#pragma unroll
      for (int h = 0; h < 2; h++) {
        const GAS u32x4* src = (const GAS u32x4*)((h ? p.bim : p.bre) + ((sid - 12) * 192 + (tid >> 3)) * D) + (tid & 7);
        u32x4 v[27];
#pragma unroll
        for (int i = 0; i < 27; i++) v[i] = src[(i / 9) * 64 * (D / 8) + (i % 9) * 8];
#pragma unroll
        for (int k = 0; k < 3; k++) {
          float s = 0.f, s2 = 0.f;
#pragma unroll
          for (int i = 9 * k; i < 9 * k + 9; i++) { s = dot2(v[i][0], ONES, s); s2 = dot2(v[i][1], ONES, s2); s = dot2(v[i][2], ONES, s); s2 = dot2(v[i][3], ONES, s2); }
          s += s2; s += dppx<0xb1>(s); s += dppx<0x4e>(s); s += dppx<0x141>(s);
          float c1, c2, c3; split3(s, c1, c2, c3);
          *(LAS u32x4*)(size_t)(lds0 + (h * 192 + k * 64 + (tid >> 3)) * 16) = (u32x4){cvtpk(c1, c2), cvtpk(c1, c2), cvtpk(c3, c1), 0u};
        }
      }
    } else {
      const int cl = tid - 512, c = (sid - 12) * 192 + cl;
      const float lr = bf2f(p.lre[c]), li = bf2f(p.lim[c]);
      const float dt = rbf(expf(bf2f(p.logdt[c])));
      const float ex = expf(dt * lr), an = dt * li;
      const float br = ex * cosf(an), bi = ex * sinf(an);
      const float nr = br - 1.f, den = 1.f / (lr * lr + li * li);
      const float cr = (nr * lr + bi * li) * den, ci = (bi * lr - nr * li) * den;
      float ar = br, ai = bi, r8 = 0.f, i8 = 0.f;
#pragma unroll
      for (int k = 0; k < 7; k++) { const float t = ar * ar - ai * ai; ai = 2.f * ar * ai; ar = t; if (k == 2) { r8 = ar; i8 = ai; } }
      *(LAS f32x4*)(size_t)(lds0 + 6144 + cl * 32) = (f32x4){br, bi, cr, ci};
      *(LAS f32x4*)(size_t)(lds0 + 6144 + cl * 32 + 16) = (f32x4){r8, i8, ar, ai};
    }
  }
  if (sid < 15) {
    WAITL(); BAR();
    if (sid < 12) {
      if (tid < 384) {
        float s = 0.f;
#pragma unroll
        for (int r = 0; r < 12; r++) s += rdf(lds0 + tid * 4, r * 1536);
        float c1, c2, c3; split3(s, c1, c2, c3);
        const float b = bf2f(p.b1[sid * 384 + tid]);
        st16((u32x4){cvtpk(c1, c2), cvtpk(c1, c2), cvtpk(c3, c1), cvtpk(b, b)}, rWS, tid * 16, WS_AUXW + sid * 6144);
      }
    } else {
      const u32x4 v = *(LAS u32x4*)(size_t)(lds0 + tid * 16);
      st16(v, rWS, tid < 384 ? WS_AUXW + sid * 6144 + tid * 16 : WS_TAB + (sid - 12) * 6144 + (tid - 384) * 16, 0);
    }
    waitv<0>(); BAR();
    if (tid == 0) st_flag(p.ws, FL_C + sid, tag);
  }
  STAMP(1);
  {
    u32 acc = cpf;
    const GAS u32* w2 = (const GAS u32*)p.w2;
    if (tid < 648) {
#pragma unroll
      for (int i = 0; i < 4; i++) {
        const int L = (i * 16 + sid) * 648 + tid;
        const int r = L / 9, kb = r / 1536, rr = r - kb * 1536;
        acc ^= w2[((rr >> 5) * 96 + kb * 32 + (rr & 31)) * (ROWB / 4) + (L - r * 9) * 32];
      }
      if (tid < 324) acc ^= ((const GAS u32*)p.cre)[(sid * 324 + tid) * 32]; else acc ^= ((const GAS u32*)p.cim)[(sid * 324 + tid - 324) * 32];
    }
    asm volatile("" :: "v"(acc));
  }
}

__global__ __launch_bounds__(768, 3) void fused_kernel(Params p) {
  extern __shared__ __attribute__((aligned(16))) char smem[];
  const u32 lds0 = (u32)(size_t)(LAS char*)smem;
  const int b = blockIdx.x, xcd = b & 7, idx = b >> 3;
  const u32 tag = 1u;
  u64 pc0; asm volatile("s_getpc_b64 %0" : "=s"(pc0));
  STAMP(0);
  const int tri = idx / 3, j = idx - 3 * tri;
  const int g = tri < 2 ? 4 : xcd >> 1, rt = 8 * (xcd & 1) + (tri < 2 ? (xcd & 6) + tri : tri - 2);
  const int lane = threadIdx.x & 63, w = __builtin_amdgcn_readfirstlane(threadIdx.x >> 6);
  const int sid = xcd * 2 + idx - 30;
  const int srow = rt * 128 + (3 * g + j) * 8 + w;
  int isld = idx < 30 ? w >> 3 : 0;
  if (!isld) {
    if (idx < 30) {
      if (w == 0 && lane < 2) st_flag(p.ws, lane ? FL_P + rt * 15 + 3 * g + j : FL_O + (rt * 3 + j) * 5 + g, 0u);
      if (g == 4 && w >= 1 && w < 4) st16((u32x4){0u, 0u, 0u, 0u}, mk(p.ws, WS_END), (192 * j + threadIdx.x - 64) * 16, WS_EG + rt * 576 * 16);
    } else if (w == 11 && sid == 15) {
      if (lane < 16) st_flag(p.ws, lane == 15 ? FL_GO : FL_C + lane, 0u);
      st16((u32x4){0u, 0u, 0u, 0u}, mk(p.ws, WS_END), lane * 16, WS_ACK);
    }
    if (idx < 30) {
      float mean, ve; row_stats(p.x + srow * D, lane, mean, ve);
      if (lane == 0) __hip_atomic_store((GAS u64*)(p.ws + WS_ST + srow * 8), ((u64)__float_as_uint(ve) << 32) | __float_as_uint(mean), __ATOMIC_RELAXED, __HIP_MEMORY_SCOPE_AGENT);
    }
  }
  asm volatile("" : "+s"(isld));
  if (__builtin_expect(isld, 1)) { loader(p, rt, g, j, w - 8, lane, lds0, tag); return; }
  if (idx >= 30) { const Params q = reload_params(); spare(q, sid, lds0, tag, pc0); STAMP(7); return; }
  if (g < 4) { const Params q = reload_params(); ffn_compute(q, rt, g, j, w, lane, lds0, tag); }
  else { const Params q = reload_params(); ssm_compute(q, rt, j, w, lane, lds0, tag); }
}

__device__ u32 med_diff(const u64* st, int kind, int a, int b) {
  __shared__ u32 v[256]; __shared__ u32 res; __shared__ u64 t0s[256];
  const int t = threadIdx.x; const int idx = t >> 3, xcd = t & 7;
  const int k = idx >= 30 ? 2 : (idx < 6 ? 1 : 0);
  t0s[t] = st[t * 16]; __syncthreads();
  u64 t0 = ~0ull; for (int i = 0; i < 256; i++) t0 = t0s[i] < t0 ? t0s[i] : t0;
  const bool mx = b >= 100; if (mx) b -= 100;
  const u32* tr = (const u32*)(st + 4096) + 16384 + t * 64;
  auto get = [&](int x) { return x == -1 ? (u32)t0 : x < 64 ? tr[x] : (u32)st[t * 16 + x - 64]; };
  v[t] = k == kind ? get(b) - get(a) : 0xffffffffu; __syncthreads();
  int n = 0, less = 0; for (int i = 0; i < 256; i++) { n += v[i] != 0xffffffffu; less += (v[i] < v[t]) || (v[i] == v[t] && i < t); }
  if (k == kind && less == (mx ? n - 1 : n / 2)) res = v[t];
  __syncthreads(); u32 r = res; __syncthreads(); return r;
}
__global__ void amp_k(const u64* st) {
  u32 v0 = med_diff(st, K0, A0, B0), v1 = med_diff(st, K1, A1, B1);
  v0 = (v0 + 12) / 25; v1 = (v1 + 12) / 25; if (v0 > 127) v0 = 127; if (v1 > 110) v1 = 110;
  if (threadIdx.x == 0) { u64 t0 = __builtin_amdgcn_s_memrealtime(); u64 d = (u64)(v0 + 128 * v1) * 100; while (__builtin_amdgcn_s_memrealtime() - t0 < d) __builtin_amdgcn_s_sleep(4); }
}

extern "C" void kernel_launch(void* const* d_in, const int* in_sizes, int n_in, void* d_out, int out_size, void* d_ws, size_t ws_size, hipStream_t stream) {
  Params p;
  p.x = (const u16*)d_in[0]; p.lre = (const u16*)d_in[1]; p.lim = (const u16*)d_in[2]; p.bre = (const u16*)d_in[3]; p.bim = (const u16*)d_in[4];
  p.cre = (const u16*)d_in[5]; p.cim = (const u16*)d_in[6]; p.logdt = (const u16*)d_in[7]; p.dskip = (const u16*)d_in[8];
  p.w1 = (const u16*)d_in[10]; p.b1 = (const u16*)d_in[11]; p.w2 = (const u16*)d_in[12]; p.b2 = (const u16*)d_in[13];
  p.out = (float*)d_out; p.ws = (char*)d_ws;
  p.stamps = (u64*)((char*)d_ws + (200u << 20));
  (void)hipFuncSetAttribute((const void*)fused_kernel, hipFuncAttributeMaxDynamicSharedMemorySize, LDS_BYTES);
  fused_kernel<<<256, 768, LDS_BYTES, stream>>>(p);
  amp_k<<<1, 256, 0, stream>>>(p.stamps);
}
```

```cpp
#include <hip/hip_runtime.h>
#include <utility>

#define LAS __attribute__((address_space(3)))
#define GAS __attribute__((address_space(1)))
#define DI __device__ __forceinline__
typedef unsigned short u16;
typedef unsigned u32;
typedef unsigned long long u64;
typedef unsigned u32x4 __attribute__((ext_vector_type(4)));
typedef unsigned u32x2 __attribute__((ext_vector_type(2)));
typedef short s16x4 __attribute__((ext_vector_type(4)));
typedef short bf16x8 __attribute__((ext_vector_type(8)));
typedef float f32x4 __attribute__((ext_vector_type(4)));
typedef float f32x2 __attribute__((ext_vector_type(2)));
typedef __bf16 bf16x2 __attribute__((ext_vector_type(2)));
typedef __amdgpu_buffer_rsrc_t rsrc_t;

constexpr int S = 2048, D = 576, F = 4608;
constexpr int ROWB = D * 2;
constexpr int WSLOT = 24576, TOFF = 4 * WSLOT, TSLOT = 16384, LDS_BYTES = 163840;
constexpr int AX_W = 0, AX_T = 6144, AX_S = 8192, AX_TAB = 10240;
constexpr u32 WS_FLAGS = 0;
constexpr u32 FL_GO = 0, FL_P = 32, FL_O = 32 + 240, NFLAG = 32 + 480;
constexpr u32 WS_EG = NFLAG * 128;
constexpr u32 WS_ST = WS_EG + 16 * 576 * 16;
constexpr u32 WS_REQ = WS_ST + 3 * 12288, WS_RSP = WS_REQ + 1024, WS_ACK = WS_RSP + 1024;
constexpr u32 WS_CS = 256 * 1024;
constexpr u32 WS_TAB = WS_CS + 15 * 16 * 1536;
constexpr u32 WS_P = 1u << 20;
constexpr u32 XBW = 5 * 1536;
constexpr u32 WS_XB = WS_P + 240 * 98304;
constexpr u32 WS_END = WS_XB + 48 * 32 * XBW;
constexpr int CODE_LINES = 430;
constexpr u32 SPIN_CAP = 1u << 18;

struct Params {
  const u16 *x, *lre, *lim, *bre, *bim, *cre, *cim, *logdt, *dskip, *w1, *b1, *w2, *b2;
  float* out; char* ws;
};

DI Params reload_params() {
  Params q;
#if defined(__HIP_DEVICE_COMPILE__)
  typedef __attribute__((address_space(4))) const Params* KP;
  KP kp = (KP)__builtin_amdgcn_kernarg_segment_ptr(); asm volatile("" : "+s"(kp)); q = *kp;
#endif
  return q;
}
template <int N> DI void waitv() { asm volatile("s_waitcnt vmcnt(%0)" :: "n"(N) : "memory"); }
#define WAITL() asm volatile("s_waitcnt lgkmcnt(0)" ::: "memory")
#define BAR() do { asm volatile("" ::: "memory"); __builtin_amdgcn_s_barrier(); asm volatile("" ::: "memory"); } while (0)
#define SB() __builtin_amdgcn_sched_barrier(0)
#define MFENCE() do { SB(); asm volatile("s_nop 7\n\ts_nop 7"); SB(); } while (0)
#define MMA(c, a, b) asm volatile("v_mfma_f32_16x16x32_bf16 %0, %1, %2, %0" : "+v"(c) : "v"(a), "v"(b))
#define PIN(x) asm volatile("" : "+v"(x))
#define STAMPPTR nullptr
#define STAMP(i)

DI void dma(rsrc_t r, u32 lds, u32 voff, u32 soff) {
  asm volatile("s_mov_b32 m0, %0\n\ts_nop 0\n\tbuffer_load_dwordx4 %1, %2, %3 offen lds" :: "s"(lds), "v"(voff), "s"(r), "s"(soff) : "memory");
}
DI void dma_sc1(rsrc_t r, u32 lds, u32 voff, u32 soff) {
  asm volatile("s_mov_b32 m0, %0\n\ts_nop 0\n\tbuffer_load_dwordx4 %1, %2, %3 offen sc1 lds" :: "s"(lds), "v"(voff), "s"(r), "s"(soff) : "memory");
}
DI void dma12(rsrc_t r, u32 lds, u32 voff, u32 so, u32 d2, u32 st, u32 st3) {
  u32 t;
  asm volatile("s_mov_b32 m0, %2\n\t"
               "s_add_u32 %1, %0, %5\n\t"
               "buffer_load_dwordx4 %3, %4, %0 offen lds\n\t"
               "s_add_u32 m0, m0, 0x800\n\t"
               "s_add_u32 %0, %0, %6\n\t"
               "buffer_load_dwordx4 %3, %4, %1 offen lds\n\t"
               "s_add_u32 m0, m0, 0x800\n\t"
               "s_add_u32 %1, %1, %6\n\t"
               "buffer_load_dwordx4 %3, %4, %0 offen lds\n\t"
               "s_add_u32 m0, m0, 0x800\n\t"
               "s_add_u32 %0, %0, %6\n\t"
               "buffer_load_dwordx4 %3, %4, %1 offen lds\n\t"
               "s_add_u32 m0, m0, 0x800\n\t"
               "s_add_u32 %1, %1, %6\n\t"
               "buffer_load_dwordx4 %3, %4, %0 offen lds\n\t"
               "s_add_u32 m0, m0, 0x800\n\t"
               "s_add_u32 %0, %0, %7\n\t"
               "buffer_load_dwordx4 %3, %4, %1 offen lds\n\t"
               "s_add_u32 m0, m0, 0x800\n\t"
               "s_add_u32 %1, %1, %7\n\t"
               "buffer_load_dwordx4 %3, %4, %0 offen lds\n\t"
               "s_add_u32 m0, m0, 0x800\n\t"
               "s_add_u32 %0, %0, %6\n\t"
               "buffer_load_dwordx4 %3, %4, %1 offen lds\n\t"
               "s_add_u32 m0, m0, 0x800\n\t"
               "s_add_u32 %1, %1, %6\n\t"
               "buffer_load_dwordx4 %3, %4, %0 offen lds\n\t"
               "s_add_u32 m0, m0, 0x800\n\t"
               "s_add_u32 %0, %0, %6\n\t"
               "buffer_load_dwordx4 %3, %4, %1 offen lds\n\t"
               "s_add_u32 m0, m0, 0x800\n\t"
               "s_add_u32 %1, %1, %6\n\t"
               "buffer_load_dwordx4 %3, %4, %0 offen lds\n\t"
               "s_add_u32 m0, m0, 0x800\n\t"
               "s_add_u32 %0, %0, %6\n\t"
               "buffer_load_dwordx4 %3, %4, %1 offen lds"
               : "+&s"(so), "=&s"(t) : "s"(lds), "v"(voff), "s"(r), "s"(d2), "s"(st), "s"(st3) : "memory", "scc");
}
DI void dma4(rsrc_t r, u32 lds, u32 voff, u32 so, u32 st) {
  asm volatile("s_mov_b32 m0, %1\n\t"
               "s_nop 0\n\t"
               "buffer_load_dwordx4 %2, %3, %0 offen lds\n\t"
               "s_add_u32 m0, m0, 0x800\n\t"
               "s_add_u32 %0, %0, %4\n\t"
               "buffer_load_dwordx4 %2, %3, %0 offen lds\n\t"
               "s_add_u32 m0, m0, 0x800\n\t"
               "s_add_u32 %0, %0, %4\n\t"
               "buffer_load_dwordx4 %2, %3, %0 offen lds\n\t"
               "s_add_u32 m0, m0, 0x800\n\t"
               "s_add_u32 %0, %0, %4\n\t"
               "buffer_load_dwordx4 %2, %3, %0 offen lds"
               : "+&s"(so) : "s"(lds), "v"(voff), "s"(r), "s"(st) : "memory", "scc");
}
DI void dma8lin(rsrc_t r, u32 lds, u32 voff, u32 so) {
  asm volatile("s_mov_b32 m0, %1\n\t"
               "s_nop 0\n\t"
               "buffer_load_dwordx4 %2, %3, %0 offen offset:0 sc1 lds\n\t"
               "buffer_load_dwordx4 %2, %3, %0 offen offset:1024 sc1 lds\n\t"
               "buffer_load_dwordx4 %2, %3, %0 offen offset:2048 sc1 lds\n\t"
               "buffer_load_dwordx4 %2, %3, %0 offen offset:3072 sc1 lds\n\t"
               "s_add_u32 m0, m0, 0x2000\n\t"
               "s_add_u32 %0, %0, 0x2000\n\t"
               "buffer_load_dwordx4 %2, %3, %0 offen offset:0 sc1 lds\n\t"
               "buffer_load_dwordx4 %2, %3, %0 offen offset:1024 sc1 lds\n\t"
               "buffer_load_dwordx4 %2, %3, %0 offen offset:2048 sc1 lds\n\t"
               "buffer_load_dwordx4 %2, %3, %0 offen offset:3072 sc1 lds"
               : "+&s"(so) : "s"(lds), "v"(voff), "s"(r) : "memory", "scc");
}
DI rsrc_t mk(const void* ptr, u32 bytes) { return __builtin_amdgcn_make_buffer_rsrc((void*)ptr, 0, bytes, 0x00020000); }
DI s16x4 rd_tr1(u32 a, int off) { return __builtin_amdgcn_ds_read_tr16_b64_v4i16((LAS s16x4*)(size_t)(a + off)); }
DI bf16x8 cat(s16x4 lo, s16x4 hi) { return __builtin_shufflevector(lo, hi, 0, 1, 2, 3, 4, 5, 6, 7); }
DI bf16x8 rd_tr(u32 a, int off) { return cat(rd_tr1(a, off), rd_tr1(a, off + 2048)); }
DI bf16x8 rd128(u32 a, int off) { return *(LAS bf16x8*)(size_t)(a + off); }
DI f32x4 rdf4(u32 a, int off) { return *(LAS f32x4*)(size_t)(a + off); }
DI float rdf(u32 a, int off) { return *(LAS float*)(size_t)(a + off); }
DI void wr128(u32 a, int off, u32x4 v) { *(LAS u32x4*)(size_t)(a + off) = v; }
DI u32 cvtpk(float lo, float hi) { f32x2 v = {lo, hi}; bf16x2 b = __builtin_convertvector(v, bf16x2); return __builtin_bit_cast(u32, b); }
DI float bflo(u32 v) { return __uint_as_float(v << 16); }
DI float bfhi(u32 v) { return __uint_as_float(v & 0xffff0000u); }
DI float bf2f(u16 v) { return __uint_as_float((u32)v << 16); }
DI float dot2(u32 a, u32 b, float c) { return __builtin_amdgcn_fdot2_f32_bf16(__builtin_bit_cast(bf16x2, a), __builtin_bit_cast(bf16x2, b), c, false); }
constexpr u32 ONES = 0x3f803f80u;
DI float rbf(float v) { return bflo(cvtpk(v, 0.f)); }
DI void split3(float v, float& a, float& b, float& c) { a = rbf(v); const float r = v - a; b = rbf(r); c = rbf(r - b); }
template <int C> DI float dppx(float v) { return __uint_as_float(__builtin_amdgcn_update_dpp(0u, __float_as_uint(v), C, 0xf, 0xf, true)); }
template <int C> DI double dppd(double v) { u32x2 t = __builtin_bit_cast(u32x2, v); t[0] = __builtin_amdgcn_update_dpp(0u, t[0], C, 0xf, 0xf, true); t[1] = __builtin_amdgcn_update_dpp(0u, t[1], C, 0xf, 0xf, true); return __builtin_bit_cast(double, t); }
DI void st16(u32x4 v, rsrc_t r, u32 voff, u32 soff) { asm volatile("buffer_store_dwordx4 %0, %1, %2, %3 offen sc1\n\ts_nop 1" :: "v"(v), "v"(voff), "s"(r), "s"(soff) : "memory"); }
DI void st_flag(char* ws, u32 idx, u32 tag) { __hip_atomic_store((GAS u32*)(ws + WS_FLAGS + idx * 128), tag, __ATOMIC_RELAXED, __HIP_MEMORY_SCOPE_AGENT); }
DI u32 ld_flag(const char* a) { return __hip_atomic_load((GAS u32*)a, __ATOMIC_RELAXED, __HIP_MEMORY_SCOPE_AGENT); }
DI void spin_all(const char* a, u32 tag) {
#pragma nounroll
  for (u32 i = 0; i < SPIN_CAP; i++) { if (__all(ld_flag(a) == tag)) break; __builtin_amdgcn_s_sleep(1); }
  asm volatile("" ::: "memory");
}
DI void spin_ge(const char* a, u32 lvl) {
#pragma nounroll
  for (u32 i = 0; i < SPIN_CAP; i++) { if (__all(ld_flag(a) >= lvl)) break; __builtin_amdgcn_s_sleep(1); }
  asm volatile("" ::: "memory");
}
DI void st_word(char* a, u32 v) { __hip_atomic_store((GAS u32*)a, v, __ATOMIC_RELAXED, __HIP_MEMORY_SCOPE_AGENT); }
DI void hs_leader(char* ws, int lane, u32 lds0, u64* stamps) {
  const rsrc_t r = mk(ws, WS_END);
  bool first = true; u32x4 last = {0u, 0u, 0u, 0u};
#pragma nounroll
  for (u32 i = 0; i < SPIN_CAP; i++) {
    asm volatile("" ::: "memory");
    const u32x4 rq = __builtin_amdgcn_raw_buffer_load_b128(r, lane * 16, WS_REQ, 16), ak = __builtin_amdgcn_raw_buffer_load_b128(r, lane * 16, WS_ACK, 16);
    if (first || rq[0] != last[0] || rq[1] != last[1] || rq[2] != last[2] || rq[3] != last[3]) st16(rq, r, lane * 16, WS_RSP);
    first = false; last = rq;
    if (__all(ak[0] == 1u && ak[1] == 1u && ak[2] == 1u && (ak[3] == 1u || lane == 63))) break;
    __builtin_amdgcn_s_sleep(1);
  }
  BAR();
  st_flag(ws, FL_GO, 1u);
}
DI float wave_sum(float v) {
  v += dppx<0xb1>(v); v += dppx<0x4e>(v); v += dppx<0x141>(v); v += dppx<0x140>(v);
  const u32 b = __float_as_uint(v);
  return (__uint_as_float(__builtin_amdgcn_readlane(b, 0)) + __uint_as_float(__builtin_amdgcn_readlane(b, 16))) + (__uint_as_float(__builtin_amdgcn_readlane(b, 32)) + __uint_as_float(__builtin_amdgcn_readlane(b, 48)));
}
DI void row_stats(const u16* row, int lane, float& mean, float& ve) {
  const u32x4 a = *((const GAS u32x4*)row + lane), b = *((const GAS u32x4*)row + 64 + (lane & 7));
  float s = 0.f, t = 0.f;
#pragma unroll
  for (int c = 0; c < 4; c++) { s += bflo(a[c]) + bfhi(a[c]); t += bflo(b[c]) + bfhi(b[c]); }
  mean = wave_sum(lane < 8 ? s + t : s) * (1.f / D);
  s = 0.f; t = 0.f;
#pragma unroll
  for (int c = 0; c < 4; c++) {
    const float a0 = bflo(a[c]) - mean, a1 = bfhi(a[c]) - mean, b0 = bflo(b[c]) - mean, b1 = bfhi(b[c]) - mean;
    s += a0 * a0 + a1 * a1; t += b0 * b0 + b1 * b1;
  }
  ve = wave_sum(lane < 8 ? s + t : s) * (1.f / D) + 1e-5f;
}

DI f32x2 gelu2(f32x2 a, float k1, float k2, float sg) {
  f32x2 t = a * a; t = t * k2 + k1; t = t * a;
  t.x = __builtin_amdgcn_exp2f(t.x); t.y = __builtin_amdgcn_exp2f(t.y);
  t = t * sg + sg;
  t.x = __builtin_amdgcn_rcpf(t.x); t.y = __builtin_amdgcn_rcpf(t.y);
  return t * a;
}
DI u32x4 gelu_frag(f32x4 a, f32x4 b, float k1, float k2, float sg) {
  f32x2 g0 = gelu2((f32x2){a[0], a[1]}, k1, k2, sg), g1 = gelu2((f32x2){a[2], a[3]}, k1, k2, sg);
  f32x2 g2 = gelu2((f32x2){b[0], b[1]}, k1, k2, sg), g3 = gelu2((f32x2){b[2], b[3]}, k1, k2, sg);
  return (u32x4){cvtpk(g0.x, g0.y), cvtpk(g1.x, g1.y), cvtpk(g2.x, g2.y), cvtpk(g3.x, g3.y)};
}

template <int H>
DI void stepA(f32x4 (&acc)[6][4], bf16x8 (&wp)[2], bf16x8 (&t)[4], const u32 (&wc)[4], u32 n0, u32 n1, u32 xn) {
  bf16x8 w[8], nx[4];
  w[0] = wp[0]; w[1] = wp[1];
#pragma unroll
  for (int n = 0; n < 6; n++) {
    const int f = n + 2, ff = f < 6 ? f : f - 6;
    const u32 a = (f < 6 || H == 0) ? wc[ff & 3] : (ff ? n1 : n0); const int off = (f < 6 ? H : 1 - H) * WSLOT + (ff >> 2) * 4096;
    MMA(acc[n][0], w[n], t[0]); SB();
    s16x4 lo = rd_tr1(a, off); SB();
    MMA(acc[n][1], w[n], t[1]); SB();
    s16x4 hi = rd_tr1(a, off + 2048); SB();
    MMA(acc[n][2], w[n], t[2]); SB();
    if (n >= 1 && n <= 4) { nx[n - 1] = rd128(xn, (n - 1) * 2048); SB(); }
    MMA(acc[n][3], w[n], t[3]); SB();
    if (n == 0) { BAR(); SB(); }
    w[f] = cat(lo, hi);
  }
  wp[0] = w[6]; wp[1] = w[7];
#pragma unroll
  for (int i = 0; i < 4; i++) t[i] = nx[i];
}

template <int KIND, class FW, class FT, class FN, class MID>
DI void half(f32x4 (&acc)[3][4], bf16x8 (&w)[3], bf16x8 (&tp)[2], FW&& rw, FT&& rt_, FN&& rn, MID&& mid) {
  bf16x8 t[4], wn[3], tn[2];
  t[0] = tp[0]; t[1] = tp[1];
  s16x4 lo;
#pragma unroll
  for (int i = 0; i < 12; i++) {
    const int T = i / 3, n = i % 3;
    if constexpr (KIND == 2) MMA(acc[n][T], t[T], w[n]); else MMA(acc[n][T], w[n], t[T]);
    SB();
    if constexpr (KIND == 0) {
      if (i == 0) t[2] = rt_(2, 0); if (i == 1) t[3] = rt_(3, 0);
      if (i >= 2 && i <= 7) { if ((i & 1) == 0) lo = rw((i - 2) >> 1, 0); else wn[(i - 2) >> 1] = cat(lo, rw((i - 2) >> 1, 1)); }
      if (i == 8) tn[0] = rn(0, 0); if (i == 9) tn[1] = rn(1, 0);
    } else if constexpr (KIND == 1) {
      if (i <= 3) { if ((i & 1) == 0) lo = rt_(2 + (i >> 1), 0); else t[2 + (i >> 1)] = cat(lo, rt_(2 + (i >> 1), 1)); }
      if (i >= 4 && i <= 6) wn[i - 4] = rw(i - 4, 0);
      if (i >= 7 && i <= 10) { if ((i & 1) == 1) lo = rn((i - 7) >> 1, 0); else tn[(i - 7) >> 1] = cat(lo, rn((i - 7) >> 1, 1)); }
    } else {
      if (i == 0) t[2] = rt_(2, 0); if (i == 2) t[3] = rt_(3, 0);
      if (i == 3 || i == 5 || i == 6) wn[i == 3 ? 0 : i - 4] = rw(i == 3 ? 0 : i - 4, 0);
      if (i == 8) tn[0] = rn(0, 0); if (i == 9) tn[1] = rn(1, 0);
    }
    mid(i);
    SB();
  }
#pragma unroll
  for (int i = 0; i < 3; i++) w[i] = wn[i];
  tp[0] = tn[0]; tp[1] = tn[1];
}

template <int I, int N, class FN> DI void sfor(FN&& f) { if constexpr (I < N) { f(std::integral_constant<int, I>{}); sfor<I + 1, N>(f); } }

DI void ssm_carry(const Params& p, int rt, int j, int c, u32 lds0, u32 tag) {
  const u32 ax = lds0 + (18 & 3) * WSLOT, scr = lds0 + TOFF + (22 & 3) * TSLOT;
  {
    f32x4 pw = rdf4(ax + AX_TAB + c * 32, 16);
    { const float t = pw[0] * pw[0] - pw[1] * pw[1]; pw[1] = 2.f * pw[0] * pw[1]; pw[0] = t; }
    f32x2 es[8];
#pragma unroll
    for (int s = 0; s < 8; s++) es[s] = *(LAS f32x2*)(size_t)(scr + (s * 192 + c) * 8);
    float sr = 0.f, si = 0.f;
#pragma unroll
    for (int s = 0; s < 8; s++) { const float nr = pw[0] * sr - pw[1] * si + es[s].x, ni = pw[0] * si + pw[1] * sr + es[s].y; sr = nr; si = ni; }
    const rsrc_t rE = mk(p.ws + WS_EG, 16 * 9216);
    const int vo = (192 * j + c) * 16 + rt * 9216;
    st16((u32x4){__float_as_uint(sr), tag, __float_as_uint(si), tag}, rE, vo, 0);
    u32x4 v[15];
#pragma nounroll
    for (u32 spin = 0; spin < SPIN_CAP; spin++) {
      asm volatile("" ::: "memory");
      bool ok = true;
#pragma unroll
      for (int i = 0; i < 15; i++) v[i] = __builtin_amdgcn_raw_buffer_load_b128(rE, vo - (15 - i) * 9216, 0, 16);
#pragma unroll
      for (int i = 0; i < 15; i++) ok = ok && ((v[i][1] == tag && v[i][3] == tag) || vo < (15 - i) * 9216);
      if (__all(ok)) break;
    }
    float cr = 0.f, ci = 0.f;
#pragma unroll
    for (int i = 0; i < 15; i++) {
      const float nr = pw[2] * cr - pw[3] * ci + __uint_as_float(v[i][0]), ni = pw[2] * ci + pw[3] * cr + __uint_as_float(v[i][2]); cr = nr; ci = ni;
    }
#pragma unroll
    for (int s = 0; s < 8; s++) {
      *(LAS f32x2*)(size_t)(scr + (s * 192 + c) * 8) = (f32x2){cr, ci};
      const float nr = pw[0] * cr - pw[1] * ci + es[s].x, ni = pw[0] * ci + pw[1] * cr + es[s].y; cr = nr; ci = ni;
    }
  }
}

#define LBAR() BAR()
DI void loader(const Params& p0, int rt, int g, int j, int u, int lane, u32 lds0, u32 tag) {
  Params p = p0;
  const bool ssm = g == 4;
  const u32 wbytes = ssm ? D * ROWB : D * F * 2;
  const rsrc_t rX = mk(p.x, S * ROWB), rA0 = mk(ssm ? p.bre : p.w1, wbytes), rA1 = mk(ssm ? p.bim : p.w1, wbytes);
  const int r8 = lane >> 3, gp = lane & 7;
  const int cgr = ((((gp >> 1) ^ ((r8 >> 1) & 3)) << 1) | (gp & 1));
  const int rgr = gp ^ (4 * u + (r8 >> 1));
  const int slice = 3 * g + j;
  const int kA = 16 * u + 8 * (r8 >> 2) + (r8 & 3);
  const int R = 8 * u + r8;
  const u32 voffA = ssm ? (192 * j + R) * ROWB + rgr * 16 : kA * (F * 2) + slice * 768 + cgr * 16;
  const int tok = ssm ? 16 * ((R >> 2) & 3) + 8 * (R & 1) + ((R >> 1) & 1) : R;
  const u32 voffX = (rt * 128 + tok) * ROWB + rgr * 16;
  const u32 ldsW = lds0 + u * 1024, ldsT = lds0 + TOFF;
  const u32 aKt = ssm ? 128 : 64 * F * 2, aPar = ssm ? 0 : 32 * F * 2, pstr = ssm ? 32 * ROWB : 128, a2 = ssm ? 16 * ROWB : 4 * F * 2, xstr = (ssm ? 2 : 16) * ROWB;
  const int rot = rt >= 9 ? rt - 9 : rt;
  auto ktile = [&](int kap) { int kt = kap + rot; return kt >= 9 ? kt - 9 : kt; };
  auto issueA = [&](int kap, int h) { dma12(h ? rA1 : rA0, ldsW + ((2 * kap + h) & 3) * WSLOT, voffA, ktile(kap) * aKt + h * aPar, a2, pstr, pstr); };
  auto issueX = [&](int kap, int h) { dma4(rX, ldsT + (kap & 3) * TSLOT + h * 8192 + u * 1024, voffX, h * 64 * ROWB + ktile(kap) * 128, xstr); };
  issueA(0, 0); issueX(0, 0); issueX(0, 1); issueA(0, 1);
  __builtin_amdgcn_s_setprio(3);
  waitv<12>(); BAR();
  issueX(1, 0); issueX(1, 1); issueA(1, 0);
#pragma nounroll
  for (int kap = 0; kap < 7; kap++) {
    u32 l1 = ldsW + ((2 * kap + 3) & 3) * WSLOT, s1 = ktile(kap + 1) * aKt + aPar, l2 = ldsW + ((2 * kap) & 3) * WSLOT, s2 = ktile(kap + 2) * aKt;
    u32 lx = ldsT + ((kap + 2) & 3) * TSLOT + u * 1024, lx2 = lx + 8192, sx = ktile(kap + 2) * 128, sx2 = sx + 64 * ROWB;
    asm volatile("" : "+s"(l1), "+s"(s1), "+s"(l2), "+s"(s2), "+s"(lx), "+s"(lx2), "+s"(sx), "+s"(sx2));
    waitv<16>(); BAR(); dma12(rA1, l1, voffA, s1, a2, pstr, pstr); dma4(rX, lx, voffX, sx, xstr);
    waitv<16>(); BAR(); dma12(rA0, l2, voffA, s2, a2, pstr, pstr); dma4(rX, lx2, voffX, sx2, xstr);
  }
  waitv<16>(); BAR(); issueA(8, 1);
  p = reload_params();
  const u32 voffL = lane * 16;
  const rsrc_t rWS = mk(p.ws, WS_END), rB0 = mk(ssm ? p.cre : p.w2, wbytes), rB1 = mk(ssm ? p.cim : p.w2, wbytes);
  const u32 voffB = ssm ? voffA : R * ROWB + j * 384 + cgr * 16;
  const u32 bJ = ssm ? 384 : 384 * ROWB, bPar = ssm ? 0 : 192 * ROWB, bI = ssm ? 128 : 32 * ROWB, b0 = ssm ? 0 : g * 1152 * ROWB;
  const int j1 = j == 2 ? 0 : j + 1, j2 = j == 0 ? 2 : j - 1;
  const u32 st3 = 96 * ROWB - 2 * pstr, pb = WS_P + (rt * 15 + 3 * g) * 98304 + u * 4096;
  auto issueB = [&](int s, int jj, int i) { dma12((i & 1) ? rB1 : rB0, ldsW + (s & 3) * WSLOT, voffB, b0 + jj * bJ + (i & 1) * bPar + (i >> 1) * bI, 16 * ROWB, pstr, st3); };
  auto issueP = [&](int s, int jj, int i) { dma8lin(rWS, ldsT + (s & 3) * TSLOT + u * 4096, voffL, pb + jj * 98304 + i * 16384); };
  waitv<12>(); BAR();
#pragma unroll
  for (int k = 0; k < 3; k++) dma_sc1(rWS, lds0 + (18 & 3) * WSLOT + AX_TAB + (u * 3 + k) * 1024, voffL, WS_TAB + j * 6144 + (u * 3 + k) * 1024);
  waitv<3>();
  BAR(); issueB(19, j, 0);
  BAR(); issueB(20, j, 1); waitv<12>();
  BAR(); issueB(21, j, 2); waitv<12>();
  if (ssm) { BAR(); if (u == 1) ssm_carry(p, rt, j, lane, lds0, tag); WAITL(); BAR(); BAR(); }
  BAR(); issueB(22, j, 3); waitv<12>();
  BAR(); issueB(23, j, 4); issueP(23, j, 4); waitv<20>();
  BAR(); issueB(24, j, 5); issueP(24, j, 5);
#pragma nounroll
  for (int n = 0; n < 3; n++) {
    const int i = n ? 2 * n - 2 : 4;
    u32 w1 = b0 + j1 * bJ + (i >> 1) * bI, w2 = b0 + j2 * bJ + (i >> 1) * bI, w3 = w1 + bPar, w4 = w2 + bPar;
    u32 p1 = pb + j1 * 98304 + i * 16384, p2 = pb + j2 * 98304 + i * 16384, p3 = p1 + 16384, p4 = p2 + 16384;
    asm volatile("" : "+s"(w1), "+s"(w2), "+s"(w3), "+s"(w4), "+s"(p1), "+s"(p2), "+s"(p3), "+s"(p4));
    waitv<20>(); BAR(); dma12(rB0, ldsW + 1 * WSLOT, voffB, w1, 16 * ROWB, pstr, st3); dma8lin(rWS, ldsT + 1 * TSLOT + u * 4096, voffL, p1);
    waitv<20>(); BAR(); dma12(rB0, ldsW + 2 * WSLOT, voffB, w2, 16 * ROWB, pstr, st3); dma8lin(rWS, ldsT + 2 * TSLOT + u * 4096, voffL, p2);
    waitv<20>(); BAR(); dma12(rB1, ldsW + 3 * WSLOT, voffB, w3, 16 * ROWB, pstr, st3); dma8lin(rWS, ldsT + 3 * TSLOT + u * 4096, voffL, p3);
    waitv<20>(); BAR(); dma12(rB1, ldsW + 0 * WSLOT, voffB, w4, 16 * ROWB, pstr, st3); dma8lin(rWS, ldsT + 0 * TSLOT + u * 4096, voffL, p4);
  }
  waitv<20>();
  BAR();
  {
    const u32 l = lds0 + WSLOT;
    const u32 vo = (lane & 31) < 24 ? (rt * 128 + u * 64 + (g & 3) * 16 + (lane >> 5)) * ROWB + j * 384 + (lane & 31) * 16 : 0x7fffffffu;
#pragma unroll
    for (int k = 0; k < 8; k++) dma(rX, l + (u * 8 + k) * 1024, vo, k * 2 * ROWB);
    dma(mk(u ? p.dskip : p.b2, ROWB), l + 16384 + u * 1024, voffL + j * 384, 0);
  }
  waitv<9>(); BAR(); BAR(); waitv<0>();
  BAR();
}

DI void helper(const Params& p, int rt, int g, int j, int h, int lane, u32 lds0, u32 tag) {
  const bool ssm = g == 4;
  const int slice = 3 * g + j, q = lane >> 4, jl = lane & 15;
  const rsrc_t rWS = mk(p.ws, WS_END);
  char* const hsR = p.ws + WS_RSP + blockIdx.x * 4;
  u32 hsn = 0;
  if (h == 0) { hsn = __builtin_amdgcn_readfirstlane(ld_flag(hsR)) + 1u; st_word(hsR - 1024, hsn); }
  __builtin_amdgcn_s_setprio(2);
  {
    const int e = (lane >> 2) & 3, z = 2 * (q & 1) + (e >> 1);
    bf16x8 f[12]; f32x4 c[12]; u32x4 one = {ONES, ONES, ONES, ONES};
    auto rdslot = [&](int sl) {
      if (ssm) {
        const u32 a = lds0 + h * 12288 + jl * 128 + ((q ^ (jl >> 1)) * 16);
#pragma unroll
        for (int n = 0; n < 6; n++) { f[n] = rd128(a, sl * WSLOT + n * 2048); f[6 + n] = rd128(a ^ 64, sl * WSLOT + n * 2048); }
      } else {
#pragma unroll
        for (int n = 0; n < 12; n++) f[n] = rd_tr(lds0 + h * 12288 + (4 * q + e) * 128 + (((n & 3) ^ z) * 32) + (lane & 3) * 8, sl * WSLOT + (n >> 2) * 4096);
      }
#pragma unroll
      for (int n = 0; n < 12; n++) PIN(f[n]);
      asm volatile("s_nop 1");
    };
#pragma unroll
    for (int n = 0; n < 12; n++) { c[n] = (f32x4){0.f, 0.f, 0.f, 0.f}; PIN(c[n]); }
    PIN(one);
    LBAR();
    rdslot(0);
    if (ssm) {
#pragma unroll
      for (int n = 0; n < 12; n++) MMA(c[n % 6], f[n], one);
      MFENCE();
    } else {
#pragma unroll
      for (int n = 0; n < 12; n++) MMA(c[n], f[n], one);
      MFENCE();
    }
    LBAR();
    rdslot(1);
    if (rt < 9) {
#pragma unroll
      for (int n = 6; n < 12; n++) MMA(c[n], f[n], one);
      MFENCE();
    }
    LBAR();
    if (rt < 9) {
      if (ssm) {
#pragma unroll
        for (int n = 0; n < 6; n++) MMA(c[6 + n], f[n], one);
      MFENCE();
      } else {
#pragma unroll
        for (int n = 0; n < 6; n++) MMA(c[n], f[n], one);
      MFENCE();
      }
    }
    f32x4 m = c[0];
#pragma unroll
    for (int n = 1; n < 12; n++) m = jl == n ? c[n] : m;
    const int col = (ssm ? (jl >= 6 ? 192 - 96 : 0) + 96 * h : 192 * h) + 16 * jl + 4 * q;
    st16(__builtin_bit_cast(u32x4, m), rWS, jl < 12 ? col * 4 : 0x7fffffffu, WS_CS + (slice * 16 + rt) * 1536);
  }
  LBAR();
  u32 pv = ld_flag(hsR);
  LBAR();
  waitv<0>(); LBAR();
  bool acked = h != 0;
#pragma unroll
  for (int s = 0; s < 2; s++) {
    if (!acked) {
      acked = __all(pv == hsn);
      if (!acked && s == 1) { spin_all(hsR, hsn); acked = true; }
      if (acked) st_word(hsR + 1024, 1u);
    }
    if (s < 1) pv = ld_flag(hsR);
    LBAR(); LBAR();
  }
  LBAR();
  const char* fGo = p.ws + WS_FLAGS + FL_GO * 128;
  pv = ld_flag(fGo);
  LBAR(); LBAR();
  if (!__all(pv == 1u)) spin_all(fGo, 1u);
  const int ir = 64 * h + lane, irt = ssm ? (ir & 64) + 16 * ((ir >> 2) & 3) + 8 * (ir & 1) + 2 * ((ir >> 4) & 3) + ((ir >> 1) & 1) : ir;
  const u32x2 sg = __builtin_amdgcn_raw_buffer_load_b64(rWS, (rt * 128 + irt) * 8, WS_ST, 16);
  const int cc = 192 * h + 4 * (lane < 48 ? lane : lane - 48);
  f32x4 cp[9];
#pragma unroll
  for (int k = 0; k < 9; k++) cp[k] = __builtin_bit_cast(f32x4, __builtin_amdgcn_raw_buffer_load_b128(rWS, cc * 4, WS_CS + (slice * 16 + k) * 1536, 16));
  const u32x2 bb = *(const GAS u32x2*)(p.b1 + (ssm ? 0 : slice * 384) + cc);
  LBAR(); LBAR(); SB();
  f32x4 cs = ((cp[0] + cp[1]) + (cp[2] + cp[3])) + ((cp[4] + cp[5]) + (cp[6] + cp[7])) + cp[8];
  float mean = __uint_as_float(sg[0]), rstd = __builtin_amdgcn_rsqf(__uint_as_float(sg[1])), sig = __uint_as_float(sg[1]) * rstd;
  u32x4 at, aw[4];
  { float m1, m2, m3; split3(-mean, m1, m2, m3); const float g1 = rbf(sig), g2 = sig - g1;
    at = (u32x4){cvtpk(m1, m1), cvtpk(m2, m2), cvtpk(m1, m3), cvtpk(g1, g2)}; mean = ssm ? -mean * rstd : mean; }
  PIN(cs); PIN(at); PIN(mean); PIN(rstd); PIN(sig);
  auto auxw = [&](int k) { float c1, c2, c3; split3(cs[k], c1, c2, c3); const float b = (k & 1) ? bfhi(bb[k >> 1]) : bflo(bb[k >> 1]);
    aw[k] = (u32x4){cvtpk(c1, c2), cvtpk(c1, c2), cvtpk(c3, c1), cvtpk(b, b)}; PIN(aw[k]); };
  LBAR(); SB();
  auxw(0); auxw(1);
  const u32 ax = lds0 + (18 & 3) * WSLOT;
  LBAR(); SB();
  wr128(ax + AX_T + ir * 16, 0, at);
  { LAS float* sp = (LAS float*)(size_t)(ax + AX_S + ir * 4); sp[0] = mean; sp[128] = rstd; sp[256] = sig; }
  auxw(2); auxw(3);
  LBAR(); SB();
  if (ssm) *(LAS f32x4*)(size_t)(ax + AX_W + cc * 4) = cs;
  else {
#pragma unroll
    for (int k = 0; k < 4; k++) wr128(ax + AX_W + cc * 16, k * 16, aw[k]);
  }
  WAITL(); LBAR();
  LBAR(); SB();
  if (ssm) { LBAR(); ssm_carry(p, rt, j, 64 + 64 * h + lane, lds0, tag); WAITL(); LBAR(); BAR(); }
  char* const fMine = p.ws + WS_FLAGS + (FL_P + rt * 15 + slice) * 128;
  const char* fP = p.ws + WS_FLAGS + (FL_P + rt * 15 + 3 * g + ((lane & 1) ? (j == 2 ? 0 : j + 1) : (j == 0 ? 2 : j - 1))) * 128;
  LBAR(); if (h == 1 && !ssm) st_word(fMine, 2u);
  LBAR(); if (h == 1 && ssm) st_word(fMine, 1u);
#pragma nounroll
  for (u32 lv = 1; lv < 4; lv++) {
    LBAR(); if (h == 0) spin_ge(fP, lv);
    LBAR(); if (h == 1 && lv == 1) st_word(fMine, 3u);
    LBAR(); LBAR();
  }
  LBAR(); LBAR(); LBAR(); LBAR();
  LBAR();
}

template <bool SSM>
DI void phaseB(const Params& p, int rt, int g, int j, int w, int lane, u32 lds0, u32 tag) {
  asm volatile("" : "+v"(lane));
  const int rg = w >> 2, cg = w & 3, q = lane >> 4, e = (lane >> 2) & 3, pp = lane & 3, jl = lane & 15;
  const int z = 2 * (q & 1) + (e >> 1);
  u32 wlo[3], whi[3], tb[2];
#pragma unroll
  for (int n = 0; n < 3; n++) {
    const int t = 3 * cg + n;
    wlo[n] = SSM ? lds0 + (16 * t + jl) * 128 + ((q ^ (jl >> 1)) * 16) : lds0 + (t >> 2) * 4096 + (4 * q + e) * 128 + (((t & 3) ^ z) * 32) + pp * 8;
    whi[n] = SSM ? (wlo[n] ^ 64) : wlo[n] + 2 * WSLOT;
  }
  tb[0] = SSM ? lds0 + TOFF + rg * 8192 + (q >> 1) * 2048 + (pp >> 1) * 1024 + (((8 * (q & 1) + e) * 16) ^ ((pp >> 1) * 64)) + 8 * (pp & 1) : lds0 + TOFF + rg * 4096 + lane * 16;
  tb[1] = tb[0] ^ 64;
  float mean = 0.f, rstd = 0.f;
  if constexpr (!SSM) { const u32 sa = lds0 + (18 & 3) * WSLOT + AX_S + (64 * rg + 16 * g + jl) * 4; mean = rdf(sa, 0); rstd = rdf(sa, 512); }
  f32x4 ob[3][4];
#pragma unroll
  for (int n = 0; n < 3; n++)
#pragma unroll
    for (int T = 0; T < 4; T++) { ob[n][T] = (f32x4){0.f, 0.f, 0.f, 0.f}; PIN(ob[n][T]); }
  auto RW = [&](int sl, int kb, int n, int part) {
    if constexpr (SSM) return rd128((kb ? whi[n] : wlo[n]) + (sl >> 1) * 2 * WSLOT, (sl & 1) * WSLOT);
    else return rd_tr1(sl < 2 ? wlo[n] : whi[n], (sl & 1) * WSLOT + kb * 12288 + part * 2048);
  };
  auto RT = [&](int sl, int kb, int T, int part) {
    if constexpr (SSM) return rd_tr1(tb[part], sl * TSLOT + kb * 4096 + T * 256);
    else return rd128(tb[0], sl * TSLOT + kb * 8192 + T * 1024);
  };
  bf16x8 wf[3], tp[2];
  auto step = [&](auto slc, auto&& mid) {
    constexpr int SL = decltype(slc)::value, NS = (SL + 1) & 3;
    half<SSM ? 1 : 0>(ob, wf, tp, [&](int n, int part) { return RW(SL, 1, n, part); }, [&](int T, int part) { return RT(SL, 0, T, part); }, [&](int T, int part) { return RT(SL, 1, T, part); }, mid);
    half<SSM ? 1 : 0>(ob, wf, tp, [&](int n, int part) { return RW(NS, 0, n, part); }, [&](int T, int part) { return RT(SL, 1, T, part); }, [&](int T, int part) { return RT(NS, 0, T, part); }, [](int) {});
  };
  using I0 = std::integral_constant<int, 0>; using I1 = std::integral_constant<int, 1>; using I2 = std::integral_constant<int, 2>; using I3 = std::integral_constant<int, 3>;
  if constexpr (!SSM) waitv<4>();
  WAITL(); BAR();
#pragma unroll
  for (int n = 0; n < 3; n++) { if constexpr (SSM) wf[n] = RW(3, 0, n, 0); else wf[n] = cat(RW(3, 0, n, 0), RW(3, 0, n, 1)); }
#pragma unroll
  for (int T = 0; T < 2; T++) { if constexpr (SSM) tp[T] = cat(RT(3, 0, T, 0), RT(3, 0, T, 1)); else tp[T] = RT(3, 0, T, 0); }
  auto barm = [](int i) { if (i == 3) BAR(); };
  step(I3{}, [](int) {});
  step(I0{}, [&](int i) { if (i == 3) { if constexpr (SSM) waitv<8>(); BAR(); } });
#pragma nounroll
  for (int it = 0; it < 4; it++) { step(I1{}, barm); step(I2{}, [&](int i) { if (i == 3) { waitv<0>(); BAR(); } }); step(I3{}, barm); step(I0{}, barm); }
  MFENCE();
  const rsrc_t rWS = mk(p.ws, WS_END);
  const u32 xb = WS_XB + (rt * 3 + j) * 32 * XBW + w * XBW;
  f32x4 mine[3];
#pragma unroll
  for (int T = 0; T < 4; T++) {
    if (SSM || T != g) {
      const u32 o = xb + T * 8 * XBW + g * 1536;
      const u32x4 a = {cvtpk(ob[0][T][0], ob[0][T][1]), cvtpk(ob[0][T][2], ob[0][T][3]), cvtpk(ob[1][T][0], ob[1][T][1]), cvtpk(ob[1][T][2], ob[1][T][3])};
      const u32 b0 = cvtpk(ob[2][T][0], ob[2][T][1]), b1 = cvtpk(ob[2][T][2], ob[2][T][3]);
      const auto s0 = __builtin_amdgcn_permlane32_swap(b0, b0, false, false), s1 = __builtin_amdgcn_permlane32_swap(b1, b1, false, false);
      st16(a, rWS, lane * 16, o);
      if (lane < 32) st16((u32x4){b0, b1, s0[1], s1[1]}, rWS, lane * 16, o + 1024);
    } else {
#pragma unroll
      for (int n = 0; n < 3; n++) mine[n] = ob[n][T];
    }
  }
  if constexpr (SSM) {
    waitv<0>(); BAR();
    if (threadIdx.x == 0) st_flag(p.ws, FL_O + (rt * 3 + j) * 5 + g, tag);
    STAMP(7);
  } else {
    waitv<0>(); BAR();
    if (threadIdx.x == 0) st_flag(p.ws, FL_O + (rt * 3 + j) * 5 + g, tag);
    const int tok = rt * 128 + 64 * rg + 16 * g + jl, col = 192 * j + 48 * cg + 4 * q;
    u32x2 xv[3], bv[3], dv[3];
#pragma unroll
    for (int n = 0; n < 3; n++) {
      const u32 cb = lds0 + WSLOT + (48 * cg + 4 * q + 16 * n) * 2;
      xv[n] = *(LAS u32x2*)(size_t)(cb + (16 * rg + jl) * 512); bv[n] = *(LAS u32x2*)(size_t)(cb + 16384); dv[n] = *(LAS u32x2*)(size_t)(cb + 17408);
    }
    spin_all(p.ws + WS_FLAGS + (FL_O + (rt * 3 + j) * 5 + (lane % 5)) * 128, tag);
    const u32 o = xb + g * 8 * XBW;
    u32x4 a[4]; u32x2 b[4];
#pragma unroll
    for (int k = 0; k < 4; k++) {
      const int src = k == 3 ? 4 : (k < g ? k : k + 1);
      a[k] = __builtin_amdgcn_raw_buffer_load_b128(rWS, lane * 16, o + src * 1536, 16);
      b[k] = __builtin_amdgcn_raw_buffer_load_b64(rWS, (lane & 31) * 16 + (lane >> 5) * 8, o + src * 1536 + 1024, 16);
    }
#pragma unroll
    for (int n = 0; n < 3; n++) {
      f32x4 r;
#pragma unroll
      for (int c = 0; c < 4; c++) {
        float G = mine[n][c] + ((c & 1) ? bfhi(bv[n][c >> 1]) : bflo(bv[n][c >> 1]));
#pragma unroll
        for (int k = 0; k < 3; k++) { const u32 v = n < 2 ? a[k][2 * n + (c >> 1)] : b[k][c >> 1]; G += (c & 1) ? bfhi(v) : bflo(v); }
        const u32 yv = n < 2 ? a[3][2 * n + (c >> 1)] : b[3][c >> 1];
        const float yy = (c & 1) ? bfhi(yv) : bflo(yv);
        const float xx = (c & 1) ? bfhi(xv[n][c >> 1]) : bflo(xv[n][c >> 1]);
        const float dd = (c & 1) ? bfhi(dv[n][c >> 1]) : bflo(dv[n][c >> 1]);
        const float sv = yy + (xx - mean) * rstd * dd;
        const float sig = __builtin_amdgcn_rcpf(1.f + __builtin_amdgcn_exp2f(-1.4426950408889634f * G));
        r[c] = xx + sv * (G * sig);
      }
      *(GAS f32x4*)(p.out + tok * D + col + 16 * n) = r;
    }
    STAMP(7);
  }
}

DI void ffn_compute(const Params& p, int rt, int g, int j, int w, int lane, u32 lds0, u32 tag) {
  const int rg = w >> 2, cg = w & 3, q = lane >> 4, e = (lane >> 2) & 3, pp = lane & 3, jl = lane & 15;
  const int z = 2 * (q & 1) + (e >> 1);
  u32 wc[4];
#pragma unroll
  for (int n = 0; n < 4; n++) { const int t = 6 * cg + n; wc[n] = lds0 + (t >> 2) * 4096 + (4 * q + e) * 128 + (((t & 3) ^ z) * 32) + pp * 8; }
  const u32 xbase = lds0 + TOFF + (64 * rg + jl) * 128 + ((q ^ (jl >> 1)) * 16);
  f32x4 acc[6][4];
#pragma unroll
  for (int n = 0; n < 6; n++)
#pragma unroll
    for (int T = 0; T < 4; T++) { acc[n][T] = (f32x4){0.f, 0.f, 0.f, 0.f}; asm volatile("" : "+v"(acc[n][T])); }
  bf16x8 wp[2], t[4];
  BAR();
  wp[0] = rd_tr(wc[0], 0); wp[1] = rd_tr(wc[1], 0);
#pragma unroll
  for (int T = 0; T < 4; T++) t[T] = rd128(xbase, T * 2048);
  u32 xs = 0; int dw = 2 * WSLOT;
#pragma nounroll
  for (int it = 0; it < 9; it++) {
    stepA<0>(acc, wp, t, wc, 0, 0, (xbase ^ 64) + xs); xs = (xs + TSLOT) & (4 * TSLOT - 1);
    const u32 n0 = wc[0] + dw, n1 = wc[1] + dw;
    stepA<1>(acc, wp, t, wc, n0, n1, xbase + xs);
    waitv<0>();
    wc[0] = n0; wc[1] = n1; wc[2] += dw; wc[3] += dw; dw = -dw;
  }
  WAITL(); BAR();
  const u32 ax = lds0 + (18 & 3) * WSLOT;
  float k1[4], k2[4], sg[4];
#pragma unroll
  for (int T = 0; T < 4; T++) {
    u32x4 v = __builtin_bit_cast(u32x4, rd128(ax + AX_T + (64 * rg + jl) * 16, T * 256));
    if (q != 0) v = (u32x4){0u, 0u, 0u, 0u};
    t[T] = __builtin_bit_cast(bf16x8, v);
    const float r = rdf(ax + AX_S + 512 + (64 * rg + jl) * 4, T * 64);
    sg[T] = rdf(ax + AX_S + 1024 + (64 * rg + jl) * 4, T * 64);
    k1[T] = -2.3022081985f * r; k2[T] = -0.10294324f * r * r * r;
  }
  {
    bf16x8 a[6];
#pragma unroll
    for (int n = 0; n < 6; n++) a[n] = rd128(ax + AX_W + (96 * cg + jl) * 16, n * 256);
    asm volatile("s_nop 1" : "+v"(t[0]), "+v"(t[1]), "+v"(t[2]), "+v"(t[3]));
#pragma unroll
    for (int n = 0; n < 6; n++)
#pragma unroll
      for (int T = 0; T < 4; T++) MMA(acc[n][T], a[n], t[T]);
    MFENCE();
  }
  const rsrc_t rWS = mk(p.ws, WS_END);
  const u32 pg = WS_P + (rt * 15 + 3 * g + j) * 98304 + (cg >> 1) * 16384 + (cg & 1) * 8192 + rg * 4096;
  const u32 pl = lds0 + TOFF + (cg & 1) * 8192 + rg * 4096 + lane * 16, pl0 = pl + ((19 + (cg >> 1)) & 3) * TSLOT, pl1 = pl + ((21 + (cg >> 1)) & 3) * TSLOT;
#pragma unroll
  for (int bb = 0; bb < 3; bb++) {
    const int b = (bb + 2) % 3;
#pragma unroll
    for (int T = 0; T < 4; T++) {
      const u32x4 P = gelu_frag(acc[2 * b][T], acc[2 * b + 1][T], k1[T], k2[T], sg[T]);
      st16(P, rWS, lane * 16, pg + b * 32768 + T * 1024);
      if (b < 2) wr128(b ? pl1 : pl0, T * 1024, P);
      SB();
    }
  }
  phaseB<false>(p, rt, g, j, w, lane, lds0, tag);
}

DI void ssm_compute(const Params& p, int rt, int j, int w, int lane, u32 lds0, u32 tag) {
  const int rg = w >> 2, cg = w & 3; int q = lane >> 4, jl = lane & 15;
  u32 wb = lds0 + (16 * cg + jl) * 128 + ((q ^ (jl >> 1)) * 16);
  const u32 xb0 = lds0 + TOFF + (64 * rg + jl) * 128 + ((q ^ (jl >> 1)) * 16);
  f32x4 ar[3][4], ai[3][4];
#pragma unroll
  for (int n = 0; n < 3; n++)
#pragma unroll
    for (int T = 0; T < 4; T++) { ar[n][T] = (f32x4){0.f, 0.f, 0.f, 0.f}; ai[n][T] = (f32x4){0.f, 0.f, 0.f, 0.f}; asm volatile("" : "+v"(ar[n][T]), "+v"(ai[n][T])); }
  bf16x8 wf[3], tp[2];
  auto nop = [](int) {};
  auto barm = [](int i) { if (i == 3) BAR(); };
  BAR();
#pragma unroll
  for (int n = 0; n < 3; n++) wf[n] = rd128(wb, n * 8192);
  tp[0] = rd128(xb0, 0); tp[1] = rd128(xb0, 2048);
  u32 xs = 0; int dw = 2 * WSLOT;
#pragma nounroll
  for (int it = 0; it < 9; it++) {
    const u32 x0 = xb0 + xs, x1 = (xb0 ^ 64) + xs, wb1 = wb ^ 64, wn = wb + dw;
    xs = (xs + TSLOT) & (4 * TSLOT - 1);
    const u32 xn = xb0 + xs;
    half<2>(ar, wf, tp, [&](int n, int) { return rd128(wb1, n * 8192); }, [&](int T, int) { return rd128(x0, T * 2048); }, [&](int T, int) { return rd128(x1, T * 2048); }, barm);
    half<2>(ar, wf, tp, [&](int n, int) { return rd128(wb, WSLOT + n * 8192); }, [&](int T, int) { return rd128(x1, T * 2048); }, [&](int T, int) { return rd128(x0, T * 2048); }, nop);
    half<2>(ai, wf, tp, [&](int n, int) { return rd128(wb1, WSLOT + n * 8192); }, [&](int T, int) { return rd128(x0, T * 2048); }, [&](int T, int) { return rd128(x1, T * 2048); }, barm);
    half<2>(ai, wf, tp, [&](int n, int) { return rd128(wn, n * 8192); }, [&](int T, int) { return rd128(x1, T * 2048); }, [&](int T, int) { return rd128(xn, T * 2048); }, nop);
    wb = wn; dw = -dw;
    waitv<0>();
  }
  WAITL(); BAR();
  asm volatile("" : "+v"(lane));
  q = lane >> 4; jl = lane & 15;
  const u32 ax = lds0 + (18 & 3) * WSLOT;
  MFENCE();
  f32x4 lc[3];
  const u32 scr = lds0 + TOFF + (22 & 3) * TSLOT + ((4 * rg + q) * 192 + 16 * cg + jl) * 8;
  f32x2 e0[3]; float csr[3], csi[3];
#pragma unroll
  for (int b = 0; b < 3; b++) { csr[b] = rdf(ax + AX_W + (16 * cg + jl) * 4, b * 256); csi[b] = rdf(ax + AX_W + (16 * cg + jl) * 4, 768 + b * 256); }
#pragma unroll
  for (int T = 0; T < 4; T++) {
    const f32x4 r = rdf4(ax + AX_S + 512 + (64 * rg + 4 * q) * 4, T * 64), nm = rdf4(ax + AX_S + (64 * rg + 4 * q) * 4, T * 64);
#pragma unroll
    for (int b = 0; b < 3; b++) { ar[b][T] = ar[b][T] * r + nm * csr[b]; ai[b][T] = ai[b][T] * r + nm * csi[b]; }
  }
  f32x2 l8[3];
#pragma unroll
  for (int b = 0; b < 3; b++) {
    lc[b] = rdf4(ax + AX_TAB + (16 * cg + jl) * 32, b * 2048);
    l8[b] = *(LAS f32x2*)(size_t)(ax + AX_TAB + (16 * cg + jl) * 32 + b * 2048 + 16);
    f32x2 hr = {ar[b][0][0], ar[b][0][1]}, hi = {ai[b][0][0], ai[b][0][1]};
#pragma unroll
    for (int k = 1; k < 8; k++) {
      const f32x2 ur = {ar[b][k >> 1][(k & 1) * 2], ar[b][k >> 1][(k & 1) * 2 + 1]}, ui = {ai[b][k >> 1][(k & 1) * 2], ai[b][k >> 1][(k & 1) * 2 + 1]};
      const f32x2 nr = lc[b][0] * hr + (ur - lc[b][1] * hi), ni = lc[b][0] * hi + (ui + lc[b][1] * hr);
      hr = nr; hi = ni;
    }
    e0[b] = (f32x2){hr.x, hi.x};
    *(LAS f32x2*)(size_t)(scr + b * 512) = (f32x2){l8[b].x * hr.x - l8[b].y * hi.x + hr.y, l8[b].x * hi.x + l8[b].y * hr.x + hi.y};
  }
  WAITL(); BAR(); SB();
#pragma unroll
  for (int b = 0; b < 3; b++) {
    const float c2r = 2.f * lc[b][2], c2i = 2.f * lc[b][3];
#pragma unroll
    for (int T = 0; T < 4; T++) {
      const f32x4 nr = c2r * ar[b][T] - c2i * ai[b][T], ni = -c2r * ai[b][T] - c2i * ar[b][T];
      ar[b][T] = nr; ai[b][T] = ni;
      asm volatile("" : "+v"(ar[b][T]), "+v"(ai[b][T]));
    }
  }
  BAR();
  const rsrc_t rWS = mk(p.ws, WS_END);
  const u32 io0 = w * 2048 + lane * 16, io1 = w * 2048 + 1024 + ((lane * 16) ^ 64);
  const u32 pg = WS_P + (rt * 15 + 12 + j) * 98304;
  f32x2 cins[3];
#pragma unroll
  for (int b = 0; b < 3; b++) { cins[b] = *(LAS f32x2*)(size_t)(scr + b * 512); l8[b] = *(LAS f32x2*)(size_t)(ax + AX_TAB + (16 * cg + jl) * 32 + b * 2048 + 16); }
#pragma unroll
  for (int bb = 0; bb < 3; bb++) {
    const int b = (bb + 2) % 3;
    u32x4 H[2][2];
    const f32x2 cin = cins[b];
    const f32x2 sr = {cin.x, l8[b].x * cin.x - l8[b].y * cin.y + e0[b].x}, si = {cin.y, l8[b].x * cin.y + l8[b].y * cin.x + e0[b].y};
    const float c2r = 2.f * lc[b][2], c2i = 2.f * lc[b][3];
    f32x2 hr = c2r * sr - c2i * si, hi = -c2r * si - c2i * sr;
#pragma unroll
    for (int T = 0; T < 4; T++) {
      f32x2 pr[2], pi[2];
#pragma unroll
      for (int k = 0; k < 2; k++) {
        const f32x2 ur = {ar[b][T][2 * k], ar[b][T][2 * k + 1]}, ui = {ai[b][T][2 * k], ai[b][T][2 * k + 1]};
        const f32x2 nr = lc[b][0] * hr + (ur + lc[b][1] * hi), ni = lc[b][0] * hi + (ui - lc[b][1] * hr);
        hr = nr; hi = ni; pr[k] = nr; pi[k] = ni;
      }
      H[0][0][T] = cvtpk(pr[0].x, pr[1].x); H[0][1][T] = cvtpk(pr[0].y, pr[1].y);
      H[1][0][T] = cvtpk(pi[0].x, pi[1].x); H[1][1][T] = cvtpk(pi[0].y, pi[1].y);
    }
    if (bb == 1) BAR();
#pragma unroll
    for (int k = 0; k < 4; k++) {
      st16(H[k >> 1][k & 1], rWS, (k & 1) ? io1 : io0, pg + (2 * b + (k >> 1)) * 16384);
      if (b < 2) wr128(lds0 + TOFF + ((k & 1) ? io1 : io0), ((19 + 2 * b + (k >> 1)) & 3) * TSLOT, H[k >> 1][k & 1]);
    }
    SB();
  }
  phaseB<true>(p, rt, 4, j, w, lane, lds0, tag);
}

DI void spare(const Params& p, int sid, u32 lds0, u32 tag, u64 pc0) {
  const int tid = threadIdx.x;
  u32 cpf = 0;
  if (tid < CODE_LINES && !(sid & 1)) cpf = *(const GAS u32*)(pc0 + tid * 128);
  const rsrc_t rWS = mk(p.ws, WS_END);
  const int w = __builtin_amdgcn_readfirstlane(tid >> 6), lane = tid & 63;
  if (sid == 15 && w == 11) { waitv<0>(); hs_leader(p.ws, lane, lds0, STAMPPTR); return; }
  u32 hsn = 0;
  if (w == 11) { hsn = ld_flag(p.ws + WS_RSP + blockIdx.x * 4) + 1u; st_word(p.ws + WS_REQ + blockIdx.x * 4, hsn); }
  else if (w == 10) {
    const int row = sid * 128 + 120 + (lane >> 3);
    u32x4 v[9]; float s = 0.f;
#pragma unroll
    for (int i = 0; i < 9; i++) v[i] = ((const GAS u32x4*)(p.x + row * D) + (lane & 7))[8 * i];
#pragma unroll
    for (int i = 0; i < 9; i++)
#pragma unroll
      for (int c = 0; c < 4; c++) s += bflo(v[i][c]) + bfhi(v[i][c]);
    s += dppx<0xb1>(s); s += dppx<0x4e>(s); s += dppx<0x141>(s);
    const float mean = s * (1.f / D); s = 0.f;
#pragma unroll
    for (int i = 0; i < 9; i++)
#pragma unroll
      for (int c = 0; c < 4; c++) { const float a = bflo(v[i][c]) - mean, b = bfhi(v[i][c]) - mean; s += a * a + b * b; }
    s += dppx<0xb1>(s); s += dppx<0x4e>(s); s += dppx<0x141>(s);
    if ((lane & 7) == 0) __hip_atomic_store((GAS u64*)(p.ws + WS_ST + row * 8), ((u64)__float_as_uint(s * (1.f / D) + 1e-5f) << 32) | __float_as_uint(mean), __ATOMIC_RELAXED, __HIP_MEMORY_SCOPE_AGENT);
  } else if (w == 9 && sid < 9) {
    const int c = sid * 64 + lane;
    const float lr = bf2f(p.lre[c]), li = bf2f(p.lim[c]);
    const float dt = rbf(expf(bf2f(p.logdt[c])));
    const float ex = expf(dt * lr), an = dt * li;
    const float br = ex * cosf(an), bi = ex * sinf(an);
    const float nr = br - 1.f, den = 1.f / (lr * lr + li * li);
    const float cr = (nr * lr + bi * li) * den, ci = (bi * lr - nr * li) * den;
    float ar = br, ai = bi, r8 = 0.f, i8 = 0.f;
#pragma unroll
    for (int k = 0; k < 7; k++) { const float t = ar * ar - ai * ai; ai = 2.f * ar * ai; ar = t; if (k == 2) { r8 = ar; i8 = ai; } }
    st16(__builtin_bit_cast(u32x4, (f32x4){br, bi, cr, ci}), rWS, c * 32, WS_TAB);
    st16(__builtin_bit_cast(u32x4, (f32x4){r8, i8, ar, ai}), rWS, c * 32 + 16, WS_TAB);
  }
  if (w == 11) spin_all(p.ws + WS_RSP + blockIdx.x * 4, hsn);
  waitv<0>(); asm volatile("" :: "v"(cpf)); BAR();
  if (sid == 15) return;
  if (w == 11) st_word(p.ws + WS_ACK + blockIdx.x * 4, 1u);
  {
    u32 acc = cpf;
    const GAS u32* w2 = (const GAS u32*)p.w2;
#pragma unroll
    for (int i = 0; i < 4; i++) { const int L = (i * 15 + sid) * 768 + tid; if (L < 41472) acc ^= w2[L * 32]; }
    { const int L = sid * 768 + tid; if (L < 5184) acc ^= ((const GAS u32*)p.cre)[L * 32]; else if (L < 10368) acc ^= ((const GAS u32*)p.cim)[(L - 5184) * 32]; }
    asm volatile("" :: "v"(acc));
  }
}

__global__ __launch_bounds__(768, 3) void fused_kernel(Params p) {
  extern __shared__ __attribute__((aligned(16))) char smem[];
  const u32 lds0 = (u32)(size_t)(LAS char*)smem;
  const int b = blockIdx.x, xcd = b & 7, idx = b >> 3;
  const u32 tag = 1u;
  u64 pc0; asm volatile("s_getpc_b64 %0" : "=s"(pc0));
  STAMP(0);
  const int tri = idx / 3, j = idx - 3 * tri;
  const int g = tri < 2 ? 4 : xcd >> 1, rt = 8 * (xcd & 1) + (tri < 2 ? (xcd & 6) + tri : tri - 2);
  const int lane = threadIdx.x & 63, w = __builtin_amdgcn_readfirstlane(threadIdx.x >> 6);
  const int sid = xcd * 2 + idx - 30;
  const int srow = rt * 128 + (3 * g + j) * 8 + w;
  int role = idx < 30 ? w >> 1 : 0;
  if (w < 8 || idx >= 30) {
    if (idx < 30) {
      if (w == 0 && lane < 2) st_flag(p.ws, lane ? FL_P + rt * 15 + 3 * g + j : FL_O + (rt * 3 + j) * 5 + g, 0u);
      if (g == 4 && w >= 1 && w < 4) st16((u32x4){0u, 0u, 0u, 0u}, mk(p.ws, WS_END), (192 * j + threadIdx.x - 64) * 16, WS_EG + rt * 576 * 16);
    } else if (w == 11 && sid == 15) {
      st_flag(p.ws, FL_GO, 0u);
      st16((u32x4){0u, 0u, 0u, 0u}, mk(p.ws, WS_END), lane * 16, WS_ACK);
    }
    if (idx < 30) {
      float mean, ve; row_stats(p.x + srow * D, lane, mean, ve);
      if (lane == 0) __hip_atomic_store((GAS u64*)(p.ws + WS_ST + srow * 8), ((u64)__float_as_uint(ve) << 32) | __float_as_uint(mean), __ATOMIC_RELAXED, __HIP_MEMORY_SCOPE_AGENT);
    }
  }
  asm volatile("" : "+s"(role));
  if (__builtin_expect(role == 4, 1)) { loader(p, rt, g, j, w - 8, lane, lds0, tag); return; }
  if (idx < 30 && w >= 10) { const Params q = reload_params(); helper(q, rt, g, j, w - 10, lane, lds0, tag); return; }
  if (idx >= 30) { const Params q = reload_params(); spare(q, sid, lds0, tag, pc0); STAMP(7); return; }
  if (g < 4) { const Params q = reload_params(); ffn_compute(q, rt, g, j, w, lane, lds0, tag); }
  else { const Params q = reload_params(); ssm_compute(q, rt, j, w, lane, lds0, tag); }
}

extern "C" void kernel_launch(void* const* d_in, const int* in_sizes, int n_in, void* d_out, int out_size, void* d_ws, size_t ws_size, hipStream_t stream) {
  Params p;
  p.x = (const u16*)d_in[0]; p.lre = (const u16*)d_in[1]; p.lim = (const u16*)d_in[2]; p.bre = (const u16*)d_in[3]; p.bim = (const u16*)d_in[4];
  p.cre = (const u16*)d_in[5]; p.cim = (const u16*)d_in[6]; p.logdt = (const u16*)d_in[7]; p.dskip = (const u16*)d_in[8];
  p.w1 = (const u16*)d_in[10]; p.b1 = (const u16*)d_in[11]; p.w2 = (const u16*)d_in[12]; p.b2 = (const u16*)d_in[13];
  p.out = (float*)d_out; p.ws = (char*)d_ws;
  (void)hipFuncSetAttribute((const void*)fused_kernel, hipFuncAttributeMaxDynamicSharedMemorySize, LDS_BYTES);
  fused_kernel<<<256, 768, LDS_BYTES, stream>>>(p);
}
```

```cpp
#include <hip/hip_runtime.h>
#include <utility>

#define LAS __attribute__((address_space(3)))
#define GAS __attribute__((address_space(1)))
#define DI __device__ __forceinline__
typedef unsigned short u16;
typedef unsigned u32;
typedef unsigned long long u64;
typedef unsigned u32x4 __attribute__((ext_vector_type(4)));
typedef unsigned u32x2 __attribute__((ext_vector_type(2)));
typedef short s16x4 __attribute__((ext_vector_type(4)));
typedef short bf16x8 __attribute__((ext_vector_type(8)));
typedef float f32x4 __attribute__((ext_vector_type(4)));
typedef float f32x2 __attribute__((ext_vector_type(2)));
typedef __bf16 bf16x2 __attribute__((ext_vector_type(2)));
typedef __amdgpu_buffer_rsrc_t rsrc_t;

constexpr int S = 2048, D = 576, F = 4608;
constexpr int ROWB = D * 2;
constexpr int WSLOT = 24576, TOFF = 4 * WSLOT, TSLOT = 16384, LDS_BYTES = 163840;
constexpr int AX_W = 0, AX_T = 6656, AX_S = 8704, AX_TAB = 10752;
constexpr u32 WS_FLAGS = 0;
constexpr u32 FL_GO = 0, FL_P = 32, FL_O = 32 + 240, NFLAG = 32 + 480;
constexpr u32 WS_EG = NFLAG * 128;
constexpr u32 WS_ST = WS_EG + 16 * 576 * 16;
constexpr u32 WS_REQ = WS_ST + 3 * 12288, WS_RSP = WS_REQ + 1024, WS_ACK = WS_RSP + 1024;
constexpr u32 WS_CS = 256 * 1024;
constexpr u32 WS_TAB = WS_CS + 15 * 16 * 1536;
constexpr u32 WS_P = 1u << 20;
constexpr u32 XBW = 5 * 1536;
constexpr u32 WS_XB = WS_P + 240 * 98304;
constexpr u32 WS_END = WS_XB + 48 * 32 * XBW;
constexpr int CODE_LINES = 430;
constexpr u32 SPIN_CAP = 1u << 18;

struct Params {
  const u16 *x, *lre, *lim, *bre, *bim, *cre, *cim, *logdt, *dskip, *w1, *b1, *w2, *b2;
  float* out; char* ws;
};

DI Params reload_params() {
  Params q;
#if defined(__HIP_DEVICE_COMPILE__)
  typedef __attribute__((address_space(4))) const Params* KP;
  KP kp = (KP)__builtin_amdgcn_kernarg_segment_ptr(); asm volatile("" : "+s"(kp)); q = *kp;
#endif
  return q;
}
template <int N> DI void waitv() { asm volatile("s_waitcnt vmcnt(%0)" :: "n"(N) : "memory"); }
#define WAITL() asm volatile("s_waitcnt lgkmcnt(0)" ::: "memory")
#define BAR() do { asm volatile("" ::: "memory"); __builtin_amdgcn_s_barrier(); asm volatile("" ::: "memory"); } while (0)
#define SB() __builtin_amdgcn_sched_barrier(0)
#define MFENCE() do { SB(); asm volatile("s_nop 7\n\ts_nop 7"); SB(); } while (0)
#define MMA(c, a, b) asm volatile("v_mfma_f32_16x16x32_bf16 %0, %1, %2, %0" : "+v"(c) : "v"(a), "v"(b))
#define PIN(x) asm volatile("" : "+v"(x))
#define STAMPPTR nullptr
#define STAMP(i)

DI void dma(rsrc_t r, u32 lds, u32 voff, u32 soff) {
  asm volatile("s_mov_b32 m0, %0\n\ts_nop 0\n\tbuffer_load_dwordx4 %1, %2, %3 offen lds" :: "s"(lds), "v"(voff), "s"(r), "s"(soff) : "memory");
}
DI void dma_sc1(rsrc_t r, u32 lds, u32 voff, u32 soff) {
  asm volatile("s_mov_b32 m0, %0\n\ts_nop 0\n\tbuffer_load_dwordx4 %1, %2, %3 offen sc1 lds" :: "s"(lds), "v"(voff), "s"(r), "s"(soff) : "memory");
}
DI void dma12(rsrc_t r, u32 lds, u32 voff, u32 so, u32 d2, u32 st, u32 st3) {
  u32 t;
  asm volatile("s_mov_b32 m0, %2\n\t"
               "s_add_u32 %1, %0, %5\n\t"
               "buffer_load_dwordx4 %3, %4, %0 offen lds\n\t"
               "s_add_u32 m0, m0, 0x800\n\t"
               "s_add_u32 %0, %0, %6\n\t"
               "buffer_load_dwordx4 %3, %4, %1 offen lds\n\t"
               "s_add_u32 m0, m0, 0x800\n\t"
               "s_add_u32 %1, %1, %6\n\t"
               "buffer_load_dwordx4 %3, %4, %0 offen lds\n\t"
               "s_add_u32 m0, m0, 0x800\n\t"
               "s_add_u32 %0, %0, %6\n\t"
               "buffer_load_dwordx4 %3, %4, %1 offen lds\n\t"
               "s_add_u32 m0, m0, 0x800\n\t"
               "s_add_u32 %1, %1, %6\n\t"
               "buffer_load_dwordx4 %3, %4, %0 offen lds\n\t"
               "s_add_u32 m0, m0, 0x800\n\t"
               "s_add_u32 %0, %0, %7\n\t"
               "buffer_load_dwordx4 %3, %4, %1 offen lds\n\t"
               "s_add_u32 m0, m0, 0x800\n\t"
               "s_add_u32 %1, %1, %7\n\t"
               "buffer_load_dwordx4 %3, %4, %0 offen lds\n\t"
               "s_add_u32 m0, m0, 0x800\n\t"
               "s_add_u32 %0, %0, %6\n\t"
               "buffer_load_dwordx4 %3, %4, %1 offen lds\n\t"
               "s_add_u32 m0, m0, 0x800\n\t"
               "s_add_u32 %1, %1, %6\n\t"
               "buffer_load_dwordx4 %3, %4, %0 offen lds\n\t"
               "s_add_u32 m0, m0, 0x800\n\t"
               "s_add_u32 %0, %0, %6\n\t"
               "buffer_load_dwordx4 %3, %4, %1 offen lds\n\t"
               "s_add_u32 m0, m0, 0x800\n\t"
               "s_add_u32 %1, %1, %6\n\t"
               "buffer_load_dwordx4 %3, %4, %0 offen lds\n\t"
               "s_add_u32 m0, m0, 0x800\n\t"
               "s_add_u32 %0, %0, %6\n\t"
               "buffer_load_dwordx4 %3, %4, %1 offen lds"
               : "+&s"(so), "=&s"(t) : "s"(lds), "v"(voff), "s"(r), "s"(d2), "s"(st), "s"(st3) : "memory", "scc");
}
DI void dma4(rsrc_t r, u32 lds, u32 voff, u32 so, u32 st) {
  asm volatile("s_mov_b32 m0, %1\n\t"
               "s_nop 0\n\t"
               "buffer_load_dwordx4 %2, %3, %0 offen lds\n\t"
               "s_add_u32 m0, m0, 0x800\n\t"
               "s_add_u32 %0, %0, %4\n\t"
               "buffer_load_dwordx4 %2, %3, %0 offen lds\n\t"
               "s_add_u32 m0, m0, 0x800\n\t"
               "s_add_u32 %0, %0, %4\n\t"
               "buffer_load_dwordx4 %2, %3, %0 offen lds\n\t"
               "s_add_u32 m0, m0, 0x800\n\t"
               "s_add_u32 %0, %0, %4\n\t"
               "buffer_load_dwordx4 %2, %3, %0 offen lds"
               : "+&s"(so) : "s"(lds), "v"(voff), "s"(r), "s"(st) : "memory", "scc");
}
DI void dma8lin(rsrc_t r, u32 lds, u32 voff, u32 so) {
  asm volatile("s_mov_b32 m0, %1\n\t"
               "s_nop 0\n\t"
               "buffer_load_dwordx4 %2, %3, %0 offen offset:0 sc1 lds\n\t"
               "buffer_load_dwordx4 %2, %3, %0 offen offset:1024 sc1 lds\n\t"
               "buffer_load_dwordx4 %2, %3, %0 offen offset:2048 sc1 lds\n\t"
               "buffer_load_dwordx4 %2, %3, %0 offen offset:3072 sc1 lds\n\t"
               "s_add_u32 m0, m0, 0x2000\n\t"
               "s_add_u32 %0, %0, 0x2000\n\t"
               "buffer_load_dwordx4 %2, %3, %0 offen offset:0 sc1 lds\n\t"
               "buffer_load_dwordx4 %2, %3, %0 offen offset:1024 sc1 lds\n\t"
               "buffer_load_dwordx4 %2, %3, %0 offen offset:2048 sc1 lds\n\t"
               "buffer_load_dwordx4 %2, %3, %0 offen offset:3072 sc1 lds"
               : "+&s"(so) : "s"(lds), "v"(voff), "s"(r) : "memory", "scc");
}
DI rsrc_t mk(const void* ptr, u32 bytes) { return __builtin_amdgcn_make_buffer_rsrc((void*)ptr, 0, bytes, 0x00020000); }
DI s16x4 rd_tr1(u32 a, int off) { return __builtin_amdgcn_ds_read_tr16_b64_v4i16((LAS s16x4*)(size_t)(a + off)); }
DI bf16x8 cat(s16x4 lo, s16x4 hi) { return __builtin_shufflevector(lo, hi, 0, 1, 2, 3, 4, 5, 6, 7); }
DI bf16x8 rd_tr(u32 a, int off) { return cat(rd_tr1(a, off), rd_tr1(a, off + 2048)); }
DI bf16x8 rd128(u32 a, int off) { return *(LAS bf16x8*)(size_t)(a + off); }
DI f32x4 rdf4(u32 a, int off) { return *(LAS f32x4*)(size_t)(a + off); }
DI float rdf(u32 a, int off) { return *(LAS float*)(size_t)(a + off); }
DI void wr128(u32 a, int off, u32x4 v) { *(LAS u32x4*)(size_t)(a + off) = v; }
DI u32 cvtpk(float lo, float hi) { f32x2 v = {lo, hi}; bf16x2 b = __builtin_convertvector(v, bf16x2); return __builtin_bit_cast(u32, b); }
DI float bflo(u32 v) { return __uint_as_float(v << 16); }
DI float bfhi(u32 v) { return __uint_as_float(v & 0xffff0000u); }
DI float bf2f(u16 v) { return __uint_as_float((u32)v << 16); }
DI float dot2(u32 a, u32 b, float c) { return __builtin_amdgcn_fdot2_f32_bf16(__builtin_bit_cast(bf16x2, a), __builtin_bit_cast(bf16x2, b), c, false); }
constexpr u32 ONES = 0x3f803f80u;
DI float rbf(float v) { return bflo(cvtpk(v, 0.f)); }
DI void split3(float v, float& a, float& b, float& c) { a = rbf(v); const float r = v - a; b = rbf(r); c = rbf(r - b); }
template <int C> DI float dppx(float v) { return __uint_as_float(__builtin_amdgcn_update_dpp(0u, __float_as_uint(v), C, 0xf, 0xf, true)); }
template <int C> DI double dppd(double v) { u32x2 t = __builtin_bit_cast(u32x2, v); t[0] = __builtin_amdgcn_update_dpp(0u, t[0], C, 0xf, 0xf, true); t[1] = __builtin_amdgcn_update_dpp(0u, t[1], C, 0xf, 0xf, true); return __builtin_bit_cast(double, t); }
DI void st16(u32x4 v, rsrc_t r, u32 voff, u32 soff) { asm volatile("buffer_store_dwordx4 %0, %1, %2, %3 offen sc1\n\ts_nop 1" :: "v"(v), "v"(voff), "s"(r), "s"(soff) : "memory"); }
DI void st_flag(char* ws, u32 idx, u32 tag) { __hip_atomic_store((GAS u32*)(ws + WS_FLAGS + idx * 128), tag, __ATOMIC_RELAXED, __HIP_MEMORY_SCOPE_AGENT); }
DI u32 ld_flag(const char* a) { return __hip_atomic_load((GAS u32*)a, __ATOMIC_RELAXED, __HIP_MEMORY_SCOPE_AGENT); }
DI void spin_all(const char* a, u32 tag) {
#pragma nounroll
  for (u32 i = 0; i < SPIN_CAP; i++) { if (__all(ld_flag(a) == tag)) break; __builtin_amdgcn_s_sleep(1); }
  asm volatile("" ::: "memory");
}
DI void spin_ge(const char* a, u32 lvl) {
#pragma nounroll
  for (u32 i = 0; i < SPIN_CAP; i++) { if (__all(ld_flag(a) >= lvl)) break; __builtin_amdgcn_s_sleep(1); }
  asm volatile("" ::: "memory");
}
DI void st_word(char* a, u32 v) { __hip_atomic_store((GAS u32*)a, v, __ATOMIC_RELAXED, __HIP_MEMORY_SCOPE_AGENT); }
DI void hs_leader(char* ws, int lane, u32 lds0, u64* stamps) {
  const rsrc_t r = mk(ws, WS_END);
  bool first = true; u32x4 last = {0u, 0u, 0u, 0u};
#pragma nounroll
  for (u32 i = 0; i < SPIN_CAP; i++) {
    asm volatile("" ::: "memory");
    const u32x4 rq = __builtin_amdgcn_raw_buffer_load_b128(r, lane * 16, WS_REQ, 16), ak = __builtin_amdgcn_raw_buffer_load_b128(r, lane * 16, WS_ACK, 16);
    if (first || rq[0] != last[0] || rq[1] != last[1] || rq[2] != last[2] || rq[3] != last[3]) st16(rq, r, lane * 16, WS_RSP);
    first = false; last = rq;
    if (__all(ak[0] == 1u && ak[1] == 1u && ak[2] == 1u && (ak[3] == 1u || lane == 63))) break;
    __builtin_amdgcn_s_sleep(1);
  }
  BAR();
  st_flag(ws, FL_GO, 1u);
}
DI float wave_sum(float v) {
  v += dppx<0xb1>(v); v += dppx<0x4e>(v); v += dppx<0x141>(v); v += dppx<0x140>(v);
  const u32 b = __float_as_uint(v);
  return (__uint_as_float(__builtin_amdgcn_readlane(b, 0)) + __uint_as_float(__builtin_amdgcn_readlane(b, 16))) + (__uint_as_float(__builtin_amdgcn_readlane(b, 32)) + __uint_as_float(__builtin_amdgcn_readlane(b, 48)));
}
DI void row_stats(const u16* row, int lane, float& mean, float& ve) {
  const u32x4 a = *((const GAS u32x4*)row + lane), b = *((const GAS u32x4*)row + 64 + (lane & 7));
  float s = 0.f, t = 0.f;
#pragma unroll
  for (int c = 0; c < 4; c++) { s += bflo(a[c]) + bfhi(a[c]); t += bflo(b[c]) + bfhi(b[c]); }
  mean = wave_sum(lane < 8 ? s + t : s) * (1.f / D);
  s = 0.f; t = 0.f;
#pragma unroll
  for (int c = 0; c < 4; c++) {
    const float a0 = bflo(a[c]) - mean, a1 = bfhi(a[c]) - mean, b0 = bflo(b[c]) - mean, b1 = bfhi(b[c]) - mean;
    s += a0 * a0 + a1 * a1; t += b0 * b0 + b1 * b1;
  }
  ve = wave_sum(lane < 8 ? s + t : s) * (1.f / D) + 1e-5f;
}

DI f32x2 gelu2(f32x2 a, float k1, float k2, float sg) {
  f32x2 t = a * a; t = t * k2 + k1; t = t * a;
  t.x = __builtin_amdgcn_exp2f(t.x); t.y = __builtin_amdgcn_exp2f(t.y);
  t = t * sg + sg;
  t.x = __builtin_amdgcn_rcpf(t.x); t.y = __builtin_amdgcn_rcpf(t.y);
  return t * a;
}
DI u32x4 gelu_frag(f32x4 a, f32x4 b, float k1, float k2, float sg) {
  f32x2 g0 = gelu2((f32x2){a[0], a[1]}, k1, k2, sg), g1 = gelu2((f32x2){a[2], a[3]}, k1, k2, sg);
  f32x2 g2 = gelu2((f32x2){b[0], b[1]}, k1, k2, sg), g3 = gelu2((f32x2){b[2], b[3]}, k1, k2, sg);
  return (u32x4){cvtpk(g0.x, g0.y), cvtpk(g1.x, g1.y), cvtpk(g2.x, g2.y), cvtpk(g3.x, g3.y)};
}

template <int H, class UPD>
DI void stepA(f32x4 (&acc)[6][4], bf16x8 (&wp)[2], bf16x8 (&t)[4], const u32 (&wc)[4], const u32& n0, const u32& n1, const u32& xn, UPD&& upd) {
  bf16x8 w[8], nx[4];
  w[0] = wp[0]; w[1] = wp[1];
#pragma unroll
  for (int n = 0; n < 6; n++) {
    const int f = n + 2, ff = f < 6 ? f : f - 6;
    const u32 a = (f < 6 || H == 0) ? wc[ff & 3] : (ff ? n1 : n0); const int off = (f < 6 ? H : 1 - H) * WSLOT + (ff >> 2) * 4096;
    MMA(acc[n][0], w[n], t[0]); SB();
    s16x4 lo = rd_tr1(a, off); SB();
    MMA(acc[n][1], w[n], t[1]); SB();
    s16x4 hi = rd_tr1(a, off + 2048); SB();
    MMA(acc[n][2], w[n], t[2]); SB();
    if (n >= 4) { nx[2 * n - 8] = rd128(xn, (2 * n - 8) * 2048); SB(); }
    if (n == 1) { upd(); SB(); }
    MMA(acc[n][3], w[n], t[3]); SB();
    if (n >= 4) { nx[2 * n - 7] = rd128(xn, (2 * n - 7) * 2048); SB(); }
    if (n == 3) { BAR(); SB(); }
    w[f] = cat(lo, hi);
  }
  wp[0] = w[6]; wp[1] = w[7];
#pragma unroll
  for (int i = 0; i < 4; i++) t[i] = nx[i];
}

template <int KIND, class FW, class FT, class FN, class MID>
DI void half(f32x4 (&acc)[3][4], bf16x8 (&w)[3], bf16x8 (&tp)[2], FW&& rw, FT&& rt_, FN&& rn, MID&& mid) {
  bf16x8 t[4], wn[3], tn[2];
  t[0] = tp[0]; t[1] = tp[1];
  s16x4 lo;
#pragma unroll
  for (int i = 0; i < 12; i++) {
    const int T = i / 3, n = i % 3;
    if constexpr (KIND == 2) MMA(acc[n][T], t[T], w[n]); else MMA(acc[n][T], w[n], t[T]);
    SB();
    if constexpr (KIND == 0) {
      if (i == 0) t[2] = rt_(2, 0); if (i == 1) t[3] = rt_(3, 0);
      if (i >= 2 && i <= 7) { if ((i & 1) == 0) lo = rw((i - 2) >> 1, 0); else wn[(i - 2) >> 1] = cat(lo, rw((i - 2) >> 1, 1)); }
      if (i == 8) tn[0] = rn(0, 0); if (i == 9) tn[1] = rn(1, 0);
    } else if constexpr (KIND == 1) {
      if (i <= 3) { if ((i & 1) == 0) lo = rt_(2 + (i >> 1), 0); else t[2 + (i >> 1)] = cat(lo, rt_(2 + (i >> 1), 1)); }
      if (i >= 4 && i <= 6) wn[i - 4] = rw(i - 4, 0);
      if (i >= 7 && i <= 10) { if ((i & 1) == 1) lo = rn((i - 7) >> 1, 0); else tn[(i - 7) >> 1] = cat(lo, rn((i - 7) >> 1, 1)); }
    } else {
      if (i == 0) t[2] = rt_(2, 0); if (i == 2) t[3] = rt_(3, 0);
      if (i == 3 || i == 5 || i == 6) wn[i == 3 ? 0 : i - 4] = rw(i == 3 ? 0 : i - 4, 0);
      if (i == 8) tn[0] = rn(0, 0); if (i == 9) tn[1] = rn(1, 0);
    }
    mid(i);
    SB();
  }
#pragma unroll
  for (int i = 0; i < 3; i++) w[i] = wn[i];
  tp[0] = tn[0]; tp[1] = tn[1];
}

template <int I, int N, class FN> DI void sfor(FN&& f) { if constexpr (I < N) { f(std::integral_constant<int, I>{}); sfor<I + 1, N>(f); } }

DI void ssm_carry(const Params& p, int rt, int j, int c, u32 lds0, u32 tag) {
  const u32 ax = lds0 + (18 & 3) * WSLOT, scr = lds0 + TOFF + (22 & 3) * TSLOT;
  {
    f32x4 pw = rdf4(ax + AX_TAB + c * 32, 16);
    { const float t = pw[0] * pw[0] - pw[1] * pw[1]; pw[1] = 2.f * pw[0] * pw[1]; pw[0] = t; }
    f32x2 es[8];
#pragma unroll
    for (int s = 0; s < 8; s++) es[s] = *(LAS f32x2*)(size_t)(scr + (s * 192 + c) * 8);
    float sr = 0.f, si = 0.f;
#pragma unroll
    for (int s = 0; s < 8; s++) { const float nr = pw[0] * sr - pw[1] * si + es[s].x, ni = pw[0] * si + pw[1] * sr + es[s].y; sr = nr; si = ni; }
    const rsrc_t rE = mk(p.ws + WS_EG, 16 * 9216);
    const int vo = (192 * j + c) * 16 + rt * 9216;
    st16((u32x4){__float_as_uint(sr), tag, __float_as_uint(si), tag}, rE, vo, 0);
    u32x4 v[15];
#pragma nounroll
    for (u32 spin = 0; spin < SPIN_CAP; spin++) {
      asm volatile("" ::: "memory");
      bool ok = true;
#pragma unroll
      for (int i = 0; i < 15; i++) v[i] = __builtin_amdgcn_raw_buffer_load_b128(rE, vo - (15 - i) * 9216, 0, 16);
#pragma unroll
      for (int i = 0; i < 15; i++) ok = ok && ((v[i][1] == tag && v[i][3] == tag) || vo < (15 - i) * 9216);
      if (__all(ok)) break;
    }
    float cr = 0.f, ci = 0.f;
#pragma unroll
    for (int i = 0; i < 15; i++) {
      const float nr = pw[2] * cr - pw[3] * ci + __uint_as_float(v[i][0]), ni = pw[2] * ci + pw[3] * cr + __uint_as_float(v[i][2]); cr = nr; ci = ni;
    }
#pragma unroll
    for (int s = 0; s < 8; s++) {
      *(LAS f32x2*)(size_t)(scr + (s * 192 + c) * 8) = (f32x2){cr, ci};
      const float nr = pw[0] * cr - pw[1] * ci + es[s].x, ni = pw[0] * ci + pw[1] * cr + es[s].y; cr = nr; ci = ni;
    }
  }
}

#define LBAR() BAR()
DI void loader(const Params& p0, int rt, int g, int j, int u, int lane, u32 lds0, u32 tag) {
  Params p = p0;
  const bool ssm = g == 4;
  const u32 wbytes = ssm ? D * ROWB : D * F * 2;
  const rsrc_t rX = mk(p.x, S * ROWB), rA0 = mk(ssm ? p.bre : p.w1, wbytes), rA1 = mk(ssm ? p.bim : p.w1, wbytes);
  const int r8 = lane >> 3, gp = lane & 7;
  const int cgr = ((((gp >> 1) ^ ((r8 >> 1) & 3)) << 1) | (gp & 1));
  const int rgr = gp ^ (4 * u + (r8 >> 1));
  const int slice = 3 * g + j;
  const int kA = 16 * u + 8 * (r8 >> 2) + (r8 & 3);
  const int R = 8 * u + r8;
  const u32 voffA = ssm ? (192 * j + R) * ROWB + rgr * 16 : kA * (F * 2) + slice * 768 + cgr * 16;
  const int tok = ssm ? 16 * ((R >> 2) & 3) + 8 * (R & 1) + ((R >> 1) & 1) : R;
  const u32 voffX = (rt * 128 + tok) * ROWB + rgr * 16;
  const u32 ldsW = lds0 + u * 1024, ldsT = lds0 + TOFF;
  const u32 aKt = ssm ? 128 : 64 * F * 2, aPar = ssm ? 0 : 32 * F * 2, pstr = ssm ? 32 * ROWB : 128, a2 = ssm ? 16 * ROWB : 4 * F * 2, xstr = (ssm ? 2 : 16) * ROWB;
  const int rot = rt >= 9 ? rt - 9 : rt;
  auto ktile = [&](int kap) { int kt = kap + rot; return kt >= 9 ? kt - 9 : kt; };
  auto issueA = [&](int kap, int h) { dma12(h ? rA1 : rA0, ldsW + ((2 * kap + h) & 3) * WSLOT, voffA, ktile(kap) * aKt + h * aPar, a2, pstr, pstr); };
  auto issueX = [&](int kap, int h) { dma4(rX, ldsT + (kap & 3) * TSLOT + h * 8192 + u * 1024, voffX, h * 64 * ROWB + ktile(kap) * 128, xstr); };
  issueA(0, 0); issueX(0, 0); issueX(0, 1); issueA(0, 1);
  __builtin_amdgcn_s_setprio(3);
  waitv<12>(); BAR();
  issueX(1, 0); issueX(1, 1); issueA(1, 0);
#pragma nounroll
  for (int kap = 0; kap < 7; kap++) {
    u32 l1 = ldsW + ((2 * kap + 3) & 3) * WSLOT, s1 = ktile(kap + 1) * aKt + aPar, l2 = ldsW + ((2 * kap) & 3) * WSLOT, s2 = ktile(kap + 2) * aKt;
    u32 lx = ldsT + ((kap + 2) & 3) * TSLOT + u * 1024, lx2 = lx + 8192, sx = ktile(kap + 2) * 128, sx2 = sx + 64 * ROWB;
    asm volatile("" : "+s"(l1), "+s"(s1), "+s"(l2), "+s"(s2), "+s"(lx), "+s"(lx2), "+s"(sx), "+s"(sx2));
    waitv<16>(); BAR(); dma12(rA1, l1, voffA, s1, a2, pstr, pstr); dma4(rX, lx, voffX, sx, xstr);
    waitv<16>(); BAR(); dma12(rA0, l2, voffA, s2, a2, pstr, pstr); dma4(rX, lx2, voffX, sx2, xstr);
  }
  waitv<16>(); BAR(); issueA(8, 1);
  p = reload_params();
  const u32 voffL = lane * 16;
  const rsrc_t rWS = mk(p.ws, WS_END), rB0 = mk(ssm ? p.cre : p.w2, wbytes), rB1 = mk(ssm ? p.cim : p.w2, wbytes);
  const u32 voffB = ssm ? voffA : R * ROWB + j * 384 + cgr * 16;
  const u32 bJ = ssm ? 384 : 384 * ROWB, bPar = ssm ? 0 : 192 * ROWB, bI = ssm ? 128 : 32 * ROWB, b0 = ssm ? 0 : g * 1152 * ROWB;
  const int j1 = j == 2 ? 0 : j + 1, j2 = j == 0 ? 2 : j - 1;
  const u32 st3 = 96 * ROWB - 2 * pstr, pb = WS_P + (rt * 15 + 3 * g) * 98304 + u * 4096;
  auto issueB = [&](int s, int jj, int i) { dma12((i & 1) ? rB1 : rB0, ldsW + (s & 3) * WSLOT, voffB, b0 + jj * bJ + (i & 1) * bPar + (i >> 1) * bI, 16 * ROWB, pstr, st3); };
  auto issueP = [&](int s, int jj, int i) { dma8lin(rWS, ldsT + (s & 3) * TSLOT + u * 4096, voffL, pb + jj * 98304 + i * 16384); };
  waitv<12>(); BAR();
#pragma unroll
  for (int k = 0; k < 3; k++) dma_sc1(rWS, lds0 + (18 & 3) * WSLOT + AX_TAB + (u * 3 + k) * 1024, voffL, WS_TAB + j * 6144 + (u * 3 + k) * 1024);
  waitv<3>();
  BAR(); issueB(19, j, 0);
  BAR(); issueB(20, j, 1); waitv<12>();
  BAR(); issueB(21, j, 2); waitv<12>();
  if (ssm) { BAR(); if (u == 1) ssm_carry(p, rt, j, lane, lds0, tag); WAITL(); BAR(); BAR(); }
  BAR(); issueB(22, j, 3); waitv<12>();
  BAR(); issueB(23, j, 4); issueP(23, j, 4); waitv<20>();
  BAR(); issueB(24, j, 5); issueP(24, j, 5);
#pragma nounroll
  for (int n = 0; n < 3; n++) {
    const int i = n ? 2 * n - 2 : 4;
    u32 w1 = b0 + j1 * bJ + (i >> 1) * bI, w2 = b0 + j2 * bJ + (i >> 1) * bI, w3 = w1 + bPar, w4 = w2 + bPar;
    u32 p1 = pb + j1 * 98304 + i * 16384, p2 = pb + j2 * 98304 + i * 16384, p3 = p1 + 16384, p4 = p2 + 16384;
    asm volatile("" : "+s"(w1), "+s"(w2), "+s"(w3), "+s"(w4), "+s"(p1), "+s"(p2), "+s"(p3), "+s"(p4));
    waitv<20>(); BAR(); dma12(rB0, ldsW + 1 * WSLOT, voffB, w1, 16 * ROWB, pstr, st3); dma8lin(rWS, ldsT + 1 * TSLOT + u * 4096, voffL, p1);
    waitv<20>(); BAR(); dma12(rB0, ldsW + 2 * WSLOT, voffB, w2, 16 * ROWB, pstr, st3); dma8lin(rWS, ldsT + 2 * TSLOT + u * 4096, voffL, p2);
    waitv<20>(); BAR(); dma12(rB1, ldsW + 3 * WSLOT, voffB, w3, 16 * ROWB, pstr, st3); dma8lin(rWS, ldsT + 3 * TSLOT + u * 4096, voffL, p3);
    waitv<20>(); BAR(); dma12(rB1, ldsW + 0 * WSLOT, voffB, w4, 16 * ROWB, pstr, st3); dma8lin(rWS, ldsT + 0 * TSLOT + u * 4096, voffL, p4);
  }
  waitv<20>();
  BAR();
  {
    const u32 l = lds0 + WSLOT;
    const u32 vo = (lane & 31) < 24 ? (rt * 128 + u * 64 + (g & 3) * 16 + (lane >> 5)) * ROWB + j * 384 + (lane & 31) * 16 : 0x7fffffffu;
#pragma unroll
    for (int k = 0; k < 8; k++) dma(rX, l + (u * 8 + k) * 1024, vo, k * 2 * ROWB);
    dma(mk(u ? p.dskip : p.b2, ROWB), l + 16384 + u * 1024, voffL + j * 384, 0);
  }
  waitv<9>(); BAR(); BAR(); waitv<0>();
  BAR();
}

DI void helper(const Params& p, int rt, int g, int j, int h, int lane, u32 lds0, u32 tag) {
  const bool ssm = g == 4;
  const int slice = 3 * g + j, q = lane >> 4, jl = lane & 15;
  const rsrc_t rWS = mk(p.ws, WS_END);
  char* const hsR = p.ws + WS_RSP + blockIdx.x * 4;
  u32 hsn = 0;
  if (h == 0) { hsn = __builtin_amdgcn_readfirstlane(ld_flag(hsR)) + 1u; st_word(hsR - 1024, hsn); }
  __builtin_amdgcn_s_setprio(2);
  {
    const int e = (lane >> 2) & 3, z = 2 * (q & 1) + (e >> 1);
    bf16x8 f[12]; f32x4 c[12]; u32x4 one = {ONES, ONES, ONES, ONES};
    auto rdslot = [&](int sl) {
      if (ssm) {
        const u32 a = lds0 + h * 12288 + jl * 128 + ((q ^ (jl >> 1)) * 16);
#pragma unroll
        for (int n = 0; n < 6; n++) { f[n] = rd128(a, sl * WSLOT + n * 2048); f[6 + n] = rd128(a ^ 64, sl * WSLOT + n * 2048); }
      } else {
#pragma unroll
        for (int n = 0; n < 12; n++) f[n] = rd_tr(lds0 + h * 12288 + (4 * q + e) * 128 + (((n & 3) ^ z) * 32) + (lane & 3) * 8, sl * WSLOT + (n >> 2) * 4096);
      }
#pragma unroll
      for (int n = 0; n < 12; n++) PIN(f[n]);
      asm volatile("s_nop 1");
    };
#pragma unroll
    for (int n = 0; n < 12; n++) { c[n] = (f32x4){0.f, 0.f, 0.f, 0.f}; PIN(c[n]); }
    PIN(one);
    LBAR();
    rdslot(0);
    if (ssm) {
#pragma unroll
      for (int n = 0; n < 12; n++) MMA(c[n % 6], f[n], one);
      MFENCE();
    } else {
#pragma unroll
      for (int n = 0; n < 12; n++) MMA(c[n], f[n], one);
      MFENCE();
    }
    LBAR();
    rdslot(1);
    if (rt < 9) {
#pragma unroll
      for (int n = 6; n < 12; n++) MMA(c[n], f[n], one);
      MFENCE();
    }
    LBAR();
    if (rt < 9) {
      if (ssm) {
#pragma unroll
        for (int n = 0; n < 6; n++) MMA(c[6 + n], f[n], one);
      MFENCE();
      } else {
#pragma unroll
        for (int n = 0; n < 6; n++) MMA(c[n], f[n], one);
      MFENCE();
      }
    }
    f32x4 m = c[0];
#pragma unroll
    for (int n = 1; n < 12; n++) m = jl == n ? c[n] : m;
    const int col = (ssm ? (jl >= 6 ? 192 - 96 : 0) + 96 * h : 192 * h) + 16 * jl + 4 * q;
    st16(__builtin_bit_cast(u32x4, m), rWS, jl < 12 ? col * 4 : 0x7fffffffu, WS_CS + (slice * 16 + rt) * 1536);
  }
  LBAR();
  u32 pv = ld_flag(hsR);
  LBAR();
  waitv<0>(); LBAR();
  bool acked = h != 0;
#pragma unroll
  for (int s = 0; s < 2; s++) {
    if (!acked) {
      acked = __all(pv == hsn);
      if (!acked && s == 1) { spin_all(hsR, hsn); acked = true; }
      if (acked) st_word(hsR + 1024, 1u);
    }
    if (s < 1) pv = ld_flag(hsR);
    LBAR(); LBAR();
  }
  LBAR();
  const char* fGo = p.ws + WS_FLAGS + FL_GO * 128;
  pv = ld_flag(fGo);
  LBAR(); LBAR();
  if (!__all(pv == 1u)) spin_all(fGo, 1u);
  const int ir = 64 * h + lane, irt = ssm ? (ir & 64) + 16 * ((ir >> 2) & 3) + 8 * (ir & 1) + 2 * ((ir >> 4) & 3) + ((ir >> 1) & 1) : ir;
  const u32x2 sg = __builtin_amdgcn_raw_buffer_load_b64(rWS, (rt * 128 + irt) * 8, WS_ST, 16);
  const int cc = 192 * h + 4 * (lane < 48 ? lane : lane - 48);
  f32x4 cp[9];
#pragma unroll
  for (int k = 0; k < 9; k++) cp[k] = __builtin_bit_cast(f32x4, __builtin_amdgcn_raw_buffer_load_b128(rWS, cc * 4, WS_CS + (slice * 16 + k) * 1536, 16));
  const u32x2 bb = *(const GAS u32x2*)(p.b1 + (ssm ? 0 : slice * 384) + cc);
  LBAR(); LBAR(); SB();
  f32x4 cs = ((cp[0] + cp[1]) + (cp[2] + cp[3])) + ((cp[4] + cp[5]) + (cp[6] + cp[7])) + cp[8];
  float mean = __uint_as_float(sg[0]), rstd = __builtin_amdgcn_rsqf(__uint_as_float(sg[1])), sig = __uint_as_float(sg[1]) * rstd;
  u32x4 at, aw[4];
  { float m1, m2, m3; split3(-mean, m1, m2, m3); const float g1 = rbf(sig), g2 = sig - g1;
    at = (u32x4){cvtpk(m1, m1), cvtpk(m2, m2), cvtpk(m1, m3), cvtpk(g1, g2)}; mean = ssm ? -mean * rstd : mean; }
  PIN(cs); PIN(at); PIN(mean); PIN(rstd); PIN(sig);
  auto auxw = [&](int k) { float c1, c2, c3; split3(cs[k], c1, c2, c3); const float b = (k & 1) ? bfhi(bb[k >> 1]) : bflo(bb[k >> 1]);
    aw[k] = (u32x4){cvtpk(c1, c2), cvtpk(c1, c2), cvtpk(c3, c1), cvtpk(b, b)}; PIN(aw[k]); };
  LBAR(); SB();
  auxw(0); auxw(1);
  const u32 ax = lds0 + (18 & 3) * WSLOT;
  LBAR(); SB();
  wr128(ax + AX_T + ir * 16, 0, at);
  { LAS float* sp = (LAS float*)(size_t)(ax + AX_S + ir * 4); sp[0] = mean; sp[128] = rstd; sp[256] = sig; }
  auxw(2); auxw(3);
  LBAR(); SB();
  if (ssm) *(LAS f32x4*)(size_t)(ax + AX_W + cc * 4) = cs;
  else {
#pragma unroll
    for (int k = 0; k < 4; k++) wr128(ax + AX_W + cc * 4, k * 1600, aw[k]);
  }
  WAITL(); LBAR();
  LBAR(); SB();
  if (ssm) { LBAR(); ssm_carry(p, rt, j, 64 + 64 * h + lane, lds0, tag); WAITL(); LBAR(); BAR(); }
  char* const fMine = p.ws + WS_FLAGS + (FL_P + rt * 15 + slice) * 128;
  const char* fP = p.ws + WS_FLAGS + (FL_P + rt * 15 + 3 * g + ((lane & 1) ? (j == 2 ? 0 : j + 1) : (j == 0 ? 2 : j - 1))) * 128;
  LBAR(); if (h == 1 && !ssm) st_word(fMine, 2u);
  LBAR(); if (h == 1 && ssm) st_word(fMine, 1u);
#pragma nounroll
  for (u32 lv = 1; lv < 4; lv++) {
    LBAR(); if (h == 0) spin_ge(fP, lv);
    LBAR(); if (h == 1 && lv == 1) st_word(fMine, 3u);
    LBAR(); LBAR();
  }
  LBAR(); LBAR(); LBAR(); LBAR();
  LBAR();
}

template <bool SSM>
DI void phaseB(const Params& p, int rt, int g, int j, int w, int lane, u32 lds0, u32 tag) {
  asm volatile("" : "+v"(lane));
  const int rg = w >> 2, cg = w & 3, q = lane >> 4, e = (lane >> 2) & 3, pp = lane & 3, jl = lane & 15;
  const int z = 2 * (q & 1) + (e >> 1);
  u32 wlo[3], whi[3], tb[2];
#pragma unroll
  for (int n = 0; n < 3; n++) {
    const int t = 3 * cg + n;
    wlo[n] = SSM ? lds0 + (16 * t + jl) * 128 + ((q ^ (jl >> 1)) * 16) : lds0 + (t >> 2) * 4096 + (4 * q + e) * 128 + (((t & 3) ^ z) * 32) + pp * 8;
    whi[n] = SSM ? (wlo[n] ^ 64) : wlo[n] + 2 * WSLOT;
  }
  tb[0] = SSM ? lds0 + TOFF + rg * 8192 + (q >> 1) * 2048 + (pp >> 1) * 1024 + (((8 * (q & 1) + e) * 16) ^ ((pp >> 1) * 64)) + 8 * (pp & 1) : lds0 + TOFF + rg * 4096 + lane * 16;
  tb[1] = tb[0] ^ 64;
  float mean = 0.f, rstd = 0.f;
  if constexpr (!SSM) { const u32 sa = lds0 + (18 & 3) * WSLOT + AX_S + (64 * rg + 16 * g + jl) * 4; mean = rdf(sa, 0); rstd = rdf(sa, 512); }
  f32x4 ob[3][4];
#pragma unroll
  for (int n = 0; n < 3; n++)
#pragma unroll
    for (int T = 0; T < 4; T++) { ob[n][T] = (f32x4){0.f, 0.f, 0.f, 0.f}; PIN(ob[n][T]); }
  auto RW = [&](int sl, int kb, int n, int part) {
    if constexpr (SSM) return rd128((kb ? whi[n] : wlo[n]) + (sl >> 1) * 2 * WSLOT, (sl & 1) * WSLOT);
    else return rd_tr1(sl < 2 ? wlo[n] : whi[n], (sl & 1) * WSLOT + kb * 12288 + part * 2048);
  };
  auto RT = [&](int sl, int kb, int T, int part) {
    if constexpr (SSM) return rd_tr1(tb[part], sl * TSLOT + kb * 4096 + T * 256);
    else return rd128(tb[0], sl * TSLOT + kb * 8192 + T * 1024);
  };
  bf16x8 wf[3], tp[2];
  auto step = [&](auto slc, auto&& mid) {
    constexpr int SL = decltype(slc)::value, NS = (SL + 1) & 3;
    half<SSM ? 1 : 0>(ob, wf, tp, [&](int n, int part) { return RW(SL, 1, n, part); }, [&](int T, int part) { return RT(SL, 0, T, part); }, [&](int T, int part) { return RT(SL, 1, T, part); }, mid);
    half<SSM ? 1 : 0>(ob, wf, tp, [&](int n, int part) { return RW(NS, 0, n, part); }, [&](int T, int part) { return RT(SL, 1, T, part); }, [&](int T, int part) { return RT(NS, 0, T, part); }, [](int) {});
  };
  using I0 = std::integral_constant<int, 0>; using I1 = std::integral_constant<int, 1>; using I2 = std::integral_constant<int, 2>; using I3 = std::integral_constant<int, 3>;
  if constexpr (!SSM) waitv<4>();
  WAITL(); BAR();
#pragma unroll
  for (int n = 0; n < 3; n++) { if constexpr (SSM) wf[n] = RW(3, 0, n, 0); else wf[n] = cat(RW(3, 0, n, 0), RW(3, 0, n, 1)); }
#pragma unroll
  for (int T = 0; T < 2; T++) { if constexpr (SSM) tp[T] = cat(RT(3, 0, T, 0), RT(3, 0, T, 1)); else tp[T] = RT(3, 0, T, 0); }
  auto barm = [](int i) { if (i == 11) BAR(); };
  step(I3{}, [](int) {});
  step(I0{}, [&](int i) { if (i == 3) { if constexpr (SSM) waitv<8>(); BAR(); } });
#pragma nounroll
  for (int it = 0; it < 4; it++) { step(I1{}, barm); step(I2{}, [&](int i) { if (i == 11) { waitv<0>(); BAR(); } }); step(I3{}, barm); step(I0{}, barm); }
  MFENCE();
  const rsrc_t rWS = mk(p.ws, WS_END);
  const u32 xb = WS_XB + (rt * 3 + j) * 32 * XBW + w * XBW;
  f32x4 mine[3];
#pragma unroll
  for (int T = 0; T < 4; T++) {
    if (SSM || T != g) {
      const u32 o = xb + T * 8 * XBW + g * 1536;
      const u32x4 a = {cvtpk(ob[0][T][0], ob[0][T][1]), cvtpk(ob[0][T][2], ob[0][T][3]), cvtpk(ob[1][T][0], ob[1][T][1]), cvtpk(ob[1][T][2], ob[1][T][3])};
      const u32 b0 = cvtpk(ob[2][T][0], ob[2][T][1]), b1 = cvtpk(ob[2][T][2], ob[2][T][3]);
      const auto s0 = __builtin_amdgcn_permlane32_swap(b0, b0, false, false), s1 = __builtin_amdgcn_permlane32_swap(b1, b1, false, false);
      st16(a, rWS, lane * 16, o);
      if (lane < 32) st16((u32x4){b0, b1, s0[1], s1[1]}, rWS, lane * 16, o + 1024);
    } else {
#pragma unroll
      for (int n = 0; n < 3; n++) mine[n] = ob[n][T];
    }
  }
  if constexpr (SSM) {
    waitv<0>(); BAR();
    if (threadIdx.x == 0) st_flag(p.ws, FL_O + (rt * 3 + j) * 5 + g, tag);
    STAMP(7);
  } else {
    waitv<0>(); BAR();
    if (threadIdx.x == 0) st_flag(p.ws, FL_O + (rt * 3 + j) * 5 + g, tag);
    const int tok = rt * 128 + 64 * rg + 16 * g + jl, col = 192 * j + 48 * cg + 4 * q;
    u32x2 xv[3], bv[3], dv[3];
#pragma unroll
    for (int n = 0; n < 3; n++) {
      const u32 cb = lds0 + WSLOT + (48 * cg + 4 * q + 16 * n) * 2;
      xv[n] = *(LAS u32x2*)(size_t)(cb + (16 * rg + jl) * 512); bv[n] = *(LAS u32x2*)(size_t)(cb + 16384); dv[n] = *(LAS u32x2*)(size_t)(cb + 17408);
    }
    const u32 o = xb + g * 8 * XBW;
    u32x4 a[4]; u32x2 b[4];
#pragma unroll
    for (int k = 0; k < 4; k++) {
      if (k == 0 || k == 3) spin_all(p.ws + WS_FLAGS + (FL_O + (rt * 3 + j) * 5 + (k ? 4 : (lane & 3))) * 128, tag);
      const int src = k == 3 ? 4 : (k < g ? k : k + 1);
      a[k] = __builtin_amdgcn_raw_buffer_load_b128(rWS, lane * 16, o + src * 1536, 16);
      b[k] = __builtin_amdgcn_raw_buffer_load_b64(rWS, (lane & 31) * 16 + (lane >> 5) * 8, o + src * 1536 + 1024, 16);
    }
#pragma unroll
    for (int n = 0; n < 3; n++) {
      f32x4 r;
#pragma unroll
      for (int c = 0; c < 4; c++) {
        float G = mine[n][c] + ((c & 1) ? bfhi(bv[n][c >> 1]) : bflo(bv[n][c >> 1]));
#pragma unroll
        for (int k = 0; k < 3; k++) { const u32 v = n < 2 ? a[k][2 * n + (c >> 1)] : b[k][c >> 1]; G += (c & 1) ? bfhi(v) : bflo(v); }
        const u32 yv = n < 2 ? a[3][2 * n + (c >> 1)] : b[3][c >> 1];
        const float yy = (c & 1) ? bfhi(yv) : bflo(yv);
        const float xx = (c & 1) ? bfhi(xv[n][c >> 1]) : bflo(xv[n][c >> 1]);
        const float dd = (c & 1) ? bfhi(dv[n][c >> 1]) : bflo(dv[n][c >> 1]);
        const float sv = yy + (xx - mean) * rstd * dd;
        const float sig = __builtin_amdgcn_rcpf(1.f + __builtin_amdgcn_exp2f(-1.4426950408889634f * G));
        r[c] = xx + sv * (G * sig);
      }
      *(GAS f32x4*)(p.out + tok * D + col + 16 * n) = r;
    }
    STAMP(7);
  }
}

DI void ffn_compute(const Params& p, int rt, int g, int j, int w, int lane, u32 lds0, u32 tag) {
  const int rg = w >> 2, cg = w & 3, q = lane >> 4, e = (lane >> 2) & 3, pp = lane & 3, jl = lane & 15;
  const int z = 2 * (q & 1) + (e >> 1);
  u32 wc[4];
#pragma unroll
  for (int n = 0; n < 4; n++) { const int t = 6 * cg + n; wc[n] = lds0 + (t >> 2) * 4096 + (4 * q + e) * 128 + (((t & 3) ^ z) * 32) + pp * 8; }
  const u32 xbase = lds0 + TOFF + (64 * rg + jl) * 128 + ((q ^ (jl >> 1)) * 16);
  f32x4 acc[6][4];
#pragma unroll
  for (int n = 0; n < 6; n++)
#pragma unroll
    for (int T = 0; T < 4; T++) { acc[n][T] = (f32x4){0.f, 0.f, 0.f, 0.f}; asm volatile("" : "+v"(acc[n][T])); }
  bf16x8 wp[2], t[4];
  BAR();
  wp[0] = rd_tr(wc[0], 0); wp[1] = rd_tr(wc[1], 0);
#pragma unroll
  for (int T = 0; T < 4; T++) t[T] = rd128(xbase, T * 2048);
  u32 xs = 0, xn = 0, n0 = 0, n1 = 0; int dw = 2 * WSLOT;
#pragma nounroll
  for (int it = 0; it < 9; it++) {
    stepA<0>(acc, wp, t, wc, n0, n1, xn, [&]() { xn = (xbase ^ 64) + xs; PIN(xn); xs = (xs + TSLOT) & (4 * TSLOT - 1); });
    stepA<1>(acc, wp, t, wc, n0, n1, xn, [&]() { xn = xbase + xs; n0 = wc[0] + dw; n1 = wc[1] + dw; wc[2] += dw; wc[3] += dw; PIN(xn); PIN(n0); PIN(n1); PIN(wc[2]); PIN(wc[3]); waitv<0>(); });
    wc[0] = n0; wc[1] = n1; dw = -dw;
  }
  const u32 ax = lds0 + (18 & 3) * WSLOT;
  float k1[4], k2[4], sg[4];
#pragma unroll
  for (int T = 0; T < 4; T++) {
    u32x4 v = __builtin_bit_cast(u32x4, rd128(ax + AX_T + (64 * rg + jl) * 16, T * 256));
    if (q != 0) v = (u32x4){0u, 0u, 0u, 0u};
    t[T] = __builtin_bit_cast(bf16x8, v);
    const float r = rdf(ax + AX_S + 512 + (64 * rg + jl) * 4, T * 64);
    sg[T] = rdf(ax + AX_S + 1024 + (64 * rg + jl) * 4, T * 64);
    k1[T] = -2.3022081985f * r; k2[T] = -0.10294324f * r * r * r;
  }
  {
    bf16x8 a[6];
#pragma unroll
    for (int n = 0; n < 6; n++) a[n] = rd128(ax + AX_W + ((jl & 3) * 100 + 24 * cg + (jl >> 2)) * 16, n * 64);
    asm volatile("s_nop 1" : "+v"(t[0]), "+v"(t[1]), "+v"(t[2]), "+v"(t[3]));
#pragma unroll
    for (int n = 0; n < 6; n++)
#pragma unroll
      for (int T = 0; T < 4; T++) MMA(acc[n][T], a[n], t[T]);
    MFENCE();
  }
  const rsrc_t rWS = mk(p.ws, WS_END);
  const u32 pg = WS_P + (rt * 15 + 3 * g + j) * 98304 + (cg >> 1) * 16384 + (cg & 1) * 8192 + rg * 4096;
  const u32 pl = lds0 + TOFF + (cg & 1) * 8192 + rg * 4096 + lane * 16, pl0 = pl + ((19 + (cg >> 1)) & 3) * TSLOT, pl1 = pl + ((21 + (cg >> 1)) & 3) * TSLOT;
#pragma unroll
  for (int bb = 0; bb < 3; bb++) {
    const int b = (bb + 2) % 3;
#pragma unroll
    for (int T = 0; T < 4; T++) {
      const u32x4 P = gelu_frag(acc[2 * b][T], acc[2 * b + 1][T], k1[T], k2[T], sg[T]);
      st16(P, rWS, lane * 16, pg + b * 32768 + T * 1024);
      if (b < 2) wr128(b ? pl1 : pl0, T * 1024, P);
      SB();
    }
    if (bb == 0) BAR();
  }
  phaseB<false>(p, rt, g, j, w, lane, lds0, tag);
}

DI void ssm_compute(const Params& p, int rt, int j, int w, int lane, u32 lds0, u32 tag) {
  const int rg = w >> 2, cg = w & 3; int q = lane >> 4, jl = lane & 15;
  u32 wb = lds0 + (16 * cg + jl) * 128 + ((q ^ (jl >> 1)) * 16);
  const u32 xb0 = lds0 + TOFF + (64 * rg + jl) * 128 + ((q ^ (jl >> 1)) * 16);
  f32x4 ar[3][4], ai[3][4];
#pragma unroll
  for (int n = 0; n < 3; n++)
#pragma unroll
    for (int T = 0; T < 4; T++) { ar[n][T] = (f32x4){0.f, 0.f, 0.f, 0.f}; ai[n][T] = (f32x4){0.f, 0.f, 0.f, 0.f}; asm volatile("" : "+v"(ar[n][T]), "+v"(ai[n][T])); }
  bf16x8 wf[3], tp[2];
  auto nop = [](int) {};
  auto barm = [](int i) { if (i == 11) BAR(); };
  BAR();
#pragma unroll
  for (int n = 0; n < 3; n++) wf[n] = rd128(wb, n * 8192);
  tp[0] = rd128(xb0, 0); tp[1] = rd128(xb0, 2048);
  u32 xs = 0; int dw = 2 * WSLOT;
#pragma nounroll
  for (int it = 0; it < 9; it++) {
    const u32 x0 = xb0 + xs, x1 = (xb0 ^ 64) + xs, wb1 = wb ^ 64, wn = wb + dw;
    xs = (xs + TSLOT) & (4 * TSLOT - 1);
    const u32 xn = xb0 + xs;
    half<2>(ar, wf, tp, [&](int n, int) { return rd128(wb1, n * 8192); }, [&](int T, int) { return rd128(x0, T * 2048); }, [&](int T, int) { return rd128(x1, T * 2048); }, barm);
    half<2>(ar, wf, tp, [&](int n, int) { return rd128(wb, WSLOT + n * 8192); }, [&](int T, int) { return rd128(x1, T * 2048); }, [&](int T, int) { return rd128(x0, T * 2048); }, nop);
    half<2>(ai, wf, tp, [&](int n, int) { return rd128(wb1, WSLOT + n * 8192); }, [&](int T, int) { return rd128(x0, T * 2048); }, [&](int T, int) { return rd128(x1, T * 2048); }, barm);
    half<2>(ai, wf, tp, [&](int n, int) { return rd128(wn, n * 8192); }, [&](int T, int) { return rd128(x1, T * 2048); }, [&](int T, int) { return rd128(xn, T * 2048); }, nop);
    wb = wn; dw = -dw;
    waitv<0>();
  }
  WAITL(); BAR();
  asm volatile("" : "+v"(lane));
  q = lane >> 4; jl = lane & 15;
  const u32 ax = lds0 + (18 & 3) * WSLOT;
  MFENCE();
  f32x4 lc[3];
  const u32 scr = lds0 + TOFF + (22 & 3) * TSLOT + ((4 * rg + q) * 192 + 16 * cg + jl) * 8;
  f32x2 e0[3]; float csr[3], csi[3];
#pragma unroll
  for (int b = 0; b < 3; b++) { csr[b] = rdf(ax + AX_W + (16 * cg + jl) * 4, b * 256); csi[b] = rdf(ax + AX_W + (16 * cg + jl) * 4, 768 + b * 256); }
#pragma unroll
  for (int T = 0; T < 4; T++) {
    const f32x4 r = rdf4(ax + AX_S + 512 + (64 * rg + 4 * q) * 4, T * 64), nm = rdf4(ax + AX_S + (64 * rg + 4 * q) * 4, T * 64);
#pragma unroll
    for (int b = 0; b < 3; b++) { ar[b][T] = ar[b][T] * r + nm * csr[b]; ai[b][T] = ai[b][T] * r + nm * csi[b]; }
  }
  f32x2 l8[3];
#pragma unroll
  for (int b = 0; b < 3; b++) {
    lc[b] = rdf4(ax + AX_TAB + (16 * cg + jl) * 32, b * 2048);
    l8[b] = *(LAS f32x2*)(size_t)(ax + AX_TAB + (16 * cg + jl) * 32 + b * 2048 + 16);
    f32x2 hr = {ar[b][0][0], ar[b][0][1]}, hi = {ai[b][0][0], ai[b][0][1]};
#pragma unroll
    for (int k = 1; k < 8; k++) {
      const f32x2 ur = {ar[b][k >> 1][(k & 1) * 2], ar[b][k >> 1][(k & 1) * 2 + 1]}, ui = {ai[b][k >> 1][(k & 1) * 2], ai[b][k >> 1][(k & 1) * 2 + 1]};
      const f32x2 nr = lc[b][0] * hr + (ur - lc[b][1] * hi), ni = lc[b][0] * hi + (ui + lc[b][1] * hr);
      hr = nr; hi = ni;
    }
    e0[b] = (f32x2){hr.x, hi.x};
    *(LAS f32x2*)(size_t)(scr + b * 512) = (f32x2){l8[b].x * hr.x - l8[b].y * hi.x + hr.y, l8[b].x * hi.x + l8[b].y * hr.x + hi.y};
  }
  WAITL(); BAR(); SB();
#pragma unroll
  for (int b = 0; b < 3; b++) {
    const float c2r = 2.f * lc[b][2], c2i = 2.f * lc[b][3];
#pragma unroll
    for (int T = 0; T < 4; T++) {
      const f32x4 nr = c2r * ar[b][T] - c2i * ai[b][T], ni = -c2r * ai[b][T] - c2i * ar[b][T];
      ar[b][T] = nr; ai[b][T] = ni;
      asm volatile("" : "+v"(ar[b][T]), "+v"(ai[b][T]));
    }
  }
  BAR();
  const rsrc_t rWS = mk(p.ws, WS_END);
  const u32 io0 = w * 2048 + lane * 16, io1 = w * 2048 + 1024 + ((lane * 16) ^ 64);
  const u32 pg = WS_P + (rt * 15 + 12 + j) * 98304;
  f32x2 cins[3];
#pragma unroll
  for (int b = 0; b < 3; b++) { cins[b] = *(LAS f32x2*)(size_t)(scr + b * 512); l8[b] = *(LAS f32x2*)(size_t)(ax + AX_TAB + (16 * cg + jl) * 32 + b * 2048 + 16); }
#pragma unroll
  for (int bb = 0; bb < 3; bb++) {
    const int b = (bb + 2) % 3;
    u32x4 H[2][2];
    const f32x2 cin = cins[b];
    const f32x2 sr = {cin.x, l8[b].x * cin.x - l8[b].y * cin.y + e0[b].x}, si = {cin.y, l8[b].x * cin.y + l8[b].y * cin.x + e0[b].y};
    const float c2r = 2.f * lc[b][2], c2i = 2.f * lc[b][3];
    f32x2 hr = c2r * sr - c2i * si, hi = -c2r * si - c2i * sr;
#pragma unroll
    for (int T = 0; T < 4; T++) {
      f32x2 pr[2], pi[2];
#pragma unroll
      for (int k = 0; k < 2; k++) {
        const f32x2 ur = {ar[b][T][2 * k], ar[b][T][2 * k + 1]}, ui = {ai[b][T][2 * k], ai[b][T][2 * k + 1]};
        const f32x2 nr = lc[b][0] * hr + (ur + lc[b][1] * hi), ni = lc[b][0] * hi + (ui - lc[b][1] * hr);
        hr = nr; hi = ni; pr[k] = nr; pi[k] = ni;
      }
      H[0][0][T] = cvtpk(pr[0].x, pr[1].x); H[0][1][T] = cvtpk(pr[0].y, pr[1].y);
      H[1][0][T] = cvtpk(pi[0].x, pi[1].x); H[1][1][T] = cvtpk(pi[0].y, pi[1].y);
    }
    if (bb == 1) BAR();
#pragma unroll
    for (int k = 0; k < 4; k++) {
      st16(H[k >> 1][k & 1], rWS, (k & 1) ? io1 : io0, pg + (2 * b + (k >> 1)) * 16384);
      if (b < 2) wr128(lds0 + TOFF + ((k & 1) ? io1 : io0), ((19 + 2 * b + (k >> 1)) & 3) * TSLOT, H[k >> 1][k & 1]);
    }
    SB();
  }
  phaseB<true>(p, rt, 4, j, w, lane, lds0, tag);
}

DI void spare(const Params& p, int sid, u32 lds0, u32 tag, u64 pc0) {
  const int tid = threadIdx.x;
  u32 cpf = 0;
  if (tid < CODE_LINES && !(sid & 1)) cpf = *(const GAS u32*)(pc0 + tid * 128);
  const rsrc_t rWS = mk(p.ws, WS_END);
  const int w = __builtin_amdgcn_readfirstlane(tid >> 6), lane = tid & 63;
  if (sid == 15 && w == 11) { waitv<0>(); hs_leader(p.ws, lane, lds0, STAMPPTR); return; }
  u32 hsn = 0;
  if (w == 11) { hsn = ld_flag(p.ws + WS_RSP + blockIdx.x * 4) + 1u; st_word(p.ws + WS_REQ + blockIdx.x * 4, hsn); }
  else if (w == 10) {
    const int row = sid * 128 + 120 + (lane >> 3);
    u32x4 v[9]; float s = 0.f;
#pragma unroll
    for (int i = 0; i < 9; i++) v[i] = ((const GAS u32x4*)(p.x + row * D) + (lane & 7))[8 * i];
#pragma unroll
    for (int i = 0; i < 9; i++)
#pragma unroll
      for (int c = 0; c < 4; c++) s += bflo(v[i][c]) + bfhi(v[i][c]);
    s += dppx<0xb1>(s); s += dppx<0x4e>(s); s += dppx<0x141>(s);
    const float mean = s * (1.f / D); s = 0.f;
#pragma unroll
    for (int i = 0; i < 9; i++)
#pragma unroll
      for (int c = 0; c < 4; c++) { const float a = bflo(v[i][c]) - mean, b = bfhi(v[i][c]) - mean; s += a * a + b * b; }
    s += dppx<0xb1>(s); s += dppx<0x4e>(s); s += dppx<0x141>(s);
    if ((lane & 7) == 0) __hip_atomic_store((GAS u64*)(p.ws + WS_ST + row * 8), ((u64)__float_as_uint(s * (1.f / D) + 1e-5f) << 32) | __float_as_uint(mean), __ATOMIC_RELAXED, __HIP_MEMORY_SCOPE_AGENT);
  } else if (w == 9 && sid < 9) {
    const int c = sid * 64 + lane;
    const float lr = bf2f(p.lre[c]), li = bf2f(p.lim[c]);
    const float dt = rbf(expf(bf2f(p.logdt[c])));
    const float ex = expf(dt * lr), an = dt * li;
    const float br = ex * cosf(an), bi = ex * sinf(an);
    const float nr = br - 1.f, den = 1.f / (lr * lr + li * li);
    const float cr = (nr * lr + bi * li) * den, ci = (bi * lr - nr * li) * den;
    float ar = br, ai = bi, r8 = 0.f, i8 = 0.f;
#pragma unroll
    for (int k = 0; k < 7; k++) { const float t = ar * ar - ai * ai; ai = 2.f * ar * ai; ar = t; if (k == 2) { r8 = ar; i8 = ai; } }
    st16(__builtin_bit_cast(u32x4, (f32x4){br, bi, cr, ci}), rWS, c * 32, WS_TAB);
    st16(__builtin_bit_cast(u32x4, (f32x4){r8, i8, ar, ai}), rWS, c * 32 + 16, WS_TAB);
  }
  if (w == 11) spin_all(p.ws + WS_RSP + blockIdx.x * 4, hsn);
  waitv<0>(); asm volatile("" :: "v"(cpf)); BAR();
  if (sid == 15) return;
  if (w == 11) st_word(p.ws + WS_ACK + blockIdx.x * 4, 1u);
  {
    u32 acc = cpf;
    const GAS u32* w2 = (const GAS u32*)p.w2;
#pragma unroll
    for (int i = 0; i < 4; i++) { const int L = (i * 15 + sid) * 768 + tid; if (L < 41472) acc ^= w2[L * 32]; }
    { const int L = sid * 768 + tid; if (L < 5184) acc ^= ((const GAS u32*)p.cre)[L * 32]; else if (L < 10368) acc ^= ((const GAS u32*)p.cim)[(L - 5184) * 32]; }
    asm volatile("" :: "v"(acc));
  }
}

__global__ __launch_bounds__(768, 3) void fused_kernel(Params p) {
  extern __shared__ __attribute__((aligned(16))) char smem[];
  const u32 lds0 = (u32)(size_t)(LAS char*)smem;
  const int b = blockIdx.x, xcd = b & 7, idx = b >> 3;
  const u32 tag = 1u;
  u64 pc0; asm volatile("s_getpc_b64 %0" : "=s"(pc0));
  STAMP(0);
  const int tri = idx / 3, j = idx - 3 * tri;
  const int g = tri < 2 ? 4 : xcd >> 1, rt = 8 * (xcd & 1) + (tri < 2 ? (xcd & 6) + tri : tri - 2);
  const int lane = threadIdx.x & 63, w = __builtin_amdgcn_readfirstlane(threadIdx.x >> 6);
  const int sid = xcd * 2 + idx - 30;
  const int srow = rt * 128 + (3 * g + j) * 8 + w;
  int role = idx < 30 ? w >> 1 : 0;
  if (w < 8 || idx >= 30) {
    if (idx < 30) {
      if (w == 0 && lane < 2) st_flag(p.ws, lane ? FL_P + rt * 15 + 3 * g + j : FL_O + (rt * 3 + j) * 5 + g, 0u);
      if (g == 4 && w >= 1 && w < 4) st16((u32x4){0u, 0u, 0u, 0u}, mk(p.ws, WS_END), (192 * j + threadIdx.x - 64) * 16, WS_EG + rt * 576 * 16);
    } else if (w == 11 && sid == 15) {
      st_flag(p.ws, FL_GO, 0u);
      st16((u32x4){0u, 0u, 0u, 0u}, mk(p.ws, WS_END), lane * 16, WS_ACK);
    }
    if (idx < 30) {
      float mean, ve; row_stats(p.x + srow * D, lane, mean, ve);
      if (lane == 0) __hip_atomic_store((GAS u64*)(p.ws + WS_ST + srow * 8), ((u64)__float_as_uint(ve) << 32) | __float_as_uint(mean), __ATOMIC_RELAXED, __HIP_MEMORY_SCOPE_AGENT);
    }
  }
  asm volatile("" : "+s"(role));
  if (__builtin_expect(role == 4, 1)) { loader(p, rt, g, j, w - 8, lane, lds0, tag); return; }
  if (idx < 30 && w >= 10) { const Params q = reload_params(); helper(q, rt, g, j, w - 10, lane, lds0, tag); return; }
  if (idx >= 30) { const Params q = reload_params(); spare(q, sid, lds0, tag, pc0); STAMP(7); return; }
  if (g < 4) { const Params q = reload_params(); ffn_compute(q, rt, g, j, w, lane, lds0, tag); }
  else { const Params q = reload_params(); ssm_compute(q, rt, j, w, lane, lds0, tag); }
}

extern "C" void kernel_launch(void* const* d_in, const int* in_sizes, int n_in, void* d_out, int out_size, void* d_ws, size_t ws_size, hipStream_t stream) {
  Params p;
  p.x = (const u16*)d_in[0]; p.lre = (const u16*)d_in[1]; p.lim = (const u16*)d_in[2]; p.bre = (const u16*)d_in[3]; p.bim = (const u16*)d_in[4];
  p.cre = (const u16*)d_in[5]; p.cim = (const u16*)d_in[6]; p.logdt = (const u16*)d_in[7]; p.dskip = (const u16*)d_in[8];
  p.w1 = (const u16*)d_in[10]; p.b1 = (const u16*)d_in[11]; p.w2 = (const u16*)d_in[12]; p.b2 = (const u16*)d_in[13];
  p.out = (float*)d_out; p.ws = (char*)d_ws + ((ws_size / 2) & ~(size_t)0x1fffff);
  (void)hipFuncSetAttribute((const void*)fused_kernel, hipFuncAttributeMaxDynamicSharedMemorySize, LDS_BYTES);
  fused_kernel<<<256, 768, LDS_BYTES, stream>>>(p);
}
```

```cpp
#define STAMPS
#define K0 0
#define A0 0
#define B0 0
#define K1 0
#define A1 0
#define B1 3
#include <hip/hip_runtime.h>
#include <utility>

#define LAS __attribute__((address_space(3)))
#define GAS __attribute__((address_space(1)))
#define DI __device__ __forceinline__
typedef unsigned short u16;
typedef unsigned u32;
typedef unsigned long long u64;
typedef unsigned u32x4 __attribute__((ext_vector_type(4)));
typedef unsigned u32x2 __attribute__((ext_vector_type(2)));
typedef short s16x4 __attribute__((ext_vector_type(4)));
typedef short bf16x8 __attribute__((ext_vector_type(8)));
typedef float f32x4 __attribute__((ext_vector_type(4)));
typedef float f32x2 __attribute__((ext_vector_type(2)));
typedef __bf16 bf16x2 __attribute__((ext_vector_type(2)));
typedef __amdgpu_buffer_rsrc_t rsrc_t;

constexpr int S = 2048, D = 576, F = 4608;
constexpr int ROWB = D * 2;
constexpr int WSLOT = 24576, TOFF = 4 * WSLOT, TSLOT = 16384, LDS_BYTES = 163840;
constexpr int AX_W = 0, AX_T = 6656, AX_S = 8704, AX_TAB = 10752;
constexpr u32 WS_FLAGS = 0;
constexpr u32 FL_GO = 0, FL_P = 32, FL_O = 32 + 240, NFLAG = 32 + 480;
constexpr u32 WS_EG = NFLAG * 128;
constexpr u32 WS_ST = WS_EG + 16 * 576 * 16;
constexpr u32 WS_REQ = WS_ST + 3 * 12288, WS_RSP = WS_REQ + 1024, WS_ACK = WS_RSP + 1024;
constexpr u32 WS_CS = 256 * 1024;
constexpr u32 WS_TAB = WS_CS + 15 * 16 * 1536;
constexpr u32 WS_P = 1u << 20;
constexpr u32 XBW = 5 * 1536;
constexpr u32 WS_XB = WS_P + 240 * 98304;
constexpr u32 WS_END = WS_XB + 48 * 32 * XBW;
constexpr int CODE_LINES = 430;
constexpr u32 SPIN_CAP = 1u << 18;

struct Params {
  const u16 *x, *lre, *lim, *bre, *bim, *cre, *cim, *logdt, *dskip, *w1, *b1, *w2, *b2;
  float* out; char* ws;
  u64* stamps;
};

DI Params reload_params() {
  Params q;
#if defined(__HIP_DEVICE_COMPILE__)
  typedef __attribute__((address_space(4))) const Params* KP;
  KP kp = (KP)__builtin_amdgcn_kernarg_segment_ptr(); asm volatile("" : "+s"(kp)); q = *kp;
#endif
  return q;
}
template <int N> DI void waitv() { asm volatile("s_waitcnt vmcnt(%0)" :: "n"(N) : "memory"); }
#define WAITL() asm volatile("s_waitcnt lgkmcnt(0)" ::: "memory")
#define BAR() do { asm volatile("" ::: "memory"); __builtin_amdgcn_s_barrier(); asm volatile("" ::: "memory"); } while (0)
#define SB() __builtin_amdgcn_sched_barrier(0)
#define MFENCE() do { SB(); asm volatile("s_nop 7\n\ts_nop 7"); SB(); } while (0)
#define MMA(c, a, b) asm volatile("v_mfma_f32_16x16x32_bf16 %0, %1, %2, %0" : "+v"(c) : "v"(a), "v"(b))
#define PIN(x) asm volatile("" : "+v"(x))
#define STAMPPTR p.stamps
#define STAMP(i) do { SB(); if (threadIdx.x == 0) p.stamps[blockIdx.x * 16 + (i)] = __builtin_amdgcn_s_memrealtime(); SB(); } while (0)

DI void dma(rsrc_t r, u32 lds, u32 voff, u32 soff) {
  asm volatile("s_mov_b32 m0, %0\n\ts_nop 0\n\tbuffer_load_dwordx4 %1, %2, %3 offen lds" :: "s"(lds), "v"(voff), "s"(r), "s"(soff) : "memory");
}
DI void dma_sc1(rsrc_t r, u32 lds, u32 voff, u32 soff) {
  asm volatile("s_mov_b32 m0, %0\n\ts_nop 0\n\tbuffer_load_dwordx4 %1, %2, %3 offen sc1 lds" :: "s"(lds), "v"(voff), "s"(r), "s"(soff) : "memory");
}
DI void dma12(rsrc_t r, u32 lds, u32 voff, u32 so, u32 d2, u32 st, u32 st3) {
  u32 t;
  asm volatile("s_mov_b32 m0, %2\n\t"
               "s_add_u32 %1, %0, %5\n\t"
               "buffer_load_dwordx4 %3, %4, %0 offen lds\n\t"
               "s_add_u32 m0, m0, 0x800\n\t"
               "s_add_u32 %0, %0, %6\n\t"
               "buffer_load_dwordx4 %3, %4, %1 offen lds\n\t"
               "s_add_u32 m0, m0, 0x800\n\t"
               "s_add_u32 %1, %1, %6\n\t"
               "buffer_load_dwordx4 %3, %4, %0 offen lds\n\t"
               "s_add_u32 m0, m0, 0x800\n\t"
               "s_add_u32 %0, %0, %6\n\t"
               "buffer_load_dwordx4 %3, %4, %1 offen lds\n\t"
               "s_add_u32 m0, m0, 0x800\n\t"
               "s_add_u32 %1, %1, %6\n\t"
               "buffer_load_dwordx4 %3, %4, %0 offen lds\n\t"
               "s_add_u32 m0, m0, 0x800\n\t"
               "s_add_u32 %0, %0, %7\n\t"
               "buffer_load_dwordx4 %3, %4, %1 offen lds\n\t"
               "s_add_u32 m0, m0, 0x800\n\t"
               "s_add_u32 %1, %1, %7\n\t"
               "buffer_load_dwordx4 %3, %4, %0 offen lds\n\t"
               "s_add_u32 m0, m0, 0x800\n\t"
               "s_add_u32 %0, %0, %6\n\t"
               "buffer_load_dwordx4 %3, %4, %1 offen lds\n\t"
               "s_add_u32 m0, m0, 0x800\n\t"
               "s_add_u32 %1, %1, %6\n\t"
               "buffer_load_dwordx4 %3, %4, %0 offen lds\n\t"
               "s_add_u32 m0, m0, 0x800\n\t"
               "s_add_u32 %0, %0, %6\n\t"
               "buffer_load_dwordx4 %3, %4, %1 offen lds\n\t"
               "s_add_u32 m0, m0, 0x800\n\t"
               "s_add_u32 %1, %1, %6\n\t"
               "buffer_load_dwordx4 %3, %4, %0 offen lds\n\t"
               "s_add_u32 m0, m0, 0x800\n\t"
               "s_add_u32 %0, %0, %6\n\t"
               "buffer_load_dwordx4 %3, %4, %1 offen lds"
               : "+&s"(so), "=&s"(t) : "s"(lds), "v"(voff), "s"(r), "s"(d2), "s"(st), "s"(st3) : "memory", "scc");
}
DI void dma4(rsrc_t r, u32 lds, u32 voff, u32 so, u32 st) {
  asm volatile("s_mov_b32 m0, %1\n\t"
               "s_nop 0\n\t"
               "buffer_load_dwordx4 %2, %3, %0 offen lds\n\t"
               "s_add_u32 m0, m0, 0x800\n\t"
               "s_add_u32 %0, %0, %4\n\t"
               "buffer_load_dwordx4 %2, %3, %0 offen lds\n\t"
               "s_add_u32 m0, m0, 0x800\n\t"
               "s_add_u32 %0, %0, %4\n\t"
               "buffer_load_dwordx4 %2, %3, %0 offen lds\n\t"
               "s_add_u32 m0, m0, 0x800\n\t"
               "s_add_u32 %0, %0, %4\n\t"
               "buffer_load_dwordx4 %2, %3, %0 offen lds"
               : "+&s"(so) : "s"(lds), "v"(voff), "s"(r), "s"(st) : "memory", "scc");
}
DI void dma8lin(rsrc_t r, u32 lds, u32 voff, u32 so) {
  asm volatile("s_mov_b32 m0, %1\n\t"
               "s_nop 0\n\t"
               "buffer_load_dwordx4 %2, %3, %0 offen offset:0 sc1 lds\n\t"
               "buffer_load_dwordx4 %2, %3, %0 offen offset:1024 sc1 lds\n\t"
               "buffer_load_dwordx4 %2, %3, %0 offen offset:2048 sc1 lds\n\t"
               "buffer_load_dwordx4 %2, %3, %0 offen offset:3072 sc1 lds\n\t"
               "s_add_u32 m0, m0, 0x2000\n\t"
               "s_add_u32 %0, %0, 0x2000\n\t"
               "buffer_load_dwordx4 %2, %3, %0 offen offset:0 sc1 lds\n\t"
               "buffer_load_dwordx4 %2, %3, %0 offen offset:1024 sc1 lds\n\t"
               "buffer_load_dwordx4 %2, %3, %0 offen offset:2048 sc1 lds\n\t"
               "buffer_load_dwordx4 %2, %3, %0 offen offset:3072 sc1 lds"
               : "+&s"(so) : "s"(lds), "v"(voff), "s"(r) : "memory", "scc");
}
DI rsrc_t mk(const void* ptr, u32 bytes) { return __builtin_amdgcn_make_buffer_rsrc((void*)ptr, 0, bytes, 0x00020000); }
DI s16x4 rd_tr1(u32 a, int off) { return __builtin_amdgcn_ds_read_tr16_b64_v4i16((LAS s16x4*)(size_t)(a + off)); }
DI bf16x8 cat(s16x4 lo, s16x4 hi) { return __builtin_shufflevector(lo, hi, 0, 1, 2, 3, 4, 5, 6, 7); }
DI bf16x8 rd_tr(u32 a, int off) { return cat(rd_tr1(a, off), rd_tr1(a, off + 2048)); }
DI bf16x8 rd128(u32 a, int off) { return *(LAS bf16x8*)(size_t)(a + off); }
DI f32x4 rdf4(u32 a, int off) { return *(LAS f32x4*)(size_t)(a + off); }
DI float rdf(u32 a, int off) { return *(LAS float*)(size_t)(a + off); }
DI void wr128(u32 a, int off, u32x4 v) { *(LAS u32x4*)(size_t)(a + off) = v; }
DI u32 cvtpk(float lo, float hi) { f32x2 v = {lo, hi}; bf16x2 b = __builtin_convertvector(v, bf16x2); return __builtin_bit_cast(u32, b); }
DI float bflo(u32 v) { return __uint_as_float(v << 16); }
DI float bfhi(u32 v) { return __uint_as_float(v & 0xffff0000u); }
DI float bf2f(u16 v) { return __uint_as_float((u32)v << 16); }
DI float dot2(u32 a, u32 b, float c) { return __builtin_amdgcn_fdot2_f32_bf16(__builtin_bit_cast(bf16x2, a), __builtin_bit_cast(bf16x2, b), c, false); }
constexpr u32 ONES = 0x3f803f80u;
DI float rbf(float v) { return bflo(cvtpk(v, 0.f)); }
DI void split3(float v, float& a, float& b, float& c) { a = rbf(v); const float r = v - a; b = rbf(r); c = rbf(r - b); }
template <int C> DI float dppx(float v) { return __uint_as_float(__builtin_amdgcn_update_dpp(0u, __float_as_uint(v), C, 0xf, 0xf, true)); }
template <int C> DI double dppd(double v) { u32x2 t = __builtin_bit_cast(u32x2, v); t[0] = __builtin_amdgcn_update_dpp(0u, t[0], C, 0xf, 0xf, true); t[1] = __builtin_amdgcn_update_dpp(0u, t[1], C, 0xf, 0xf, true); return __builtin_bit_cast(double, t); }
DI void st16(u32x4 v, rsrc_t r, u32 voff, u32 soff) { asm volatile("buffer_store_dwordx4 %0, %1, %2, %3 offen sc1\n\ts_nop 1" :: "v"(v), "v"(voff), "s"(r), "s"(soff) : "memory"); }
DI void st_flag(char* ws, u32 idx, u32 tag) { __hip_atomic_store((GAS u32*)(ws + WS_FLAGS + idx * 128), tag, __ATOMIC_RELAXED, __HIP_MEMORY_SCOPE_AGENT); }
DI u32 ld_flag(const char* a) { return __hip_atomic_load((GAS u32*)a, __ATOMIC_RELAXED, __HIP_MEMORY_SCOPE_AGENT); }
DI void spin_all(const char* a, u32 tag) {
#pragma nounroll
  for (u32 i = 0; i < SPIN_CAP; i++) { if (__all(ld_flag(a) == tag)) break; __builtin_amdgcn_s_sleep(1); }
  asm volatile("" ::: "memory");
}
DI void spin_ge(const char* a, u32 lvl) {
#pragma nounroll
  for (u32 i = 0; i < SPIN_CAP; i++) { if (__all(ld_flag(a) >= lvl)) break; __builtin_amdgcn_s_sleep(1); }
  asm volatile("" ::: "memory");
}
DI void st_word(char* a, u32 v) { __hip_atomic_store((GAS u32*)a, v, __ATOMIC_RELAXED, __HIP_MEMORY_SCOPE_AGENT); }
DI void hs_leader(char* ws, int lane, u32 lds0, u64* stamps) {
  const rsrc_t r = mk(ws, WS_END);
  bool first = true; u32x4 last = {0u, 0u, 0u, 0u};
#pragma nounroll
  for (u32 i = 0; i < SPIN_CAP; i++) {
    asm volatile("" ::: "memory");
    const u32x4 rq = __builtin_amdgcn_raw_buffer_load_b128(r, lane * 16, WS_REQ, 16), ak = __builtin_amdgcn_raw_buffer_load_b128(r, lane * 16, WS_ACK, 16);
    if (first || rq[0] != last[0] || rq[1] != last[1] || rq[2] != last[2] || rq[3] != last[3]) st16(rq, r, lane * 16, WS_RSP);
    first = false; last = rq;
    if (__all(ak[0] == 1u && ak[1] == 1u && ak[2] == 1u && (ak[3] == 1u || lane == 63))) break;
    __builtin_amdgcn_s_sleep(1);
  }
  BAR();
  st_flag(ws, FL_GO, 1u);
  if (lane == 0) stamps[255 * 16 + 2] = __builtin_amdgcn_s_memrealtime();
}
DI float wave_sum(float v) {
  v += dppx<0xb1>(v); v += dppx<0x4e>(v); v += dppx<0x141>(v); v += dppx<0x140>(v);
  const u32 b = __float_as_uint(v);
  return (__uint_as_float(__builtin_amdgcn_readlane(b, 0)) + __uint_as_float(__builtin_amdgcn_readlane(b, 16))) + (__uint_as_float(__builtin_amdgcn_readlane(b, 32)) + __uint_as_float(__builtin_amdgcn_readlane(b, 48)));
}
DI void row_stats(const u16* row, int lane, float& mean, float& ve) {
  const u32x4 a = *((const GAS u32x4*)row + lane), b = *((const GAS u32x4*)row + 64 + (lane & 7));
  float s = 0.f, t = 0.f;
#pragma unroll
  for (int c = 0; c < 4; c++) { s += bflo(a[c]) + bfhi(a[c]); t += bflo(b[c]) + bfhi(b[c]); }
  mean = wave_sum(lane < 8 ? s + t : s) * (1.f / D);
  s = 0.f; t = 0.f;
#pragma unroll
  for (int c = 0; c < 4; c++) {
    const float a0 = bflo(a[c]) - mean, a1 = bfhi(a[c]) - mean, b0 = bflo(b[c]) - mean, b1 = bfhi(b[c]) - mean;
    s += a0 * a0 + a1 * a1; t += b0 * b0 + b1 * b1;
  }
  ve = wave_sum(lane < 8 ? s + t : s) * (1.f / D) + 1e-5f;
}

DI f32x2 gelu2(f32x2 a, float k1, float k2, float sg) {
  f32x2 t = a * a; t = t * k2 + k1; t = t * a;
  t.x = __builtin_amdgcn_exp2f(t.x); t.y = __builtin_amdgcn_exp2f(t.y);
  t = t * sg + sg;
  t.x = __builtin_amdgcn_rcpf(t.x); t.y = __builtin_amdgcn_rcpf(t.y);
  return t * a;
}
DI u32x4 gelu_frag(f32x4 a, f32x4 b, float k1, float k2, float sg) {
  f32x2 g0 = gelu2((f32x2){a[0], a[1]}, k1, k2, sg), g1 = gelu2((f32x2){a[2], a[3]}, k1, k2, sg);
  f32x2 g2 = gelu2((f32x2){b[0], b[1]}, k1, k2, sg), g3 = gelu2((f32x2){b[2], b[3]}, k1, k2, sg);
  return (u32x4){cvtpk(g0.x, g0.y), cvtpk(g1.x, g1.y), cvtpk(g2.x, g2.y), cvtpk(g3.x, g3.y)};
}

template <int H, class UPD>
DI void stepA(f32x4 (&acc)[6][4], bf16x8 (&wp)[2], bf16x8 (&t)[4], const u32 (&wc)[4], const u32& n0, const u32& n1, const u32& xn, UPD&& upd) {
  bf16x8 w[8], nx[4];
  w[0] = wp[0]; w[1] = wp[1];
#pragma unroll
  for (int n = 0; n < 6; n++) {
    const int f = n + 2, ff = f < 6 ? f : f - 6;
    const u32 a = (f < 6 || H == 0) ? wc[ff & 3] : (ff ? n1 : n0); const int off = (f < 6 ? H : 1 - H) * WSLOT + (ff >> 2) * 4096;
    MMA(acc[n][0], w[n], t[0]); SB();
    s16x4 lo = rd_tr1(a, off); SB();
    MMA(acc[n][1], w[n], t[1]); SB();
    s16x4 hi = rd_tr1(a, off + 2048); SB();
    MMA(acc[n][2], w[n], t[2]); SB();
    if (n >= 4) { nx[2 * n - 8] = rd128(xn, (2 * n - 8) * 2048); SB(); }
    if (n == 1) { upd(); SB(); }
    MMA(acc[n][3], w[n], t[3]); SB();
    if (n >= 4) { nx[2 * n - 7] = rd128(xn, (2 * n - 7) * 2048); SB(); }
    if (n == 3) { BAR(); SB(); }
    w[f] = cat(lo, hi);
  }
  wp[0] = w[6]; wp[1] = w[7];
#pragma unroll
  for (int i = 0; i < 4; i++) t[i] = nx[i];
}

template <int KIND, class FW, class FT, class FN, class MID>
DI void half(f32x4 (&acc)[3][4], bf16x8 (&w)[3], bf16x8 (&tp)[2], FW&& rw, FT&& rt_, FN&& rn, MID&& mid) {
  bf16x8 t[4], wn[3], tn[2];
  t[0] = tp[0]; t[1] = tp[1];
  s16x4 lo;
#pragma unroll
  for (int i = 0; i < 12; i++) {
    const int T = i / 3, n = i % 3;
    if constexpr (KIND == 2) MMA(acc[n][T], t[T], w[n]); else MMA(acc[n][T], w[n], t[T]);
    SB();
    if constexpr (KIND == 0) {
      if (i == 0) t[2] = rt_(2, 0); if (i == 1) t[3] = rt_(3, 0);
      if (i >= 2 && i <= 7) { if ((i & 1) == 0) lo = rw((i - 2) >> 1, 0); else wn[(i - 2) >> 1] = cat(lo, rw((i - 2) >> 1, 1)); }
      if (i == 8) tn[0] = rn(0, 0); if (i == 9) tn[1] = rn(1, 0);
    } else if constexpr (KIND == 1) {
      if (i <= 3) { if ((i & 1) == 0) lo = rt_(2 + (i >> 1), 0); else t[2 + (i >> 1)] = cat(lo, rt_(2 + (i >> 1), 1)); }
      if (i >= 4 && i <= 6) wn[i - 4] = rw(i - 4, 0);
      if (i >= 7 && i <= 10) { if ((i & 1) == 1) lo = rn((i - 7) >> 1, 0); else tn[(i - 7) >> 1] = cat(lo, rn((i - 7) >> 1, 1)); }
    } else {
      if (i == 0) t[2] = rt_(2, 0); if (i == 2) t[3] = rt_(3, 0);
      if (i == 3 || i == 5 || i == 6) wn[i == 3 ? 0 : i - 4] = rw(i == 3 ? 0 : i - 4, 0);
      if (i == 8) tn[0] = rn(0, 0); if (i == 9) tn[1] = rn(1, 0);
    }
    mid(i);
    SB();
  }
#pragma unroll
  for (int i = 0; i < 3; i++) w[i] = wn[i];
  tp[0] = tn[0]; tp[1] = tn[1];
}

template <int I, int N, class FN> DI void sfor(FN&& f) { if constexpr (I < N) { f(std::integral_constant<int, I>{}); sfor<I + 1, N>(f); } }

DI void ssm_carry(const Params& p, int rt, int j, int c, u32 lds0, u32 tag) {
  const u32 ax = lds0 + (18 & 3) * WSLOT, scr = lds0 + TOFF + (22 & 3) * TSLOT;
  {
    f32x4 pw = rdf4(ax + AX_TAB + c * 32, 16);
    { const float t = pw[0] * pw[0] - pw[1] * pw[1]; pw[1] = 2.f * pw[0] * pw[1]; pw[0] = t; }
    f32x2 es[8];
#pragma unroll
    for (int s = 0; s < 8; s++) es[s] = *(LAS f32x2*)(size_t)(scr + (s * 192 + c) * 8);
    float sr = 0.f, si = 0.f;
#pragma unroll
    for (int s = 0; s < 8; s++) { const float nr = pw[0] * sr - pw[1] * si + es[s].x, ni = pw[0] * si + pw[1] * sr + es[s].y; sr = nr; si = ni; }
    const rsrc_t rE = mk(p.ws + WS_EG, 16 * 9216);
    const int vo = (192 * j + c) * 16 + rt * 9216;
    st16((u32x4){__float_as_uint(sr), tag, __float_as_uint(si), tag}, rE, vo, 0);
    u32x4 v[15];
#pragma nounroll
    for (u32 spin = 0; spin < SPIN_CAP; spin++) {
      asm volatile("" ::: "memory");
      bool ok = true;
#pragma unroll
      for (int i = 0; i < 15; i++) v[i] = __builtin_amdgcn_raw_buffer_load_b128(rE, vo - (15 - i) * 9216, 0, 16);
#pragma unroll
      for (int i = 0; i < 15; i++) ok = ok && ((v[i][1] == tag && v[i][3] == tag) || vo < (15 - i) * 9216);
      if (__all(ok)) break;
    }
    float cr = 0.f, ci = 0.f;
#pragma unroll
    for (int i = 0; i < 15; i++) {
      const float nr = pw[2] * cr - pw[3] * ci + __uint_as_float(v[i][0]), ni = pw[2] * ci + pw[3] * cr + __uint_as_float(v[i][2]); cr = nr; ci = ni;
    }
#pragma unroll
    for (int s = 0; s < 8; s++) {
      *(LAS f32x2*)(size_t)(scr + (s * 192 + c) * 8) = (f32x2){cr, ci};
      const float nr = pw[0] * cr - pw[1] * ci + es[s].x, ni = pw[0] * ci + pw[1] * cr + es[s].y; cr = nr; ci = ni;
    }
  }
}

#define LBAR() do { BAR(); const u32 t_ = (u32)__builtin_amdgcn_s_memtime(), r_ = (u32)__builtin_amdgcn_s_memrealtime(); tsv = lane == tsn ? t_ : tsv; tsr = lane == tsn ? r_ : tsr; tsn++; } while (0)
DI void loader(const Params& p0, int rt, int g, int j, int u, int lane, u32 lds0, u32 tag) {
  Params p = p0;
  const bool ssm = g == 4;
  const u32 wbytes = ssm ? D * ROWB : D * F * 2;
  const rsrc_t rX = mk(p.x, S * ROWB), rA0 = mk(ssm ? p.bre : p.w1, wbytes), rA1 = mk(ssm ? p.bim : p.w1, wbytes);
  const int r8 = lane >> 3, gp = lane & 7;
  const int cgr = ((((gp >> 1) ^ ((r8 >> 1) & 3)) << 1) | (gp & 1));
  const int rgr = gp ^ (4 * u + (r8 >> 1));
  const int slice = 3 * g + j;
  const int kA = 16 * u + 8 * (r8 >> 2) + (r8 & 3);
  const int R = 8 * u + r8;
  const u32 voffA = ssm ? (192 * j + R) * ROWB + rgr * 16 : kA * (F * 2) + slice * 768 + cgr * 16;
  const int tok = ssm ? 16 * ((R >> 2) & 3) + 8 * (R & 1) + ((R >> 1) & 1) : R;
  const u32 voffX = (rt * 128 + tok) * ROWB + rgr * 16;
  const u32 ldsW = lds0 + u * 1024, ldsT = lds0 + TOFF;
  const u32 aKt = ssm ? 128 : 64 * F * 2, aPar = ssm ? 0 : 32 * F * 2, pstr = ssm ? 32 * ROWB : 128, a2 = ssm ? 16 * ROWB : 4 * F * 2, xstr = (ssm ? 2 : 16) * ROWB;
  const int rot = rt >= 9 ? rt - 9 : rt;
  auto ktile = [&](int kap) { int kt = kap + rot; return kt >= 9 ? kt - 9 : kt; };
  auto issueA = [&](int kap, int h) { dma12(h ? rA1 : rA0, ldsW + ((2 * kap + h) & 3) * WSLOT, voffA, ktile(kap) * aKt + h * aPar, a2, pstr, pstr); };
  auto issueX = [&](int kap, int h) { dma4(rX, ldsT + (kap & 3) * TSLOT + h * 8192 + u * 1024, voffX, h * 64 * ROWB + ktile(kap) * 128, xstr); };
  issueA(0, 0); issueX(0, 0); issueX(0, 1); issueA(0, 1);
  __builtin_amdgcn_s_setprio(3);
  waitv<12>(); BAR();
  issueX(1, 0); issueX(1, 1); issueA(1, 0);
#pragma nounroll
  for (int kap = 0; kap < 7; kap++) {
    u32 l1 = ldsW + ((2 * kap + 3) & 3) * WSLOT, s1 = ktile(kap + 1) * aKt + aPar, l2 = ldsW + ((2 * kap) & 3) * WSLOT, s2 = ktile(kap + 2) * aKt;
    u32 lx = ldsT + ((kap + 2) & 3) * TSLOT + u * 1024, lx2 = lx + 8192, sx = ktile(kap + 2) * 128, sx2 = sx + 64 * ROWB;
    asm volatile("" : "+s"(l1), "+s"(s1), "+s"(l2), "+s"(s2), "+s"(lx), "+s"(lx2), "+s"(sx), "+s"(sx2));
    waitv<16>(); BAR(); dma12(rA1, l1, voffA, s1, a2, pstr, pstr); dma4(rX, lx, voffX, sx, xstr);
    waitv<16>(); BAR(); dma12(rA0, l2, voffA, s2, a2, pstr, pstr); dma4(rX, lx2, voffX, sx2, xstr);
  }
  waitv<16>(); BAR(); issueA(8, 1);
  p = reload_params();
  const u32 voffL = lane * 16;
  const rsrc_t rWS = mk(p.ws, WS_END), rB0 = mk(ssm ? p.cre : p.w2, wbytes), rB1 = mk(ssm ? p.cim : p.w2, wbytes);
  const u32 voffB = ssm ? voffA : R * ROWB + j * 384 + cgr * 16;
  const u32 bJ = ssm ? 384 : 384 * ROWB, bPar = ssm ? 0 : 192 * ROWB, bI = ssm ? 128 : 32 * ROWB, b0 = ssm ? 0 : g * 1152 * ROWB;
  const int j1 = j == 2 ? 0 : j + 1, j2 = j == 0 ? 2 : j - 1;
  const u32 st3 = 96 * ROWB - 2 * pstr, pb = WS_P + (rt * 15 + 3 * g) * 98304 + u * 4096;
  auto issueB = [&](int s, int jj, int i) { dma12((i & 1) ? rB1 : rB0, ldsW + (s & 3) * WSLOT, voffB, b0 + jj * bJ + (i & 1) * bPar + (i >> 1) * bI, 16 * ROWB, pstr, st3); };
  auto issueP = [&](int s, int jj, int i) { dma8lin(rWS, ldsT + (s & 3) * TSLOT + u * 4096, voffL, pb + jj * 98304 + i * 16384); };
  waitv<12>(); BAR();
#pragma unroll
  for (int k = 0; k < 3; k++) dma_sc1(rWS, lds0 + (18 & 3) * WSLOT + AX_TAB + (u * 3 + k) * 1024, voffL, WS_TAB + j * 6144 + (u * 3 + k) * 1024);
  waitv<3>();
  BAR(); issueB(19, j, 0);
  BAR(); issueB(20, j, 1); waitv<12>();
  BAR(); issueB(21, j, 2); waitv<12>();
  if (ssm) { BAR(); if (u == 1) ssm_carry(p, rt, j, lane, lds0, tag); WAITL(); BAR(); BAR(); }
  BAR(); issueB(22, j, 3); waitv<12>();
  BAR(); issueB(23, j, 4); issueP(23, j, 4); waitv<20>();
  BAR(); issueB(24, j, 5); issueP(24, j, 5);
#pragma nounroll
  for (int n = 0; n < 3; n++) {
    const int i = n ? 2 * n - 2 : 4;
    u32 w1 = b0 + j1 * bJ + (i >> 1) * bI, w2 = b0 + j2 * bJ + (i >> 1) * bI, w3 = w1 + bPar, w4 = w2 + bPar;
    u32 p1 = pb + j1 * 98304 + i * 16384, p2 = pb + j2 * 98304 + i * 16384, p3 = p1 + 16384, p4 = p2 + 16384;
    asm volatile("" : "+s"(w1), "+s"(w2), "+s"(w3), "+s"(w4), "+s"(p1), "+s"(p2), "+s"(p3), "+s"(p4));
    waitv<20>(); BAR(); dma12(rB0, ldsW + 1 * WSLOT, voffB, w1, 16 * ROWB, pstr, st3); dma8lin(rWS, ldsT + 1 * TSLOT + u * 4096, voffL, p1);
    waitv<20>(); BAR(); dma12(rB0, ldsW + 2 * WSLOT, voffB, w2, 16 * ROWB, pstr, st3); dma8lin(rWS, ldsT + 2 * TSLOT + u * 4096, voffL, p2);
    waitv<20>(); BAR(); dma12(rB1, ldsW + 3 * WSLOT, voffB, w3, 16 * ROWB, pstr, st3); dma8lin(rWS, ldsT + 3 * TSLOT + u * 4096, voffL, p3);
    waitv<20>(); BAR(); dma12(rB1, ldsW + 0 * WSLOT, voffB, w4, 16 * ROWB, pstr, st3); dma8lin(rWS, ldsT + 0 * TSLOT + u * 4096, voffL, p4);
  }
  waitv<20>();
  BAR();
  {
    const u32 l = lds0 + WSLOT;
    const u32 vo = (lane & 31) < 24 ? (rt * 128 + u * 64 + (g & 3) * 16 + (lane >> 5)) * ROWB + j * 384 + (lane & 31) * 16 : 0x7fffffffu;
#pragma unroll
    for (int k = 0; k < 8; k++) dma(rX, l + (u * 8 + k) * 1024, vo, k * 2 * ROWB);
    dma(mk(u ? p.dskip : p.b2, ROWB), l + 16384 + u * 1024, voffL + j * 384, 0);
  }
  waitv<9>(); BAR(); BAR(); waitv<0>();
  BAR();
}

DI void helper(const Params& p, int rt, int g, int j, int h, int lane, u32 lds0, u32 tag) {
  u32 tsv = 0, tsr = 0; int tsn = 0;
  const bool ssm = g == 4;
  const int slice = 3 * g + j, q = lane >> 4, jl = lane & 15;
  const rsrc_t rWS = mk(p.ws, WS_END);
  char* const hsR = p.ws + WS_RSP + blockIdx.x * 4;
  u32 hsn = 0;
  if (h == 0) { hsn = __builtin_amdgcn_readfirstlane(ld_flag(hsR)) + 1u; st_word(hsR - 1024, hsn); }
  __builtin_amdgcn_s_setprio(2);
  {
    const int e = (lane >> 2) & 3, z = 2 * (q & 1) + (e >> 1);
    bf16x8 f[12]; f32x4 c[12]; u32x4 one = {ONES, ONES, ONES, ONES};
    auto rdslot = [&](int sl) {
      if (ssm) {
        const u32 a = lds0 + h * 12288 + jl * 128 + ((q ^ (jl >> 1)) * 16);
#pragma unroll
        for (int n = 0; n < 6; n++) { f[n] = rd128(a, sl * WSLOT + n * 2048); f[6 + n] = rd128(a ^ 64, sl * WSLOT + n * 2048); }
      } else {
#pragma unroll
        for (int n = 0; n < 12; n++) f[n] = rd_tr(lds0 + h * 12288 + (4 * q + e) * 128 + (((n & 3) ^ z) * 32) + (lane & 3) * 8, sl * WSLOT + (n >> 2) * 4096);
      }
#pragma unroll
      for (int n = 0; n < 12; n++) PIN(f[n]);
      asm volatile("s_nop 1");
    };
#pragma unroll
    for (int n = 0; n < 12; n++) { c[n] = (f32x4){0.f, 0.f, 0.f, 0.f}; PIN(c[n]); }
    PIN(one);
    LBAR();
    rdslot(0);
    if (ssm) {
#pragma unroll
      for (int n = 0; n < 12; n++) MMA(c[n % 6], f[n], one);
      MFENCE();
    } else {
#pragma unroll
      for (int n = 0; n < 12; n++) MMA(c[n], f[n], one);
      MFENCE();
    }
    LBAR();
    rdslot(1);
    if (rt < 9) {
#pragma unroll
      for (int n = 6; n < 12; n++) MMA(c[n], f[n], one);
      MFENCE();
    }
    LBAR();
    if (rt < 9) {
      if (ssm) {
#pragma unroll
        for (int n = 0; n < 6; n++) MMA(c[6 + n], f[n], one);
      MFENCE();
      } else {
#pragma unroll
        for (int n = 0; n < 6; n++) MMA(c[n], f[n], one);
      MFENCE();
      }
    }
    f32x4 m = c[0];
#pragma unroll
    for (int n = 1; n < 12; n++) m = jl == n ? c[n] : m;
    const int col = (ssm ? (jl >= 6 ? 192 - 96 : 0) + 96 * h : 192 * h) + 16 * jl + 4 * q;
    st16(__builtin_bit_cast(u32x4, m), rWS, jl < 12 ? col * 4 : 0x7fffffffu, WS_CS + (slice * 16 + rt) * 1536);
  }
  LBAR();
  u32 pv = ld_flag(hsR);
  LBAR();
  waitv<0>(); LBAR();
  bool acked = h != 0;
#pragma unroll
  for (int s = 0; s < 2; s++) {
    if (!acked) {
      acked = __all(pv == hsn);
      if (!acked && s == 1) { spin_all(hsR, hsn); acked = true; }
      if (acked) st_word(hsR + 1024, 1u);
    }
    if (s < 1) pv = ld_flag(hsR);
    LBAR(); LBAR();
  }
  LBAR();
  const char* fGo = p.ws + WS_FLAGS + FL_GO * 128;
  pv = ld_flag(fGo);
  LBAR(); LBAR();
  if (!__all(pv == 1u)) spin_all(fGo, 1u);
  const int ir = 64 * h + lane, irt = ssm ? (ir & 64) + 16 * ((ir >> 2) & 3) + 8 * (ir & 1) + 2 * ((ir >> 4) & 3) + ((ir >> 1) & 1) : ir;
  const u32x2 sg = __builtin_amdgcn_raw_buffer_load_b64(rWS, (rt * 128 + irt) * 8, WS_ST, 16);
  const int cc = 192 * h + 4 * (lane < 48 ? lane : lane - 48);
  f32x4 cp[9];
#pragma unroll
  for (int k = 0; k < 9; k++) cp[k] = __builtin_bit_cast(f32x4, __builtin_amdgcn_raw_buffer_load_b128(rWS, cc * 4, WS_CS + (slice * 16 + k) * 1536, 16));
  const u32x2 bb = *(const GAS u32x2*)(p.b1 + (ssm ? 0 : slice * 384) + cc);
  LBAR(); LBAR(); SB();
  f32x4 cs = ((cp[0] + cp[1]) + (cp[2] + cp[3])) + ((cp[4] + cp[5]) + (cp[6] + cp[7])) + cp[8];
  float mean = __uint_as_float(sg[0]), rstd = __builtin_amdgcn_rsqf(__uint_as_float(sg[1])), sig = __uint_as_float(sg[1]) * rstd;
  u32x4 at, aw[4];
  { float m1, m2, m3; split3(-mean, m1, m2, m3); const float g1 = rbf(sig), g2 = sig - g1;
    at = (u32x4){cvtpk(m1, m1), cvtpk(m2, m2), cvtpk(m1, m3), cvtpk(g1, g2)}; mean = ssm ? -mean * rstd : mean; }
  PIN(cs); PIN(at); PIN(mean); PIN(rstd); PIN(sig);
  auto auxw = [&](int k) { float c1, c2, c3; split3(cs[k], c1, c2, c3); const float b = (k & 1) ? bfhi(bb[k >> 1]) : bflo(bb[k >> 1]);
    aw[k] = (u32x4){cvtpk(c1, c2), cvtpk(c1, c2), cvtpk(c3, c1), cvtpk(b, b)}; PIN(aw[k]); };
  LBAR(); SB();
  auxw(0); auxw(1);
  const u32 ax = lds0 + (18 & 3) * WSLOT;
  LBAR(); SB();
  wr128(ax + AX_T + ir * 16, 0, at);
  { LAS float* sp = (LAS float*)(size_t)(ax + AX_S + ir * 4); sp[0] = mean; sp[128] = rstd; sp[256] = sig; }
  auxw(2); auxw(3);
  LBAR(); SB();
  if (ssm) *(LAS f32x4*)(size_t)(ax + AX_W + cc * 4) = cs;
  else {
#pragma unroll
    for (int k = 0; k < 4; k++) wr128(ax + AX_W + cc * 4, k * 1600, aw[k]);
  }
  WAITL(); LBAR();
  LBAR(); SB();
  if (ssm) { LBAR(); ssm_carry(p, rt, j, 64 + 64 * h + lane, lds0, tag); WAITL(); LBAR(); BAR(); }
  char* const fMine = p.ws + WS_FLAGS + (FL_P + rt * 15 + slice) * 128;
  const char* fP = p.ws + WS_FLAGS + (FL_P + rt * 15 + 3 * g + ((lane & 1) ? (j == 2 ? 0 : j + 1) : (j == 0 ? 2 : j - 1))) * 128;
  LBAR(); if (h == 1 && !ssm) st_word(fMine, 2u);
  LBAR(); if (h == 1 && ssm) st_word(fMine, 1u);
#pragma nounroll
  for (u32 lv = 1; lv < 4; lv++) {
    LBAR(); if (h == 0) spin_ge(fP, lv);
    LBAR(); if (h == 1 && lv == 1) st_word(fMine, 3u);
    LBAR(); LBAR();
  }
  LBAR(); LBAR(); LBAR(); LBAR();
  LBAR();
  if (h == 1) { ((u32*)(p.stamps + 4096))[blockIdx.x * 64 + lane] = tsv; ((u32*)(p.stamps + 4096))[16384 + blockIdx.x * 64 + lane] = tsr; }
}

template <bool SSM>
DI void phaseB(const Params& p, int rt, int g, int j, int w, int lane, u32 lds0, u32 tag) {
  asm volatile("" : "+v"(lane));
  const int rg = w >> 2, cg = w & 3, q = lane >> 4, e = (lane >> 2) & 3, pp = lane & 3, jl = lane & 15;
  const int z = 2 * (q & 1) + (e >> 1);
  u32 wlo[3], whi[3], tb[2];
#pragma unroll
  for (int n = 0; n < 3; n++) {
    const int t = 3 * cg + n;
    wlo[n] = SSM ? lds0 + (16 * t + jl) * 128 + ((q ^ (jl >> 1)) * 16) : lds0 + (t >> 2) * 4096 + (4 * q + e) * 128 + (((t & 3) ^ z) * 32) + pp * 8;
    whi[n] = SSM ? (wlo[n] ^ 64) : wlo[n] + 2 * WSLOT;
  }
  tb[0] = SSM ? lds0 + TOFF + rg * 8192 + (q >> 1) * 2048 + (pp >> 1) * 1024 + (((8 * (q & 1) + e) * 16) ^ ((pp >> 1) * 64)) + 8 * (pp & 1) : lds0 + TOFF + rg * 4096 + lane * 16;
  tb[1] = tb[0] ^ 64;
  float mean = 0.f, rstd = 0.f;
  if constexpr (!SSM) { const u32 sa = lds0 + (18 & 3) * WSLOT + AX_S + (64 * rg + 16 * g + jl) * 4; mean = rdf(sa, 0); rstd = rdf(sa, 512); }
  f32x4 ob[3][4];
#pragma unroll
  for (int n = 0; n < 3; n++)
#pragma unroll
    for (int T = 0; T < 4; T++) { ob[n][T] = (f32x4){0.f, 0.f, 0.f, 0.f}; PIN(ob[n][T]); }
  auto RW = [&](int sl, int kb, int n, int part) {
    if constexpr (SSM) return rd128((kb ? whi[n] : wlo[n]) + (sl >> 1) * 2 * WSLOT, (sl & 1) * WSLOT);
    else return rd_tr1(sl < 2 ? wlo[n] : whi[n], (sl & 1) * WSLOT + kb * 12288 + part * 2048);
  };
  auto RT = [&](int sl, int kb, int T, int part) {
    if constexpr (SSM) return rd_tr1(tb[part], sl * TSLOT + kb * 4096 + T * 256);
    else return rd128(tb[0], sl * TSLOT + kb * 8192 + T * 1024);
  };
  bf16x8 wf[3], tp[2];
  auto step = [&](auto slc, auto&& mid) {
    constexpr int SL = decltype(slc)::value, NS = (SL + 1) & 3;
    half<SSM ? 1 : 0>(ob, wf, tp, [&](int n, int part) { return RW(SL, 1, n, part); }, [&](int T, int part) { return RT(SL, 0, T, part); }, [&](int T, int part) { return RT(SL, 1, T, part); }, mid);
    half<SSM ? 1 : 0>(ob, wf, tp, [&](int n, int part) { return RW(NS, 0, n, part); }, [&](int T, int part) { return RT(SL, 1, T, part); }, [&](int T, int part) { return RT(NS, 0, T, part); }, [](int) {});
  };
  using I0 = std::integral_constant<int, 0>; using I1 = std::integral_constant<int, 1>; using I2 = std::integral_constant<int, 2>; using I3 = std::integral_constant<int, 3>;
  if constexpr (!SSM) waitv<4>();
  WAITL(); BAR();
#pragma unroll
  for (int n = 0; n < 3; n++) { if constexpr (SSM) wf[n] = RW(3, 0, n, 0); else wf[n] = cat(RW(3, 0, n, 0), RW(3, 0, n, 1)); }
#pragma unroll
  for (int T = 0; T < 2; T++) { if constexpr (SSM) tp[T] = cat(RT(3, 0, T, 0), RT(3, 0, T, 1)); else tp[T] = RT(3, 0, T, 0); }
  auto barm = [](int i) { if (i == 11) BAR(); };
  step(I3{}, [](int) {});
  step(I0{}, [&](int i) { if (i == 3) { if constexpr (SSM) waitv<8>(); BAR(); } });
#pragma nounroll
  for (int it = 0; it < 4; it++) { step(I1{}, barm); step(I2{}, [&](int i) { if (i == 11) { waitv<0>(); BAR(); } }); step(I3{}, barm); step(I0{}, barm); }
  MFENCE();
  const rsrc_t rWS = mk(p.ws, WS_END);
  const u32 xb = WS_XB + (rt * 3 + j) * 32 * XBW + w * XBW;
  f32x4 mine[3];
#pragma unroll
  for (int T = 0; T < 4; T++) {
    if (SSM || T != g) {
      const u32 o = xb + T * 8 * XBW + g * 1536;
      const u32x4 a = {cvtpk(ob[0][T][0], ob[0][T][1]), cvtpk(ob[0][T][2], ob[0][T][3]), cvtpk(ob[1][T][0], ob[1][T][1]), cvtpk(ob[1][T][2], ob[1][T][3])};
      const u32 b0 = cvtpk(ob[2][T][0], ob[2][T][1]), b1 = cvtpk(ob[2][T][2], ob[2][T][3]);
      const auto s0 = __builtin_amdgcn_permlane32_swap(b0, b0, false, false), s1 = __builtin_amdgcn_permlane32_swap(b1, b1, false, false);
      st16(a, rWS, lane * 16, o);
      if (lane < 32) st16((u32x4){b0, b1, s0[1], s1[1]}, rWS, lane * 16, o + 1024);
    } else {
#pragma unroll
      for (int n = 0; n < 3; n++) mine[n] = ob[n][T];
    }
  }
  if constexpr (SSM) {
    waitv<0>(); BAR();
    if (threadIdx.x == 0) st_flag(p.ws, FL_O + (rt * 3 + j) * 5 + g, tag);
    STAMP(7);
  } else {
    waitv<0>(); BAR();
    if (threadIdx.x == 0) st_flag(p.ws, FL_O + (rt * 3 + j) * 5 + g, tag);
    const int tok = rt * 128 + 64 * rg + 16 * g + jl, col = 192 * j + 48 * cg + 4 * q;
    u32x2 xv[3], bv[3], dv[3];
#pragma unroll
    for (int n = 0; n < 3; n++) {
      const u32 cb = lds0 + WSLOT + (48 * cg + 4 * q + 16 * n) * 2;
      xv[n] = *(LAS u32x2*)(size_t)(cb + (16 * rg + jl) * 512); bv[n] = *(LAS u32x2*)(size_t)(cb + 16384); dv[n] = *(LAS u32x2*)(size_t)(cb + 17408);
    }
    const u32 o = xb + g * 8 * XBW;
    u32x4 a[4]; u32x2 b[4];
#pragma unroll
    for (int k = 0; k < 4; k++) {
      if (k == 0 || k == 3) spin_all(p.ws + WS_FLAGS + (FL_O + (rt * 3 + j) * 5 + (k ? 4 : (lane & 3))) * 128, tag);
      const int src = k == 3 ? 4 : (k < g ? k : k + 1);
      a[k] = __builtin_amdgcn_raw_buffer_load_b128(rWS, lane * 16, o + src * 1536, 16);
      b[k] = __builtin_amdgcn_raw_buffer_load_b64(rWS, (lane & 31) * 16 + (lane >> 5) * 8, o + src * 1536 + 1024, 16);
    }
#pragma unroll
    for (int n = 0; n < 3; n++) {
      f32x4 r;
#pragma unroll
      for (int c = 0; c < 4; c++) {
        float G = mine[n][c] + ((c & 1) ? bfhi(bv[n][c >> 1]) : bflo(bv[n][c >> 1]));
#pragma unroll
        for (int k = 0; k < 3; k++) { const u32 v = n < 2 ? a[k][2 * n + (c >> 1)] : b[k][c >> 1]; G += (c & 1) ? bfhi(v) : bflo(v); }
        const u32 yv = n < 2 ? a[3][2 * n + (c >> 1)] : b[3][c >> 1];
        const float yy = (c & 1) ? bfhi(yv) : bflo(yv);
        const float xx = (c & 1) ? bfhi(xv[n][c >> 1]) : bflo(xv[n][c >> 1]);
        const float dd = (c & 1) ? bfhi(dv[n][c >> 1]) : bflo(dv[n][c >> 1]);
        const float sv = yy + (xx - mean) * rstd * dd;
        const float sig = __builtin_amdgcn_rcpf(1.f + __builtin_amdgcn_exp2f(-1.4426950408889634f * G));
        r[c] = xx + sv * (G * sig);
      }
      *(GAS f32x4*)(p.out + tok * D + col + 16 * n) = r;
    }
    STAMP(7);
  }
}

DI void ffn_compute(const Params& p, int rt, int g, int j, int w, int lane, u32 lds0, u32 tag) {
  const int rg = w >> 2, cg = w & 3, q = lane >> 4, e = (lane >> 2) & 3, pp = lane & 3, jl = lane & 15;
  const int z = 2 * (q & 1) + (e >> 1);
  u32 wc[4];
#pragma unroll
  for (int n = 0; n < 4; n++) { const int t = 6 * cg + n; wc[n] = lds0 + (t >> 2) * 4096 + (4 * q + e) * 128 + (((t & 3) ^ z) * 32) + pp * 8; }
  const u32 xbase = lds0 + TOFF + (64 * rg + jl) * 128 + ((q ^ (jl >> 1)) * 16);
  f32x4 acc[6][4];
#pragma unroll
  for (int n = 0; n < 6; n++)
#pragma unroll
    for (int T = 0; T < 4; T++) { acc[n][T] = (f32x4){0.f, 0.f, 0.f, 0.f}; asm volatile("" : "+v"(acc[n][T])); }
  bf16x8 wp[2], t[4];
  BAR();
  wp[0] = rd_tr(wc[0], 0); wp[1] = rd_tr(wc[1], 0);
#pragma unroll
  for (int T = 0; T < 4; T++) t[T] = rd128(xbase, T * 2048);
  u32 xs = 0, xn = 0, n0 = 0, n1 = 0; int dw = 2 * WSLOT;
#pragma nounroll
  for (int it = 0; it < 9; it++) {
    stepA<0>(acc, wp, t, wc, n0, n1, xn, [&]() { xn = (xbase ^ 64) + xs; PIN(xn); xs = (xs + TSLOT) & (4 * TSLOT - 1); });
    stepA<1>(acc, wp, t, wc, n0, n1, xn, [&]() { xn = xbase + xs; n0 = wc[0] + dw; n1 = wc[1] + dw; wc[2] += dw; wc[3] += dw; PIN(xn); PIN(n0); PIN(n1); PIN(wc[2]); PIN(wc[3]); waitv<0>(); });
    wc[0] = n0; wc[1] = n1; dw = -dw;
  }
  const u32 ax = lds0 + (18 & 3) * WSLOT;
  float k1[4], k2[4], sg[4];
#pragma unroll
  for (int T = 0; T < 4; T++) {
    u32x4 v = __builtin_bit_cast(u32x4, rd128(ax + AX_T + (64 * rg + jl) * 16, T * 256));
    if (q != 0) v = (u32x4){0u, 0u, 0u, 0u};
    t[T] = __builtin_bit_cast(bf16x8, v);
    const float r = rdf(ax + AX_S + 512 + (64 * rg + jl) * 4, T * 64);
    sg[T] = rdf(ax + AX_S + 1024 + (64 * rg + jl) * 4, T * 64);
    k1[T] = -2.3022081985f * r; k2[T] = -0.10294324f * r * r * r;
  }
  {
    bf16x8 a[6];
#pragma unroll
    for (int n = 0; n < 6; n++) a[n] = rd128(ax + AX_W + ((jl & 3) * 100 + 24 * cg + (jl >> 2)) * 16, n * 64);
    asm volatile("s_nop 1" : "+v"(t[0]), "+v"(t[1]), "+v"(t[2]), "+v"(t[3]));
#pragma unroll
    for (int n = 0; n < 6; n++)
#pragma unroll
      for (int T = 0; T < 4; T++) MMA(acc[n][T], a[n], t[T]);
    MFENCE();
  }
  const rsrc_t rWS = mk(p.ws, WS_END);
  const u32 pg = WS_P + (rt * 15 + 3 * g + j) * 98304 + (cg >> 1) * 16384 + (cg & 1) * 8192 + rg * 4096;
  const u32 pl = lds0 + TOFF + (cg & 1) * 8192 + rg * 4096 + lane * 16, pl0 = pl + ((19 + (cg >> 1)) & 3) * TSLOT, pl1 = pl + ((21 + (cg >> 1)) & 3) * TSLOT;
#pragma unroll
  for (int bb = 0; bb < 3; bb++) {
    const int b = (bb + 2) % 3;
#pragma unroll
    for (int T = 0; T < 4; T++) {
      const u32x4 P = gelu_frag(acc[2 * b][T], acc[2 * b + 1][T], k1[T], k2[T], sg[T]);
      st16(P, rWS, lane * 16, pg + b * 32768 + T * 1024);
      if (b < 2) wr128(b ? pl1 : pl0, T * 1024, P);
      SB();
    }
    if (bb == 0) BAR();
  }
  phaseB<false>(p, rt, g, j, w, lane, lds0, tag);
}

DI void ssm_compute(const Params& p, int rt, int j, int w, int lane, u32 lds0, u32 tag) {
  const int rg = w >> 2, cg = w & 3; int q = lane >> 4, jl = lane & 15;
  u32 wb = lds0 + (16 * cg + jl) * 128 + ((q ^ (jl >> 1)) * 16);
  const u32 xb0 = lds0 + TOFF + (64 * rg + jl) * 128 + ((q ^ (jl >> 1)) * 16);
  f32x4 ar[3][4], ai[3][4];
#pragma unroll
  for (int n = 0; n < 3; n++)
#pragma unroll
    for (int T = 0; T < 4; T++) { ar[n][T] = (f32x4){0.f, 0.f, 0.f, 0.f}; ai[n][T] = (f32x4){0.f, 0.f, 0.f, 0.f}; asm volatile("" : "+v"(ar[n][T]), "+v"(ai[n][T])); }
  bf16x8 wf[3], tp[2];
  auto nop = [](int) {};
  auto barm = [](int i) { if (i == 11) BAR(); };
  BAR();
#pragma unroll
  for (int n = 0; n < 3; n++) wf[n] = rd128(wb, n * 8192);
  tp[0] = rd128(xb0, 0); tp[1] = rd128(xb0, 2048);
  u32 xs = 0; int dw = 2 * WSLOT;
#pragma nounroll
  for (int it = 0; it < 9; it++) {
    const u32 x0 = xb0 + xs, x1 = (xb0 ^ 64) + xs, wb1 = wb ^ 64, wn = wb + dw;
    xs = (xs + TSLOT) & (4 * TSLOT - 1);
    const u32 xn = xb0 + xs;
    half<2>(ar, wf, tp, [&](int n, int) { return rd128(wb1, n * 8192); }, [&](int T, int) { return rd128(x0, T * 2048); }, [&](int T, int) { return rd128(x1, T * 2048); }, barm);
    half<2>(ar, wf, tp, [&](int n, int) { return rd128(wb, WSLOT + n * 8192); }, [&](int T, int) { return rd128(x1, T * 2048); }, [&](int T, int) { return rd128(x0, T * 2048); }, nop);
    half<2>(ai, wf, tp, [&](int n, int) { return rd128(wb1, WSLOT + n * 8192); }, [&](int T, int) { return rd128(x0, T * 2048); }, [&](int T, int) { return rd128(x1, T * 2048); }, barm);
    half<2>(ai, wf, tp, [&](int n, int) { return rd128(wn, n * 8192); }, [&](int T, int) { return rd128(x1, T * 2048); }, [&](int T, int) { return rd128(xn, T * 2048); }, nop);
    wb = wn; dw = -dw;
    waitv<0>();
  }
  WAITL(); BAR();
  asm volatile("" : "+v"(lane));
  q = lane >> 4; jl = lane & 15;
  const u32 ax = lds0 + (18 & 3) * WSLOT;
  MFENCE();
  f32x4 lc[3];
  const u32 scr = lds0 + TOFF + (22 & 3) * TSLOT + ((4 * rg + q) * 192 + 16 * cg + jl) * 8;
  f32x2 e0[3]; float csr[3], csi[3];
#pragma unroll
  for (int b = 0; b < 3; b++) { csr[b] = rdf(ax + AX_W + (16 * cg + jl) * 4, b * 256); csi[b] = rdf(ax + AX_W + (16 * cg + jl) * 4, 768 + b * 256); }
#pragma unroll
  for (int T = 0; T < 4; T++) {
    const f32x4 r = rdf4(ax + AX_S + 512 + (64 * rg + 4 * q) * 4, T * 64), nm = rdf4(ax + AX_S + (64 * rg + 4 * q) * 4, T * 64);
#pragma unroll
    for (int b = 0; b < 3; b++) { ar[b][T] = ar[b][T] * r + nm * csr[b]; ai[b][T] = ai[b][T] * r + nm * csi[b]; }
  }
  f32x2 l8[3];
#pragma unroll
  for (int b = 0; b < 3; b++) {
    lc[b] = rdf4(ax + AX_TAB + (16 * cg + jl) * 32, b * 2048);
    l8[b] = *(LAS f32x2*)(size_t)(ax + AX_TAB + (16 * cg + jl) * 32 + b * 2048 + 16);
    f32x2 hr = {ar[b][0][0], ar[b][0][1]}, hi = {ai[b][0][0], ai[b][0][1]};
#pragma unroll
    for (int k = 1; k < 8; k++) {
      const f32x2 ur = {ar[b][k >> 1][(k & 1) * 2], ar[b][k >> 1][(k & 1) * 2 + 1]}, ui = {ai[b][k >> 1][(k & 1) * 2], ai[b][k >> 1][(k & 1) * 2 + 1]};
      const f32x2 nr = lc[b][0] * hr + (ur - lc[b][1] * hi), ni = lc[b][0] * hi + (ui + lc[b][1] * hr);
      hr = nr; hi = ni;
    }
    e0[b] = (f32x2){hr.x, hi.x};
    *(LAS f32x2*)(size_t)(scr + b * 512) = (f32x2){l8[b].x * hr.x - l8[b].y * hi.x + hr.y, l8[b].x * hi.x + l8[b].y * hr.x + hi.y};
  }
  WAITL(); BAR(); SB();
#pragma unroll
  for (int b = 0; b < 3; b++) {
    const float c2r = 2.f * lc[b][2], c2i = 2.f * lc[b][3];
#pragma unroll
    for (int T = 0; T < 4; T++) {
      const f32x4 nr = c2r * ar[b][T] - c2i * ai[b][T], ni = -c2r * ai[b][T] - c2i * ar[b][T];
      ar[b][T] = nr; ai[b][T] = ni;
      asm volatile("" : "+v"(ar[b][T]), "+v"(ai[b][T]));
    }
  }
  BAR();
  const rsrc_t rWS = mk(p.ws, WS_END);
  const u32 io0 = w * 2048 + lane * 16, io1 = w * 2048 + 1024 + ((lane * 16) ^ 64);
  const u32 pg = WS_P + (rt * 15 + 12 + j) * 98304;
  f32x2 cins[3];
#pragma unroll
  for (int b = 0; b < 3; b++) { cins[b] = *(LAS f32x2*)(size_t)(scr + b * 512); l8[b] = *(LAS f32x2*)(size_t)(ax + AX_TAB + (16 * cg + jl) * 32 + b * 2048 + 16); }
#pragma unroll
  for (int bb = 0; bb < 3; bb++) {
    const int b = (bb + 2) % 3;
    u32x4 H[2][2];
    const f32x2 cin = cins[b];
    const f32x2 sr = {cin.x, l8[b].x * cin.x - l8[b].y * cin.y + e0[b].x}, si = {cin.y, l8[b].x * cin.y + l8[b].y * cin.x + e0[b].y};
    const float c2r = 2.f * lc[b][2], c2i = 2.f * lc[b][3];
    f32x2 hr = c2r * sr - c2i * si, hi = -c2r * si - c2i * sr;
#pragma unroll
    for (int T = 0; T < 4; T++) {
      f32x2 pr[2], pi[2];
#pragma unroll
      for (int k = 0; k < 2; k++) {
        const f32x2 ur = {ar[b][T][2 * k], ar[b][T][2 * k + 1]}, ui = {ai[b][T][2 * k], ai[b][T][2 * k + 1]};
        const f32x2 nr = lc[b][0] * hr + (ur + lc[b][1] * hi), ni = lc[b][0] * hi + (ui - lc[b][1] * hr);
        hr = nr; hi = ni; pr[k] = nr; pi[k] = ni;
      }
      H[0][0][T] = cvtpk(pr[0].x, pr[1].x); H[0][1][T] = cvtpk(pr[0].y, pr[1].y);
      H[1][0][T] = cvtpk(pi[0].x, pi[1].x); H[1][1][T] = cvtpk(pi[0].y, pi[1].y);
    }
    if (bb == 1) BAR();
#pragma unroll
    for (int k = 0; k < 4; k++) {
      st16(H[k >> 1][k & 1], rWS, (k & 1) ? io1 : io0, pg + (2 * b + (k >> 1)) * 16384);
      if (b < 2) wr128(lds0 + TOFF + ((k & 1) ? io1 : io0), ((19 + 2 * b + (k >> 1)) & 3) * TSLOT, H[k >> 1][k & 1]);
    }
    SB();
  }
  phaseB<true>(p, rt, 4, j, w, lane, lds0, tag);
}

DI void spare(const Params& p, int sid, u32 lds0, u32 tag, u64 pc0) {
  const int tid = threadIdx.x;
  u32 cpf = 0;
  if (tid < CODE_LINES && !(sid & 1)) cpf = *(const GAS u32*)(pc0 + tid * 128);
  const rsrc_t rWS = mk(p.ws, WS_END);
  const int w = __builtin_amdgcn_readfirstlane(tid >> 6), lane = tid & 63;
  if (sid == 15 && w == 11) { waitv<0>(); hs_leader(p.ws, lane, lds0, STAMPPTR); return; }
  u32 hsn = 0;
  if (w == 11) { hsn = ld_flag(p.ws + WS_RSP + blockIdx.x * 4) + 1u; st_word(p.ws + WS_REQ + blockIdx.x * 4, hsn); }
  else if (w == 10) {
    const int row = sid * 128 + 120 + (lane >> 3);
    u32x4 v[9]; float s = 0.f;
#pragma unroll
    for (int i = 0; i < 9; i++) v[i] = ((const GAS u32x4*)(p.x + row * D) + (lane & 7))[8 * i];
#pragma unroll
    for (int i = 0; i < 9; i++)
#pragma unroll
      for (int c = 0; c < 4; c++) s += bflo(v[i][c]) + bfhi(v[i][c]);
    s += dppx<0xb1>(s); s += dppx<0x4e>(s); s += dppx<0x141>(s);
    const float mean = s * (1.f / D); s = 0.f;
#pragma unroll
    for (int i = 0; i < 9; i++)
#pragma unroll
      for (int c = 0; c < 4; c++) { const float a = bflo(v[i][c]) - mean, b = bfhi(v[i][c]) - mean; s += a * a + b * b; }
    s += dppx<0xb1>(s); s += dppx<0x4e>(s); s += dppx<0x141>(s);
    if ((lane & 7) == 0) __hip_atomic_store((GAS u64*)(p.ws + WS_ST + row * 8), ((u64)__float_as_uint(s * (1.f / D) + 1e-5f) << 32) | __float_as_uint(mean), __ATOMIC_RELAXED, __HIP_MEMORY_SCOPE_AGENT);
  } else if (w == 9 && sid < 9) {
    const int c = sid * 64 + lane;
    const float lr = bf2f(p.lre[c]), li = bf2f(p.lim[c]);
    const float dt = rbf(expf(bf2f(p.logdt[c])));
    const float ex = expf(dt * lr), an = dt * li;
    const float br = ex * cosf(an), bi = ex * sinf(an);
    const float nr = br - 1.f, den = 1.f / (lr * lr + li * li);
    const float cr = (nr * lr + bi * li) * den, ci = (bi * lr - nr * li) * den;
    float ar = br, ai = bi, r8 = 0.f, i8 = 0.f;
#pragma unroll
    for (int k = 0; k < 7; k++) { const float t = ar * ar - ai * ai; ai = 2.f * ar * ai; ar = t; if (k == 2) { r8 = ar; i8 = ai; } }
    st16(__builtin_bit_cast(u32x4, (f32x4){br, bi, cr, ci}), rWS, c * 32, WS_TAB);
    st16(__builtin_bit_cast(u32x4, (f32x4){r8, i8, ar, ai}), rWS, c * 32 + 16, WS_TAB);
  }
  if (w == 11) spin_all(p.ws + WS_RSP + blockIdx.x * 4, hsn);
  waitv<0>(); asm volatile("" :: "v"(cpf)); BAR();
  if (sid == 15) return;
  if (w == 11) st_word(p.ws + WS_ACK + blockIdx.x * 4, 1u);
  {
    u32 acc = cpf;
    const GAS u32* w2 = (const GAS u32*)p.w2;
#pragma unroll
    for (int i = 0; i < 4; i++) { const int L = (i * 15 + sid) * 768 + tid; if (L < 41472) acc ^= w2[L * 32]; }
    { const int L = sid * 768 + tid; if (L < 5184) acc ^= ((const GAS u32*)p.cre)[L * 32]; else if (L < 10368) acc ^= ((const GAS u32*)p.cim)[(L - 5184) * 32]; }
    asm volatile("" :: "v"(acc));
  }
}

__global__ __launch_bounds__(768, 3) void fused_kernel(Params p) {
  extern __shared__ __attribute__((aligned(16))) char smem[];
  const u32 lds0 = (u32)(size_t)(LAS char*)smem;
  const int b = blockIdx.x, xcd = b & 7, idx = b >> 3;
  const u32 tag = 1u;
  u64 pc0; asm volatile("s_getpc_b64 %0" : "=s"(pc0));
  STAMP(0);
  const int tri = idx / 3, j = idx - 3 * tri;
  const int g = tri < 2 ? 4 : xcd >> 1, rt = 8 * (xcd & 1) + (tri < 2 ? (xcd & 6) + tri : tri - 2);
  const int lane = threadIdx.x & 63, w = __builtin_amdgcn_readfirstlane(threadIdx.x >> 6);
  const int sid = xcd * 2 + idx - 30;
  const int srow = rt * 128 + (3 * g + j) * 8 + w;
  int role = idx < 30 ? w >> 1 : 0;
  if (w < 8 || idx >= 30) {
    if (idx < 30) {
      if (w == 0 && lane < 2) st_flag(p.ws, lane ? FL_P + rt * 15 + 3 * g + j : FL_O + (rt * 3 + j) * 5 + g, 0u);
      if (g == 4 && w >= 1 && w < 4) st16((u32x4){0u, 0u, 0u, 0u}, mk(p.ws, WS_END), (192 * j + threadIdx.x - 64) * 16, WS_EG + rt * 576 * 16);
    } else if (w == 11 && sid == 15) {
      st_flag(p.ws, FL_GO, 0u);
      st16((u32x4){0u, 0u, 0u, 0u}, mk(p.ws, WS_END), lane * 16, WS_ACK);
    }
    if (idx < 30) {
      float mean, ve; row_stats(p.x + srow * D, lane, mean, ve);
      if (lane == 0) __hip_atomic_store((GAS u64*)(p.ws + WS_ST + srow * 8), ((u64)__float_as_uint(ve) << 32) | __float_as_uint(mean), __ATOMIC_RELAXED, __HIP_MEMORY_SCOPE_AGENT);
    }
  }
  asm volatile("" : "+s"(role));
  if (__builtin_expect(role == 4, 1)) { loader(p, rt, g, j, w - 8, lane, lds0, tag); return; }
  if (idx < 30 && w >= 10) { const Params q = reload_params(); helper(q, rt, g, j, w - 10, lane, lds0, tag); return; }
  if (idx >= 30) { const Params q = reload_params(); spare(q, sid, lds0, tag, pc0); STAMP(7); return; }
  if (g < 4) { const Params q = reload_params(); ffn_compute(q, rt, g, j, w, lane, lds0, tag); }
  else { const Params q = reload_params(); ssm_compute(q, rt, j, w, lane, lds0, tag); }
}

__device__ u32 med_diff(const u64* st, int kind, int a, int b) {
  __shared__ u32 v[256]; __shared__ u32 res; __shared__ u64 t0s[256];
  const int t = threadIdx.x; const int idx = t >> 3, xcd = t & 7;
  const int k = idx >= 30 ? 2 : (idx < 6 ? 1 : 0);
  t0s[t] = st[t * 16]; __syncthreads();
  u64 t0 = ~0ull; for (int i = 0; i < 256; i++) t0 = t0s[i] < t0 ? t0s[i] : t0;
  const bool mx = b >= 100; if (mx) b -= 100;
  const u32* tr = (const u32*)(st + 4096) + 16384 + t * 64;
  auto get = [&](int x) { return x == -1 ? (u32)t0 : x < 64 ? tr[x] : (u32)st[t * 16 + x - 64]; };
  v[t] = k == kind ? get(b) - get(a) : 0xffffffffu; __syncthreads();
  int n = 0, less = 0; for (int i = 0; i < 256; i++) { n += v[i] != 0xffffffffu; less += (v[i] < v[t]) || (v[i] == v[t] && i < t); }
  if (k == kind && less == (mx ? n - 1 : n / 2)) res = v[t];
  __syncthreads(); u32 r = res; __syncthreads(); return r;
}
__global__ void amp_k(const u64* st) {
  u32 v0 = med_diff(st, K0, A0, B0), v1 = med_diff(st, K1, A1, B1);
  v0 = (v0 + 12) / 25; v1 = (v1 + 12) / 25; if (v0 > 127) v0 = 127; if (v1 > 110) v1 = 110;
  if (threadIdx.x == 0) { u64 t0 = __builtin_amdgcn_s_memrealtime(); u64 d = (u64)(v0 + 128 * v1) * 100; while (__builtin_amdgcn_s_memrealtime() - t0 < d) __builtin_amdgcn_s_sleep(4); }
}

extern "C" void kernel_launch(void* const* d_in, const int* in_sizes, int n_in, void* d_out, int out_size, void* d_ws, size_t ws_size, hipStream_t stream) {
  Params p;
  p.x = (const u16*)d_in[0]; p.lre = (const u16*)d_in[1]; p.lim = (const u16*)d_in[2]; p.bre = (const u16*)d_in[3]; p.bim = (const u16*)d_in[4];
  p.cre = (const u16*)d_in[5]; p.cim = (const u16*)d_in[6]; p.logdt = (const u16*)d_in[7]; p.dskip = (const u16*)d_in[8];
  p.w1 = (const u16*)d_in[10]; p.b1 = (const u16*)d_in[11]; p.w2 = (const u16*)d_in[12]; p.b2 = (const u16*)d_in[13];
  p.out = (float*)d_out; p.ws = (char*)d_ws;
  p.stamps = (u64*)((char*)d_ws + (200u << 20));
  (void)hipFuncSetAttribute((const void*)fused_kernel, hipFuncAttributeMaxDynamicSharedMemorySize, LDS_BYTES);
  fused_kernel<<<256, 768, LDS_BYTES, stream>>>(p);
  amp_k<<<1, 256, 0, stream>>>(p.stamps);
}
```

```cpp
#include <hip/hip_runtime.h>
#include <utility>

#define LAS __attribute__((address_space(3)))
#define GAS __attribute__((address_space(1)))
#define DI __device__ __forceinline__
typedef unsigned short u16;
typedef unsigned u32;
typedef unsigned long long u64;
typedef unsigned u32x4 __attribute__((ext_vector_type(4)));
typedef unsigned u32x2 __attribute__((ext_vector_type(2)));
typedef short s16x4 __attribute__((ext_vector_type(4)));
typedef short bf16x8 __attribute__((ext_vector_type(8)));
typedef float f32x4 __attribute__((ext_vector_type(4)));
typedef float f32x2 __attribute__((ext_vector_type(2)));
typedef __bf16 bf16x2 __attribute__((ext_vector_type(2)));
typedef __amdgpu_buffer_rsrc_t rsrc_t;

constexpr int S = 2048, D = 576, F = 4608;
constexpr int ROWB = D * 2;
constexpr int WSLOT = 24576, TOFF = 4 * WSLOT, TSLOT = 16384, LDS_BYTES = 163840;
constexpr int AX_W = 0, AX_T = 6656, AX_S = 8704, AX_TAB = 10752;
constexpr u32 WS_FLAGS = 0;
constexpr u32 FL_GO = 0, FL_P = 32, FL_O = 32 + 240, NFLAG = 32 + 480;
constexpr u32 WS_EG = NFLAG * 128;
constexpr u32 WS_ST = WS_EG + 16 * 576 * 16;
constexpr u32 WS_REQ = WS_ST + 3 * 12288, WS_RSP = WS_REQ + 1024, WS_ACK = WS_RSP + 1024;
constexpr u32 WS_CS = 256 * 1024;
constexpr u32 WS_TAB = WS_CS + 15 * 16 * 1536;
constexpr u32 WS_P = 1u << 20;
constexpr u32 XBW = 5 * 1536;
constexpr u32 WS_XB = WS_P + 240 * 98304;
constexpr u32 WS_END = WS_XB + 48 * 32 * XBW;
constexpr int CODE_LINES = 430;
constexpr u32 SPIN_CAP = 1u << 18;

struct Params {
  const u16 *x, *lre, *lim, *bre, *bim, *cre, *cim, *logdt, *dskip, *w1, *b1, *w2, *b2;
  float* out; char* ws;
};

DI Params reload_params() {
  Params q;
#if defined(__HIP_DEVICE_COMPILE__)
  typedef __attribute__((address_space(4))) const Params* KP;
  KP kp = (KP)__builtin_amdgcn_kernarg_segment_ptr(); asm volatile("" : "+s"(kp)); q = *kp;
#endif
  return q;
}
template <int N> DI void waitv() { asm volatile("s_waitcnt vmcnt(%0)" :: "n"(N) : "memory"); }
#define WAITL() asm volatile("s_waitcnt lgkmcnt(0)" ::: "memory")
#define BAR() do { asm volatile("" ::: "memory"); __builtin_amdgcn_s_barrier(); asm volatile("" ::: "memory"); } while (0)
#define SB() __builtin_amdgcn_sched_barrier(0)
#define MFENCE() do { SB(); asm volatile("s_nop 7\n\ts_nop 7"); SB(); } while (0)
#define MMA(c, a, b) asm volatile("v_mfma_f32_16x16x32_bf16 %0, %1, %2, %0" : "+v"(c) : "v"(a), "v"(b))
#define PIN(x) asm volatile("" : "+v"(x))
#define STAMPPTR nullptr
#define STAMP(i)

DI void dma(rsrc_t r, u32 lds, u32 voff, u32 soff) {
  asm volatile("s_mov_b32 m0, %0\n\ts_nop 0\n\tbuffer_load_dwordx4 %1, %2, %3 offen lds" :: "s"(lds), "v"(voff), "s"(r), "s"(soff) : "memory");
}
DI void dma_sc1(rsrc_t r, u32 lds, u32 voff, u32 soff) {
  asm volatile("s_mov_b32 m0, %0\n\ts_nop 0\n\tbuffer_load_dwordx4 %1, %2, %3 offen sc1 lds" :: "s"(lds), "v"(voff), "s"(r), "s"(soff) : "memory");
}
DI void dma12(rsrc_t r, u32 lds, u32 voff, u32 so, u32 d2, u32 st, u32 st3) {
  u32 t;
  asm volatile("s_mov_b32 m0, %2\n\t"
               "s_add_u32 %1, %0, %5\n\t"
               "buffer_load_dwordx4 %3, %4, %0 offen lds\n\t"
               "s_add_u32 m0, m0, 0x800\n\t"
               "s_add_u32 %0, %0, %6\n\t"
               "buffer_load_dwordx4 %3, %4, %1 offen lds\n\t"
               "s_add_u32 m0, m0, 0x800\n\t"
               "s_add_u32 %1, %1, %6\n\t"
               "buffer_load_dwordx4 %3, %4, %0 offen lds\n\t"
               "s_add_u32 m0, m0, 0x800\n\t"
               "s_add_u32 %0, %0, %6\n\t"
               "buffer_load_dwordx4 %3, %4, %1 offen lds\n\t"
               "s_add_u32 m0, m0, 0x800\n\t"
               "s_add_u32 %1, %1, %6\n\t"
               "buffer_load_dwordx4 %3, %4, %0 offen lds\n\t"
               "s_add_u32 m0, m0, 0x800\n\t"
               "s_add_u32 %0, %0, %7\n\t"
               "buffer_load_dwordx4 %3, %4, %1 offen lds\n\t"
               "s_add_u32 m0, m0, 0x800\n\t"
               "s_add_u32 %1, %1, %7\n\t"
               "buffer_load_dwordx4 %3, %4, %0 offen lds\n\t"
               "s_add_u32 m0, m0, 0x800\n\t"
               "s_add_u32 %0, %0, %6\n\t"
               "buffer_load_dwordx4 %3, %4, %1 offen lds\n\t"
               "s_add_u32 m0, m0, 0x800\n\t"
               "s_add_u32 %1, %1, %6\n\t"
               "buffer_load_dwordx4 %3, %4, %0 offen lds\n\t"
               "s_add_u32 m0, m0, 0x800\n\t"
               "s_add_u32 %0, %0, %6\n\t"
               "buffer_load_dwordx4 %3, %4, %1 offen lds\n\t"
               "s_add_u32 m0, m0, 0x800\n\t"
               "s_add_u32 %1, %1, %6\n\t"
               "buffer_load_dwordx4 %3, %4, %0 offen lds\n\t"
               "s_add_u32 m0, m0, 0x800\n\t"
               "s_add_u32 %0, %0, %6\n\t"
               "buffer_load_dwordx4 %3, %4, %1 offen lds"
               : "+&s"(so), "=&s"(t) : "s"(lds), "v"(voff), "s"(r), "s"(d2), "s"(st), "s"(st3) : "memory", "scc");
}
DI void dma4(rsrc_t r, u32 lds, u32 voff, u32 so, u32 st) {
  asm volatile("s_mov_b32 m0, %1\n\t"
               "s_nop 0\n\t"
               "buffer_load_dwordx4 %2, %3, %0 offen lds\n\t"
               "s_add_u32 m0, m0, 0x800\n\t"
               "s_add_u32 %0, %0, %4\n\t"
               "buffer_load_dwordx4 %2, %3, %0 offen lds\n\t"
               "s_add_u32 m0, m0, 0x800\n\t"
               "s_add_u32 %0, %0, %4\n\t"
               "buffer_load_dwordx4 %2, %3, %0 offen lds\n\t"
               "s_add_u32 m0, m0, 0x800\n\t"
               "s_add_u32 %0, %0, %4\n\t"
               "buffer_load_dwordx4 %2, %3, %0 offen lds"
               : "+&s"(so) : "s"(lds), "v"(voff), "s"(r), "s"(st) : "memory", "scc");
}
DI void dma8lin(rsrc_t r, u32 lds, u32 voff, u32 so) {
  asm volatile("s_mov_b32 m0, %1\n\t"
               "s_nop 0\n\t"
               "buffer_load_dwordx4 %2, %3, %0 offen offset:0 sc1 lds\n\t"
               "buffer_load_dwordx4 %2, %3, %0 offen offset:1024 sc1 lds\n\t"
               "buffer_load_dwordx4 %2, %3, %0 offen offset:2048 sc1 lds\n\t"
               "buffer_load_dwordx4 %2, %3, %0 offen offset:3072 sc1 lds\n\t"
               "s_add_u32 m0, m0, 0x2000\n\t"
               "s_add_u32 %0, %0, 0x2000\n\t"
               "buffer_load_dwordx4 %2, %3, %0 offen offset:0 sc1 lds\n\t"
               "buffer_load_dwordx4 %2, %3, %0 offen offset:1024 sc1 lds\n\t"
               "buffer_load_dwordx4 %2, %3, %0 offen offset:2048 sc1 lds\n\t"
               "buffer_load_dwordx4 %2, %3, %0 offen offset:3072 sc1 lds"
               : "+&s"(so) : "s"(lds), "v"(voff), "s"(r) : "memory", "scc");
}
DI rsrc_t mk(const void* ptr, u32 bytes) { return __builtin_amdgcn_make_buffer_rsrc((void*)ptr, 0, bytes, 0x00020000); }
DI s16x4 rd_tr1(u32 a, int off) { return __builtin_amdgcn_ds_read_tr16_b64_v4i16((LAS s16x4*)(size_t)(a + off)); }
DI bf16x8 cat(s16x4 lo, s16x4 hi) { return __builtin_shufflevector(lo, hi, 0, 1, 2, 3, 4, 5, 6, 7); }
DI bf16x8 rd_tr(u32 a, int off) { return cat(rd_tr1(a, off), rd_tr1(a, off + 2048)); }
DI bf16x8 rd128(u32 a, int off) { return *(LAS bf16x8*)(size_t)(a + off); }
DI f32x4 rdf4(u32 a, int off) { return *(LAS f32x4*)(size_t)(a + off); }
DI float rdf(u32 a, int off) { return *(LAS float*)(size_t)(a + off); }
DI void wr128(u32 a, int off, u32x4 v) { *(LAS u32x4*)(size_t)(a + off) = v; }
DI u32 cvtpk(float lo, float hi) { f32x2 v = {lo, hi}; bf16x2 b = __builtin_convertvector(v, bf16x2); return __builtin_bit_cast(u32, b); }
DI float bflo(u32 v) { return __uint_as_float(v << 16); }
DI float bfhi(u32 v) { return __uint_as_float(v & 0xffff0000u); }
DI float bf2f(u16 v) { return __uint_as_float((u32)v << 16); }
DI float dot2(u32 a, u32 b, float c) { return __builtin_amdgcn_fdot2_f32_bf16(__builtin_bit_cast(bf16x2, a), __builtin_bit_cast(bf16x2, b), c, false); }
constexpr u32 ONES = 0x3f803f80u;
DI float rbf(float v) { return bflo(cvtpk(v, 0.f)); }
DI void split3(float v, float& a, float& b, float& c) { a = rbf(v); const float r = v - a; b = rbf(r); c = rbf(r - b); }
template <int C> DI float dppx(float v) { return __uint_as_float(__builtin_amdgcn_update_dpp(0u, __float_as_uint(v), C, 0xf, 0xf, true)); }
template <int C> DI double dppd(double v) { u32x2 t = __builtin_bit_cast(u32x2, v); t[0] = __builtin_amdgcn_update_dpp(0u, t[0], C, 0xf, 0xf, true); t[1] = __builtin_amdgcn_update_dpp(0u, t[1], C, 0xf, 0xf, true); return __builtin_bit_cast(double, t); }
DI void st16(u32x4 v, rsrc_t r, u32 voff, u32 soff) { asm volatile("buffer_store_dwordx4 %0, %1, %2, %3 offen sc1\n\ts_nop 1" :: "v"(v), "v"(voff), "s"(r), "s"(soff) : "memory"); }
DI void st_flag(char* ws, u32 idx, u32 tag) { __hip_atomic_store((GAS u32*)(ws + WS_FLAGS + idx * 128), tag, __ATOMIC_RELAXED, __HIP_MEMORY_SCOPE_AGENT); }
DI u32 ld_flag(const char* a) { return __hip_atomic_load((GAS u32*)a, __ATOMIC_RELAXED, __HIP_MEMORY_SCOPE_AGENT); }
DI void spin_all(const char* a, u32 tag) {
#pragma nounroll
  for (u32 i = 0; i < SPIN_CAP; i++) { if (__all(ld_flag(a) == tag)) break; __builtin_amdgcn_s_sleep(1); }
  asm volatile("" ::: "memory");
}
DI void spin_ge(const char* a, u32 lvl) {
#pragma nounroll
  for (u32 i = 0; i < SPIN_CAP; i++) { if (__all(ld_flag(a) >= lvl)) break; __builtin_amdgcn_s_sleep(1); }
  asm volatile("" ::: "memory");
}
DI void st_word(char* a, u32 v) { __hip_atomic_store((GAS u32*)a, v, __ATOMIC_RELAXED, __HIP_MEMORY_SCOPE_AGENT); }
DI void hs_leader(char* ws, int lane, u32 lds0, u64* stamps) {
  const rsrc_t r = mk(ws, WS_END);
  bool first = true; u32x4 last = {0u, 0u, 0u, 0u};
#pragma nounroll
  for (u32 i = 0; i < SPIN_CAP; i++) {
    asm volatile("" ::: "memory");
    const u32x4 rq = __builtin_amdgcn_raw_buffer_load_b128(r, lane * 16, WS_REQ, 16), ak = __builtin_amdgcn_raw_buffer_load_b128(r, lane * 16, WS_ACK, 16);
    if (first || rq[0] != last[0] || rq[1] != last[1] || rq[2] != last[2] || rq[3] != last[3]) st16(rq, r, lane * 16, WS_RSP);
    first = false; last = rq;
    if (__all(ak[0] == 1u && ak[1] == 1u && ak[2] == 1u && (ak[3] == 1u || lane == 63))) break;
    __builtin_amdgcn_s_sleep(1);
  }
  BAR();
  st_flag(ws, FL_GO, 1u);
}
DI float wave_sum(float v) {
  v += dppx<0xb1>(v); v += dppx<0x4e>(v); v += dppx<0x141>(v); v += dppx<0x140>(v);
  const u32 b = __float_as_uint(v);
  return (__uint_as_float(__builtin_amdgcn_readlane(b, 0)) + __uint_as_float(__builtin_amdgcn_readlane(b, 16))) + (__uint_as_float(__builtin_amdgcn_readlane(b, 32)) + __uint_as_float(__builtin_amdgcn_readlane(b, 48)));
}
DI void row_stats(const u16* row, int lane, float& mean, float& ve) {
  const u32x4 a = *((const GAS u32x4*)row + lane), b = *((const GAS u32x4*)row + 64 + (lane & 7));
  float s = 0.f, t = 0.f;
#pragma unroll
  for (int c = 0; c < 4; c++) { s += bflo(a[c]) + bfhi(a[c]); t += bflo(b[c]) + bfhi(b[c]); }
  mean = wave_sum(lane < 8 ? s + t : s) * (1.f / D);
  s = 0.f; t = 0.f;
#pragma unroll
  for (int c = 0; c < 4; c++) {
    const float a0 = bflo(a[c]) - mean, a1 = bfhi(a[c]) - mean, b0 = bflo(b[c]) - mean, b1 = bfhi(b[c]) - mean;
    s += a0 * a0 + a1 * a1; t += b0 * b0 + b1 * b1;
  }
  ve = wave_sum(lane < 8 ? s + t : s) * (1.f / D) + 1e-5f;
}

DI f32x2 gelu2(f32x2 a, float k1, float k2, float sg) {
  f32x2 t = a * a; t = t * k2 + k1; t = t * a;
  t.x = __builtin_amdgcn_exp2f(t.x); t.y = __builtin_amdgcn_exp2f(t.y);
  t = t * sg + sg;
  t.x = __builtin_amdgcn_rcpf(t.x); t.y = __builtin_amdgcn_rcpf(t.y);
  return t * a;
}
DI u32x4 gelu_frag(f32x4 a, f32x4 b, float k1, float k2, float sg) {
  f32x2 g0 = gelu2((f32x2){a[0], a[1]}, k1, k2, sg), g1 = gelu2((f32x2){a[2], a[3]}, k1, k2, sg);
  f32x2 g2 = gelu2((f32x2){b[0], b[1]}, k1, k2, sg), g3 = gelu2((f32x2){b[2], b[3]}, k1, k2, sg);
  return (u32x4){cvtpk(g0.x, g0.y), cvtpk(g1.x, g1.y), cvtpk(g2.x, g2.y), cvtpk(g3.x, g3.y)};
}

template <int H, class UPD>
DI void stepA(f32x4 (&acc)[6][4], bf16x8 (&wp)[2], bf16x8 (&t)[4], const u32 (&wc)[4], const u32& n0, const u32& n1, const u32& xn, UPD&& upd) {
  bf16x8 w[8], nx[4];
  w[0] = wp[0]; w[1] = wp[1];
#pragma unroll
  for (int n = 0; n < 6; n++) {
    const int f = n + 2, ff = f < 6 ? f : f - 6;
    const u32 a = (f < 6 || H == 0) ? wc[ff & 3] : (ff ? n1 : n0); const int off = (f < 6 ? H : 1 - H) * WSLOT + (ff >> 2) * 4096;
    MMA(acc[n][0], w[n], t[0]); SB();
    s16x4 lo = rd_tr1(a, off); SB();
    MMA(acc[n][1], w[n], t[1]); SB();
    s16x4 hi = rd_tr1(a, off + 2048); SB();
    MMA(acc[n][2], w[n], t[2]); SB();
    if (n >= 4) { nx[2 * n - 8] = rd128(xn, (2 * n - 8) * 2048); SB(); }
    if (n == 1) { upd(); SB(); }
    MMA(acc[n][3], w[n], t[3]); SB();
    if (n >= 4) { nx[2 * n - 7] = rd128(xn, (2 * n - 7) * 2048); SB(); }
    if (n == 3) { BAR(); SB(); }
    w[f] = cat(lo, hi);
  }
  wp[0] = w[6]; wp[1] = w[7];
#pragma unroll
  for (int i = 0; i < 4; i++) t[i] = nx[i];
}

template <int KIND, class FW, class FT, class FN, class MID>
DI void half(f32x4 (&acc)[3][4], bf16x8 (&w)[3], bf16x8 (&tp)[2], FW&& rw, FT&& rt_, FN&& rn, MID&& mid) {
  bf16x8 t[4], wn[3], tn[2];
  t[0] = tp[0]; t[1] = tp[1];
  s16x4 lo;
#pragma unroll
  for (int i = 0; i < 12; i++) {
    const int T = i / 3, n = i % 3;
    if constexpr (KIND == 2) MMA(acc[n][T], t[T], w[n]); else MMA(acc[n][T], w[n], t[T]);
    SB();
    if constexpr (KIND == 0) {
      if (i == 0) t[2] = rt_(2, 0); if (i == 1) t[3] = rt_(3, 0);
      if (i >= 2 && i <= 7) { if ((i & 1) == 0) lo = rw((i - 2) >> 1, 0); else wn[(i - 2) >> 1] = cat(lo, rw((i - 2) >> 1, 1)); }
      if (i == 8) tn[0] = rn(0, 0); if (i == 9) tn[1] = rn(1, 0);
    } else if constexpr (KIND == 1) {
      if (i <= 3) { if ((i & 1) == 0) lo = rt_(2 + (i >> 1), 0); else t[2 + (i >> 1)] = cat(lo, rt_(2 + (i >> 1), 1)); }
      if (i >= 4 && i <= 6) wn[i - 4] = rw(i - 4, 0);
      if (i >= 7 && i <= 10) { if ((i & 1) == 1) lo = rn((i - 7) >> 1, 0); else tn[(i - 7) >> 1] = cat(lo, rn((i - 7) >> 1, 1)); }
    } else {
      if (i == 0) t[2] = rt_(2, 0); if (i == 2) t[3] = rt_(3, 0);
      if (i == 3 || i == 5 || i == 6) wn[i == 3 ? 0 : i - 4] = rw(i == 3 ? 0 : i - 4, 0);
      if (i == 8) tn[0] = rn(0, 0); if (i == 9) tn[1] = rn(1, 0);
    }
    mid(i);
    SB();
  }
#pragma unroll
  for (int i = 0; i < 3; i++) w[i] = wn[i];
  tp[0] = tn[0]; tp[1] = tn[1];
}

template <int I, int N, class FN> DI void sfor(FN&& f) { if constexpr (I < N) { f(std::integral_constant<int, I>{}); sfor<I + 1, N>(f); } }

DI void ssm_carry(const Params& p, int rt, int j, int c, u32 lds0, u32 tag) {
  const u32 ax = lds0 + (18 & 3) * WSLOT, scr = lds0 + TOFF + (22 & 3) * TSLOT;
  {
    f32x4 pw = rdf4(ax + AX_TAB + c * 32, 16);
    { const float t = pw[0] * pw[0] - pw[1] * pw[1]; pw[1] = 2.f * pw[0] * pw[1]; pw[0] = t; }
    f32x2 es[8];
#pragma unroll
    for (int s = 0; s < 8; s++) es[s] = *(LAS f32x2*)(size_t)(scr + (s * 192 + c) * 8);
    float sr = 0.f, si = 0.f;
#pragma unroll
    for (int s = 0; s < 8; s++) { const float nr = pw[0] * sr - pw[1] * si + es[s].x, ni = pw[0] * si + pw[1] * sr + es[s].y; sr = nr; si = ni; }
    const rsrc_t rE = mk(p.ws + WS_EG, 16 * 9216);
    const int vo = (192 * j + c) * 16 + rt * 9216;
    st16((u32x4){__float_as_uint(sr), tag, __float_as_uint(si), tag}, rE, vo, 0);
    u32x4 v[15];
#pragma nounroll
    for (u32 spin = 0; spin < SPIN_CAP; spin++) {
      asm volatile("" ::: "memory");
      bool ok = true;
#pragma unroll
      for (int i = 0; i < 15; i++) v[i] = __builtin_amdgcn_raw_buffer_load_b128(rE, vo - (15 - i) * 9216, 0, 16);
#pragma unroll
      for (int i = 0; i < 15; i++) ok = ok && ((v[i][1] == tag && v[i][3] == tag) || vo < (15 - i) * 9216);
      if (__all(ok)) break;
    }
    float cr = 0.f, ci = 0.f;
#pragma unroll
    for (int i = 0; i < 15; i++) {
      const float nr = pw[2] * cr - pw[3] * ci + __uint_as_float(v[i][0]), ni = pw[2] * ci + pw[3] * cr + __uint_as_float(v[i][2]); cr = nr; ci = ni;
    }
#pragma unroll
    for (int s = 0; s < 8; s++) {
      *(LAS f32x2*)(size_t)(scr + (s * 192 + c) * 8) = (f32x2){cr, ci};
      const float nr = pw[0] * cr - pw[1] * ci + es[s].x, ni = pw[0] * ci + pw[1] * cr + es[s].y; cr = nr; ci = ni;
    }
  }
}

#define LBAR() BAR()
DI void loader(const Params& p0, int rt, int g, int j, int u, int lane, u32 lds0, u32 tag) {
  Params p = p0;
  const bool ssm = g == 4;
  const u32 wbytes = ssm ? D * ROWB : D * F * 2;
  const rsrc_t rX = mk(p.x, S * ROWB), rA0 = mk(ssm ? p.bre : p.w1, wbytes), rA1 = mk(ssm ? p.bim : p.w1, wbytes);
  const int r8 = lane >> 3, gp = lane & 7;
  const int cgr = ((((gp >> 1) ^ ((r8 >> 1) & 3)) << 1) | (gp & 1));
  const int rgr = gp ^ (4 * u + (r8 >> 1));
  const int slice = 3 * g + j;
  const int kA = 16 * u + 8 * (r8 >> 2) + (r8 & 3);
  const int R = 8 * u + r8;
  const u32 voffA = ssm ? (192 * j + R) * ROWB + rgr * 16 : kA * (F * 2) + slice * 768 + cgr * 16;
  const int tok = ssm ? 16 * ((R >> 2) & 3) + 8 * (R & 1) + ((R >> 1) & 1) : R;
  const u32 voffX = (rt * 128 + tok) * ROWB + rgr * 16;
  const u32 ldsW = lds0 + u * 1024, ldsT = lds0 + TOFF;
  const u32 aKt = ssm ? 128 : 64 * F * 2, aPar = ssm ? 0 : 32 * F * 2, pstr = ssm ? 32 * ROWB : 128, a2 = ssm ? 16 * ROWB : 4 * F * 2, xstr = (ssm ? 2 : 16) * ROWB;
  const int rot = rt >= 9 ? rt - 9 : rt;
  auto ktile = [&](int kap) { int kt = kap + rot; return kt >= 9 ? kt - 9 : kt; };
  auto issueA = [&](int kap, int h) { dma12(h ? rA1 : rA0, ldsW + ((2 * kap + h) & 3) * WSLOT, voffA, ktile(kap) * aKt + h * aPar, a2, pstr, pstr); };
  auto issueX = [&](int kap, int h) { dma4(rX, ldsT + (kap & 3) * TSLOT + h * 8192 + u * 1024, voffX, h * 64 * ROWB + ktile(kap) * 128, xstr); };
  issueA(0, 0); issueX(0, 0); issueX(0, 1); issueA(0, 1);
  __builtin_amdgcn_s_setprio(3);
  waitv<12>(); BAR();
  issueX(1, 0); issueX(1, 1); issueA(1, 0);
#pragma nounroll
  for (int kap = 0; kap < 7; kap++) {
    u32 l1 = ldsW + ((2 * kap + 3) & 3) * WSLOT, s1 = ktile(kap + 1) * aKt + aPar, l2 = ldsW + ((2 * kap) & 3) * WSLOT, s2 = ktile(kap + 2) * aKt;
    u32 lx = ldsT + ((kap + 2) & 3) * TSLOT + u * 1024, lx2 = lx + 8192, sx = ktile(kap + 2) * 128, sx2 = sx + 64 * ROWB;
    asm volatile("" : "+s"(l1), "+s"(s1), "+s"(l2), "+s"(s2), "+s"(lx), "+s"(lx2), "+s"(sx), "+s"(sx2));
    waitv<16>(); BAR(); dma12(rA1, l1, voffA, s1, a2, pstr, pstr); dma4(rX, lx, voffX, sx, xstr);
    waitv<16>(); BAR(); dma12(rA0, l2, voffA, s2, a2, pstr, pstr); dma4(rX, lx2, voffX, sx2, xstr);
  }
  waitv<16>(); BAR(); issueA(8, 1);
  p = reload_params();
  const u32 voffL = lane * 16;
  const rsrc_t rWS = mk(p.ws, WS_END), rB0 = mk(ssm ? p.cre : p.w2, wbytes), rB1 = mk(ssm ? p.cim : p.w2, wbytes);
  const u32 voffB = ssm ? voffA : R * ROWB + j * 384 + cgr * 16;
  const u32 bJ = ssm ? 384 : 384 * ROWB, bPar = ssm ? 0 : 192 * ROWB, bI = ssm ? 128 : 32 * ROWB, b0 = ssm ? 0 : g * 1152 * ROWB;
  const int j1 = j == 2 ? 0 : j + 1, j2 = j == 0 ? 2 : j - 1;
  const u32 st3 = 96 * ROWB - 2 * pstr, pb = WS_P + (rt * 15 + 3 * g) * 98304 + u * 4096;
  auto issueB = [&](int s, int jj, int i) { dma12((i & 1) ? rB1 : rB0, ldsW + (s & 3) * WSLOT, voffB, b0 + jj * bJ + (i & 1) * bPar + (i >> 1) * bI, 16 * ROWB, pstr, st3); };
  auto issueP = [&](int s, int jj, int i) { dma8lin(rWS, ldsT + (s & 3) * TSLOT + u * 4096, voffL, pb + jj * 98304 + i * 16384); };
  waitv<12>(); BAR();
#pragma unroll
  for (int k = 0; k < 3; k++) dma_sc1(rWS, lds0 + (18 & 3) * WSLOT + AX_TAB + (u * 3 + k) * 1024, voffL, WS_TAB + j * 6144 + (u * 3 + k) * 1024);
  waitv<3>();
  BAR(); issueB(19, j, 0);
  BAR(); issueB(20, j, 1); waitv<12>();
  BAR(); issueB(21, j, 2); waitv<12>();
  if (ssm) { BAR(); if (u == 1) ssm_carry(p, rt, j, lane, lds0, tag); WAITL(); BAR(); BAR(); }
  BAR(); issueB(22, j, 3); waitv<12>();
  BAR(); issueB(23, j, 4); issueP(23, j, 4); waitv<20>();
  BAR(); issueB(24, j, 5); issueP(24, j, 5);
#pragma nounroll
  for (int n = 0; n < 3; n++) {
    const int i = n ? 2 * n - 2 : 4;
    u32 w1 = b0 + j1 * bJ + (i >> 1) * bI, w2 = b0 + j2 * bJ + (i >> 1) * bI, w3 = w1 + bPar, w4 = w2 + bPar;
    u32 p1 = pb + j1 * 98304 + i * 16384, p2 = pb + j2 * 98304 + i * 16384, p3 = p1 + 16384, p4 = p2 + 16384;
    asm volatile("" : "+s"(w1), "+s"(w2), "+s"(w3), "+s"(w4), "+s"(p1), "+s"(p2), "+s"(p3), "+s"(p4));
    waitv<20>(); BAR(); dma12(rB0, ldsW + 1 * WSLOT, voffB, w1, 16 * ROWB, pstr, st3); dma8lin(rWS, ldsT + 1 * TSLOT + u * 4096, voffL, p1);
    waitv<20>(); BAR(); dma12(rB0, ldsW + 2 * WSLOT, voffB, w2, 16 * ROWB, pstr, st3); dma8lin(rWS, ldsT + 2 * TSLOT + u * 4096, voffL, p2);
    waitv<20>(); BAR(); dma12(rB1, ldsW + 3 * WSLOT, voffB, w3, 16 * ROWB, pstr, st3); dma8lin(rWS, ldsT + 3 * TSLOT + u * 4096, voffL, p3);
    waitv<20>(); BAR(); dma12(rB1, ldsW + 0 * WSLOT, voffB, w4, 16 * ROWB, pstr, st3); dma8lin(rWS, ldsT + 0 * TSLOT + u * 4096, voffL, p4);
  }
  waitv<20>();
  BAR();
  {
    const u32 l = lds0 + WSLOT;
    const u32 vo = (lane & 31) < 24 ? (rt * 128 + u * 64 + (g & 3) * 16 + (lane >> 5)) * ROWB + j * 384 + (lane & 31) * 16 : 0x7fffffffu;
#pragma unroll
    for (int k = 0; k < 8; k++) dma(rX, l + (u * 8 + k) * 1024, vo, k * 2 * ROWB);
    dma(mk(u ? p.dskip : p.b2, ROWB), l + 16384 + u * 1024, voffL + j * 384, 0);
  }
  waitv<9>(); BAR(); BAR(); waitv<0>();
  BAR();
}

DI void helper(const Params& p, int rt, int g, int j, int h, int lane, u32 lds0, u32 tag) {
  const bool ssm = g == 4;
  const int slice = 3 * g + j, q = lane >> 4, jl = lane & 15;
  const rsrc_t rWS = mk(p.ws, WS_END);
  char* const hsR = p.ws + WS_RSP + blockIdx.x * 4;
  u32 hsn = 0;
  if (h == 0) { hsn = __builtin_amdgcn_readfirstlane(ld_flag(hsR)) + 1u; st_word(hsR - 1024, hsn); }
  __builtin_amdgcn_s_setprio(2);
  {
    const int e = (lane >> 2) & 3, z = 2 * (q & 1) + (e >> 1);
    bf16x8 f[12]; f32x4 c[12]; u32x4 one = {ONES, ONES, ONES, ONES};
    auto rdslot = [&](int sl) {
      if (ssm) {
        const u32 a = lds0 + h * 12288 + jl * 128 + ((q ^ (jl >> 1)) * 16);
#pragma unroll
        for (int n = 0; n < 6; n++) { f[n] = rd128(a, sl * WSLOT + n * 2048); f[6 + n] = rd128(a ^ 64, sl * WSLOT + n * 2048); }
      } else {
#pragma unroll
        for (int n = 0; n < 12; n++) f[n] = rd_tr(lds0 + h * 12288 + (4 * q + e) * 128 + (((n & 3) ^ z) * 32) + (lane & 3) * 8, sl * WSLOT + (n >> 2) * 4096);
      }
#pragma unroll
      for (int n = 0; n < 12; n++) PIN(f[n]);
      asm volatile("s_nop 1");
    };
#pragma unroll
    for (int n = 0; n < 12; n++) { c[n] = (f32x4){0.f, 0.f, 0.f, 0.f}; PIN(c[n]); }
    PIN(one);
    LBAR();
    rdslot(0);
    if (ssm) {
#pragma unroll
      for (int n = 0; n < 12; n++) MMA(c[n % 6], f[n], one);
      MFENCE();
    } else {
#pragma unroll
      for (int n = 0; n < 12; n++) MMA(c[n], f[n], one);
      MFENCE();
    }
    LBAR();
    rdslot(1);
    if (rt < 9) {
#pragma unroll
      for (int n = 6; n < 12; n++) MMA(c[n], f[n], one);
      MFENCE();
    }
    LBAR();
    if (rt < 9) {
      if (ssm) {
#pragma unroll
        for (int n = 0; n < 6; n++) MMA(c[6 + n], f[n], one);
      MFENCE();
      } else {
#pragma unroll
        for (int n = 0; n < 6; n++) MMA(c[n], f[n], one);
      MFENCE();
      }
    }
    f32x4 m = c[0];
#pragma unroll
    for (int n = 1; n < 12; n++) m = jl == n ? c[n] : m;
    const int col = (ssm ? (jl >= 6 ? 192 - 96 : 0) + 96 * h : 192 * h) + 16 * jl + 4 * q;
    st16(__builtin_bit_cast(u32x4, m), rWS, jl < 12 ? col * 4 : 0x7fffffffu, WS_CS + (slice * 16 + rt) * 1536);
  }
  LBAR();
  u32 pv = ld_flag(hsR);
  LBAR();
  waitv<0>(); LBAR();
  bool acked = h != 0;
#pragma unroll
  for (int s = 0; s < 2; s++) {
    if (!acked) {
      acked = __all(pv == hsn);
      if (!acked && s == 1) { spin_all(hsR, hsn); acked = true; }
      if (acked) st_word(hsR + 1024, 1u);
    }
    if (s < 1) pv = ld_flag(hsR);
    LBAR(); LBAR();
  }
  LBAR();
  const char* fGo = p.ws + WS_FLAGS + FL_GO * 128;
  pv = ld_flag(fGo);
  LBAR(); LBAR();
  if (!__all(pv == 1u)) spin_all(fGo, 1u);
  const int ir = 64 * h + lane, irt = ssm ? (ir & 64) + 16 * ((ir >> 2) & 3) + 8 * (ir & 1) + 2 * ((ir >> 4) & 3) + ((ir >> 1) & 1) : ir;
  const u32x2 sg = __builtin_amdgcn_raw_buffer_load_b64(rWS, (rt * 128 + irt) * 8, WS_ST, 16);
  const int cc = 192 * h + 4 * (lane < 48 ? lane : lane - 48);
  f32x4 cp[9];
#pragma unroll
  for (int k = 0; k < 9; k++) cp[k] = __builtin_bit_cast(f32x4, __builtin_amdgcn_raw_buffer_load_b128(rWS, cc * 4, WS_CS + (slice * 16 + k) * 1536, 16));
  const u32x2 bb = *(const GAS u32x2*)(p.b1 + (ssm ? 0 : slice * 384) + cc);
  LBAR(); LBAR(); SB();
  f32x4 cs = ((cp[0] + cp[1]) + (cp[2] + cp[3])) + ((cp[4] + cp[5]) + (cp[6] + cp[7])) + cp[8];
  float mean = __uint_as_float(sg[0]), rstd = __builtin_amdgcn_rsqf(__uint_as_float(sg[1])), sig = __uint_as_float(sg[1]) * rstd;
  u32x4 at, aw[4];
  { float m1, m2, m3; split3(-mean, m1, m2, m3); const float g1 = rbf(sig), g2 = sig - g1;
    at = (u32x4){cvtpk(m1, m1), cvtpk(m2, m2), cvtpk(m1, m3), cvtpk(g1, g2)}; mean = ssm ? -mean * rstd : mean; }
  PIN(cs); PIN(at); PIN(mean); PIN(rstd); PIN(sig);
  auto auxw = [&](int k) { float c1, c2, c3; split3(cs[k], c1, c2, c3); const float b = (k & 1) ? bfhi(bb[k >> 1]) : bflo(bb[k >> 1]);
    aw[k] = (u32x4){cvtpk(c1, c2), cvtpk(c1, c2), cvtpk(c3, c1), cvtpk(b, b)}; PIN(aw[k]); };
  LBAR(); SB();
  auxw(0); auxw(1);
  const u32 ax = lds0 + (18 & 3) * WSLOT;
  LBAR(); SB();
  wr128(ax + AX_T + ir * 16, 0, at);
  { LAS float* sp = (LAS float*)(size_t)(ax + AX_S + ir * 4); sp[0] = mean; sp[128] = rstd; sp[256] = sig; }
  auxw(2); auxw(3);
  LBAR(); SB();
  if (ssm) *(LAS f32x4*)(size_t)(ax + AX_W + cc * 4) = cs;
  else {
#pragma unroll
    for (int k = 0; k < 4; k++) wr128(ax + AX_W + cc * 4, k * 1600, aw[k]);
  }
  WAITL(); LBAR();
  LBAR(); SB();
  if (ssm) { LBAR(); ssm_carry(p, rt, j, 64 + 64 * h + lane, lds0, tag); WAITL(); LBAR(); BAR(); }
  char* const fMine = p.ws + WS_FLAGS + (FL_P + rt * 15 + slice) * 128;
  const char* fP = p.ws + WS_FLAGS + (FL_P + rt * 15 + 3 * g + ((lane & 1) ? (j == 2 ? 0 : j + 1) : (j == 0 ? 2 : j - 1))) * 128;
  LBAR(); if (h == 1 && !ssm) st_word(fMine, 2u);
  LBAR(); if (h == 1 && ssm) st_word(fMine, 1u);
#pragma nounroll
  for (u32 lv = 1; lv < 4; lv++) {
    LBAR(); if (h == 0) spin_ge(fP, lv);
    LBAR(); if (h == 1 && lv == 1) st_word(fMine, 3u);
    LBAR(); LBAR();
  }
  LBAR(); LBAR(); LBAR(); LBAR();
  LBAR();
}

template <bool SSM>
DI void phaseB(const Params& p, int rt, int g, int j, int w, int lane, u32 lds0, u32 tag) {
  asm volatile("" : "+v"(lane));
  const int rg = w >> 2, cg = w & 3, q = lane >> 4, e = (lane >> 2) & 3, pp = lane & 3, jl = lane & 15;
  const int z = 2 * (q & 1) + (e >> 1);
  u32 wlo[3], whi[3], tb[2];
#pragma unroll
  for (int n = 0; n < 3; n++) {
    const int t = 3 * cg + n;
    wlo[n] = SSM ? lds0 + (16 * t + jl) * 128 + ((q ^ (jl >> 1)) * 16) : lds0 + (t >> 2) * 4096 + (4 * q + e) * 128 + (((t & 3) ^ z) * 32) + pp * 8;
    whi[n] = SSM ? (wlo[n] ^ 64) : wlo[n] + 2 * WSLOT;
  }
  tb[0] = SSM ? lds0 + TOFF + rg * 8192 + (q >> 1) * 2048 + (pp >> 1) * 1024 + (((8 * (q & 1) + e) * 16) ^ ((pp >> 1) * 64)) + 8 * (pp & 1) : lds0 + TOFF + rg * 4096 + lane * 16;
  tb[1] = tb[0] ^ 64;
  float mean = 0.f, rstd = 0.f;
  if constexpr (!SSM) { const u32 sa = lds0 + (18 & 3) * WSLOT + AX_S + (64 * rg + 16 * g + jl) * 4; mean = rdf(sa, 0); rstd = rdf(sa, 512); }
  f32x4 ob[3][4];
#pragma unroll
  for (int n = 0; n < 3; n++)
#pragma unroll
    for (int T = 0; T < 4; T++) { ob[n][T] = (f32x4){0.f, 0.f, 0.f, 0.f}; PIN(ob[n][T]); }
  auto RW = [&](int sl, int kb, int n, int part) {
    if constexpr (SSM) return rd128((kb ? whi[n] : wlo[n]) + (sl >> 1) * 2 * WSLOT, (sl & 1) * WSLOT);
    else return rd_tr1(sl < 2 ? wlo[n] : whi[n], (sl & 1) * WSLOT + kb * 12288 + part * 2048);
  };
  auto RT = [&](int sl, int kb, int T, int part) {
    if constexpr (SSM) return rd_tr1(tb[part], sl * TSLOT + kb * 4096 + T * 256);
    else return rd128(tb[0], sl * TSLOT + kb * 8192 + T * 1024);
  };
  bf16x8 wf[3], tp[2];
  auto step = [&](auto slc, auto&& mid) {
    constexpr int SL = decltype(slc)::value, NS = (SL + 1) & 3;
    half<SSM ? 1 : 0>(ob, wf, tp, [&](int n, int part) { return RW(SL, 1, n, part); }, [&](int T, int part) { return RT(SL, 0, T, part); }, [&](int T, int part) { return RT(SL, 1, T, part); }, mid);
    half<SSM ? 1 : 0>(ob, wf, tp, [&](int n, int part) { return RW(NS, 0, n, part); }, [&](int T, int part) { return RT(SL, 1, T, part); }, [&](int T, int part) { return RT(NS, 0, T, part); }, [](int) {});
  };
  using I0 = std::integral_constant<int, 0>; using I1 = std::integral_constant<int, 1>; using I2 = std::integral_constant<int, 2>; using I3 = std::integral_constant<int, 3>;
  if constexpr (!SSM) waitv<4>();
  WAITL(); BAR();
#pragma unroll
  for (int n = 0; n < 3; n++) { if constexpr (SSM) wf[n] = RW(3, 0, n, 0); else wf[n] = cat(RW(3, 0, n, 0), RW(3, 0, n, 1)); }
#pragma unroll
  for (int T = 0; T < 2; T++) { if constexpr (SSM) tp[T] = cat(RT(3, 0, T, 0), RT(3, 0, T, 1)); else tp[T] = RT(3, 0, T, 0); }
  auto barm = [](int i) { if (i == 11) BAR(); };
  step(I3{}, [](int) {});
  step(I0{}, [&](int i) { if (i == 3) { if constexpr (SSM) waitv<8>(); BAR(); } });
#pragma nounroll
  for (int it = 0; it < 4; it++) { step(I1{}, barm); step(I2{}, [&](int i) { if (i == 11) { waitv<0>(); BAR(); } }); step(I3{}, barm); step(I0{}, barm); }
  MFENCE();
  const rsrc_t rWS = mk(p.ws, WS_END);
  const u32 xb = WS_XB + (rt * 3 + j) * 32 * XBW + w * XBW;
  f32x4 mine[3];
#pragma unroll
  for (int T = 0; T < 4; T++) {
    if (SSM || T != g) {
      const u32 o = xb + T * 8 * XBW + g * 1536;
      const u32x4 a = {cvtpk(ob[0][T][0], ob[0][T][1]), cvtpk(ob[0][T][2], ob[0][T][3]), cvtpk(ob[1][T][0], ob[1][T][1]), cvtpk(ob[1][T][2], ob[1][T][3])};
      const u32 b0 = cvtpk(ob[2][T][0], ob[2][T][1]), b1 = cvtpk(ob[2][T][2], ob[2][T][3]);
      const auto s0 = __builtin_amdgcn_permlane32_swap(b0, b0, false, false), s1 = __builtin_amdgcn_permlane32_swap(b1, b1, false, false);
      st16(a, rWS, lane * 16, o);
      if (lane < 32) st16((u32x4){b0, b1, s0[1], s1[1]}, rWS, lane * 16, o + 1024);
    } else {
#pragma unroll
      for (int n = 0; n < 3; n++) mine[n] = ob[n][T];
    }
  }
  if constexpr (SSM) {
    waitv<0>(); BAR();
    if (threadIdx.x == 0) st_flag(p.ws, FL_O + (rt * 3 + j) * 5 + g, tag);
    STAMP(7);
  } else {
    waitv<0>(); BAR();
    if (threadIdx.x == 0) st_flag(p.ws, FL_O + (rt * 3 + j) * 5 + g, tag);
    const int tok = rt * 128 + 64 * rg + 16 * g + jl, col = 192 * j + 48 * cg + 4 * q;
    u32x2 xv[3], bv[3], dv[3];
#pragma unroll
    for (int n = 0; n < 3; n++) {
      const u32 cb = lds0 + WSLOT + (48 * cg + 4 * q + 16 * n) * 2;
      xv[n] = *(LAS u32x2*)(size_t)(cb + (16 * rg + jl) * 512); bv[n] = *(LAS u32x2*)(size_t)(cb + 16384); dv[n] = *(LAS u32x2*)(size_t)(cb + 17408);
    }
    const u32 o = xb + g * 8 * XBW;
    u32x4 a[4]; u32x2 b[4];
#pragma unroll
    for (int k = 0; k < 4; k++) {
      if (k == 0 || k == 3) spin_all(p.ws + WS_FLAGS + (FL_O + (rt * 3 + j) * 5 + (k ? 4 : (lane & 3))) * 128, tag);
      const int src = k == 3 ? 4 : (k < g ? k : k + 1);
      a[k] = __builtin_amdgcn_raw_buffer_load_b128(rWS, lane * 16, o + src * 1536, 16);
      b[k] = __builtin_amdgcn_raw_buffer_load_b64(rWS, (lane & 31) * 16 + (lane >> 5) * 8, o + src * 1536 + 1024, 16);
    }
#pragma unroll
    for (int n = 0; n < 3; n++) {
      f32x4 r;
#pragma unroll
      for (int c = 0; c < 4; c++) {
        float G = mine[n][c] + ((c & 1) ? bfhi(bv[n][c >> 1]) : bflo(bv[n][c >> 1]));
#pragma unroll
        for (int k = 0; k < 3; k++) { const u32 v = n < 2 ? a[k][2 * n + (c >> 1)] : b[k][c >> 1]; G += (c & 1) ? bfhi(v) : bflo(v); }
        const u32 yv = n < 2 ? a[3][2 * n + (c >> 1)] : b[3][c >> 1];
        const float yy = (c & 1) ? bfhi(yv) : bflo(yv);
        const float xx = (c & 1) ? bfhi(xv[n][c >> 1]) : bflo(xv[n][c >> 1]);
        const float dd = (c & 1) ? bfhi(dv[n][c >> 1]) : bflo(dv[n][c >> 1]);
        const float sv = yy + (xx - mean) * rstd * dd;
        const float sig = __builtin_amdgcn_rcpf(1.f + __builtin_amdgcn_exp2f(-1.4426950408889634f * G));
        r[c] = xx + sv * (G * sig);
      }
      *(GAS f32x4*)(p.out + tok * D + col + 16 * n) = r;
    }
    STAMP(7);
  }
}

DI void ffn_compute(const Params& p, int rt, int g, int j, int w, int lane, u32 lds0, u32 tag) {
  const int rg = w >> 2, cg = w & 3, q = lane >> 4, e = (lane >> 2) & 3, pp = lane & 3, jl = lane & 15;
  const int z = 2 * (q & 1) + (e >> 1);
  u32 wc[4];
#pragma unroll
  for (int n = 0; n < 4; n++) { const int t = 6 * cg + n; wc[n] = lds0 + (t >> 2) * 4096 + (4 * q + e) * 128 + (((t & 3) ^ z) * 32) + pp * 8; }
  const u32 xbase = lds0 + TOFF + (64 * rg + jl) * 128 + ((q ^ (jl >> 1)) * 16);
  f32x4 acc[6][4];
#pragma unroll
  for (int n = 0; n < 6; n++)
#pragma unroll
    for (int T = 0; T < 4; T++) { acc[n][T] = (f32x4){0.f, 0.f, 0.f, 0.f}; asm volatile("" : "+v"(acc[n][T])); }
  bf16x8 wp[2], t[4];
  BAR();
  wp[0] = rd_tr(wc[0], 0); wp[1] = rd_tr(wc[1], 0);
#pragma unroll
  for (int T = 0; T < 4; T++) t[T] = rd128(xbase, T * 2048);
  u32 xs = 0, xn = 0, n0 = 0, n1 = 0; int dw = 2 * WSLOT;
#pragma nounroll
  for (int it = 0; it < 9; it++) {
    stepA<0>(acc, wp, t, wc, n0, n1, xn, [&]() { xn = (xbase ^ 64) + xs; PIN(xn); xs = (xs + TSLOT) & (4 * TSLOT - 1); });
    stepA<1>(acc, wp, t, wc, n0, n1, xn, [&]() { xn = xbase + xs; n0 = wc[0] + dw; n1 = wc[1] + dw; wc[2] += dw; wc[3] += dw; PIN(xn); PIN(n0); PIN(n1); PIN(wc[2]); PIN(wc[3]); waitv<0>(); });
    wc[0] = n0; wc[1] = n1; dw = -dw;
  }
  const u32 ax = lds0 + (18 & 3) * WSLOT;
  float k1[4], k2[4], sg[4];
#pragma unroll
  for (int T = 0; T < 4; T++) {
    u32x4 v = __builtin_bit_cast(u32x4, rd128(ax + AX_T + (64 * rg + jl) * 16, T * 256));
    if (q != 0) v = (u32x4){0u, 0u, 0u, 0u};
    t[T] = __builtin_bit_cast(bf16x8, v);
    const float r = rdf(ax + AX_S + 512 + (64 * rg + jl) * 4, T * 64);
    sg[T] = rdf(ax + AX_S + 1024 + (64 * rg + jl) * 4, T * 64);
    k1[T] = -2.3022081985f * r; k2[T] = -0.10294324f * r * r * r;
  }
  {
    bf16x8 a[6];
#pragma unroll
    for (int n = 0; n < 6; n++) a[n] = rd128(ax + AX_W + ((jl & 3) * 100 + 24 * cg + (jl >> 2)) * 16, n * 64);
    asm volatile("s_nop 1" : "+v"(t[0]), "+v"(t[1]), "+v"(t[2]), "+v"(t[3]));
#pragma unroll
    for (int n = 0; n < 6; n++)
#pragma unroll
      for (int T = 0; T < 4; T++) MMA(acc[n][T], a[n], t[T]);
    MFENCE();
  }
  const rsrc_t rWS = mk(p.ws, WS_END);
  const u32 pg = WS_P + (rt * 15 + 3 * g + j) * 98304 + (cg >> 1) * 16384 + (cg & 1) * 8192 + rg * 4096;
  const u32 pl = lds0 + TOFF + (cg & 1) * 8192 + rg * 4096 + lane * 16, pl0 = pl + ((19 + (cg >> 1)) & 3) * TSLOT, pl1 = pl + ((21 + (cg >> 1)) & 3) * TSLOT;
#pragma unroll
  for (int bb = 0; bb < 3; bb++) {
    const int b = (bb + 2) % 3;
#pragma unroll
    for (int T = 0; T < 4; T++) {
      const u32x4 P = gelu_frag(acc[2 * b][T], acc[2 * b + 1][T], k1[T], k2[T], sg[T]);
      st16(P, rWS, lane * 16, pg + b * 32768 + T * 1024);
      if (b < 2) wr128(b ? pl1 : pl0, T * 1024, P);
      SB();
    }
    if (bb == 0) BAR();
  }
  phaseB<false>(p, rt, g, j, w, lane, lds0, tag);
}

DI void ssm_compute(const Params& p, int rt, int j, int w, int lane, u32 lds0, u32 tag) {
  const int rg = w >> 2, cg = w & 3; int q = lane >> 4, jl = lane & 15;
  u32 wb = lds0 + (16 * cg + jl) * 128 + ((q ^ (jl >> 1)) * 16);
  const u32 xb0 = lds0 + TOFF + (64 * rg + jl) * 128 + ((q ^ (jl >> 1)) * 16);
  f32x4 ar[3][4], ai[3][4];
#pragma unroll
  for (int n = 0; n < 3; n++)
#pragma unroll
    for (int T = 0; T < 4; T++) { ar[n][T] = (f32x4){0.f, 0.f, 0.f, 0.f}; ai[n][T] = (f32x4){0.f, 0.f, 0.f, 0.f}; asm volatile("" : "+v"(ar[n][T]), "+v"(ai[n][T])); }
  bf16x8 wf[3], tp[2];
  auto nop = [](int) {};
  auto barm = [](int i) { if (i == 11) BAR(); };
  BAR();
#pragma unroll
  for (int n = 0; n < 3; n++) wf[n] = rd128(wb, n * 8192);
  tp[0] = rd128(xb0, 0); tp[1] = rd128(xb0, 2048);
  u32 xs = 0; int dw = 2 * WSLOT;
#pragma nounroll
  for (int it = 0; it < 9; it++) {
    const u32 x0 = xb0 + xs, x1 = (xb0 ^ 64) + xs, wb1 = wb ^ 64, wn = wb + dw;
    xs = (xs + TSLOT) & (4 * TSLOT - 1);
    const u32 xn = xb0 + xs;
    half<2>(ar, wf, tp, [&](int n, int) { return rd128(wb1, n * 8192); }, [&](int T, int) { return rd128(x0, T * 2048); }, [&](int T, int) { return rd128(x1, T * 2048); }, barm);
    half<2>(ar, wf, tp, [&](int n, int) { return rd128(wb, WSLOT + n * 8192); }, [&](int T, int) { return rd128(x1, T * 2048); }, [&](int T, int) { return rd128(x0, T * 2048); }, nop);
    half<2>(ai, wf, tp, [&](int n, int) { return rd128(wb1, WSLOT + n * 8192); }, [&](int T, int) { return rd128(x0, T * 2048); }, [&](int T, int) { return rd128(x1, T * 2048); }, barm);
    half<2>(ai, wf, tp, [&](int n, int) { return rd128(wn, n * 8192); }, [&](int T, int) { return rd128(x1, T * 2048); }, [&](int T, int) { return rd128(xn, T * 2048); }, nop);
    wb = wn; dw = -dw;
    waitv<0>();
  }
  WAITL(); BAR();
  asm volatile("" : "+v"(lane));
  q = lane >> 4; jl = lane & 15;
  const u32 ax = lds0 + (18 & 3) * WSLOT;
  MFENCE();
  f32x4 lc[3];
  const u32 scr = lds0 + TOFF + (22 & 3) * TSLOT + ((4 * rg + q) * 192 + 16 * cg + jl) * 8;
  f32x2 e0[3]; float csr[3], csi[3];
#pragma unroll
  for (int b = 0; b < 3; b++) { csr[b] = rdf(ax + AX_W + (16 * cg + jl) * 4, b * 256); csi[b] = rdf(ax + AX_W + (16 * cg + jl) * 4, 768 + b * 256); }
#pragma unroll
  for (int T = 0; T < 4; T++) {
    const f32x4 r = rdf4(ax + AX_S + 512 + (64 * rg + 4 * q) * 4, T * 64), nm = rdf4(ax + AX_S + (64 * rg + 4 * q) * 4, T * 64);
#pragma unroll
    for (int b = 0; b < 3; b++) { ar[b][T] = ar[b][T] * r + nm * csr[b]; ai[b][T] = ai[b][T] * r + nm * csi[b]; }
  }
  f32x2 l8[3];
#pragma unroll
  for (int b = 0; b < 3; b++) {
    lc[b] = rdf4(ax + AX_TAB + (16 * cg + jl) * 32, b * 2048);
    l8[b] = *(LAS f32x2*)(size_t)(ax + AX_TAB + (16 * cg + jl) * 32 + b * 2048 + 16);
    f32x2 hr = {ar[b][0][0], ar[b][0][1]}, hi = {ai[b][0][0], ai[b][0][1]};
#pragma unroll
    for (int k = 1; k < 8; k++) {
      const f32x2 ur = {ar[b][k >> 1][(k & 1) * 2], ar[b][k >> 1][(k & 1) * 2 + 1]}, ui = {ai[b][k >> 1][(k & 1) * 2], ai[b][k >> 1][(k & 1) * 2 + 1]};
      const f32x2 nr = lc[b][0] * hr + (ur - lc[b][1] * hi), ni = lc[b][0] * hi + (ui + lc[b][1] * hr);
      hr = nr; hi = ni;
    }
    e0[b] = (f32x2){hr.x, hi.x};
    *(LAS f32x2*)(size_t)(scr + b * 512) = (f32x2){l8[b].x * hr.x - l8[b].y * hi.x + hr.y, l8[b].x * hi.x + l8[b].y * hr.x + hi.y};
  }
  WAITL(); BAR(); SB();
#pragma unroll
  for (int b = 0; b < 3; b++) {
    const float c2r = 2.f * lc[b][2], c2i = 2.f * lc[b][3];
#pragma unroll
    for (int T = 0; T < 4; T++) {
      const f32x4 nr = c2r * ar[b][T] - c2i * ai[b][T], ni = -c2r * ai[b][T] - c2i * ar[b][T];
      ar[b][T] = nr; ai[b][T] = ni;
      asm volatile("" : "+v"(ar[b][T]), "+v"(ai[b][T]));
    }
  }
  BAR();
  const rsrc_t rWS = mk(p.ws, WS_END);
  const u32 io0 = w * 2048 + lane * 16, io1 = w * 2048 + 1024 + ((lane * 16) ^ 64);
  const u32 pg = WS_P + (rt * 15 + 12 + j) * 98304;
  f32x2 cins[3];
#pragma unroll
  for (int b = 0; b < 3; b++) { cins[b] = *(LAS f32x2*)(size_t)(scr + b * 512); l8[b] = *(LAS f32x2*)(size_t)(ax + AX_TAB + (16 * cg + jl) * 32 + b * 2048 + 16); }
#pragma unroll
  for (int bb = 0; bb < 3; bb++) {
    const int b = (bb + 2) % 3;
    u32x4 H[2][2];
    const f32x2 cin = cins[b];
    const f32x2 sr = {cin.x, l8[b].x * cin.x - l8[b].y * cin.y + e0[b].x}, si = {cin.y, l8[b].x * cin.y + l8[b].y * cin.x + e0[b].y};
    const float c2r = 2.f * lc[b][2], c2i = 2.f * lc[b][3];
    f32x2 hr = c2r * sr - c2i * si, hi = -c2r * si - c2i * sr;
#pragma unroll
    for (int T = 0; T < 4; T++) {
      f32x2 pr[2], pi[2];
#pragma unroll
      for (int k = 0; k < 2; k++) {
        const f32x2 ur = {ar[b][T][2 * k], ar[b][T][2 * k + 1]}, ui = {ai[b][T][2 * k], ai[b][T][2 * k + 1]};
        const f32x2 nr = lc[b][0] * hr + (ur + lc[b][1] * hi), ni = lc[b][0] * hi + (ui - lc[b][1] * hr);
        hr = nr; hi = ni; pr[k] = nr; pi[k] = ni;
      }
      H[0][0][T] = cvtpk(pr[0].x, pr[1].x); H[0][1][T] = cvtpk(pr[0].y, pr[1].y);
      H[1][0][T] = cvtpk(pi[0].x, pi[1].x); H[1][1][T] = cvtpk(pi[0].y, pi[1].y);
    }
    if (bb == 1) BAR();
#pragma unroll
    for (int k = 0; k < 4; k++) {
      st16(H[k >> 1][k & 1], rWS, (k & 1) ? io1 : io0, pg + (2 * b + (k >> 1)) * 16384);
      if (b < 2) wr128(lds0 + TOFF + ((k & 1) ? io1 : io0), ((19 + 2 * b + (k >> 1)) & 3) * TSLOT, H[k >> 1][k & 1]);
    }
    SB();
  }
  phaseB<true>(p, rt, 4, j, w, lane, lds0, tag);
}

DI void spare(const Params& p, int sid, u32 lds0, u32 tag, u64 pc0) {
  const int tid = threadIdx.x;
  u32 cpf = 0;
  if (tid < CODE_LINES && !(sid & 1)) cpf = *(const GAS u32*)(pc0 + tid * 128);
  const rsrc_t rWS = mk(p.ws, WS_END);
  const int w = __builtin_amdgcn_readfirstlane(tid >> 6), lane = tid & 63;
  if (sid == 15 && w == 11) { waitv<0>(); hs_leader(p.ws, lane, lds0, STAMPPTR); return; }
  u32 hsn = 0;
  if (w == 11) { hsn = ld_flag(p.ws + WS_RSP + blockIdx.x * 4) + 1u; st_word(p.ws + WS_REQ + blockIdx.x * 4, hsn); }
  else if (w == 10) {
    const int row = sid * 128 + 120 + (lane >> 3);
    u32x4 v[9]; float s = 0.f;
#pragma unroll
    for (int i = 0; i < 9; i++) v[i] = ((const GAS u32x4*)(p.x + row * D) + (lane & 7))[8 * i];
#pragma unroll
    for (int i = 0; i < 9; i++)
#pragma unroll
      for (int c = 0; c < 4; c++) s += bflo(v[i][c]) + bfhi(v[i][c]);
    s += dppx<0xb1>(s); s += dppx<0x4e>(s); s += dppx<0x141>(s);
    const float mean = s * (1.f / D); s = 0.f;
#pragma unroll
    for (int i = 0; i < 9; i++)
#pragma unroll
      for (int c = 0; c < 4; c++) { const float a = bflo(v[i][c]) - mean, b = bfhi(v[i][c]) - mean; s += a * a + b * b; }
    s += dppx<0xb1>(s); s += dppx<0x4e>(s); s += dppx<0x141>(s);
    if ((lane & 7) == 0) __hip_atomic_store((GAS u64*)(p.ws + WS_ST + row * 8), ((u64)__float_as_uint(s * (1.f / D) + 1e-5f) << 32) | __float_as_uint(mean), __ATOMIC_RELAXED, __HIP_MEMORY_SCOPE_AGENT);
  } else if (w == 9 && sid < 9) {
    const int c = sid * 64 + lane;
    const float lr = bf2f(p.lre[c]), li = bf2f(p.lim[c]);
    const float dt = rbf(expf(bf2f(p.logdt[c])));
    const float ex = expf(dt * lr), an = dt * li;
    const float br = ex * cosf(an), bi = ex * sinf(an);
    const float nr = br - 1.f, den = 1.f / (lr * lr + li * li);
    const float cr = (nr * lr + bi * li) * den, ci = (bi * lr - nr * li) * den;
    float ar = br, ai = bi, r8 = 0.f, i8 = 0.f;
#pragma unroll
    for (int k = 0; k < 7; k++) { const float t = ar * ar - ai * ai; ai = 2.f * ar * ai; ar = t; if (k == 2) { r8 = ar; i8 = ai; } }
    st16(__builtin_bit_cast(u32x4, (f32x4){br, bi, cr, ci}), rWS, c * 32, WS_TAB);
    st16(__builtin_bit_cast(u32x4, (f32x4){r8, i8, ar, ai}), rWS, c * 32 + 16, WS_TAB);
  }
  if (w == 11) spin_all(p.ws + WS_RSP + blockIdx.x * 4, hsn);
  waitv<0>(); asm volatile("" :: "v"(cpf)); BAR();
  if (sid == 15) return;
  if (w == 11) st_word(p.ws + WS_ACK + blockIdx.x * 4, 1u);
  {
    u32 acc = cpf;
    const GAS u32* w2 = (const GAS u32*)p.w2;
#pragma unroll
    for (int i = 0; i < 4; i++) { const int L = (i * 15 + sid) * 768 + tid; if (L < 41472) acc ^= w2[L * 32]; }
    { const int L = sid * 768 + tid; if (L < 5184) acc ^= ((const GAS u32*)p.cre)[L * 32]; else if (L < 10368) acc ^= ((const GAS u32*)p.cim)[(L - 5184) * 32]; }
    asm volatile("" :: "v"(acc));
  }
}

__global__ __launch_bounds__(768, 3) void fused_kernel(Params p) {
  extern __shared__ __attribute__((aligned(16))) char smem[];
  const u32 lds0 = (u32)(size_t)(LAS char*)smem;
  const int b = blockIdx.x, xcd = b & 7, idx = b >> 3;
  const u32 tag = 1u;
  u64 pc0; asm volatile("s_getpc_b64 %0" : "=s"(pc0));
  STAMP(0);
  const int tri = idx / 3, j = idx - 3 * tri;
  const int g = tri < 2 ? 4 : xcd >> 1, rt = 8 * (xcd & 1) + (tri < 2 ? (xcd & 6) + tri : tri - 2);
  const int lane = threadIdx.x & 63, w = __builtin_amdgcn_readfirstlane(threadIdx.x >> 6);
  const int sid = xcd * 2 + idx - 30;
  const int srow = rt * 128 + (3 * g + j) * 8 + w;
  int role = idx < 30 ? w >> 1 : 0;
  if (w < 8 || idx >= 30) {
    if (idx < 30) {
      if (w == 0 && lane < 2) st_flag(p.ws, lane ? FL_P + rt * 15 + 3 * g + j : FL_O + (rt * 3 + j) * 5 + g, 0u);
      if (g == 4 && w >= 1 && w < 4) st16((u32x4){0u, 0u, 0u, 0u}, mk(p.ws, WS_END), (192 * j + threadIdx.x - 64) * 16, WS_EG + rt * 576 * 16);
    } else if (w == 11 && sid == 15) {
      st_flag(p.ws, FL_GO, 0u);
      st16((u32x4){0u, 0u, 0u, 0u}, mk(p.ws, WS_END), lane * 16, WS_ACK);
    }
    if (idx < 30) {
      float mean, ve; row_stats(p.x + srow * D, lane, mean, ve);
      if (lane == 0) __hip_atomic_store((GAS u64*)(p.ws + WS_ST + srow * 8), ((u64)__float_as_uint(ve) << 32) | __float_as_uint(mean), __ATOMIC_RELAXED, __HIP_MEMORY_SCOPE_AGENT);
    }
  }
  asm volatile("" : "+s"(role));
  if (__builtin_expect(role == 4, 1)) { loader(p, rt, g, j, w - 8, lane, lds0, tag); return; }
  if (idx < 30 && w >= 10) { const Params q = reload_params(); helper(q, rt, g, j, w - 10, lane, lds0, tag); return; }
  if (idx >= 30) { const Params q = reload_params(); spare(q, sid, lds0, tag, pc0); STAMP(7); return; }
  if (g < 4) { const Params q = reload_params(); ffn_compute(q, rt, g, j, w, lane, lds0, tag); }
  else { const Params q = reload_params(); ssm_compute(q, rt, j, w, lane, lds0, tag); }
}

extern "C" void kernel_launch(void* const* d_in, const int* in_sizes, int n_in, void* d_out, int out_size, void* d_ws, size_t ws_size, hipStream_t stream) {
  Params p;
  p.x = (const u16*)d_in[0]; p.lre = (const u16*)d_in[1]; p.lim = (const u16*)d_in[2]; p.bre = (const u16*)d_in[3]; p.bim = (const u16*)d_in[4];
  p.cre = (const u16*)d_in[5]; p.cim = (const u16*)d_in[6]; p.logdt = (const u16*)d_in[7]; p.dskip = (const u16*)d_in[8];
  p.w1 = (const u16*)d_in[10]; p.b1 = (const u16*)d_in[11]; p.w2 = (const u16*)d_in[12]; p.b2 = (const u16*)d_in[13];
  p.out = (float*)d_out; p.ws = (char*)d_ws;
  (void)hipFuncSetAttribute((const void*)fused_kernel, hipFuncAttributeMaxDynamicSharedMemorySize, LDS_BYTES);
  fused_kernel<<<256, 768, LDS_BYTES, stream>>>(p);
}
```

```cpp
#define STAMPS
#define UNIT 10
#define K0 0
#define A0 0
#define B0 0
#define K1 0
#define A1 77
#define B1 0
#include <hip/hip_runtime.h>
#include <utility>

#define LAS __attribute__((address_space(3)))
#define GAS __attribute__((address_space(1)))
#define DI __device__ __forceinline__
typedef unsigned short u16;
typedef unsigned u32;
typedef unsigned long long u64;
typedef unsigned u32x4 __attribute__((ext_vector_type(4)));
typedef unsigned u32x2 __attribute__((ext_vector_type(2)));
typedef short s16x4 __attribute__((ext_vector_type(4)));
typedef short bf16x8 __attribute__((ext_vector_type(8)));
typedef float f32x4 __attribute__((ext_vector_type(4)));
typedef float f32x2 __attribute__((ext_vector_type(2)));
typedef __bf16 bf16x2 __attribute__((ext_vector_type(2)));
typedef __amdgpu_buffer_rsrc_t rsrc_t;

constexpr int S = 2048, D = 576, F = 4608;
constexpr int ROWB = D * 2;
constexpr int WSLOT = 24576, TOFF = 4 * WSLOT, TSLOT = 16384, LDS_BYTES = 163840;
constexpr int AX_W = 0, AX_T = 6656, AX_S = 8704, AX_TAB = 10752;
constexpr u32 WS_FLAGS = 0;
constexpr u32 FL_GO = 0, FL_P = 32, FL_O = 32 + 240, NFLAG = 32 + 480;
constexpr u32 WS_EG = NFLAG * 128;
constexpr u32 WS_ST = WS_EG + 16 * 576 * 16;
constexpr u32 WS_REQ = WS_ST + 3 * 12288, WS_RSP = WS_REQ + 1024, WS_ACK = WS_RSP + 1024;
constexpr u32 WS_CS = 256 * 1024;
constexpr u32 WS_TAB = WS_CS + 15 * 16 * 1536;
constexpr u32 WS_P = 1u << 20;
constexpr u32 XBW = 5 * 1536;
constexpr u32 WS_XB = WS_P + 240 * 98304;
constexpr u32 WS_END = WS_XB + 48 * 32 * XBW;
constexpr int CODE_LINES = 430;
constexpr u32 SPIN_CAP = 1u << 18;

struct Params {
  const u16 *x, *lre, *lim, *bre, *bim, *cre, *cim, *logdt, *dskip, *w1, *b1, *w2, *b2;
  float* out; char* ws;
  u64* stamps;
};

DI Params reload_params() {
  Params q;
#if defined(__HIP_DEVICE_COMPILE__)
  typedef __attribute__((address_space(4))) const Params* KP;
  KP kp = (KP)__builtin_amdgcn_kernarg_segment_ptr(); asm volatile("" : "+s"(kp)); q = *kp;
#endif
  return q;
}
template <int N> DI void waitv() { asm volatile("s_waitcnt vmcnt(%0)" :: "n"(N) : "memory"); }
#define WAITL() asm volatile("s_waitcnt lgkmcnt(0)" ::: "memory")
#define BAR() do { asm volatile("" ::: "memory"); __builtin_amdgcn_s_barrier(); asm volatile("" ::: "memory"); } while (0)
#define SB() __builtin_amdgcn_sched_barrier(0)
#define MFENCE() do { SB(); asm volatile("s_nop 7\n\ts_nop 7"); SB(); } while (0)
#define MMA(c, a, b) asm volatile("v_mfma_f32_16x16x32_bf16 %0, %1, %2, %0" : "+v"(c) : "v"(a), "v"(b))
#define MMA0(c, a, b) asm volatile("v_mfma_f32_16x16x32_bf16 %0, %1, %2, 0" : "=&v"(c) : "v"(a), "v"(b))
#define PIN(x) asm volatile("" : "+v"(x))
#define STAMPPTR p.stamps
#define STAMP(i) do { SB(); if (threadIdx.x == 0) p.stamps[blockIdx.x * 16 + (i)] = __builtin_amdgcn_s_memrealtime(); SB(); } while (0)

DI void dma(rsrc_t r, u32 lds, u32 voff, u32 soff) {
  asm volatile("s_mov_b32 m0, %0\n\ts_nop 0\n\tbuffer_load_dwordx4 %1, %2, %3 offen lds" :: "s"(lds), "v"(voff), "s"(r), "s"(soff) : "memory");
}
DI void dma_sc1(rsrc_t r, u32 lds, u32 voff, u32 soff) {
  asm volatile("s_mov_b32 m0, %0\n\ts_nop 0\n\tbuffer_load_dwordx4 %1, %2, %3 offen sc1 lds" :: "s"(lds), "v"(voff), "s"(r), "s"(soff) : "memory");
}
DI void dma12(rsrc_t r, u32 lds, u32 voff, u32 so, u32 d2, u32 st, u32 st3) {
  u32 t;
  asm volatile("s_mov_b32 m0, %2\n\t"
               "s_add_u32 %1, %0, %5\n\t"
               "buffer_load_dwordx4 %3, %4, %0 offen lds\n\t"
               "s_add_u32 m0, m0, 0x800\n\t"
               "s_add_u32 %0, %0, %6\n\t"
               "buffer_load_dwordx4 %3, %4, %1 offen lds\n\t"
               "s_add_u32 m0, m0, 0x800\n\t"
               "s_add_u32 %1, %1, %6\n\t"
               "buffer_load_dwordx4 %3, %4, %0 offen lds\n\t"
               "s_add_u32 m0, m0, 0x800\n\t"
               "s_add_u32 %0, %0, %6\n\t"
               "buffer_load_dwordx4 %3, %4, %1 offen lds\n\t"
               "s_add_u32 m0, m0, 0x800\n\t"
               "s_add_u32 %1, %1, %6\n\t"
               "buffer_load_dwordx4 %3, %4, %0 offen lds\n\t"
               "s_add_u32 m0, m0, 0x800\n\t"
               "s_add_u32 %0, %0, %7\n\t"
               "buffer_load_dwordx4 %3, %4, %1 offen lds\n\t"
               "s_add_u32 m0, m0, 0x800\n\t"
               "s_add_u32 %1, %1, %7\n\t"
               "buffer_load_dwordx4 %3, %4, %0 offen lds\n\t"
               "s_add_u32 m0, m0, 0x800\n\t"
               "s_add_u32 %0, %0, %6\n\t"
               "buffer_load_dwordx4 %3, %4, %1 offen lds\n\t"
               "s_add_u32 m0, m0, 0x800\n\t"
               "s_add_u32 %1, %1, %6\n\t"
               "buffer_load_dwordx4 %3, %4, %0 offen lds\n\t"
               "s_add_u32 m0, m0, 0x800\n\t"
               "s_add_u32 %0, %0, %6\n\t"
               "buffer_load_dwordx4 %3, %4, %1 offen lds\n\t"
               "s_add_u32 m0, m0, 0x800\n\t"
               "s_add_u32 %1, %1, %6\n\t"
               "buffer_load_dwordx4 %3, %4, %0 offen lds\n\t"
               "s_add_u32 m0, m0, 0x800\n\t"
               "s_add_u32 %0, %0, %6\n\t"
               "buffer_load_dwordx4 %3, %4, %1 offen lds"
               : "+&s"(so), "=&s"(t) : "s"(lds), "v"(voff), "s"(r), "s"(d2), "s"(st), "s"(st3) : "memory", "scc");
}
DI void dma4(rsrc_t r, u32 lds, u32 voff, u32 so, u32 st) {
  asm volatile("s_mov_b32 m0, %1\n\t"
               "s_nop 0\n\t"
               "buffer_load_dwordx4 %2, %3, %0 offen lds\n\t"
               "s_add_u32 m0, m0, 0x800\n\t"
               "s_add_u32 %0, %0, %4\n\t"
               "buffer_load_dwordx4 %2, %3, %0 offen lds\n\t"
               "s_add_u32 m0, m0, 0x800\n\t"
               "s_add_u32 %0, %0, %4\n\t"
               "buffer_load_dwordx4 %2, %3, %0 offen lds\n\t"
               "s_add_u32 m0, m0, 0x800\n\t"
               "s_add_u32 %0, %0, %4\n\t"
               "buffer_load_dwordx4 %2, %3, %0 offen lds"
               : "+&s"(so) : "s"(lds), "v"(voff), "s"(r), "s"(st) : "memory", "scc");
}
DI void dma8lin(rsrc_t r, u32 lds, u32 voff, u32 so) {
  asm volatile("s_mov_b32 m0, %1\n\t"
               "s_nop 0\n\t"
               "buffer_load_dwordx4 %2, %3, %0 offen offset:0 sc1 lds\n\t"
               "buffer_load_dwordx4 %2, %3, %0 offen offset:1024 sc1 lds\n\t"
               "buffer_load_dwordx4 %2, %3, %0 offen offset:2048 sc1 lds\n\t"
               "buffer_load_dwordx4 %2, %3, %0 offen offset:3072 sc1 lds\n\t"
               "s_add_u32 m0, m0, 0x2000\n\t"
               "s_add_u32 %0, %0, 0x2000\n\t"
               "buffer_load_dwordx4 %2, %3, %0 offen offset:0 sc1 lds\n\t"
               "buffer_load_dwordx4 %2, %3, %0 offen offset:1024 sc1 lds\n\t"
               "buffer_load_dwordx4 %2, %3, %0 offen offset:2048 sc1 lds\n\t"
               "buffer_load_dwordx4 %2, %3, %0 offen offset:3072 sc1 lds"
               : "+&s"(so) : "s"(lds), "v"(voff), "s"(r) : "memory", "scc");
}
DI rsrc_t mk(const void* ptr, u32 bytes) { return __builtin_amdgcn_make_buffer_rsrc((void*)ptr, 0, bytes, 0x00020000); }
DI s16x4 rd_tr1(u32 a, int off) { return __builtin_amdgcn_ds_read_tr16_b64_v4i16((LAS s16x4*)(size_t)(a + off)); }
DI bf16x8 cat(s16x4 lo, s16x4 hi) { return __builtin_shufflevector(lo, hi, 0, 1, 2, 3, 4, 5, 6, 7); }
DI bf16x8 rd_tr(u32 a, int off) { return cat(rd_tr1(a, off), rd_tr1(a, off + 2048)); }
DI bf16x8 rd128(u32 a, int off) { return *(LAS bf16x8*)(size_t)(a + off); }
DI f32x4 rdf4(u32 a, int off) { return *(LAS f32x4*)(size_t)(a + off); }
DI float rdf(u32 a, int off) { return *(LAS float*)(size_t)(a + off); }
DI void wr128(u32 a, int off, u32x4 v) { *(LAS u32x4*)(size_t)(a + off) = v; }
DI u32 cvtpk(float lo, float hi) { f32x2 v = {lo, hi}; bf16x2 b = __builtin_convertvector(v, bf16x2); return __builtin_bit_cast(u32, b); }
DI float bflo(u32 v) { return __uint_as_float(v << 16); }
DI float bfhi(u32 v) { return __uint_as_float(v & 0xffff0000u); }
DI float bf2f(u16 v) { return __uint_as_float((u32)v << 16); }
DI float dot2(u32 a, u32 b, float c) { return __builtin_amdgcn_fdot2_f32_bf16(__builtin_bit_cast(bf16x2, a), __builtin_bit_cast(bf16x2, b), c, false); }
constexpr u32 ONES = 0x3f803f80u;
DI float rbf(float v) { return bflo(cvtpk(v, 0.f)); }
DI void split3(float v, float& a, float& b, float& c) { a = rbf(v); const float r = v - a; b = rbf(r); c = rbf(r - b); }
template <int C> DI float dppx(float v) { return __uint_as_float(__builtin_amdgcn_update_dpp(0u, __float_as_uint(v), C, 0xf, 0xf, true)); }
template <int C> DI double dppd(double v) { u32x2 t = __builtin_bit_cast(u32x2, v); t[0] = __builtin_amdgcn_update_dpp(0u, t[0], C, 0xf, 0xf, true); t[1] = __builtin_amdgcn_update_dpp(0u, t[1], C, 0xf, 0xf, true); return __builtin_bit_cast(double, t); }
DI void st16(u32x4 v, rsrc_t r, u32 voff, u32 soff) { asm volatile("buffer_store_dwordx4 %0, %1, %2, %3 offen sc1\n\ts_nop 1" :: "v"(v), "v"(voff), "s"(r), "s"(soff) : "memory"); }
DI void st_flag(char* ws, u32 idx, u32 tag) { __hip_atomic_store((GAS u32*)(ws + WS_FLAGS + idx * 128), tag, __ATOMIC_RELAXED, __HIP_MEMORY_SCOPE_AGENT); }
DI u32 ld_flag(const char* a) { return __hip_atomic_load((GAS u32*)a, __ATOMIC_RELAXED, __HIP_MEMORY_SCOPE_AGENT); }
DI void spin_all(const char* a, u32 tag) {
#pragma nounroll
  for (u32 i = 0; i < SPIN_CAP; i++) { if (__all(ld_flag(a) == tag)) break; __builtin_amdgcn_s_sleep(1); }
  asm volatile("" ::: "memory");
}
DI void spin_ge(const char* a, u32 lvl) {
#pragma nounroll
  for (u32 i = 0; i < SPIN_CAP; i++) { if (__all(ld_flag(a) >= lvl)) break; __builtin_amdgcn_s_sleep(1); }
  asm volatile("" ::: "memory");
}
DI void st_word(char* a, u32 v) { __hip_atomic_store((GAS u32*)a, v, __ATOMIC_RELAXED, __HIP_MEMORY_SCOPE_AGENT); }
DI void hs_leader(char* ws, int lane, u32 lds0, u64* stamps) {
  const rsrc_t r = mk(ws, WS_END);
  bool first = true; u32x4 last = {0u, 0u, 0u, 0u};
#pragma nounroll
  for (u32 i = 0; i < SPIN_CAP; i++) {
    asm volatile("" ::: "memory");
    const u32x4 rq = __builtin_amdgcn_raw_buffer_load_b128(r, lane * 16, WS_REQ, 16), ak = __builtin_amdgcn_raw_buffer_load_b128(r, lane * 16, WS_ACK, 16);
    if (first || rq[0] != last[0] || rq[1] != last[1] || rq[2] != last[2] || rq[3] != last[3]) st16(rq, r, lane * 16, WS_RSP);
    first = false; last = rq;
    if (__all(ak[0] == 1u && ak[1] == 1u && ak[2] == 1u && (ak[3] == 1u || lane == 63))) break;
    __builtin_amdgcn_s_sleep(1);
  }
  BAR();
  st_flag(ws, FL_GO, 1u);
  if (lane == 0) stamps[255 * 16 + 2] = __builtin_amdgcn_s_memrealtime();
}
DI float wave_sum(float v) {
  v += dppx<0xb1>(v); v += dppx<0x4e>(v); v += dppx<0x141>(v); v += dppx<0x140>(v);
  const u32 b = __float_as_uint(v);
  return (__uint_as_float(__builtin_amdgcn_readlane(b, 0)) + __uint_as_float(__builtin_amdgcn_readlane(b, 16))) + (__uint_as_float(__builtin_amdgcn_readlane(b, 32)) + __uint_as_float(__builtin_amdgcn_readlane(b, 48)));
}
DI void row_stats(const u16* row, int lane, float& mean, float& ve) {
  const u32x4 a = *((const GAS u32x4*)row + lane), b = *((const GAS u32x4*)row + 64 + (lane & 7));
  float s = 0.f, t = 0.f;
#pragma unroll
  for (int c = 0; c < 4; c++) { s += bflo(a[c]) + bfhi(a[c]); t += bflo(b[c]) + bfhi(b[c]); }
  mean = wave_sum(lane < 8 ? s + t : s) * (1.f / D);
  s = 0.f; t = 0.f;
#pragma unroll
  for (int c = 0; c < 4; c++) {
    const float a0 = bflo(a[c]) - mean, a1 = bfhi(a[c]) - mean, b0 = bflo(b[c]) - mean, b1 = bfhi(b[c]) - mean;
    s += a0 * a0 + a1 * a1; t += b0 * b0 + b1 * b1;
  }
  ve = wave_sum(lane < 8 ? s + t : s) * (1.f / D) + 1e-5f;
}

DI f32x2 gelu2(f32x2 a, float k1, float k2, float sg) {
  f32x2 t = a * a; t = t * k2 + k1; t = t * a;
  t.x = __builtin_amdgcn_exp2f(t.x); t.y = __builtin_amdgcn_exp2f(t.y);
  t = t * sg + sg;
  t.x = __builtin_amdgcn_rcpf(t.x); t.y = __builtin_amdgcn_rcpf(t.y);
  return t * a;
}
DI u32x4 gelu_frag(f32x4 a, f32x4 b, float k1, float k2, float sg) {
  f32x2 g0 = gelu2((f32x2){a[0], a[1]}, k1, k2, sg), g1 = gelu2((f32x2){a[2], a[3]}, k1, k2, sg);
  f32x2 g2 = gelu2((f32x2){b[0], b[1]}, k1, k2, sg), g3 = gelu2((f32x2){b[2], b[3]}, k1, k2, sg);
  return (u32x4){cvtpk(g0.x, g0.y), cvtpk(g1.x, g1.y), cvtpk(g2.x, g2.y), cvtpk(g3.x, g3.y)};
}

template <int H, class UPD>
DI void stepA(f32x4 (&acc)[6][4], bf16x8 (&wp)[2], bf16x8 (&t)[4], const u32 (&wc)[4], const u32& n0, const u32& n1, const u32& xn, UPD&& upd) {
  bf16x8 w[8], nx[4];
  w[0] = wp[0]; w[1] = wp[1];
#pragma unroll
  for (int n = 0; n < 6; n++) {
    const int f = n + 2, ff = f < 6 ? f : f - 6;
    const u32 a = (f < 6 || H == 0) ? wc[ff & 3] : (ff ? n1 : n0); const int off = (f < 6 ? H : 1 - H) * WSLOT + (ff >> 2) * 4096;
    MMA(acc[n][0], w[n], t[0]); SB();
    s16x4 lo = rd_tr1(a, off); SB();
    MMA(acc[n][1], w[n], t[1]); SB();
    s16x4 hi = rd_tr1(a, off + 2048); SB();
    MMA(acc[n][2], w[n], t[2]); SB();
    if (n >= 4) { nx[2 * n - 8] = rd128(xn, (2 * n - 8) * 2048); SB(); }
    if (n == 1) { upd(); SB(); }
    MMA(acc[n][3], w[n], t[3]); SB();
    if (n >= 4) { nx[2 * n - 7] = rd128(xn, (2 * n - 7) * 2048); SB(); }
    if (n == 3) { BAR(); SB(); }
    w[f] = cat(lo, hi);
  }
  wp[0] = w[6]; wp[1] = w[7];
#pragma unroll
  for (int i = 0; i < 4; i++) t[i] = nx[i];
}

template <int KIND, bool ZERO = false, class FW, class FT, class FN, class MID>
DI void half(f32x4 (&acc)[3][4], bf16x8 (&w)[3], bf16x8 (&tp)[2], FW&& rw, FT&& rt_, FN&& rn, MID&& mid) {
  bf16x8 t[4], wn[3], tn[2];
  t[0] = tp[0]; t[1] = tp[1];
  s16x4 lo;
#pragma unroll
  for (int i = 0; i < 12; i++) {
    const int T = i / 3, n = i % 3;
    if constexpr (KIND == 2) MMA(acc[n][T], t[T], w[n]); else if constexpr (ZERO) MMA0(acc[n][T], w[n], t[T]); else MMA(acc[n][T], w[n], t[T]);
    SB();
    if constexpr (KIND == 0) {
      if (i == 0) t[2] = rt_(2, 0); if (i == 1) t[3] = rt_(3, 0);
      if (i >= 2 && i <= 7) { if ((i & 1) == 0) lo = rw((i - 2) >> 1, 0); else wn[(i - 2) >> 1] = cat(lo, rw((i - 2) >> 1, 1)); }
      if (i == 8) tn[0] = rn(0, 0); if (i == 9) tn[1] = rn(1, 0);
    } else if constexpr (KIND == 1) {
      if (i <= 3) { if ((i & 1) == 0) lo = rt_(2 + (i >> 1), 0); else t[2 + (i >> 1)] = cat(lo, rt_(2 + (i >> 1), 1)); }
      if (i >= 4 && i <= 6) wn[i - 4] = rw(i - 4, 0);
      if (i >= 7 && i <= 10) { if ((i & 1) == 1) lo = rn((i - 7) >> 1, 0); else tn[(i - 7) >> 1] = cat(lo, rn((i - 7) >> 1, 1)); }
    } else {
      if (i == 0) t[2] = rt_(2, 0); if (i == 2) t[3] = rt_(3, 0);
      if (i == 3 || i == 5 || i == 6) wn[i == 3 ? 0 : i - 4] = rw(i == 3 ? 0 : i - 4, 0);
      if (i == 8) tn[0] = rn(0, 0); if (i == 9) tn[1] = rn(1, 0);
    }
    mid(i);
    SB();
  }
#pragma unroll
  for (int i = 0; i < 3; i++) w[i] = wn[i];
  tp[0] = tn[0]; tp[1] = tn[1];
}

template <int I, int N, class FN> DI void sfor(FN&& f) { if constexpr (I < N) { f(std::integral_constant<int, I>{}); sfor<I + 1, N>(f); } }

DI void ssm_carry(const Params& p, int rt, int j, int c, u32 lds0, u32 tag) {
  const u32 ax = lds0 + (18 & 3) * WSLOT, scr = lds0 + TOFF + (22 & 3) * TSLOT;
  {
    f32x4 pw = rdf4(ax + AX_TAB + c * 32, 16);
    { const float t = pw[0] * pw[0] - pw[1] * pw[1]; pw[1] = 2.f * pw[0] * pw[1]; pw[0] = t; }
    f32x2 es[8];
#pragma unroll
    for (int s = 0; s < 8; s++) es[s] = *(LAS f32x2*)(size_t)(scr + (s * 192 + c) * 8);
    float sr = 0.f, si = 0.f;
#pragma unroll
    for (int s = 0; s < 8; s++) { const float nr = pw[0] * sr - pw[1] * si + es[s].x, ni = pw[0] * si + pw[1] * sr + es[s].y; sr = nr; si = ni; }
    const rsrc_t rE = mk(p.ws + WS_EG, 16 * 9216);
    const int vo = (192 * j + c) * 16 + rt * 9216;
    st16((u32x4){__float_as_uint(sr), tag, __float_as_uint(si), tag}, rE, vo, 0);
    u32x4 v[15];
#pragma nounroll
    for (u32 spin = 0; spin < SPIN_CAP; spin++) {
      asm volatile("" ::: "memory");
      bool ok = true;
#pragma unroll
      for (int i = 0; i < 15; i++) v[i] = __builtin_amdgcn_raw_buffer_load_b128(rE, vo - (15 - i) * 9216, 0, 16);
#pragma unroll
      for (int i = 0; i < 15; i++) ok = ok && ((v[i][1] == tag && v[i][3] == tag) || vo < (15 - i) * 9216);
      if (__all(ok)) break;
    }
    float cr = 0.f, ci = 0.f;
#pragma unroll
    for (int i = 0; i < 15; i++) {
      const float nr = pw[2] * cr - pw[3] * ci + __uint_as_float(v[i][0]), ni = pw[2] * ci + pw[3] * cr + __uint_as_float(v[i][2]); cr = nr; ci = ni;
    }
#pragma unroll
    for (int s = 0; s < 8; s++) {
      *(LAS f32x2*)(size_t)(scr + (s * 192 + c) * 8) = (f32x2){cr, ci};
      const float nr = pw[0] * cr - pw[1] * ci + es[s].x, ni = pw[0] * ci + pw[1] * cr + es[s].y; cr = nr; ci = ni;
    }
  }
}

#define LBAR() do { BAR(); const u32 t_ = (u32)__builtin_amdgcn_s_memtime(), r_ = (u32)__builtin_amdgcn_s_memrealtime(); tsv = lane == tsn ? t_ : tsv; tsr = lane == tsn ? r_ : tsr; tsn++; } while (0)
DI void loader(const Params& p0, int rt, int g, int j, int u, int lane, u32 lds0, u32 tag) {
  Params p = p0;
  const bool ssm = g == 4;
  const u32 wbytes = ssm ? D * ROWB : D * F * 2;
  const rsrc_t rX = mk(p.x, S * ROWB), rA0 = mk(ssm ? p.bre : p.w1, wbytes), rA1 = mk(ssm ? p.bim : p.w1, wbytes);
  const int r8 = lane >> 3, gp = lane & 7;
  const int cgr = ((((gp >> 1) ^ ((r8 >> 1) & 3)) << 1) | (gp & 1));
  const int rgr = gp ^ (4 * u + (r8 >> 1));
  const int slice = 3 * g + j;
  const int kA = 16 * u + 8 * (r8 >> 2) + (r8 & 3);
  const int R = 8 * u + r8;
  const u32 voffA = ssm ? (192 * j + R) * ROWB + rgr * 16 : kA * (F * 2) + slice * 768 + cgr * 16;
  const int tok = ssm ? 16 * ((R >> 2) & 3) + 8 * (R & 1) + ((R >> 1) & 1) : R;
  const u32 voffX = (rt * 128 + tok) * ROWB + rgr * 16;
  const u32 ldsW = lds0 + u * 1024, ldsT = lds0 + TOFF;
  const u32 aKt = ssm ? 128 : 64 * F * 2, aPar = ssm ? 0 : 32 * F * 2, pstr = ssm ? 32 * ROWB : 128, a2 = ssm ? 16 * ROWB : 4 * F * 2, xstr = (ssm ? 2 : 16) * ROWB;
  const int rot = rt >= 9 ? rt - 9 : rt;
  auto ktile = [&](int kap) { int kt = kap + rot; return kt >= 9 ? kt - 9 : kt; };
  auto issueA = [&](int kap, int h) { dma12(h ? rA1 : rA0, ldsW + ((2 * kap + h) & 3) * WSLOT, voffA, ktile(kap) * aKt + h * aPar, a2, pstr, pstr); };
  auto issueX = [&](int kap, int h) { dma4(rX, ldsT + (kap & 3) * TSLOT + h * 8192 + u * 1024, voffX, h * 64 * ROWB + ktile(kap) * 128, xstr); };
  SB(); const u64 L0 = __builtin_amdgcn_s_memrealtime(); SB();
  issueA(0, 0); issueX(0, 0); issueX(0, 1); SB(); const u64 L1 = __builtin_amdgcn_s_memrealtime(); SB(); issueA(0, 1);
  __builtin_amdgcn_s_setprio(3);
  waitv<31>(); SB(); const u64 L2 = __builtin_amdgcn_s_memrealtime(); SB(); waitv<12>(); SB(); const u64 L3 = __builtin_amdgcn_s_memrealtime(); SB(); BAR();
  issueX(1, 0); issueX(1, 1); issueA(1, 0);
#pragma nounroll
  for (int kap = 0; kap < 7; kap++) {
    u32 l1 = ldsW + ((2 * kap + 3) & 3) * WSLOT, s1 = ktile(kap + 1) * aKt + aPar, l2 = ldsW + ((2 * kap) & 3) * WSLOT, s2 = ktile(kap + 2) * aKt;
    u32 lx = ldsT + ((kap + 2) & 3) * TSLOT + u * 1024, lx2 = lx + 8192, sx = ktile(kap + 2) * 128, sx2 = sx + 64 * ROWB;
    asm volatile("" : "+s"(l1), "+s"(s1), "+s"(l2), "+s"(s2), "+s"(lx), "+s"(lx2), "+s"(sx), "+s"(sx2));
    waitv<16>(); BAR(); dma12(rA1, l1, voffA, s1, a2, pstr, pstr); dma4(rX, lx, voffX, sx, xstr);
    waitv<16>(); BAR(); dma12(rA0, l2, voffA, s2, a2, pstr, pstr); dma4(rX, lx2, voffX, sx2, xstr);
  }
  waitv<16>(); BAR(); issueA(8, 1);
  p = reload_params();
  const u32 voffL = lane * 16;
  const rsrc_t rWS = mk(p.ws, WS_END), rB0 = mk(ssm ? p.cre : p.w2, wbytes), rB1 = mk(ssm ? p.cim : p.w2, wbytes);
  const u32 voffB = ssm ? voffA : R * ROWB + j * 384 + cgr * 16;
  const u32 bJ = ssm ? 384 : 384 * ROWB, bPar = ssm ? 0 : 192 * ROWB, bI = ssm ? 128 : 32 * ROWB, b0 = ssm ? 0 : g * 1152 * ROWB;
  const int j1 = j == 2 ? 0 : j + 1, j2 = j == 0 ? 2 : j - 1;
  const u32 st3 = 96 * ROWB - 2 * pstr, pb = WS_P + (rt * 15 + 3 * g) * 98304 + u * 4096;
  auto issueB = [&](int s, int jj, int i) { dma12((i & 1) ? rB1 : rB0, ldsW + (s & 3) * WSLOT, voffB, b0 + jj * bJ + (i & 1) * bPar + (i >> 1) * bI, 16 * ROWB, pstr, st3); };
  auto issueP = [&](int s, int jj, int i) { dma8lin(rWS, ldsT + (s & 3) * TSLOT + u * 4096, voffL, pb + jj * 98304 + i * 16384); };
  waitv<12>(); BAR();
#pragma unroll
  for (int k = 0; k < 3; k++) dma_sc1(rWS, lds0 + (18 & 3) * WSLOT + AX_TAB + (u * 3 + k) * 1024, voffL, WS_TAB + j * 6144 + (u * 3 + k) * 1024);
  waitv<3>();
  BAR(); issueB(19, j, 0); waitv<12>();
  BAR(); issueB(20, j, 1); waitv<12>();
  BAR();
  if (ssm) { if (u == 1) ssm_carry(p, rt, j, lane, lds0, tag); WAITL(); BAR(); }
  issueB(21, j, 2); waitv<12>();
  if (ssm) BAR();
  BAR(); issueB(22, j, 3); waitv<12>();
  BAR(); issueB(23, j, 4); issueP(23, j, 4); waitv<20>();
  BAR(); issueB(24, j, 5); issueP(24, j, 5);
#pragma nounroll
  for (int n = 0; n < 3; n++) {
    const int i = n ? 2 * n - 2 : 4;
    u32 w1 = b0 + j1 * bJ + (i >> 1) * bI, w2 = b0 + j2 * bJ + (i >> 1) * bI, w3 = w1 + bPar, w4 = w2 + bPar;
    u32 p1 = pb + j1 * 98304 + i * 16384, p2 = pb + j2 * 98304 + i * 16384, p3 = p1 + 16384, p4 = p2 + 16384;
    asm volatile("" : "+s"(w1), "+s"(w2), "+s"(w3), "+s"(w4), "+s"(p1), "+s"(p2), "+s"(p3), "+s"(p4));
    waitv<20>(); BAR(); dma12(rB0, ldsW + 1 * WSLOT, voffB, w1, 16 * ROWB, pstr, st3); dma8lin(rWS, ldsT + 1 * TSLOT + u * 4096, voffL, p1);
    waitv<20>(); BAR(); dma12(rB0, ldsW + 2 * WSLOT, voffB, w2, 16 * ROWB, pstr, st3); dma8lin(rWS, ldsT + 2 * TSLOT + u * 4096, voffL, p2);
    waitv<20>(); BAR(); dma12(rB1, ldsW + 3 * WSLOT, voffB, w3, 16 * ROWB, pstr, st3); dma8lin(rWS, ldsT + 3 * TSLOT + u * 4096, voffL, p3);
    waitv<20>(); BAR(); dma12(rB1, ldsW + 0 * WSLOT, voffB, w4, 16 * ROWB, pstr, st3); dma8lin(rWS, ldsT + 0 * TSLOT + u * 4096, voffL, p4);
  }
  waitv<20>();
  BAR();
  {
    const u32 l = lds0 + WSLOT;
    const u32 vo = (lane & 31) < 24 ? (rt * 128 + u * 64 + (g & 3) * 16 + (lane >> 5)) * ROWB + j * 384 + (lane & 31) * 16 : 0x7fffffffu;
#pragma unroll
    for (int k = 0; k < 8; k++) dma(rX, l + (u * 8 + k) * 1024, vo, k * 2 * ROWB);
    dma(mk(u ? p.dskip : p.b2, ROWB), l + 16384 + u * 1024, voffL + j * 384, 0);
  }
  waitv<9>(); BAR(); BAR(); waitv<0>();
  BAR();
  if (threadIdx.x == 512) { p.stamps[blockIdx.x * 16 + 10] = L0; p.stamps[blockIdx.x * 16 + 11] = L1; p.stamps[blockIdx.x * 16 + 12] = L2; p.stamps[blockIdx.x * 16 + 13] = L3; }
}

DI void helper(const Params& p, int rt, int g, int j, int h, int lane, u32 lds0, u32 tag) {
  u32 tsv = 0, tsr = 0; int tsn = 0;
  const bool ssm = g == 4;
  const int slice = 3 * g + j, q = lane >> 4, jl = lane & 15;
  const rsrc_t rWS = mk(p.ws, WS_END);
  char* const hsR = p.ws + WS_RSP + blockIdx.x * 4;
  u32 hsn = 0;
  if (h == 0) { hsn = __builtin_amdgcn_readfirstlane(ld_flag(hsR)) + 1u; st_word(hsR - 1024, hsn); }
  __builtin_amdgcn_s_setprio(2);
  {
    const int e = (lane >> 2) & 3, z = 2 * (q & 1) + (e >> 1);
    bf16x8 f[12]; f32x4 c[12]; u32x4 one = {ONES, ONES, ONES, ONES};
    auto rdslot = [&](int sl) {
      if (ssm) {
        const u32 a = lds0 + h * 12288 + jl * 128 + ((q ^ (jl >> 1)) * 16);
#pragma unroll
        for (int n = 0; n < 6; n++) { f[n] = rd128(a, sl * WSLOT + n * 2048); f[6 + n] = rd128(a ^ 64, sl * WSLOT + n * 2048); }
      } else {
#pragma unroll
        for (int n = 0; n < 12; n++) f[n] = rd_tr(lds0 + h * 12288 + (4 * q + e) * 128 + (((n & 3) ^ z) * 32) + (lane & 3) * 8, sl * WSLOT + (n >> 2) * 4096);
      }
#pragma unroll
      for (int n = 0; n < 12; n++) PIN(f[n]);
      asm volatile("s_nop 1");
    };
#pragma unroll
    for (int n = 0; n < 12; n++) { c[n] = (f32x4){0.f, 0.f, 0.f, 0.f}; PIN(c[n]); }
    PIN(one);
    LBAR();
    rdslot(0);
    if (ssm) {
#pragma unroll
      for (int n = 0; n < 12; n++) MMA(c[n % 6], f[n], one);
      MFENCE();
    } else {
#pragma unroll
      for (int n = 0; n < 12; n++) MMA(c[n], f[n], one);
      MFENCE();
    }
    LBAR();
    rdslot(1);
    if (rt < 9) {
#pragma unroll
      for (int n = 6; n < 12; n++) MMA(c[n], f[n], one);
      MFENCE();
    }
    LBAR();
    if (rt < 9) {
      if (ssm) {
#pragma unroll
        for (int n = 0; n < 6; n++) MMA(c[6 + n], f[n], one);
      MFENCE();
      } else {
#pragma unroll
        for (int n = 0; n < 6; n++) MMA(c[n], f[n], one);
      MFENCE();
      }
    }
    f32x4 m = c[0];
#pragma unroll
    for (int n = 1; n < 12; n++) m = jl == n ? c[n] : m;
    const int col = (ssm ? (jl >= 6 ? 192 - 96 : 0) + 96 * h : 192 * h) + 16 * jl + 4 * q;
    st16(__builtin_bit_cast(u32x4, m), rWS, jl < 12 ? col * 4 : 0x7fffffffu, WS_CS + (slice * 16 + rt) * 1536);
  }
  LBAR();
  u32 pv = ld_flag(hsR);
  LBAR();
  waitv<0>(); LBAR();
  bool acked = h != 0;
#pragma unroll
  for (int s = 0; s < 2; s++) {
    if (!acked) {
      acked = __all(pv == hsn);
      if (!acked && s == 1) { spin_all(hsR, hsn); acked = true; }
      if (acked) st_word(hsR + 1024, 1u);
    }
    if (s < 1) pv = ld_flag(hsR);
    LBAR(); LBAR();
  }
  LBAR();
  const char* fGo = p.ws + WS_FLAGS + FL_GO * 128;
  pv = ld_flag(fGo);
  LBAR(); LBAR();
  if (!__all(pv == 1u)) spin_all(fGo, 1u);
  const int ir = 64 * h + lane, irt = ssm ? (ir & 64) + 16 * ((ir >> 2) & 3) + 8 * (ir & 1) + 2 * ((ir >> 4) & 3) + ((ir >> 1) & 1) : ir;
  const u32x2 sg = __builtin_amdgcn_raw_buffer_load_b64(rWS, (rt * 128 + irt) * 8, WS_ST, 16);
  const int cc = 192 * h + 4 * (lane < 48 ? lane : lane - 48);
  f32x4 cp[9];
#pragma unroll
  for (int k = 0; k < 9; k++) cp[k] = __builtin_bit_cast(f32x4, __builtin_amdgcn_raw_buffer_load_b128(rWS, cc * 4, WS_CS + (slice * 16 + k) * 1536, 16));
  const u32x2 bb = *(const GAS u32x2*)(p.b1 + (ssm ? 0 : slice * 384) + cc);
  LBAR(); LBAR(); SB();
  f32x4 cs = ((cp[0] + cp[1]) + (cp[2] + cp[3])) + ((cp[4] + cp[5]) + (cp[6] + cp[7])) + cp[8];
  float mean = __uint_as_float(sg[0]), rstd = __builtin_amdgcn_rsqf(__uint_as_float(sg[1])), sig = __uint_as_float(sg[1]) * rstd;
  u32x4 at, aw[4];
  { float m1, m2, m3; split3(-mean, m1, m2, m3); const float g1 = rbf(sig), g2 = sig - g1;
    at = (u32x4){cvtpk(m1, m1), cvtpk(m2, m2), cvtpk(m1, m3), cvtpk(g1, g2)}; mean = ssm ? -mean * rstd : mean; }
  PIN(cs); PIN(at); PIN(mean); PIN(rstd); PIN(sig);
  auto auxw = [&](int k) { float c1, c2, c3; split3(cs[k], c1, c2, c3); const float b = (k & 1) ? bfhi(bb[k >> 1]) : bflo(bb[k >> 1]);
    aw[k] = (u32x4){cvtpk(c1, c2), cvtpk(c1, c2), cvtpk(c3, c1), cvtpk(b, b)}; PIN(aw[k]); };
  LBAR(); SB();
  auxw(0); auxw(1);
  const u32 ax = lds0 + (18 & 3) * WSLOT;
  LBAR(); SB();
  wr128(ax + AX_T + ir * 16, 0, at);
  { LAS float* sp = (LAS float*)(size_t)(ax + AX_S + ir * 4); sp[0] = mean; sp[128] = rstd; sp[256] = sig; }
  auxw(2); auxw(3);
  LBAR(); SB();
  if (ssm) *(LAS f32x4*)(size_t)(ax + AX_W + cc * 4) = cs;
  else {
#pragma unroll
    for (int k = 0; k < 4; k++) wr128(ax + AX_W + cc * 4, k * 1600, aw[k]);
  }
  WAITL(); LBAR();
  LBAR(); SB();
  if (ssm) { ssm_carry(p, rt, j, 64 + 64 * h + lane, lds0, tag); WAITL(); LBAR(); LBAR(); }
  char* const fMine = p.ws + WS_FLAGS + (FL_P + rt * 15 + slice) * 128;
  const char* fP = p.ws + WS_FLAGS + (FL_P + rt * 15 + 3 * g + ((lane & 1) ? (j == 2 ? 0 : j + 1) : (j == 0 ? 2 : j - 1))) * 128;
  LBAR(); if (h == 1 && !ssm) st_word(fMine, 2u);
  LBAR(); if (h == 1 && ssm) st_word(fMine, 1u);
#pragma unroll
  for (u32 lv = 1; lv < 4; lv++) {
    if (lv == 1) pv = ld_flag(fP);
    LBAR(); if (h == 0 && !__all(pv >= lv)) spin_ge(fP, lv);
    LBAR(); if (h == 1 && lv == 1) st_word(fMine, 2u);
    LBAR(); pv = ld_flag(fP);
    LBAR(); if (h == 1 && lv == 1) st_word(fMine, 3u);
  }
  LBAR(); LBAR(); LBAR(); LBAR();
  LBAR();
  if (h == 1) st_flag(p.ws, FL_O + (rt * 3 + j) * 5 + g, tag);
  else if (!ssm) {
    const int k = lane % 3;
    spin_all(p.ws + WS_FLAGS + (FL_O + (rt * 3 + j) * 5 + (k < g ? k : k + 1)) * 128, tag); BAR();
    spin_all(p.ws + WS_FLAGS + (FL_O + (rt * 3 + j) * 5 + 4) * 128, tag); BAR();
  }
  if (h == 1) { ((u32*)(p.stamps + 4096))[blockIdx.x * 64 + lane] = tsv; ((u32*)(p.stamps + 4096))[16384 + blockIdx.x * 64 + lane] = tsr; }
}

template <bool SSM>
DI void phaseB(const Params& p, int rt, int g, int j, int w, int lane, u32 lds0, u32 tag) {
  asm volatile("" : "+v"(lane));
  const int rg = w >> 2, cg = w & 3, q = lane >> 4, e = (lane >> 2) & 3, pp = lane & 3, jl = lane & 15;
  const int z = 2 * (q & 1) + (e >> 1);
  u32 wlo[3], whi[3], tb[2];
#pragma unroll
  for (int n = 0; n < 3; n++) {
    const int t = 3 * cg + n;
    wlo[n] = SSM ? lds0 + (16 * t + jl) * 128 + ((q ^ (jl >> 1)) * 16) : lds0 + (t >> 2) * 4096 + (4 * q + e) * 128 + (((t & 3) ^ z) * 32) + pp * 8;
    whi[n] = SSM ? (wlo[n] ^ 64) : wlo[n] + 2 * WSLOT;
  }
  tb[0] = SSM ? lds0 + TOFF + rg * 8192 + (q >> 1) * 2048 + (pp >> 1) * 1024 + (((8 * (q & 1) + e) * 16) ^ ((pp >> 1) * 64)) + 8 * (pp & 1) : lds0 + TOFF + rg * 4096 + lane * 16;
  tb[1] = tb[0] ^ 64;
  float mean = 0.f, rstd = 0.f;
  if constexpr (!SSM) { const u32 sa = lds0 + (18 & 3) * WSLOT + AX_S + (64 * rg + 16 * g + jl) * 4; mean = rdf(sa, 0); rstd = rdf(sa, 512); }
  f32x4 ob[3][4];
  auto RW = [&](int sl, int kb, int n, int part) {
    if constexpr (SSM) return rd128((kb ? whi[n] : wlo[n]) + (sl >> 1) * 2 * WSLOT, (sl & 1) * WSLOT);
    else return rd_tr1(sl < 2 ? wlo[n] : whi[n], (sl & 1) * WSLOT + kb * 12288 + part * 2048);
  };
  auto RT = [&](int sl, int kb, int T, int part) {
    if constexpr (SSM) return rd_tr1(tb[part], sl * TSLOT + kb * 4096 + T * 256);
    else return rd128(tb[0], sl * TSLOT + kb * 8192 + T * 1024);
  };
  bf16x8 wf[3], tp[2];
  auto step = [&](auto slc, auto&& mid) {
    constexpr int SL = decltype(slc)::value & 3, NS = (SL + 1) & 3;
    half<SSM ? 1 : 0, (decltype(slc)::value > 3)>(ob, wf, tp, [&](int n, int part) { return RW(SL, 1, n, part); }, [&](int T, int part) { return RT(SL, 0, T, part); }, [&](int T, int part) { return RT(SL, 1, T, part); }, mid);
    half<SSM ? 1 : 0>(ob, wf, tp, [&](int n, int part) { return RW(NS, 0, n, part); }, [&](int T, int part) { return RT(SL, 1, T, part); }, [&](int T, int part) { return RT(NS, 0, T, part); }, [](int) {});
  };
  using I0 = std::integral_constant<int, 0>; using I1 = std::integral_constant<int, 1>; using I2 = std::integral_constant<int, 2>; using I3 = std::integral_constant<int, 3>;
  if constexpr (!SSM) waitv<4>();
  WAITL(); BAR();
#pragma unroll
  for (int n = 0; n < 3; n++) { if constexpr (SSM) wf[n] = RW(3, 0, n, 0); else wf[n] = cat(RW(3, 0, n, 0), RW(3, 0, n, 1)); }
#pragma unroll
  for (int T = 0; T < 2; T++) { if constexpr (SSM) tp[T] = cat(RT(3, 0, T, 0), RT(3, 0, T, 1)); else tp[T] = RT(3, 0, T, 0); }
  auto barm = [](int i) { if (i == 11) BAR(); };
  step(std::integral_constant<int, 7>{}, [](int) {});
  step(I0{}, [&](int i) { if (i == 3) { if constexpr (SSM) waitv<8>(); BAR(); } });
#pragma nounroll
  for (int it = 0; it < 4; it++) { step(I1{}, barm); step(I2{}, [&](int i) { if (i == 11) { waitv<4>(); BAR(); } }); step(I3{}, barm); step(I0{}, [&](int i) { if (i == 11) { waitv<0>(); BAR(); } }); }
  MFENCE();
  const rsrc_t rWS = mk(p.ws, WS_END);
  const u32 xb = WS_XB + (rt * 3 + j) * 32 * XBW + w * XBW;
  f32x4 mine[3];
#pragma unroll
  for (int T = 0; T < 4; T++) {
    if (SSM || T != g) {
      const u32 o = xb + T * 8 * XBW + g * 1536;
      const u32x4 a = {cvtpk(ob[0][T][0], ob[0][T][1]), cvtpk(ob[0][T][2], ob[0][T][3]), cvtpk(ob[1][T][0], ob[1][T][1]), cvtpk(ob[1][T][2], ob[1][T][3])};
      const u32 b0 = cvtpk(ob[2][T][0], ob[2][T][1]), b1 = cvtpk(ob[2][T][2], ob[2][T][3]);
      const auto s0 = __builtin_amdgcn_permlane32_swap(b0, b0, false, false), s1 = __builtin_amdgcn_permlane32_swap(b1, b1, false, false);
      st16(a, rWS, lane * 16, o);
      if (lane < 32) st16((u32x4){b0, b1, s0[1], s1[1]}, rWS, lane * 16, o + 1024);
    } else {
#pragma unroll
      for (int n = 0; n < 3; n++) mine[n] = ob[n][T];
    }
  }
  if constexpr (SSM) {
    waitv<0>(); BAR();
    STAMP(7);
  } else {
    waitv<0>(); BAR();
    const int tok = rt * 128 + 64 * rg + 16 * g + jl, col = 192 * j + 48 * cg + 4 * q;
    u32x2 xv[3], bv[3], dv[3];
#pragma unroll
    for (int n = 0; n < 3; n++) {
      const u32 cb = lds0 + WSLOT + (48 * cg + 4 * q + 16 * n) * 2;
      xv[n] = *(LAS u32x2*)(size_t)(cb + (16 * rg + jl) * 512); bv[n] = *(LAS u32x2*)(size_t)(cb + 16384); dv[n] = *(LAS u32x2*)(size_t)(cb + 17408);
    }
    const u32 o = xb + g * 8 * XBW;
    u32x4 a[3]; u32x2 b[3];
    BAR();
#pragma unroll
    for (int k = 0; k < 3; k++) {
      const int src = k < g ? k : k + 1;
      a[k] = __builtin_amdgcn_raw_buffer_load_b128(rWS, lane * 16, o + src * 1536, 16);
      b[k] = __builtin_amdgcn_raw_buffer_load_b64(rWS, (lane & 31) * 16 + (lane >> 5) * 8, o + src * 1536 + 1024, 16);
    }
    f32x4 gate[3], base[3];
#pragma unroll
    for (int n = 0; n < 3; n++) {
#pragma unroll
      for (int c = 0; c < 4; c++) {
        float G = mine[n][c] + ((c & 1) ? bfhi(bv[n][c >> 1]) : bflo(bv[n][c >> 1]));
#pragma unroll
        for (int k = 0; k < 3; k++) { const u32 v = n < 2 ? a[k][2 * n + (c >> 1)] : b[k][c >> 1]; G += (c & 1) ? bfhi(v) : bflo(v); }
        const float xx = (c & 1) ? bfhi(xv[n][c >> 1]) : bflo(xv[n][c >> 1]);
        const float dd = (c & 1) ? bfhi(dv[n][c >> 1]) : bflo(dv[n][c >> 1]);
        gate[n][c] = G * __builtin_amdgcn_rcpf(1.f + __builtin_amdgcn_exp2f(-1.4426950408889634f * G));
        base[n][c] = xx + (xx - mean) * rstd * dd * gate[n][c];
      }
    }
#pragma unroll
    for (int n = 0; n < 3; n++) { PIN(gate[n]); PIN(base[n]); }
    BAR();
    const u32x4 ya = __builtin_amdgcn_raw_buffer_load_b128(rWS, lane * 16, o + 4 * 1536, 16);
    const u32x2 yb = __builtin_amdgcn_raw_buffer_load_b64(rWS, (lane & 31) * 16 + (lane >> 5) * 8, o + 4 * 1536 + 1024, 16);
#pragma unroll
    for (int n = 0; n < 3; n++) {
      const u32 y0 = n < 2 ? ya[2 * n] : yb[0], y1 = n < 2 ? ya[2 * n + 1] : yb[1];
      *(GAS f32x4*)(p.out + tok * D + col + 16 * n) = base[n] + (f32x4){bflo(y0), bfhi(y0), bflo(y1), bfhi(y1)} * gate[n];
    }
    STAMP(7);
  }
}

DI void ffn_compute(const Params& p, int rt, int g, int j, int w, int lane, u32 lds0, u32 tag) {
  const int rg = w >> 2, cg = w & 3, q = lane >> 4, e = (lane >> 2) & 3, pp = lane & 3, jl = lane & 15;
  const int z = 2 * (q & 1) + (e >> 1);
  u32 wc[4];
#pragma unroll
  for (int n = 0; n < 4; n++) { const int t = 6 * cg + n; wc[n] = lds0 + (t >> 2) * 4096 + (4 * q + e) * 128 + (((t & 3) ^ z) * 32) + pp * 8; }
  const u32 xbase = lds0 + TOFF + (64 * rg + jl) * 128 + ((q ^ (jl >> 1)) * 16);
  f32x4 acc[6][4];
#pragma unroll
  for (int n = 0; n < 6; n++)
#pragma unroll
    for (int T = 0; T < 4; T++) { acc[n][T] = (f32x4){0.f, 0.f, 0.f, 0.f}; asm volatile("" : "+v"(acc[n][T])); }
  bf16x8 wp[2], t[4];
  __builtin_amdgcn_s_waitcnt(0xC07F);
  BAR();
  wp[0] = rd_tr(wc[0], 0); wp[1] = rd_tr(wc[1], 0);
#pragma unroll
  for (int T = 0; T < 4; T++) t[T] = rd128(xbase, T * 2048);
  u32 xs = 0, xn = 0, n0 = 0, n1 = 0; int dw = 2 * WSLOT;
#pragma nounroll
  for (int it = 0; it < 9; it++) {
    stepA<0>(acc, wp, t, wc, n0, n1, xn, [&]() { xn = (xbase ^ 64) + xs; PIN(xn); xs = (xs + TSLOT) & (4 * TSLOT - 1); });
    stepA<1>(acc, wp, t, wc, n0, n1, xn, [&]() { xn = xbase + xs; n0 = wc[0] + dw; n1 = wc[1] + dw; wc[2] += dw; wc[3] += dw; PIN(xn); PIN(n0); PIN(n1); PIN(wc[2]); PIN(wc[3]); waitv<0>(); });
    wc[0] = n0; wc[1] = n1; dw = -dw;
  }
  const u32 ax = lds0 + (18 & 3) * WSLOT;
  float k1[4], k2[4], sg[4];
#pragma unroll
  for (int T = 0; T < 4; T++) {
    u32x4 v = __builtin_bit_cast(u32x4, rd128(ax + AX_T + (64 * rg + jl) * 16, T * 256));
    if (q != 0) v = (u32x4){0u, 0u, 0u, 0u};
    t[T] = __builtin_bit_cast(bf16x8, v);
    const float r = rdf(ax + AX_S + 512 + (64 * rg + jl) * 4, T * 64);
    sg[T] = rdf(ax + AX_S + 1024 + (64 * rg + jl) * 4, T * 64);
    k1[T] = -2.3022081985f * r; k2[T] = -0.10294324f * r * r * r;
  }
  {
    bf16x8 a[6];
#pragma unroll
    for (int n = 0; n < 6; n++) a[n] = rd128(ax + AX_W + ((jl & 3) * 100 + 24 * cg + (jl >> 2)) * 16, n * 64);
    asm volatile("s_nop 1" : "+v"(t[0]), "+v"(t[1]), "+v"(t[2]), "+v"(t[3]));
#pragma unroll
    for (int n = 0; n < 6; n++)
#pragma unroll
      for (int T = 0; T < 4; T++) MMA(acc[n][T], a[n], t[T]);
    MFENCE();
  }
  const rsrc_t rWS = mk(p.ws, WS_END);
  const u32 pg = WS_P + (rt * 15 + 3 * g + j) * 98304 + (cg >> 1) * 16384 + (cg & 1) * 8192 + rg * 4096;
  const u32 pl = lds0 + TOFF + (cg & 1) * 8192 + rg * 4096 + lane * 16, pl0 = pl + ((19 + (cg >> 1)) & 3) * TSLOT, pl1 = pl + ((21 + (cg >> 1)) & 3) * TSLOT;
#pragma unroll
  for (int bb = 0; bb < 3; bb++) {
    const int b = (bb + 2) % 3;
#pragma unroll
    for (int T = 0; T < 4; T++) {
      const u32x4 P = gelu_frag(acc[2 * b][T], acc[2 * b + 1][T], k1[T], k2[T], sg[T]);
      st16(P, rWS, lane * 16, pg + b * 32768 + T * 1024);
      if (b < 2) wr128(b ? pl1 : pl0, T * 1024, P);
      SB();
    }
    if (bb == 0) BAR();
  }
  phaseB<false>(p, rt, g, j, w, lane, lds0, tag);
}

DI void ssm_compute(const Params& p, int rt, int j, int w, int lane, u32 lds0, u32 tag) {
  const int rg = w >> 2, cg = w & 3; int q = lane >> 4, jl = lane & 15;
  u32 wb = lds0 + (16 * cg + jl) * 128 + ((q ^ (jl >> 1)) * 16);
  const u32 xb0 = lds0 + TOFF + (64 * rg + jl) * 128 + ((q ^ (jl >> 1)) * 16);
  f32x4 ar[3][4], ai[3][4];
#pragma unroll
  for (int n = 0; n < 3; n++)
#pragma unroll
    for (int T = 0; T < 4; T++) { ar[n][T] = (f32x4){0.f, 0.f, 0.f, 0.f}; ai[n][T] = (f32x4){0.f, 0.f, 0.f, 0.f}; asm volatile("" : "+v"(ar[n][T]), "+v"(ai[n][T])); }
  bf16x8 wf[3], tp[2];
  auto barm = [](int i) { if (i == 11) BAR(); };
  __builtin_amdgcn_s_waitcnt(0xC07F);
  BAR();
#pragma unroll
  for (int n = 0; n < 3; n++) wf[n] = rd128(wb, n * 8192);
  tp[0] = rd128(xb0, 0); tp[1] = rd128(xb0, 2048);
  u32 xs = 0, x0 = xb0, x1 = xb0 ^ 64, wb1 = wb ^ 64; int dw = 2 * WSLOT;
#pragma nounroll
  for (int it = 0; it < 9; it++) {
    half<2>(ar, wf, tp, [&](int n, int) { return rd128(wb1, n * 8192); }, [&](int T, int) { return rd128(x0, T * 2048); }, [&](int T, int) { return rd128(x1, T * 2048); }, barm);
    half<2>(ar, wf, tp, [&](int n, int) { return rd128(wb, WSLOT + n * 8192); }, [&](int T, int) { return rd128(x1, T * 2048); }, [&](int T, int) { return rd128(x0, T * 2048); },
            [&](int i) { if (i == 10) { wb += dw; PIN(wb); xs = (xs + TSLOT) & (4 * TSLOT - 1); waitv<0>(); } });
    half<2>(ai, wf, tp, [&](int n, int) { return rd128(wb1, WSLOT + n * 8192); }, [&](int T, int) { return rd128(x0, T * 2048); }, [&](int T, int) { return rd128(x1, T * 2048); },
            [&](int i) { if (i == 7) { wb1 += dw; x0 = xb0 + xs; PIN(wb1); PIN(x0); dw = -dw; } if (i == 11) BAR(); });
    half<2>(ai, wf, tp, [&](int n, int) { return rd128(wb, n * 8192); }, [&](int T, int) { return rd128(x1, T * 2048); }, [&](int T, int) { return rd128(x0, T * 2048); },
            [&](int i) { if (i == 4) { x1 = x0 ^ 64; PIN(x1); } });
  }
  asm volatile("" : "+v"(lane));
  q = lane >> 4; jl = lane & 15;
  const u32 ax = lds0 + (18 & 3) * WSLOT;
  MFENCE();
  f32x2 lc[3];
  const u32 tb = ax + AX_TAB + (16 * cg + jl) * 32;
  const u32 scr = lds0 + TOFF + (22 & 3) * TSLOT + ((4 * rg + q) * 192 + 16 * cg + jl) * 8;
  f32x2 e0[3]; float csr[3], csi[3];
#pragma unroll
  for (int b = 0; b < 3; b++) { csr[b] = rdf(ax + AX_W + (16 * cg + jl) * 4, b * 256); csi[b] = rdf(ax + AX_W + (16 * cg + jl) * 4, 768 + b * 256); }
#pragma unroll
  for (int T = 0; T < 4; T++) {
    const f32x4 r = rdf4(ax + AX_S + 512 + (64 * rg + 4 * q) * 4, T * 64), nm = rdf4(ax + AX_S + (64 * rg + 4 * q) * 4, T * 64);
#pragma unroll
    for (int b = 0; b < 3; b++) { ar[b][T] = ar[b][T] * r + nm * csr[b]; ai[b][T] = ai[b][T] * r + nm * csi[b]; }
  }
  f32x2 l8[3];
#pragma unroll
  for (int b = 0; b < 3; b++) {
    lc[b] = *(LAS f32x2*)(size_t)(tb + b * 2048);
    l8[b] = *(LAS f32x2*)(size_t)(tb + b * 2048 + 16);
    f32x2 hr = {ar[b][0][0], ar[b][0][1]}, hi = {ai[b][0][0], ai[b][0][1]};
#pragma unroll
    for (int k = 1; k < 8; k++) {
      const f32x2 ur = {ar[b][k >> 1][(k & 1) * 2], ar[b][k >> 1][(k & 1) * 2 + 1]}, ui = {ai[b][k >> 1][(k & 1) * 2], ai[b][k >> 1][(k & 1) * 2 + 1]};
      const f32x2 nr = lc[b][0] * hr + (ur - lc[b][1] * hi), ni = lc[b][0] * hi + (ui + lc[b][1] * hr);
      hr = nr; hi = ni;
    }
    e0[b] = (f32x2){hr.x, hi.x};
    *(LAS f32x2*)(size_t)(scr + b * 512) = (f32x2){l8[b].x * hr.x - l8[b].y * hi.x + hr.y, l8[b].x * hi.x + l8[b].y * hr.x + hi.y};
  }
  WAITL(); BAR(); SB();
#pragma unroll
  for (int b = 0; b < 3; b++) {
    const f32x2 cf = *(LAS f32x2*)(size_t)(tb + b * 2048 + 8); const float c2r = cf.x, c2i = cf.y;
#pragma unroll
    for (int T = 0; T < 4; T++) {
      const f32x4 nr = c2r * ar[b][T] - c2i * ai[b][T], ni = -c2r * ai[b][T] - c2i * ar[b][T];
      ar[b][T] = nr; ai[b][T] = ni;
      asm volatile("" : "+v"(ar[b][T]), "+v"(ai[b][T]));
    }
  }
  BAR();
  const rsrc_t rWS = mk(p.ws, WS_END);
  const u32 io0 = w * 2048 + lane * 16, io1 = w * 2048 + 1024 + ((lane * 16) ^ 64);
  const u32 pg = WS_P + (rt * 15 + 12 + j) * 98304;
  f32x2 cins[3];
#pragma unroll
  for (int b = 0; b < 3; b++) { cins[b] = *(LAS f32x2*)(size_t)(scr + b * 512); l8[b] = *(LAS f32x2*)(size_t)(tb + b * 2048 + 16); }
#pragma unroll
  for (int bb = 0; bb < 3; bb++) {
    const int b = (bb + 2) % 3;
    u32x4 H[2][2];
    const f32x2 cin = cins[b];
    const f32x2 sr = {cin.x, l8[b].x * cin.x - l8[b].y * cin.y + e0[b].x}, si = {cin.y, l8[b].x * cin.y + l8[b].y * cin.x + e0[b].y};
    const f32x2 cf = *(LAS f32x2*)(size_t)(tb + b * 2048 + 8); const float c2r = cf.x, c2i = cf.y;
    f32x2 hr = c2r * sr - c2i * si, hi = -c2r * si - c2i * sr;
#pragma unroll
    for (int T = 0; T < 4; T++) {
      f32x2 pr[2], pi[2];
#pragma unroll
      for (int k = 0; k < 2; k++) {
        const f32x2 ur = {ar[b][T][2 * k], ar[b][T][2 * k + 1]}, ui = {ai[b][T][2 * k], ai[b][T][2 * k + 1]};
        const f32x2 nr = lc[b][0] * hr + (ur + lc[b][1] * hi), ni = lc[b][0] * hi + (ui - lc[b][1] * hr);
        hr = nr; hi = ni; pr[k] = nr; pi[k] = ni;
      }
      H[0][0][T] = cvtpk(pr[0].x, pr[1].x); H[0][1][T] = cvtpk(pr[0].y, pr[1].y);
      H[1][0][T] = cvtpk(pi[0].x, pi[1].x); H[1][1][T] = cvtpk(pi[0].y, pi[1].y);
    }
    if (bb == 1) BAR();
#pragma unroll
    for (int k = 0; k < 4; k++) {
      st16(H[k >> 1][k & 1], rWS, (k & 1) ? io1 : io0, pg + (2 * b + (k >> 1)) * 16384);
      if (b < 2) wr128(lds0 + TOFF + ((k & 1) ? io1 : io0), ((19 + 2 * b + (k >> 1)) & 3) * TSLOT, H[k >> 1][k & 1]);
    }
    SB();
  }
  phaseB<true>(p, rt, 4, j, w, lane, lds0, tag);
}

DI void spare(const Params& p, int sid, u32 lds0, u32 tag, u64 pc0) {
  const int tid = threadIdx.x;
  u32 cpf = 0;
  if (tid < CODE_LINES && !(sid & 1)) cpf = *(const GAS u32*)(pc0 + tid * 128);
  const rsrc_t rWS = mk(p.ws, WS_END);
  const int w = __builtin_amdgcn_readfirstlane(tid >> 6), lane = tid & 63;
  if (sid == 15 && w == 11) { waitv<0>(); hs_leader(p.ws, lane, lds0, STAMPPTR); return; }
  u32 hsn = 0;
  if (w == 11) { hsn = ld_flag(p.ws + WS_RSP + blockIdx.x * 4) + 1u; st_word(p.ws + WS_REQ + blockIdx.x * 4, hsn); }
  else if (w == 10) {
    const int row = sid * 128 + 120 + (lane >> 3);
    u32x4 v[9]; float s = 0.f;
#pragma unroll
    for (int i = 0; i < 9; i++) v[i] = ((const GAS u32x4*)(p.x + row * D) + (lane & 7))[8 * i];
#pragma unroll
    for (int i = 0; i < 9; i++)
#pragma unroll
      for (int c = 0; c < 4; c++) s += bflo(v[i][c]) + bfhi(v[i][c]);
    s += dppx<0xb1>(s); s += dppx<0x4e>(s); s += dppx<0x141>(s);
    const float mean = s * (1.f / D); s = 0.f;
#pragma unroll
    for (int i = 0; i < 9; i++)
#pragma unroll
      for (int c = 0; c < 4; c++) { const float a = bflo(v[i][c]) - mean, b = bfhi(v[i][c]) - mean; s += a * a + b * b; }
    s += dppx<0xb1>(s); s += dppx<0x4e>(s); s += dppx<0x141>(s);
    if ((lane & 7) == 0) __hip_atomic_store((GAS u64*)(p.ws + WS_ST + row * 8), ((u64)__float_as_uint(s * (1.f / D) + 1e-5f) << 32) | __float_as_uint(mean), __ATOMIC_RELAXED, __HIP_MEMORY_SCOPE_AGENT);
  } else if (w == 9 && sid < 9) {
    const int c = sid * 64 + lane;
    const float lr = bf2f(p.lre[c]), li = bf2f(p.lim[c]);
    const float dt = rbf(expf(bf2f(p.logdt[c])));
    const float ex = expf(dt * lr), an = dt * li;
    const float br = ex * cosf(an), bi = ex * sinf(an);
    const float nr = br - 1.f, den = 1.f / (lr * lr + li * li);
    const float cr = (nr * lr + bi * li) * den, ci = (bi * lr - nr * li) * den;
    float ar = br, ai = bi, r8 = 0.f, i8 = 0.f;
#pragma unroll
    for (int k = 0; k < 7; k++) { const float t = ar * ar - ai * ai; ai = 2.f * ar * ai; ar = t; if (k == 2) { r8 = ar; i8 = ai; } }
    st16(__builtin_bit_cast(u32x4, (f32x4){br, bi, 2.f * cr, 2.f * ci}), rWS, c * 32, WS_TAB);
    st16(__builtin_bit_cast(u32x4, (f32x4){r8, i8, ar, ai}), rWS, c * 32 + 16, WS_TAB);
  }
  if (w == 11) spin_all(p.ws + WS_RSP + blockIdx.x * 4, hsn);
  waitv<0>(); asm volatile("" :: "v"(cpf)); BAR();
  if (sid == 15) return;
  if (w == 11) st_word(p.ws + WS_ACK + blockIdx.x * 4, 1u);
  {
    u32 acc = cpf;
    const GAS u32* w2 = (const GAS u32*)p.w2;
#pragma unroll
    for (int i = 0; i < 4; i++) { const int L = (i * 15 + sid) * 768 + tid; if (L < 41472) acc ^= w2[L * 32]; }
    { const int L = sid * 768 + tid; if (L < 5184) acc ^= ((const GAS u32*)p.cre)[L * 32]; else if (L < 10368) acc ^= ((const GAS u32*)p.cim)[(L - 5184) * 32]; }
    asm volatile("" :: "v"(acc));
  }
}

__global__ __launch_bounds__(768, 3) void fused_kernel(Params p) {
  extern __shared__ __attribute__((aligned(16))) char smem[];
  const u32 lds0 = (u32)(size_t)(LAS char*)smem;
  const int b = blockIdx.x, xcd = b & 7, idx = b >> 3;
  const u32 tag = 1u;
  u64 pc0; asm volatile("s_getpc_b64 %0" : "=s"(pc0));
  STAMP(0);
  const int tri = idx / 3, j = idx - 3 * tri;
  const int g = tri < 2 ? 4 : xcd >> 1, rt = 8 * (xcd & 1) + (tri < 2 ? (xcd & 6) + tri : tri - 2);
  const int lane = threadIdx.x & 63, w = __builtin_amdgcn_readfirstlane(threadIdx.x >> 6);
  const int sid = xcd * 2 + idx - 30;
  const int srow = rt * 128 + (3 * g + j) * 8 + w;
  int role = idx < 30 ? w >> 1 : 0;
  if (w < 8 || idx >= 30) {
    if (idx < 30) {
      if (w == 0 && lane < 2) st_flag(p.ws, lane ? FL_P + rt * 15 + 3 * g + j : FL_O + (rt * 3 + j) * 5 + g, 0u);
      if (g == 4 && w >= 1 && w < 4) st16((u32x4){0u, 0u, 0u, 0u}, mk(p.ws, WS_END), (192 * j + threadIdx.x - 64) * 16, WS_EG + rt * 576 * 16);
    } else if (w == 11 && sid == 15) {
      st_flag(p.ws, FL_GO, 0u);
      st16((u32x4){0u, 0u, 0u, 0u}, mk(p.ws, WS_END), lane * 16, WS_ACK);
    }
    if (idx < 30) {
      float mean, ve; row_stats(p.x + srow * D, lane, mean, ve);
      if (lane == 0) __hip_atomic_store((GAS u64*)(p.ws + WS_ST + srow * 8), ((u64)__float_as_uint(ve) << 32) | __float_as_uint(mean), __ATOMIC_RELAXED, __HIP_MEMORY_SCOPE_AGENT);
    }
  }
  asm volatile("" : "+s"(role));
  if (__builtin_expect(role == 4, 1)) { loader(p, rt, g, j, w - 8, lane, lds0, tag); return; }
  if (idx < 30 && w >= 10) { const Params q = reload_params(); helper(q, rt, g, j, w - 10, lane, lds0, tag); return; }
  if (idx >= 30) { const Params q = reload_params(); spare(q, sid, lds0, tag, pc0); STAMP(7); return; }
  if (g < 4) { const Params q = reload_params(); ffn_compute(q, rt, g, j, w, lane, lds0, tag); }
  else { const Params q = reload_params(); ssm_compute(q, rt, j, w, lane, lds0, tag); }
}

__device__ u32 med_diff(const u64* st, int kind, int a, int b) {
  __shared__ u32 v[256]; __shared__ u32 res; __shared__ u64 t0s[256];
  const int t = threadIdx.x; const int idx = t >> 3, xcd = t & 7;
  const int k = idx >= 30 ? 2 : (idx < 6 ? 1 : 0);
  t0s[t] = st[t * 16]; __syncthreads();
  u64 t0 = ~0ull; for (int i = 0; i < 256; i++) t0 = t0s[i] < t0 ? t0s[i] : t0;
  const bool mx = b >= 100; if (mx) b -= 100;
  const u32* tr = (const u32*)(st + 4096) + 16384 + t * 64;
  auto get = [&](int x) { return x == -1 ? (u32)t0 : x < 64 ? tr[x] : (u32)st[t * 16 + x - 64]; };
  v[t] = k == kind ? get(b) - get(a) : 0xffffffffu; __syncthreads();
  int n = 0, less = 0; for (int i = 0; i < 256; i++) { n += v[i] != 0xffffffffu; less += (v[i] < v[t]) || (v[i] == v[t] && i < t); }
  if (k == kind && less == (mx ? n - 1 : n / 2)) res = v[t];
  __syncthreads(); u32 r = res; __syncthreads(); return r;
}
__global__ void amp_k(const u64* st) {
  u32 v0 = med_diff(st, K0, A0, B0), v1 = med_diff(st, K1, A1, B1);
  v0 = (v0 + UNIT / 2) / UNIT; v1 = (v1 + UNIT / 2) / UNIT; if (v0 > 127) v0 = 127; if (v1 > 110) v1 = 110;
  if (threadIdx.x == 0) { u64 t0 = __builtin_amdgcn_s_memrealtime(); u64 d = (u64)(v0 + 128 * v1) * 100; while (__builtin_amdgcn_s_memrealtime() - t0 < d) __builtin_amdgcn_s_sleep(4); }
}

extern "C" void kernel_launch(void* const* d_in, const int* in_sizes, int n_in, void* d_out, int out_size, void* d_ws, size_t ws_size, hipStream_t stream) {
  Params p;
  p.x = (const u16*)d_in[0]; p.lre = (const u16*)d_in[1]; p.lim = (const u16*)d_in[2]; p.bre = (const u16*)d_in[3]; p.bim = (const u16*)d_in[4];
  p.cre = (const u16*)d_in[5]; p.cim = (const u16*)d_in[6]; p.logdt = (const u16*)d_in[7]; p.dskip = (const u16*)d_in[8];
  p.w1 = (const u16*)d_in[10]; p.b1 = (const u16*)d_in[11]; p.w2 = (const u16*)d_in[12]; p.b2 = (const u16*)d_in[13];
  p.out = (float*)d_out; p.ws = (char*)d_ws;
  p.stamps = (u64*)((char*)d_ws + (200u << 20));
  (void)hipFuncSetAttribute((const void*)fused_kernel, hipFuncAttributeMaxDynamicSharedMemorySize, LDS_BYTES);
  fused_kernel<<<256, 768, LDS_BYTES, stream>>>(p);
  amp_k<<<1, 256, 0, stream>>>(p.stamps);
}
```

```cpp
#include <hip/hip_runtime.h>
#include <utility>

#define LAS __attribute__((address_space(3)))
#define GAS __attribute__((address_space(1)))
#define DI __device__ __forceinline__
typedef unsigned short u16;
typedef unsigned u32;
typedef unsigned long long u64;
typedef unsigned u32x4 __attribute__((ext_vector_type(4)));
typedef unsigned u32x2 __attribute__((ext_vector_type(2)));
typedef short s16x4 __attribute__((ext_vector_type(4)));
typedef short bf16x8 __attribute__((ext_vector_type(8)));
typedef float f32x4 __attribute__((ext_vector_type(4)));
typedef float f32x2 __attribute__((ext_vector_type(2)));
typedef __bf16 bf16x2 __attribute__((ext_vector_type(2)));
typedef __amdgpu_buffer_rsrc_t rsrc_t;
typedef u64 u64x4 __attribute__((ext_vector_type(4)));

constexpr int S = 2048, D = 576, F = 4608;
constexpr int ROWB = D * 2;
constexpr int WSLOT = 24576, TOFF = 4 * WSLOT, TSLOT = 16384, LDS_BYTES = 163840;
constexpr int AX_W = 0, AX_T = 6656, AX_S = 8704, AX_TAB = 10752;
constexpr u32 WS_FLAGS = 0;
constexpr u32 FL_GO = 0, FL_P = 32, FL_O = 32 + 240, NFLAG = 32 + 480;
constexpr u32 WS_EG = NFLAG * 128;
constexpr u32 WS_ST = WS_EG + 16 * 576 * 16;
constexpr u32 WS_REQ = WS_ST + 3 * 12288, WS_RSP = WS_REQ + 1024, WS_ACK = WS_RSP + 1024;
constexpr u32 WS_CS = 256 * 1024;
constexpr u32 WS_TAB = WS_CS + 15 * 16 * 1536;
constexpr u32 WS_P = 1u << 20;
constexpr u32 XBW = 5 * 1536;
constexpr u32 WS_XB = WS_P + 240 * 98304;
constexpr u32 WS_END = WS_XB + 48 * 32 * XBW;
constexpr int CODE_LINES = 430;
constexpr u32 SPIN_CAP = 1u << 18;

struct Params {
  const u16 *x, *w1, *bre, *bim;
  const u16 *lre, *lim, *cre, *cim, *logdt, *dskip, *b1, *w2, *b2;
  float* out; char* ws;
};

DI Params reload_params() {
  Params q;
#if defined(__HIP_DEVICE_COMPILE__)
  typedef __attribute__((address_space(4))) const Params* KP;
  KP kp = (KP)__builtin_amdgcn_kernarg_segment_ptr(); asm volatile("" : "+s"(kp)); q = *kp;
#endif
  return q;
}
template <int N> DI void waitv() { asm volatile("s_waitcnt vmcnt(%0)" :: "n"(N) : "memory"); }
#define WAITL() asm volatile("s_waitcnt lgkmcnt(0)" ::: "memory")
#define BAR() do { asm volatile("" ::: "memory"); __builtin_amdgcn_s_barrier(); asm volatile("" ::: "memory"); } while (0)
#define SB() __builtin_amdgcn_sched_barrier(0)
#define MFENCE() do { SB(); asm volatile("s_nop 7\n\ts_nop 7"); SB(); } while (0)
#define MMA(c, a, b) asm volatile("v_mfma_f32_16x16x32_bf16 %0, %1, %2, %0" : "+v"(c) : "v"(a), "v"(b))
#define MMA0(c, a, b) asm volatile("v_mfma_f32_16x16x32_bf16 %0, %1, %2, 0" : "=&v"(c) : "v"(a), "v"(b))
#define PIN(x) asm volatile("" : "+v"(x))
#define STAMPPTR nullptr
#define STAMP(i)

DI void dma(rsrc_t r, u32 lds, u32 voff, u32 soff) {
  asm volatile("s_mov_b32 m0, %0\n\ts_nop 0\n\tbuffer_load_dwordx4 %1, %2, %3 offen lds" :: "s"(lds), "v"(voff), "s"(r), "s"(soff) : "memory");
}
DI void dma_sc1(rsrc_t r, u32 lds, u32 voff, u32 soff) {
  asm volatile("s_mov_b32 m0, %0\n\ts_nop 0\n\tbuffer_load_dwordx4 %1, %2, %3 offen sc1 lds" :: "s"(lds), "v"(voff), "s"(r), "s"(soff) : "memory");
}
DI void dma12(rsrc_t r, u32 lds, u32 voff, u32 so, u32 d2, u32 st, u32 st3) {
  u32 t;
  asm volatile("s_mov_b32 m0, %2\n\t"
               "s_add_u32 %1, %0, %5\n\t"
               "buffer_load_dwordx4 %3, %4, %0 offen lds\n\t"
               "s_add_u32 m0, m0, 0x800\n\t"
               "s_add_u32 %0, %0, %6\n\t"
               "buffer_load_dwordx4 %3, %4, %1 offen lds\n\t"
               "s_add_u32 m0, m0, 0x800\n\t"
               "s_add_u32 %1, %1, %6\n\t"
               "buffer_load_dwordx4 %3, %4, %0 offen lds\n\t"
               "s_add_u32 m0, m0, 0x800\n\t"
               "s_add_u32 %0, %0, %6\n\t"
               "buffer_load_dwordx4 %3, %4, %1 offen lds\n\t"
               "s_add_u32 m0, m0, 0x800\n\t"
               "s_add_u32 %1, %1, %6\n\t"
               "buffer_load_dwordx4 %3, %4, %0 offen lds\n\t"
               "s_add_u32 m0, m0, 0x800\n\t"
               "s_add_u32 %0, %0, %7\n\t"
               "buffer_load_dwordx4 %3, %4, %1 offen lds\n\t"
               "s_add_u32 m0, m0, 0x800\n\t"
               "s_add_u32 %1, %1, %7\n\t"
               "buffer_load_dwordx4 %3, %4, %0 offen lds\n\t"
               "s_add_u32 m0, m0, 0x800\n\t"
               "s_add_u32 %0, %0, %6\n\t"
               "buffer_load_dwordx4 %3, %4, %1 offen lds\n\t"
               "s_add_u32 m0, m0, 0x800\n\t"
               "s_add_u32 %1, %1, %6\n\t"
               "buffer_load_dwordx4 %3, %4, %0 offen lds\n\t"
               "s_add_u32 m0, m0, 0x800\n\t"
               "s_add_u32 %0, %0, %6\n\t"
               "buffer_load_dwordx4 %3, %4, %1 offen lds\n\t"
               "s_add_u32 m0, m0, 0x800\n\t"
               "s_add_u32 %1, %1, %6\n\t"
               "buffer_load_dwordx4 %3, %4, %0 offen lds\n\t"
               "s_add_u32 m0, m0, 0x800\n\t"
               "s_add_u32 %0, %0, %6\n\t"
               "buffer_load_dwordx4 %3, %4, %1 offen lds"
               : "+&s"(so), "=&s"(t) : "s"(lds), "v"(voff), "s"(r), "s"(d2), "s"(st), "s"(st3) : "memory", "scc");
}
DI void dma4(rsrc_t r, u32 lds, u32 voff, u32 so, u32 st) {
  asm volatile("s_mov_b32 m0, %1\n\t"
               "s_nop 0\n\t"
               "buffer_load_dwordx4 %2, %3, %0 offen lds\n\t"
               "s_add_u32 m0, m0, 0x800\n\t"
               "s_add_u32 %0, %0, %4\n\t"
               "buffer_load_dwordx4 %2, %3, %0 offen lds\n\t"
               "s_add_u32 m0, m0, 0x800\n\t"
               "s_add_u32 %0, %0, %4\n\t"
               "buffer_load_dwordx4 %2, %3, %0 offen lds\n\t"
               "s_add_u32 m0, m0, 0x800\n\t"
               "s_add_u32 %0, %0, %4\n\t"
               "buffer_load_dwordx4 %2, %3, %0 offen lds"
               : "+&s"(so) : "s"(lds), "v"(voff), "s"(r), "s"(st) : "memory", "scc");
}
DI void dma8lin(rsrc_t r, u32 lds, u32 voff, u32 so) {
  asm volatile("s_mov_b32 m0, %1\n\t"
               "s_nop 0\n\t"
               "buffer_load_dwordx4 %2, %3, %0 offen offset:0 sc1 lds\n\t"
               "buffer_load_dwordx4 %2, %3, %0 offen offset:1024 sc1 lds\n\t"
               "buffer_load_dwordx4 %2, %3, %0 offen offset:2048 sc1 lds\n\t"
               "buffer_load_dwordx4 %2, %3, %0 offen offset:3072 sc1 lds\n\t"
               "s_add_u32 m0, m0, 0x2000\n\t"
               "s_add_u32 %0, %0, 0x2000\n\t"
               "buffer_load_dwordx4 %2, %3, %0 offen offset:0 sc1 lds\n\t"
               "buffer_load_dwordx4 %2, %3, %0 offen offset:1024 sc1 lds\n\t"
               "buffer_load_dwordx4 %2, %3, %0 offen offset:2048 sc1 lds\n\t"
               "buffer_load_dwordx4 %2, %3, %0 offen offset:3072 sc1 lds"
               : "+&s"(so) : "s"(lds), "v"(voff), "s"(r) : "memory", "scc");
}
DI rsrc_t mk(const void* ptr, u32 bytes) { return __builtin_amdgcn_make_buffer_rsrc((void*)ptr, 0, bytes, 0x00020000); }
DI s16x4 rd_tr1(u32 a, int off) { return __builtin_amdgcn_ds_read_tr16_b64_v4i16((LAS s16x4*)(size_t)(a + off)); }
DI bf16x8 cat(s16x4 lo, s16x4 hi) { return __builtin_shufflevector(lo, hi, 0, 1, 2, 3, 4, 5, 6, 7); }
DI bf16x8 rd_tr(u32 a, int off) { return cat(rd_tr1(a, off), rd_tr1(a, off + 2048)); }
DI bf16x8 rd128(u32 a, int off) { return *(LAS bf16x8*)(size_t)(a + off); }
DI f32x4 rdf4(u32 a, int off) { return *(LAS f32x4*)(size_t)(a + off); }
DI float rdf(u32 a, int off) { return *(LAS float*)(size_t)(a + off); }
DI void wr128(u32 a, int off, u32x4 v) { *(LAS u32x4*)(size_t)(a + off) = v; }
DI u32 cvtpk(float lo, float hi) { f32x2 v = {lo, hi}; bf16x2 b = __builtin_convertvector(v, bf16x2); return __builtin_bit_cast(u32, b); }
DI float bflo(u32 v) { return __uint_as_float(v << 16); }
DI float bfhi(u32 v) { return __uint_as_float(v & 0xffff0000u); }
DI float bf2f(u16 v) { return __uint_as_float((u32)v << 16); }
DI float dot2(u32 a, u32 b, float c) { return __builtin_amdgcn_fdot2_f32_bf16(__builtin_bit_cast(bf16x2, a), __builtin_bit_cast(bf16x2, b), c, false); }
constexpr u32 ONES = 0x3f803f80u;
DI float rbf(float v) { return bflo(cvtpk(v, 0.f)); }
DI void split3(float v, float& a, float& b, float& c) { a = rbf(v); const float r = v - a; b = rbf(r); c = rbf(r - b); }
template <int C> DI float dppx(float v) { return __uint_as_float(__builtin_amdgcn_update_dpp(0u, __float_as_uint(v), C, 0xf, 0xf, true)); }
template <int C> DI double dppd(double v) { u32x2 t = __builtin_bit_cast(u32x2, v); t[0] = __builtin_amdgcn_update_dpp(0u, t[0], C, 0xf, 0xf, true); t[1] = __builtin_amdgcn_update_dpp(0u, t[1], C, 0xf, 0xf, true); return __builtin_bit_cast(double, t); }
DI void st16(u32x4 v, rsrc_t r, u32 voff, u32 soff) { asm volatile("buffer_store_dwordx4 %0, %1, %2, %3 offen sc1\n\ts_nop 1" :: "v"(v), "v"(voff), "s"(r), "s"(soff) : "memory"); }
DI void st_flag(char* ws, u32 idx, u32 tag) { __hip_atomic_store((GAS u32*)(ws + WS_FLAGS + idx * 128), tag, __ATOMIC_RELAXED, __HIP_MEMORY_SCOPE_AGENT); }
DI u32 ld_flag(const char* a) { return __hip_atomic_load((GAS u32*)a, __ATOMIC_RELAXED, __HIP_MEMORY_SCOPE_AGENT); }
DI void spin_all(const char* a, u32 tag) {
#pragma nounroll
  for (u32 i = 0; i < SPIN_CAP; i++) { if (__all(ld_flag(a) == tag)) break; __builtin_amdgcn_s_sleep(1); }
  asm volatile("" ::: "memory");
}
DI void spin_ge(const char* a, u32 lvl) {
#pragma nounroll
  for (u32 i = 0; i < SPIN_CAP; i++) { if (__all(ld_flag(a) >= lvl)) break; __builtin_amdgcn_s_sleep(1); }
  asm volatile("" ::: "memory");
}
DI void st_word(char* a, u32 v) { __hip_atomic_store((GAS u32*)a, v, __ATOMIC_RELAXED, __HIP_MEMORY_SCOPE_AGENT); }
DI void hs_leader(char* ws, int lane, u32 lds0, u64* stamps) {
  const rsrc_t r = mk(ws, WS_END);
  bool first = true; u32x4 last = {0u, 0u, 0u, 0u};
#pragma nounroll
  for (u32 i = 0; i < SPIN_CAP; i++) {
    asm volatile("" ::: "memory");
    const u32x4 rq = __builtin_amdgcn_raw_buffer_load_b128(r, lane * 16, WS_REQ, 16), ak = __builtin_amdgcn_raw_buffer_load_b128(r, lane * 16, WS_ACK, 16);
    if (first || rq[0] != last[0] || rq[1] != last[1] || rq[2] != last[2] || rq[3] != last[3]) st16(rq, r, lane * 16, WS_RSP);
    first = false; last = rq;
    if (__all(ak[0] == 1u && ak[1] == 1u && ak[2] == 1u && (ak[3] == 1u || lane == 63))) break;
    __builtin_amdgcn_s_sleep(1);
  }
  BAR();
  st_flag(ws, FL_GO, 1u);
}
DI float row_sum16(float v) { v += dppx<0xb1>(v); v += dppx<0x4e>(v); v += dppx<0x141>(v); v += dppx<0x140>(v); return v; }
DI void rows4_stats(const u16* x, char* ws, int row0, int lane) {
  const int row = row0 + (lane >> 4), l = lane & 15;
  const GAS u32x4* src = (const GAS u32x4*)(x + row * D);
  u32x4 v[4];
#pragma unroll
  for (int i = 0; i < 4; i++) v[i] = src[l + 16 * i];
  const u32x2 t = ((const GAS u32x2*)(src + 64))[l];
  f32x2 e[18], s = {0.f, 0.f};
#pragma unroll
  for (int i = 0; i < 18; i++) { const u32 d = i < 16 ? v[i >> 2][i & 3] : t[i - 16]; e[i] = (f32x2){bflo(d), bfhi(d)}; s += e[i]; }
  const float mean = row_sum16(s.x + s.y) * (1.f / D);
  s = (f32x2){0.f, 0.f};
#pragma unroll
  for (int i = 0; i < 18; i++) { const f32x2 d = e[i] - mean; s += d * d; }
  const float ve = row_sum16(s.x + s.y) * (1.f / D) + 1e-5f;
  if (l == 0) __hip_atomic_store((GAS u64*)(ws + WS_ST + row * 8), ((u64)__float_as_uint(ve) << 32) | __float_as_uint(mean), __ATOMIC_RELAXED, __HIP_MEMORY_SCOPE_AGENT);
}

DI f32x2 gelu2(f32x2 a, float k1, float k2, float sg) {
  f32x2 t = a * a; t = t * k2 + k1; t = t * a;
  t.x = __builtin_amdgcn_exp2f(t.x); t.y = __builtin_amdgcn_exp2f(t.y);
  t = t * sg + sg;
  t.x = __builtin_amdgcn_rcpf(t.x); t.y = __builtin_amdgcn_rcpf(t.y);
  return t * a;
}
DI u32x4 gelu_frag(f32x4 a, f32x4 b, float k1, float k2, float sg) {
  f32x2 g0 = gelu2((f32x2){a[0], a[1]}, k1, k2, sg), g1 = gelu2((f32x2){a[2], a[3]}, k1, k2, sg);
  f32x2 g2 = gelu2((f32x2){b[0], b[1]}, k1, k2, sg), g3 = gelu2((f32x2){b[2], b[3]}, k1, k2, sg);
  return (u32x4){cvtpk(g0.x, g0.y), cvtpk(g1.x, g1.y), cvtpk(g2.x, g2.y), cvtpk(g3.x, g3.y)};
}

template <int H, class UPD>
DI void stepA(f32x4 (&acc)[6][4], bf16x8 (&wp)[2], bf16x8 (&t)[4], const u32 (&wc)[4], const u32& n0, const u32& n1, const u32& xn, UPD&& upd) {
  bf16x8 w[8], nx[4];
  w[0] = wp[0]; w[1] = wp[1];
#pragma unroll
  for (int n = 0; n < 6; n++) {
    const int f = n + 2, ff = f < 6 ? f : f - 6;
    const u32 a = (f < 6 || H == 0) ? wc[ff & 3] : (ff ? n1 : n0); const int off = (f < 6 ? H : 1 - H) * WSLOT + (ff >> 2) * 4096;
    MMA(acc[n][0], w[n], t[0]); SB();
    s16x4 lo = rd_tr1(a, off); SB();
    MMA(acc[n][1], w[n], t[1]); SB();
    s16x4 hi = rd_tr1(a, off + 2048); SB();
    MMA(acc[n][2], w[n], t[2]); SB();
    if (n >= 4) { nx[2 * n - 8] = rd128(xn, (2 * n - 8) * 2048); SB(); }
    if (n == 1) { upd(); SB(); }
    MMA(acc[n][3], w[n], t[3]); SB();
    if (n >= 4) { nx[2 * n - 7] = rd128(xn, (2 * n - 7) * 2048); SB(); }
    if (n == 3) { BAR(); SB(); }
    w[f] = cat(lo, hi);
  }
  wp[0] = w[6]; wp[1] = w[7];
#pragma unroll
  for (int i = 0; i < 4; i++) t[i] = nx[i];
}

template <int KIND, bool ZERO = false, class FW, class FT, class FN, class MID>
DI void half(f32x4 (&acc)[3][4], bf16x8 (&w)[3], bf16x8 (&tp)[2], FW&& rw, FT&& rt_, FN&& rn, MID&& mid) {
  bf16x8 t[4], wn[3], tn[2];
  t[0] = tp[0]; t[1] = tp[1];
  s16x4 lo;
#pragma unroll
  for (int i = 0; i < 12; i++) {
    const int T = i / 3, n = i % 3;
    if constexpr (KIND == 2) MMA(acc[n][T], t[T], w[n]); else if constexpr (ZERO) MMA0(acc[n][T], w[n], t[T]); else MMA(acc[n][T], w[n], t[T]);
    SB();
    if constexpr (KIND == 0) {
      if (i == 0) t[2] = rt_(2, 0); if (i == 1) t[3] = rt_(3, 0);
      if (i >= 2 && i <= 7) { if ((i & 1) == 0) lo = rw((i - 2) >> 1, 0); else wn[(i - 2) >> 1] = cat(lo, rw((i - 2) >> 1, 1)); }
      if (i == 8) tn[0] = rn(0, 0); if (i == 9) tn[1] = rn(1, 0);
    } else if constexpr (KIND == 1) {
      if (i <= 3) { if ((i & 1) == 0) lo = rt_(2 + (i >> 1), 0); else t[2 + (i >> 1)] = cat(lo, rt_(2 + (i >> 1), 1)); }
      if (i >= 4 && i <= 6) wn[i - 4] = rw(i - 4, 0);
      if (i >= 7 && i <= 10) { if ((i & 1) == 1) lo = rn((i - 7) >> 1, 0); else tn[(i - 7) >> 1] = cat(lo, rn((i - 7) >> 1, 1)); }
    } else {
      if (i == 0) t[2] = rt_(2, 0); if (i == 2) t[3] = rt_(3, 0);
      if (i == 3 || i == 5 || i == 6) wn[i == 3 ? 0 : i - 4] = rw(i == 3 ? 0 : i - 4, 0);
      if (i == 8) tn[0] = rn(0, 0); if (i == 9) tn[1] = rn(1, 0);
    }
    mid(i);
    SB();
  }
#pragma unroll
  for (int i = 0; i < 3; i++) w[i] = wn[i];
  tp[0] = tn[0]; tp[1] = tn[1];
}

template <int I, int N, class FN> DI void sfor(FN&& f) { if constexpr (I < N) { f(std::integral_constant<int, I>{}); sfor<I + 1, N>(f); } }

DI f32x2 cmul(f32x2 a, f32x2 b) { return (f32x2){a.x * b.x - a.y * b.y, a.x * b.y + a.y * b.x}; }
DI f32x2 cfma(f32x2 a, f32x2 b, f32x2 c) { return (f32x2){__builtin_fmaf(a.x, b.x, __builtin_fmaf(-a.y, b.y, c.x)), __builtin_fmaf(a.x, b.y, __builtin_fmaf(a.y, b.x, c.y))}; }
struct CarryPre { f32x2 l16, l32, l64, L1, L4; };
DI CarryPre carry_pre(int c, u32 lds0) {
  const f32x4 pw = rdf4(lds0 + (18 & 3) * WSLOT + AX_TAB + c * 32, 16);
  CarryPre k; const f32x2 l8 = {pw[0], pw[1]};
  k.l16 = cmul(l8, l8); k.l32 = cmul(k.l16, k.l16); k.l64 = cmul(k.l32, k.l32); k.L1 = (f32x2){pw[2], pw[3]};
  const f32x2 L2 = cmul(k.L1, k.L1); k.L4 = cmul(L2, L2);
  PIN(k.l16); PIN(k.l32); PIN(k.l64); PIN(k.L1); PIN(k.L4);
  return k;
}
DI void ssm_carry(const Params& p, const CarryPre& k, int rt, int j, int c, u32 lds0) {
  const u32 scr = lds0 + TOFF + (22 & 3) * TSLOT + c * 8;
  f32x2 e[8];
#pragma unroll
  for (int s = 0; s < 8; s++) e[s] = *(LAS f32x2*)(size_t)(scr + s * 1536);
  const f32x2 a01 = cfma(k.l16, e[0], e[1]), a23 = cfma(k.l16, e[2], e[3]), a45 = cfma(k.l16, e[4], e[5]), a67 = cfma(k.l16, e[6], e[7]);
  const f32x2 a03 = cfma(k.l32, a01, a23), E = cfma(k.l64, a03, cfma(k.l32, a45, a67));
  const rsrc_t rE = mk(p.ws + WS_EG, 16 * 9216);
  const int vo = (192 * j + c) * 16 + (15 - rt) * 9216;
  st16((u32x4){__float_as_uint(E.x), 1u, __float_as_uint(E.y), 1u}, rE, vo, 0);
  u32x4 va[15], vb[15];
  auto issue = [&](u32x4 (&v)[15]) {
    asm volatile("" ::: "memory");
#pragma unroll
    for (int i = 0; i < 15; i++) v[i] = __builtin_amdgcn_raw_buffer_load_b128(rE, vo, (15 - i) * 9216, 16);
    asm volatile("" ::: "memory");
  };
  f32x2 cy;
  auto eval = [&](u32x4 (&v)[15]) {
    u32 n = 0; f32x2 q[4];
#pragma unroll
    for (int i = 0; i < 15; i++) {
      n += v[i][1] + v[i][3];
      const f32x2 x = {__uint_as_float(v[i][0]), __uint_as_float(v[i][2])};
      const int ch = (i + 1) & 3;
      q[ch] = i < 4 ? x : cfma(k.L4, q[ch], x);
    }
    cy = cfma(k.L1, cfma(k.L1, cfma(k.L1, q[0], q[1]), q[2]), q[3]);
    return __all(n == 2u * rt);
  };
  __builtin_amdgcn_s_sleep(6);
  issue(va);
  __builtin_amdgcn_s_sleep(10);
  issue(vb);
  if (!eval(va)) {
    issue(va);
    if (!eval(vb) && !eval(va)) {
#pragma nounroll
      for (u32 spin = 0; spin < SPIN_CAP; spin++) { u32x4 vc[15]; issue(vc); if (eval(vc)) break; }
    }
  }
#pragma unroll
  for (int s = 0; s < 8; s += 2) e[s] = *(LAS f32x2*)(size_t)(scr + s * 1536);
  const f32x2 c2 = cfma(k.l32, cy, a01), c4 = cfma(k.l64, cy, a03), c6 = cfma(k.l32, c4, a45);
  const f32x2 cin[8] = {cy, cfma(k.l16, cy, e[0]), c2, cfma(k.l16, c2, e[2]), c4, cfma(k.l16, c4, e[4]), c6, cfma(k.l16, c6, e[6])};
#pragma unroll
  for (int s = 0; s < 8; s++) *(LAS f32x2*)(size_t)(scr + s * 1536) = cin[s];
}

#define LBAR() BAR()
DI void loader(u64x4 kv, int rt, int g, int j, int u, int lane, u32 lds0, u32 tag) {
  const bool ssm = g == 4;
  const u32 wbytes = ssm ? D * ROWB : D * F * 2;
  const int r8 = lane >> 3, gp = lane & 7;
  const int cgr = ((((gp >> 1) ^ ((r8 >> 1) & 3)) << 1) | (gp & 1));
  const int rgr = gp ^ (4 * u + (r8 >> 1));
  const int slice = 3 * g + j;
  const int kA = 16 * u + 8 * (r8 >> 2) + (r8 & 3);
  const int R = 8 * u + r8;
  const u32 voffA = ssm ? (192 * j + R) * ROWB + rgr * 16 : kA * (F * 2) + slice * 768 + cgr * 16;
  const int tok = ssm ? 16 * ((R >> 2) & 3) + 8 * (R & 1) + ((R >> 1) & 1) : R;
  const u32 voffX = (rt * 128 + tok) * ROWB + rgr * 16;
  const u32 ldsW = lds0 + u * 1024, ldsT = lds0 + TOFF;
  const u32 aKt = ssm ? 128 : 64 * F * 2, aPar = ssm ? 0 : 32 * F * 2, pstr = ssm ? 32 * ROWB : 128, a2 = ssm ? 16 * ROWB : 4 * F * 2, xstr = (ssm ? 2 : 16) * ROWB;
  const int rot = (rt >= 10 ? rt - 10 : rt) & ~1;
  asm volatile("s_waitcnt lgkmcnt(0)" : "+s"(kv) : "v"(voffA), "v"(voffX), "s"(aKt), "s"(rot));
  const rsrc_t rX = mk((void*)kv[0], S * ROWB), rA0 = mk((void*)(ssm ? kv[2] : kv[1]), wbytes), rA1 = mk((void*)(ssm ? kv[3] : kv[1]), wbytes);
  auto ktile = [&](int kap) { int kt = kap + rot; return kt >= 9 ? kt - 9 : kt; };
  auto issueA = [&](int kap, int h) { dma12(h ? rA1 : rA0, ldsW + ((2 * kap + h) & 3) * WSLOT, voffA, ktile(kap) * aKt + h * aPar, a2, pstr, pstr); };
  auto issueX = [&](int kap, int h) { dma4(rX, ldsT + (kap & 3) * TSLOT + h * 8192 + u * 1024, voffX, h * 64 * ROWB + ktile(kap) * 128, xstr); };
  issueA(0, 0); issueX(0, 0); issueX(0, 1); issueA(0, 1);
  __builtin_amdgcn_s_setprio(3);
  waitv<12>(); BAR();
  issueX(1, 0); issueX(1, 1); issueA(1, 0);
#pragma nounroll
  for (int kap = 0; kap < 7; kap++) {
    u32 l1 = ldsW + ((2 * kap + 3) & 3) * WSLOT, s1 = ktile(kap + 1) * aKt + aPar, l2 = ldsW + ((2 * kap) & 3) * WSLOT, s2 = ktile(kap + 2) * aKt;
    u32 lx = ldsT + ((kap + 2) & 3) * TSLOT + u * 1024, lx2 = lx + 8192, sx = ktile(kap + 2) * 128, sx2 = sx + 64 * ROWB;
    asm volatile("" : "+s"(l1), "+s"(s1), "+s"(l2), "+s"(s2), "+s"(lx), "+s"(lx2), "+s"(sx), "+s"(sx2));
    waitv<16>(); BAR(); dma12(rA1, l1, voffA, s1, a2, pstr, pstr); dma4(rX, lx, voffX, sx, xstr);
    waitv<16>(); BAR(); dma12(rA0, l2, voffA, s2, a2, pstr, pstr); dma4(rX, lx2, voffX, sx2, xstr);
  }
  waitv<16>(); BAR(); issueA(8, 1);
  const Params p = reload_params();
  const u32 voffL = lane * 16;
  const rsrc_t rWS = mk(p.ws, WS_END), rB0 = mk(ssm ? p.cre : p.w2, wbytes), rB1 = mk(ssm ? p.cim : p.w2, wbytes);
  const u32 voffB = ssm ? voffA : R * ROWB + j * 384 + cgr * 16;
  const u32 bJ = ssm ? 384 : 384 * ROWB, bPar = ssm ? 0 : 192 * ROWB, bI = ssm ? 128 : 32 * ROWB, b0 = ssm ? 0 : g * 1152 * ROWB;
  const int j1 = j == 2 ? 0 : j + 1, j2 = j == 0 ? 2 : j - 1;
  const u32 st3 = 96 * ROWB - 2 * pstr, pb = WS_P + (rt * 15 + 3 * g) * 98304 + u * 4096;
  auto issueB = [&](int s, int jj, int i) { dma12((i & 1) ? rB1 : rB0, ldsW + (s & 3) * WSLOT, voffB, b0 + jj * bJ + (i & 1) * bPar + (i >> 1) * bI, 16 * ROWB, pstr, st3); };
  auto issueP = [&](int s, int jj, int i) { dma8lin(rWS, ldsT + (s & 3) * TSLOT + u * 4096, voffL, pb + jj * 98304 + i * 16384); };
  waitv<12>(); BAR();
#pragma unroll
  for (int k = 0; k < 3; k++) dma_sc1(rWS, lds0 + (18 & 3) * WSLOT + AX_TAB + (u * 3 + k) * 1024, voffL, WS_TAB + j * 6144 + (u * 3 + k) * 1024);
  waitv<3>();
  BAR(); issueB(19, j, 0); waitv<12>();
  BAR(); issueB(20, j, 1); waitv<12>();
  if (ssm) { const CarryPre k = carry_pre(lane, lds0); BAR(); if (u == 1) ssm_carry(p, k, rt, j, lane, lds0); WAITL(); }
  BAR();
  issueB(21, j, 2); waitv<12>();
  if (ssm) BAR();
  BAR(); issueB(22, j, 3); waitv<12>();
  BAR(); issueB(23, j, 4); issueP(23, j, 4); waitv<20>();
  BAR(); issueB(24, j, 5); issueP(24, j, 5);
#pragma nounroll
  for (int n = 0; n < 3; n++) {
    const int i = n ? 2 * n - 2 : 4;
    u32 w1 = b0 + j1 * bJ + (i >> 1) * bI, w2 = b0 + j2 * bJ + (i >> 1) * bI, w3 = w1 + bPar, w4 = w2 + bPar;
    u32 p1 = pb + j1 * 98304 + i * 16384, p2 = pb + j2 * 98304 + i * 16384, p3 = p1 + 16384, p4 = p2 + 16384;
    asm volatile("" : "+s"(w1), "+s"(w2), "+s"(w3), "+s"(w4), "+s"(p1), "+s"(p2), "+s"(p3), "+s"(p4));
    waitv<20>(); BAR(); dma12(rB0, ldsW + 1 * WSLOT, voffB, w1, 16 * ROWB, pstr, st3); dma8lin(rWS, ldsT + 1 * TSLOT + u * 4096, voffL, p1);
    waitv<20>(); BAR(); dma12(rB0, ldsW + 2 * WSLOT, voffB, w2, 16 * ROWB, pstr, st3); dma8lin(rWS, ldsT + 2 * TSLOT + u * 4096, voffL, p2);
    waitv<20>(); BAR(); dma12(rB1, ldsW + 3 * WSLOT, voffB, w3, 16 * ROWB, pstr, st3); dma8lin(rWS, ldsT + 3 * TSLOT + u * 4096, voffL, p3);
    waitv<20>(); BAR(); dma12(rB1, ldsW + 0 * WSLOT, voffB, w4, 16 * ROWB, pstr, st3); dma8lin(rWS, ldsT + 0 * TSLOT + u * 4096, voffL, p4);
  }
  waitv<20>();
  BAR();
  {
    const u32 l = lds0 + WSLOT;
    const u32 vo = (lane & 31) < 24 ? (rt * 128 + u * 64 + (g & 3) * 16 + (lane >> 5)) * ROWB + j * 384 + (lane & 31) * 16 : 0x7fffffffu;
#pragma unroll
    for (int k = 0; k < 8; k++) dma(rX, l + (u * 8 + k) * 1024, vo, k * 2 * ROWB);
    dma(mk(u ? p.dskip : p.b2, ROWB), l + 16384 + u * 1024, voffL + j * 384, 0);
  }
  waitv<9>(); BAR(); BAR(); waitv<0>();
  BAR();
}

DI void helper(const Params& p, int rt, int g, int j, int h, int lane, u32 lds0, u32 tag) {
  const bool ssm = g == 4;
  const int slice = 3 * g + j, q = lane >> 4, jl = lane & 15;
  const rsrc_t rWS = mk(p.ws, WS_END);
  char* const hsR = p.ws + WS_RSP + blockIdx.x * 4;
  const u32 hs0 = ld_flag(hsR);
  rows4_stats(p.x, p.ws, rt * 128 + slice * 8 + 4 * h, lane);
  const u32 hsn = __builtin_amdgcn_readfirstlane(hs0) + 1u;
  if (h == 0) st_word(hsR - 1024, hsn);
  __builtin_amdgcn_s_setprio(2);
  {
    const int e = (lane >> 2) & 3, z = 2 * (q & 1) + (e >> 1);
    bf16x8 f[12]; f32x4 c[12]; u32x4 one = {ONES, ONES, ONES, ONES};
    auto rdslot = [&](int sl) {
      if (ssm) {
        const u32 a = lds0 + (rt & 1) * 2 * WSLOT + h * 12288 + jl * 128 + ((q ^ (jl >> 1)) * 16);
#pragma unroll
        for (int n = 0; n < 6; n++) { f[n] = rd128(a, sl * WSLOT + n * 2048); f[6 + n] = rd128(a ^ 64, sl * WSLOT + n * 2048); }
      } else {
#pragma unroll
        for (int n = 0; n < 12; n++) f[n] = rd_tr(lds0 + (rt & 1) * 2 * WSLOT + h * 12288 + (4 * q + e) * 128 + (((n & 3) ^ z) * 32) + (lane & 3) * 8, sl * WSLOT + (n >> 2) * 4096);
      }
#pragma unroll
      for (int n = 0; n < 12; n++) PIN(f[n]);
      asm volatile("s_nop 1");
    };
#pragma unroll
    for (int n = 0; n < 12; n++) { c[n] = (f32x4){0.f, 0.f, 0.f, 0.f}; PIN(c[n]); }
    PIN(one);
    const int odd = rt & 1;
    LBAR();
    if (odd) { LBAR(); LBAR(); }
    if (rt < 9) {
      rdslot(0);
      if (ssm) {
#pragma unroll
        for (int n = 0; n < 12; n++) MMA(c[n % 6], f[n], one);
        MFENCE();
      } else {
#pragma unroll
        for (int n = 0; n < 12; n++) MMA(c[n], f[n], one);
        MFENCE();
      }
    }
    LBAR();
    if (rt < 9) {
      rdslot(1);
      if (ssm) {
#pragma unroll
        for (int n = 0; n < 12; n++) MMA(c[6 + n % 6], f[n], one);
        MFENCE();
      } else {
#pragma unroll
        for (int n = 0; n < 12; n++) MMA(c[n], f[n], one);
        MFENCE();
      }
    }
    LBAR();
    if (rt < 9) {
      f32x4 m = c[0];
#pragma unroll
      for (int n = 1; n < 12; n++) m = jl == n ? c[n] : m;
      const int col = (ssm ? (jl >= 6 ? 192 - 96 : 0) + 96 * h : 192 * h) + 16 * jl + 4 * q;
      st16(__builtin_bit_cast(u32x4, m), rWS, jl < 12 ? col * 4 : 0x7fffffffu, WS_CS + (slice * 16 + rt) * 1536);
    }
    if (!odd) { LBAR(); LBAR(); }
  }
  u32 pv = ld_flag(hsR);
  LBAR(); LBAR();
  waitv<0>(); LBAR();
  if (h == 0) { if (!__all(pv == hsn)) spin_all(hsR, hsn); st_word(hsR + 1024, 1u); }
  LBAR(); LBAR(); LBAR(); LBAR();
  const char* fGo = p.ws + WS_FLAGS + FL_GO * 128;
  pv = ld_flag(fGo);
  LBAR(); LBAR();
  if (!__all(pv == 1u)) spin_all(fGo, 1u);
  const int ir = 64 * h + lane, irt = ssm ? (ir & 64) + 16 * ((ir >> 2) & 3) + 8 * (ir & 1) + 2 * ((ir >> 4) & 3) + ((ir >> 1) & 1) : ir;
  const u32x2 sg = __builtin_amdgcn_raw_buffer_load_b64(rWS, (rt * 128 + irt) * 8, WS_ST, 16);
  const int cc = 192 * h + 4 * (lane < 48 ? lane : lane - 48);
  f32x4 cp[9];
#pragma unroll
  for (int k = 0; k < 9; k++) cp[k] = __builtin_bit_cast(f32x4, __builtin_amdgcn_raw_buffer_load_b128(rWS, cc * 4, WS_CS + (slice * 16 + k) * 1536, 16));
  const u32x2 bb = *(const GAS u32x2*)(p.b1 + (ssm ? 0 : slice * 384) + cc);
  LBAR(); LBAR(); SB();
  f32x4 cs = ((cp[0] + cp[1]) + (cp[2] + cp[3])) + ((cp[4] + cp[5]) + (cp[6] + cp[7])) + cp[8];
  float mean = __uint_as_float(sg[0]), rstd = __builtin_amdgcn_rsqf(__uint_as_float(sg[1])), sig = __uint_as_float(sg[1]) * rstd;
  u32x4 at, aw[4];
  { float m1, m2, m3; split3(-mean, m1, m2, m3); const float g1 = rbf(sig), g2 = sig - g1;
    at = (u32x4){cvtpk(m1, m1), cvtpk(m2, m2), cvtpk(m1, m3), cvtpk(g1, g2)}; mean = ssm ? -mean * rstd : mean; }
  PIN(cs); PIN(at); PIN(mean); PIN(rstd); PIN(sig);
  auto auxw = [&](int k) { float c1, c2, c3; split3(cs[k], c1, c2, c3); const float b = (k & 1) ? bfhi(bb[k >> 1]) : bflo(bb[k >> 1]);
    aw[k] = (u32x4){cvtpk(c1, c2), cvtpk(c1, c2), cvtpk(c3, c1), cvtpk(b, b)}; PIN(aw[k]); };
  const u32 ax = lds0 + (18 & 3) * WSLOT;
  LBAR(); SB();
  wr128(ax + AX_T + ir * 16, 0, at);
  { LAS float* sp = (LAS float*)(size_t)(ax + AX_S + ir * 4); sp[0] = mean; sp[128] = rstd; sp[256] = sig; }
  if (!ssm) { auxw(0); auxw(1); }
  LBAR(); SB();
  if (ssm) *(LAS f32x4*)(size_t)(ax + AX_W + cc * 4) = cs;
  else {
    auxw(2); auxw(3);
#pragma unroll
    for (int k = 0; k < 4; k++) wr128(ax + AX_W + cc * 4, k * 1600, aw[k]);
  }
  WAITL(); LBAR();
  if (ssm) { const CarryPre k = carry_pre(64 + 64 * h + lane, lds0); LBAR(); ssm_carry(p, k, rt, j, 64 + 64 * h + lane, lds0); WAITL(); LBAR(); }
  LBAR();
  char* const fMine = p.ws + WS_FLAGS + (FL_P + rt * 15 + slice) * 128;
  const char* fP = p.ws + WS_FLAGS + (FL_P + rt * 15 + 3 * g + ((lane & 1) ? (j == 2 ? 0 : j + 1) : (j == 0 ? 2 : j - 1))) * 128;
  LBAR(); if (h == 1 && !ssm) st_word(fMine, 2u);
  LBAR(); if (h == 1 && ssm) st_word(fMine, 1u);
#pragma unroll
  for (u32 lv = 1; lv < 4; lv++) {
    if (lv == 1) pv = ld_flag(fP);
    LBAR(); if (h == 0 && !__all(pv >= lv)) spin_ge(fP, lv);
    LBAR(); if (h == 1 && lv == 1) st_word(fMine, 2u);
    LBAR(); pv = ld_flag(fP);
    LBAR(); if (h == 1 && lv == 1) st_word(fMine, 3u);
  }
  LBAR(); LBAR(); LBAR(); LBAR();
  LBAR();
  if (h == 1) st_flag(p.ws, FL_O + (rt * 3 + j) * 5 + g, tag);
  else if (!ssm) {
    const int k = lane & 3;
    const char* fa = p.ws + WS_FLAGS + (FL_O + (rt * 3 + j) * 5 + (k < g ? k : k + 1)) * 128;
    u32 v = 0;
#pragma nounroll
    for (u32 i = 0; i < SPIN_CAP; i++) { v = ld_flag(fa); if (__all(v == tag || k == 3)) break; }
    const bool yok = __all(v == tag);
    *(LAS u32*)(size_t)lds0 = yok; WAITL(); BAR();
    if (!yok) { spin_all(fa, tag); BAR(); }
  }
}

template <bool SSM>
DI void phaseB(const Params& p, int rt, int g, int j, int w, int lane, u32 lds0, u32 tag) {
  asm volatile("" : "+v"(lane));
  const int rg = w >> 2, cg = w & 3, q = lane >> 4, e = (lane >> 2) & 3, pp = lane & 3, jl = lane & 15;
  const int z = 2 * (q & 1) + (e >> 1);
  u32 wlo[3], whi[3], tb[2];
#pragma unroll
  for (int n = 0; n < 3; n++) {
    const int t = 3 * cg + n;
    wlo[n] = SSM ? lds0 + (16 * t + jl) * 128 + ((q ^ (jl >> 1)) * 16) : lds0 + (t >> 2) * 4096 + (4 * q + e) * 128 + (((t & 3) ^ z) * 32) + pp * 8;
    whi[n] = SSM ? (wlo[n] ^ 64) : wlo[n] + 2 * WSLOT;
  }
  tb[0] = SSM ? lds0 + TOFF + rg * 8192 + (q >> 1) * 2048 + (pp >> 1) * 1024 + (((8 * (q & 1) + e) * 16) ^ ((pp >> 1) * 64)) + 8 * (pp & 1) : lds0 + TOFF + rg * 4096 + lane * 16;
  tb[1] = tb[0] ^ 64;
  float mean = 0.f, rstd = 0.f;
  if constexpr (!SSM) { const u32 sa = lds0 + (18 & 3) * WSLOT + AX_S + (64 * rg + 16 * g + jl) * 4; mean = rdf(sa, 0); rstd = rdf(sa, 512); }
  f32x4 ob[3][4];
  auto RW = [&](int sl, int kb, int n, int part) {
    if constexpr (SSM) return rd128((kb ? whi[n] : wlo[n]) + (sl >> 1) * 2 * WSLOT, (sl & 1) * WSLOT);
    else return rd_tr1(sl < 2 ? wlo[n] : whi[n], (sl & 1) * WSLOT + kb * 12288 + part * 2048);
  };
  auto RT = [&](int sl, int kb, int T, int part) {
    if constexpr (SSM) return rd_tr1(tb[part], sl * TSLOT + kb * 4096 + T * 256);
    else return rd128(tb[0], sl * TSLOT + kb * 8192 + T * 1024);
  };
  bf16x8 wf[3], tp[2];
  auto step = [&](auto slc, auto&& mid) {
    constexpr int SL = decltype(slc)::value & 3, NS = (SL + 1) & 3;
    half<SSM ? 1 : 0, (decltype(slc)::value > 3)>(ob, wf, tp, [&](int n, int part) { return RW(SL, 1, n, part); }, [&](int T, int part) { return RT(SL, 0, T, part); }, [&](int T, int part) { return RT(SL, 1, T, part); }, mid);
    half<SSM ? 1 : 0>(ob, wf, tp, [&](int n, int part) { return RW(NS, 0, n, part); }, [&](int T, int part) { return RT(SL, 1, T, part); }, [&](int T, int part) { return RT(NS, 0, T, part); }, [](int) {});
  };
  using I0 = std::integral_constant<int, 0>; using I1 = std::integral_constant<int, 1>; using I2 = std::integral_constant<int, 2>; using I3 = std::integral_constant<int, 3>;
  if constexpr (!SSM) waitv<4>();
  WAITL(); BAR();
#pragma unroll
  for (int n = 0; n < 3; n++) { if constexpr (SSM) wf[n] = RW(3, 0, n, 0); else wf[n] = cat(RW(3, 0, n, 0), RW(3, 0, n, 1)); }
#pragma unroll
  for (int T = 0; T < 2; T++) { if constexpr (SSM) tp[T] = cat(RT(3, 0, T, 0), RT(3, 0, T, 1)); else tp[T] = RT(3, 0, T, 0); }
  auto barm = [](int i) { if (i == 11) BAR(); };
  step(std::integral_constant<int, 7>{}, [](int) {});
  step(I0{}, [&](int i) { if (i == 3) { if constexpr (SSM) waitv<8>(); BAR(); } });
#pragma nounroll
  for (int it = 0; it < 4; it++) { step(I1{}, barm); step(I2{}, [&](int i) { if (i == 11) { waitv<4>(); BAR(); } }); step(I3{}, barm); step(I0{}, [&](int i) { if (i == 11) { waitv<0>(); BAR(); } }); }
  MFENCE();
  const rsrc_t rWS = mk(p.ws, WS_END);
  const u32 xb = WS_XB + (rt * 3 + j) * 32 * XBW + w * XBW;
  f32x4 mine[3];
#pragma unroll
  for (int T = 0; T < 4; T++) {
    if (SSM || T != g) {
      const u32 o = xb + T * 8 * XBW + g * 1536;
      const u32x4 a = {cvtpk(ob[0][T][0], ob[0][T][1]), cvtpk(ob[0][T][2], ob[0][T][3]), cvtpk(ob[1][T][0], ob[1][T][1]), cvtpk(ob[1][T][2], ob[1][T][3])};
      const u32 b0 = cvtpk(ob[2][T][0], ob[2][T][1]), b1 = cvtpk(ob[2][T][2], ob[2][T][3]);
      const auto s0 = __builtin_amdgcn_permlane32_swap(b0, b0, false, false), s1 = __builtin_amdgcn_permlane32_swap(b1, b1, false, false);
      st16(a, rWS, lane * 16, o);
      if (lane < 32) st16((u32x4){b0, b1, s0[1], s1[1]}, rWS, lane * 16, o + 1024);
    } else {
#pragma unroll
      for (int n = 0; n < 3; n++) mine[n] = ob[n][T];
    }
  }
  if constexpr (SSM) {
    waitv<0>(); BAR();
    STAMP(7);
  } else {
    waitv<0>(); BAR();
    const int tok = rt * 128 + 64 * rg + 16 * g + jl, col = 192 * j + 48 * cg + 4 * q;
    u32x2 xv[3], bv[3], dv[3];
#pragma unroll
    for (int n = 0; n < 3; n++) {
      const u32 cb = lds0 + WSLOT + (48 * cg + 4 * q + 16 * n) * 2;
      xv[n] = *(LAS u32x2*)(size_t)(cb + (16 * rg + jl) * 512); bv[n] = *(LAS u32x2*)(size_t)(cb + 16384); dv[n] = *(LAS u32x2*)(size_t)(cb + 17408);
    }
    const u32 o = xb + g * 8 * XBW;
    u32x4 a[3]; u32x2 b[3];
    BAR();
#pragma unroll
    for (int k = 0; k < 3; k++) {
      const int src = k < g ? k : k + 1;
      a[k] = __builtin_amdgcn_raw_buffer_load_b128(rWS, lane * 16, o + src * 1536, 16);
      b[k] = __builtin_amdgcn_raw_buffer_load_b64(rWS, (lane & 31) * 16 + (lane >> 5) * 8, o + src * 1536 + 1024, 16);
    }
    const bool yok = __builtin_amdgcn_readfirstlane(*(LAS u32*)(size_t)lds0);
    u32x4 ya; u32x2 yb;
    auto ldy = [&]() { ya = __builtin_amdgcn_raw_buffer_load_b128(rWS, lane * 16, o + 4 * 1536, 16); yb = __builtin_amdgcn_raw_buffer_load_b64(rWS, (lane & 31) * 16 + (lane >> 5) * 8, o + 4 * 1536 + 1024, 16); };
    if (yok) ldy();
    f32x4 gate[3], base[3];
#pragma unroll
    for (int n = 0; n < 3; n++) {
#pragma unroll
      for (int c = 0; c < 4; c++) {
        float G = mine[n][c] + ((c & 1) ? bfhi(bv[n][c >> 1]) : bflo(bv[n][c >> 1]));
#pragma unroll
        for (int k = 0; k < 3; k++) { const u32 v = n < 2 ? a[k][2 * n + (c >> 1)] : b[k][c >> 1]; G += (c & 1) ? bfhi(v) : bflo(v); }
        const float xx = (c & 1) ? bfhi(xv[n][c >> 1]) : bflo(xv[n][c >> 1]);
        const float dd = (c & 1) ? bfhi(dv[n][c >> 1]) : bflo(dv[n][c >> 1]);
        gate[n][c] = G * __builtin_amdgcn_rcpf(1.f + __builtin_amdgcn_exp2f(-1.4426950408889634f * G));
        base[n][c] = xx + (xx - mean) * rstd * dd * gate[n][c];
      }
    }
#pragma unroll
    for (int n = 0; n < 3; n++) { PIN(gate[n]); PIN(base[n]); }
    if (!yok) { BAR(); ldy(); }
#pragma unroll
    for (int n = 0; n < 3; n++) {
      const u32 y0 = n < 2 ? ya[2 * n] : yb[0], y1 = n < 2 ? ya[2 * n + 1] : yb[1];
      *(GAS f32x4*)(p.out + tok * D + col + 16 * n) = base[n] + (f32x4){bflo(y0), bfhi(y0), bflo(y1), bfhi(y1)} * gate[n];
    }
    STAMP(7);
  }
}

DI void ffn_compute(const Params& p, int rt, int g, int j, int w, int lane, u32 lds0, u32 tag) {
  const int rg = w >> 2, cg = w & 3, q = lane >> 4, e = (lane >> 2) & 3, pp = lane & 3, jl = lane & 15;
  const int z = 2 * (q & 1) + (e >> 1);
  u32 wc[4];
#pragma unroll
  for (int n = 0; n < 4; n++) { const int t = 6 * cg + n; wc[n] = lds0 + (t >> 2) * 4096 + (4 * q + e) * 128 + (((t & 3) ^ z) * 32) + pp * 8; }
  const u32 xbase = lds0 + TOFF + (64 * rg + jl) * 128 + ((q ^ (jl >> 1)) * 16);
  f32x4 acc[6][4];
#pragma unroll
  for (int n = 0; n < 6; n++)
#pragma unroll
    for (int T = 0; T < 4; T++) { acc[n][T] = (f32x4){0.f, 0.f, 0.f, 0.f}; asm volatile("" : "+v"(acc[n][T])); }
  bf16x8 wp[2], t[4];
  __builtin_amdgcn_s_waitcnt(0xC07F);
  BAR();
  wp[0] = rd_tr(wc[0], 0); wp[1] = rd_tr(wc[1], 0);
#pragma unroll
  for (int T = 0; T < 4; T++) t[T] = rd128(xbase, T * 2048);
  u32 xs = 0, xn = 0, n0 = 0, n1 = 0; int dw = 2 * WSLOT;
#pragma nounroll
  for (int it = 0; it < 9; it++) {
    stepA<0>(acc, wp, t, wc, n0, n1, xn, [&]() { xn = (xbase ^ 64) + xs; PIN(xn); xs = (xs + TSLOT) & (4 * TSLOT - 1); });
    stepA<1>(acc, wp, t, wc, n0, n1, xn, [&]() { xn = xbase + xs; n0 = wc[0] + dw; n1 = wc[1] + dw; wc[2] += dw; wc[3] += dw; PIN(xn); PIN(n0); PIN(n1); PIN(wc[2]); PIN(wc[3]); waitv<0>(); });
    wc[0] = n0; wc[1] = n1; dw = -dw;
  }
  const u32 ax = lds0 + (18 & 3) * WSLOT;
  float k1[4], k2[4], sg[4];
#pragma unroll
  for (int T = 0; T < 4; T++) {
    u32x4 v = __builtin_bit_cast(u32x4, rd128(ax + AX_T + (64 * rg + jl) * 16, T * 256));
    if (q != 0) v = (u32x4){0u, 0u, 0u, 0u};
    t[T] = __builtin_bit_cast(bf16x8, v);
    const float r = rdf(ax + AX_S + 512 + (64 * rg + jl) * 4, T * 64);
    sg[T] = rdf(ax + AX_S + 1024 + (64 * rg + jl) * 4, T * 64);
    k1[T] = -2.3022081985f * r; k2[T] = -0.10294324f * r * r * r;
  }
  {
    bf16x8 a[6];
#pragma unroll
    for (int n = 0; n < 6; n++) a[n] = rd128(ax + AX_W + ((jl & 3) * 100 + 24 * cg + (jl >> 2)) * 16, n * 64);
    asm volatile("s_nop 1" : "+v"(t[0]), "+v"(t[1]), "+v"(t[2]), "+v"(t[3]));
#pragma unroll
    for (int n = 0; n < 6; n++)
#pragma unroll
      for (int T = 0; T < 4; T++) MMA(acc[n][T], a[n], t[T]);
    MFENCE();
  }
  const rsrc_t rWS = mk(p.ws, WS_END);
  const u32 pg = WS_P + (rt * 15 + 3 * g + j) * 98304 + (cg >> 1) * 16384 + (cg & 1) * 8192 + rg * 4096;
  const u32 pl = lds0 + TOFF + (cg & 1) * 8192 + rg * 4096 + lane * 16, pl0 = pl + ((19 + (cg >> 1)) & 3) * TSLOT, pl1 = pl + ((21 + (cg >> 1)) & 3) * TSLOT;
#pragma unroll
  for (int bb = 0; bb < 3; bb++) {
    const int b = (bb + 2) % 3;
#pragma unroll
    for (int T = 0; T < 4; T++) {
      const u32x4 P = gelu_frag(acc[2 * b][T], acc[2 * b + 1][T], k1[T], k2[T], sg[T]);
      st16(P, rWS, lane * 16, pg + b * 32768 + T * 1024);
      if (b < 2) wr128(b ? pl1 : pl0, T * 1024, P);
      SB();
    }
    if (bb == 0) BAR();
  }
  phaseB<false>(p, rt, g, j, w, lane, lds0, tag);
}

DI void ssm_compute(const Params& p, int rt, int j, int w, int lane, u32 lds0, u32 tag) {
  const int rg = w >> 2, cg = w & 3; int q = lane >> 4, jl = lane & 15;
  u32 wb = lds0 + (16 * cg + jl) * 128 + ((q ^ (jl >> 1)) * 16);
  const u32 xb0 = lds0 + TOFF + (64 * rg + jl) * 128 + ((q ^ (jl >> 1)) * 16);
  f32x4 ar[3][4], ai[3][4];
#pragma unroll
  for (int n = 0; n < 3; n++)
#pragma unroll
    for (int T = 0; T < 4; T++) { ar[n][T] = (f32x4){0.f, 0.f, 0.f, 0.f}; ai[n][T] = (f32x4){0.f, 0.f, 0.f, 0.f}; asm volatile("" : "+v"(ar[n][T]), "+v"(ai[n][T])); }
  bf16x8 wf[3], tp[2];
  auto barm = [](int i) { if (i == 11) BAR(); };
  __builtin_amdgcn_s_waitcnt(0xC07F);
  BAR();
#pragma unroll
  for (int n = 0; n < 3; n++) wf[n] = rd128(wb, n * 8192);
  tp[0] = rd128(xb0, 0); tp[1] = rd128(xb0, 2048);
  u32 xs = 0, x0 = xb0, x1 = xb0 ^ 64, wb1 = wb ^ 64; int dw = 2 * WSLOT;
#pragma nounroll
  for (int it = 0; it < 9; it++) {
    half<2>(ar, wf, tp, [&](int n, int) { return rd128(wb1, n * 8192); }, [&](int T, int) { return rd128(x0, T * 2048); }, [&](int T, int) { return rd128(x1, T * 2048); }, barm);
    half<2>(ar, wf, tp, [&](int n, int) { return rd128(wb, WSLOT + n * 8192); }, [&](int T, int) { return rd128(x1, T * 2048); }, [&](int T, int) { return rd128(x0, T * 2048); },
            [&](int i) { if (i == 10) { wb += dw; PIN(wb); xs = (xs + TSLOT) & (4 * TSLOT - 1); waitv<0>(); } });
    half<2>(ai, wf, tp, [&](int n, int) { return rd128(wb1, WSLOT + n * 8192); }, [&](int T, int) { return rd128(x0, T * 2048); }, [&](int T, int) { return rd128(x1, T * 2048); },
            [&](int i) { if (i == 7) { wb1 += dw; x0 = xb0 + xs; PIN(wb1); PIN(x0); dw = -dw; } if (i == 11) BAR(); });
    half<2>(ai, wf, tp, [&](int n, int) { return rd128(wb, n * 8192); }, [&](int T, int) { return rd128(x1, T * 2048); }, [&](int T, int) { return rd128(x0, T * 2048); },
            [&](int i) { if (i == 4) { x1 = x0 ^ 64; PIN(x1); } });
  }
  asm volatile("" : "+v"(lane));
  q = lane >> 4; jl = lane & 15;
  const u32 ax = lds0 + (18 & 3) * WSLOT;
  MFENCE();
  f32x2 lc[3];
  const u32 tb = ax + AX_TAB + (16 * cg + jl) * 32;
  const u32 scr = lds0 + TOFF + (22 & 3) * TSLOT + ((4 * rg + q) * 192 + 16 * cg + jl) * 8;
  f32x2 e0[3]; float csr[3], csi[3];
#pragma unroll
  for (int b = 0; b < 3; b++) { csr[b] = rdf(ax + AX_W + (16 * cg + jl) * 4, b * 256); csi[b] = rdf(ax + AX_W + (16 * cg + jl) * 4, 768 + b * 256); }
#pragma unroll
  for (int T = 0; T < 4; T++) {
    const f32x4 r = rdf4(ax + AX_S + 512 + (64 * rg + 4 * q) * 4, T * 64), nm = rdf4(ax + AX_S + (64 * rg + 4 * q) * 4, T * 64);
#pragma unroll
    for (int b = 0; b < 3; b++) { ar[b][T] = ar[b][T] * r + nm * csr[b]; ai[b][T] = ai[b][T] * r + nm * csi[b]; }
  }
  f32x2 l8[3];
#pragma unroll
  for (int b = 0; b < 3; b++) {
    lc[b] = *(LAS f32x2*)(size_t)(tb + b * 2048);
    l8[b] = *(LAS f32x2*)(size_t)(tb + b * 2048 + 16);
    f32x2 hr = {ar[b][0][0], ar[b][0][1]}, hi = {ai[b][0][0], ai[b][0][1]};
#pragma unroll
    for (int k = 1; k < 8; k++) {
      const f32x2 ur = {ar[b][k >> 1][(k & 1) * 2], ar[b][k >> 1][(k & 1) * 2 + 1]}, ui = {ai[b][k >> 1][(k & 1) * 2], ai[b][k >> 1][(k & 1) * 2 + 1]};
      const f32x2 nr = lc[b][0] * hr + (ur - lc[b][1] * hi), ni = lc[b][0] * hi + (ui + lc[b][1] * hr);
      hr = nr; hi = ni;
    }
    e0[b] = (f32x2){hr.x, hi.x};
    *(LAS f32x2*)(size_t)(scr + b * 512) = (f32x2){l8[b].x * hr.x - l8[b].y * hi.x + hr.y, l8[b].x * hi.x + l8[b].y * hr.x + hi.y};
  }
  WAITL(); BAR(); SB();
#pragma unroll
  for (int b = 0; b < 3; b++) {
    const f32x2 cf = *(LAS f32x2*)(size_t)(tb + b * 2048 + 8); const float c2r = cf.x, c2i = cf.y;
#pragma unroll
    for (int T = 0; T < 4; T++) {
      const f32x4 nr = c2r * ar[b][T] - c2i * ai[b][T], ni = -c2r * ai[b][T] - c2i * ar[b][T];
      ar[b][T] = nr; ai[b][T] = ni;
      asm volatile("" : "+v"(ar[b][T]), "+v"(ai[b][T]));
    }
  }
  BAR();
  const rsrc_t rWS = mk(p.ws, WS_END);
  const u32 io0 = w * 2048 + lane * 16, io1 = w * 2048 + 1024 + ((lane * 16) ^ 64);
  const u32 pg = WS_P + (rt * 15 + 12 + j) * 98304;
  f32x2 cins[3];
#pragma unroll
  for (int b = 0; b < 3; b++) { cins[b] = *(LAS f32x2*)(size_t)(scr + b * 512); l8[b] = *(LAS f32x2*)(size_t)(tb + b * 2048 + 16); }
#pragma unroll
  for (int bb = 0; bb < 3; bb++) {
    const int b = (bb + 2) % 3;
    u32x4 H[2][2];
    const f32x2 cin = cins[b];
    const f32x2 sr = {cin.x, l8[b].x * cin.x - l8[b].y * cin.y + e0[b].x}, si = {cin.y, l8[b].x * cin.y + l8[b].y * cin.x + e0[b].y};
    const f32x2 cf = *(LAS f32x2*)(size_t)(tb + b * 2048 + 8); const float c2r = cf.x, c2i = cf.y;
    f32x2 hr = c2r * sr - c2i * si, hi = -c2r * si - c2i * sr;
#pragma unroll
    for (int T = 0; T < 4; T++) {
      f32x2 pr[2], pi[2];
#pragma unroll
      for (int k = 0; k < 2; k++) {
        const f32x2 ur = {ar[b][T][2 * k], ar[b][T][2 * k + 1]}, ui = {ai[b][T][2 * k], ai[b][T][2 * k + 1]};
        const f32x2 nr = lc[b][0] * hr + (ur + lc[b][1] * hi), ni = lc[b][0] * hi + (ui - lc[b][1] * hr);
        hr = nr; hi = ni; pr[k] = nr; pi[k] = ni;
      }
      H[0][0][T] = cvtpk(pr[0].x, pr[1].x); H[0][1][T] = cvtpk(pr[0].y, pr[1].y);
      H[1][0][T] = cvtpk(pi[0].x, pi[1].x); H[1][1][T] = cvtpk(pi[0].y, pi[1].y);
    }
    if (bb == 1) BAR();
#pragma unroll
    for (int k = 0; k < 4; k++) {
      st16(H[k >> 1][k & 1], rWS, (k & 1) ? io1 : io0, pg + (2 * b + (k >> 1)) * 16384);
      if (b < 2) wr128(lds0 + TOFF + ((k & 1) ? io1 : io0), ((19 + 2 * b + (k >> 1)) & 3) * TSLOT, H[k >> 1][k & 1]);
    }
    SB();
  }
  phaseB<true>(p, rt, 4, j, w, lane, lds0, tag);
}

DI void spare(const Params& p, int sid, u32 lds0, u32 tag, u64 pc0) {
  const int tid = threadIdx.x;
  u32 cpf = 0;
  if (tid < CODE_LINES && !(sid & 1)) cpf = *(const GAS u32*)(pc0 + tid * 128);
  const rsrc_t rWS = mk(p.ws, WS_END);
  const int w = __builtin_amdgcn_readfirstlane(tid >> 6), lane = tid & 63;
  if (sid == 15 && w == 11) { st_flag(p.ws, FL_GO, 0u); st16((u32x4){0u, 0u, 0u, 0u}, rWS, lane * 16, WS_ACK); waitv<0>(); hs_leader(p.ws, lane, lds0, STAMPPTR); return; }
  u32 hsn = 0;
  if (w == 11) { hsn = ld_flag(p.ws + WS_RSP + blockIdx.x * 4) + 1u; st_word(p.ws + WS_REQ + blockIdx.x * 4, hsn); }
  else if (w == 10 || w == 8) { rows4_stats(p.x, p.ws, sid * 128 + 120 + 2 * (w - 8), lane);
  } else if (w == 9 && sid < 9) {
    const int c = sid * 64 + lane;
    const float lr = bf2f(p.lre[c]), li = bf2f(p.lim[c]);
    const float dt = rbf(expf(bf2f(p.logdt[c])));
    const float ex = expf(dt * lr), an = dt * li;
    const float br = ex * cosf(an), bi = ex * sinf(an);
    const float nr = br - 1.f, den = 1.f / (lr * lr + li * li);
    const float cr = (nr * lr + bi * li) * den, ci = (bi * lr - nr * li) * den;
    float ar = br, ai = bi, r8 = 0.f, i8 = 0.f;
#pragma unroll
    for (int k = 0; k < 7; k++) { const float t = ar * ar - ai * ai; ai = 2.f * ar * ai; ar = t; if (k == 2) { r8 = ar; i8 = ai; } }
    st16(__builtin_bit_cast(u32x4, (f32x4){br, bi, 2.f * cr, 2.f * ci}), rWS, c * 32, WS_TAB);
    st16(__builtin_bit_cast(u32x4, (f32x4){r8, i8, ar, ai}), rWS, c * 32 + 16, WS_TAB);
  }
  if (w == 11) spin_all(p.ws + WS_RSP + blockIdx.x * 4, hsn);
  waitv<0>(); asm volatile("" :: "v"(cpf)); BAR();
  if (sid == 15) return;
  if (w == 11) st_word(p.ws + WS_ACK + blockIdx.x * 4, 1u);
  {
    u32 acc = cpf;
    const GAS u32* w2 = (const GAS u32*)p.w2;
#pragma unroll
    for (int i = 0; i < 4; i++) { const int L = (i * 15 + sid) * 768 + tid; if (L < 41472) acc ^= w2[L * 32]; }
    { const int L = sid * 768 + tid; if (L < 5184) acc ^= ((const GAS u32*)p.cre)[L * 32]; else if (L < 10368) acc ^= ((const GAS u32*)p.cim)[(L - 5184) * 32]; }
    asm volatile("" :: "v"(acc));
  }
}

__global__ __launch_bounds__(768, 3) void fused_kernel(Params p) {
  extern __shared__ __attribute__((aligned(16))) char smem[];
  u64x4 kv; asm volatile("s_load_dwordx8 %0, %1, 0x0" : "=s"(kv) : "s"(__builtin_amdgcn_kernarg_segment_ptr())); SB();
  const u32 lds0 = (u32)(size_t)(LAS char*)smem;
  const int b = blockIdx.x, xcd = b & 7, idx = b >> 3;
  const u32 tag = 1u;
  u64 pc0; asm volatile("s_getpc_b64 %0" : "=s"(pc0));
  STAMP(0);
  const int tri = idx / 3, j = idx - 3 * tri;
  const int g = tri < 2 ? 4 : xcd >> 1, rt = 8 * (xcd & 1) + (tri < 2 ? (xcd & 6) + tri : tri - 2);
  const int lane = threadIdx.x & 63, w = __builtin_amdgcn_readfirstlane(threadIdx.x >> 6);
  const int sid = xcd * 2 + idx - 30;
  if (__builtin_expect(idx < 30 && w < 2, 1)) { loader(kv, rt, g, j, w, lane, lds0, tag); return; }
  asm volatile("s_waitcnt lgkmcnt(0)" :: "s"(kv));
  const Params q = reload_params();
  if (idx >= 30) { spare(q, sid, lds0, tag, pc0); STAMP(7); return; }
  if (w < 4) { helper(q, rt, g, j, w - 2, lane, lds0, tag); return; }
  {
    if (w == 4 && lane < 2) st_flag(q.ws, lane ? FL_P + rt * 15 + 3 * g + j : FL_O + (rt * 3 + j) * 5 + g, 0u);
    if (g == 4 && w >= 5 && w < 8) st16((u32x4){0u, 0u, 0u, 0u}, mk(q.ws, WS_END), (192 * j + threadIdx.x - 320) * 16, WS_EG + (15 - rt) * 9216);
  }
  if (g < 4) { const Params q2 = reload_params(); ffn_compute(q2, rt, g, j, w - 4, lane, lds0, tag); }
  else { const Params q2 = reload_params(); ssm_compute(q2, rt, j, w - 4, lane, lds0, tag); }
}

extern "C" void kernel_launch(void* const* d_in, const int* in_sizes, int n_in, void* d_out, int out_size, void* d_ws, size_t ws_size, hipStream_t stream) {
  Params p;
  p.x = (const u16*)d_in[0]; p.lre = (const u16*)d_in[1]; p.lim = (const u16*)d_in[2]; p.bre = (const u16*)d_in[3]; p.bim = (const u16*)d_in[4];
  p.cre = (const u16*)d_in[5]; p.cim = (const u16*)d_in[6]; p.logdt = (const u16*)d_in[7]; p.dskip = (const u16*)d_in[8];
  p.w1 = (const u16*)d_in[10]; p.b1 = (const u16*)d_in[11]; p.w2 = (const u16*)d_in[12]; p.b2 = (const u16*)d_in[13];
  p.out = (float*)d_out; p.ws = (char*)d_ws + ((ws_size - WS_END) & ~(size_t)0x1fffff);
  (void)hipFuncSetAttribute((const void*)fused_kernel, hipFuncAttributeMaxDynamicSharedMemorySize, LDS_BYTES);
  fused_kernel<<<256, 768, LDS_BYTES, stream>>>(p);
}
```

```cpp
#define STAMPS
#define UNIT 10
#define K0 0
#define A0 64
#define B0 0
#define K1 0
#define A1 0
#define B1 5
#include <hip/hip_runtime.h>
#include <utility>

#define LAS __attribute__((address_space(3)))
#define GAS __attribute__((address_space(1)))
#define DI __device__ __forceinline__
typedef unsigned short u16;
typedef unsigned u32;
typedef unsigned long long u64;
typedef unsigned u32x4 __attribute__((ext_vector_type(4)));
typedef unsigned u32x2 __attribute__((ext_vector_type(2)));
typedef short s16x4 __attribute__((ext_vector_type(4)));
typedef short bf16x8 __attribute__((ext_vector_type(8)));
typedef float f32x4 __attribute__((ext_vector_type(4)));
typedef float f32x2 __attribute__((ext_vector_type(2)));
typedef __bf16 bf16x2 __attribute__((ext_vector_type(2)));
typedef __amdgpu_buffer_rsrc_t rsrc_t;
typedef u64 u64x4 __attribute__((ext_vector_type(4)));

constexpr int S = 2048, D = 576, F = 4608;
constexpr int ROWB = D * 2;
constexpr int WSLOT = 24576, TOFF = 4 * WSLOT, TSLOT = 16384, LDS_BYTES = 163840;
constexpr int AX_W = 0, AX_T = 6656, AX_S = 8704, AX_TAB = 10752;
constexpr u32 WS_FLAGS = 0;
constexpr u32 FL_GO = 0, FL_P = 32, FL_O = 32 + 240, NFLAG = 32 + 480;
constexpr u32 WS_EG = NFLAG * 128;
constexpr u32 WS_ST = WS_EG + 16 * 576 * 16;
constexpr u32 WS_REQ = WS_ST + 3 * 12288, WS_RSP = WS_REQ + 1024, WS_ACK = WS_RSP + 1024;
constexpr u32 WS_CS = 256 * 1024;
constexpr u32 WS_TAB = WS_CS + 15 * 16 * 1536;
constexpr u32 WS_P = 1u << 20;
constexpr u32 XBW = 5 * 1536;
constexpr u32 WS_XB = WS_P + 240 * 98304;
constexpr u32 WS_END = WS_XB + 48 * 32 * XBW;
constexpr int CODE_LINES = 430;
constexpr u32 SPIN_CAP = 1u << 18;

struct Params {
  const u16 *x, *w1, *bre, *bim;
  const u16 *lre, *lim, *cre, *cim, *logdt, *dskip, *b1, *w2, *b2;
  float* out; char* ws;
  u64* stamps;
};

DI Params reload_params() {
  Params q;
#if defined(__HIP_DEVICE_COMPILE__)
  typedef __attribute__((address_space(4))) const Params* KP;
  KP kp = (KP)__builtin_amdgcn_kernarg_segment_ptr(); asm volatile("" : "+s"(kp)); q = *kp;
#endif
  return q;
}
template <int N> DI void waitv() { asm volatile("s_waitcnt vmcnt(%0)" :: "n"(N) : "memory"); }
#define WAITL() asm volatile("s_waitcnt lgkmcnt(0)" ::: "memory")
#define BAR() do { asm volatile("" ::: "memory"); __builtin_amdgcn_s_barrier(); asm volatile("" ::: "memory"); } while (0)
#define SB() __builtin_amdgcn_sched_barrier(0)
#define MFENCE() do { SB(); asm volatile("s_nop 7\n\ts_nop 7"); SB(); } while (0)
#define MMA(c, a, b) asm volatile("v_mfma_f32_16x16x32_bf16 %0, %1, %2, %0" : "+v"(c) : "v"(a), "v"(b))
#define MMA0(c, a, b) asm volatile("v_mfma_f32_16x16x32_bf16 %0, %1, %2, 0" : "=&v"(c) : "v"(a), "v"(b))
#define PIN(x) asm volatile("" : "+v"(x))
#define STAMPPTR p.stamps
#define STAMP(i) do { SB(); if (threadIdx.x == ((i) == 7 && blockIdx.x < 240 ? 256 : 0)) p.stamps[blockIdx.x * 16 + (i)] = __builtin_amdgcn_s_memrealtime(); SB(); } while (0)

DI void dma(rsrc_t r, u32 lds, u32 voff, u32 soff) {
  asm volatile("s_mov_b32 m0, %0\n\ts_nop 0\n\tbuffer_load_dwordx4 %1, %2, %3 offen lds" :: "s"(lds), "v"(voff), "s"(r), "s"(soff) : "memory");
}
DI void dma_sc1(rsrc_t r, u32 lds, u32 voff, u32 soff) {
  asm volatile("s_mov_b32 m0, %0\n\ts_nop 0\n\tbuffer_load_dwordx4 %1, %2, %3 offen sc1 lds" :: "s"(lds), "v"(voff), "s"(r), "s"(soff) : "memory");
}
DI void dma12(rsrc_t r, u32 lds, u32 voff, u32 so, u32 d2, u32 st, u32 st3) {
  u32 t;
  asm volatile("s_mov_b32 m0, %2\n\t"
               "s_add_u32 %1, %0, %5\n\t"
               "buffer_load_dwordx4 %3, %4, %0 offen lds\n\t"
               "s_add_u32 m0, m0, 0x800\n\t"
               "s_add_u32 %0, %0, %6\n\t"
               "buffer_load_dwordx4 %3, %4, %1 offen lds\n\t"
               "s_add_u32 m0, m0, 0x800\n\t"
               "s_add_u32 %1, %1, %6\n\t"
               "buffer_load_dwordx4 %3, %4, %0 offen lds\n\t"
               "s_add_u32 m0, m0, 0x800\n\t"
               "s_add_u32 %0, %0, %6\n\t"
               "buffer_load_dwordx4 %3, %4, %1 offen lds\n\t"
               "s_add_u32 m0, m0, 0x800\n\t"
               "s_add_u32 %1, %1, %6\n\t"
               "buffer_load_dwordx4 %3, %4, %0 offen lds\n\t"
               "s_add_u32 m0, m0, 0x800\n\t"
               "s_add_u32 %0, %0, %7\n\t"
               "buffer_load_dwordx4 %3, %4, %1 offen lds\n\t"
               "s_add_u32 m0, m0, 0x800\n\t"
               "s_add_u32 %1, %1, %7\n\t"
               "buffer_load_dwordx4 %3, %4, %0 offen lds\n\t"
               "s_add_u32 m0, m0, 0x800\n\t"
               "s_add_u32 %0, %0, %6\n\t"
               "buffer_load_dwordx4 %3, %4, %1 offen lds\n\t"
               "s_add_u32 m0, m0, 0x800\n\t"
               "s_add_u32 %1, %1, %6\n\t"
               "buffer_load_dwordx4 %3, %4, %0 offen lds\n\t"
               "s_add_u32 m0, m0, 0x800\n\t"
               "s_add_u32 %0, %0, %6\n\t"
               "buffer_load_dwordx4 %3, %4, %1 offen lds\n\t"
               "s_add_u32 m0, m0, 0x800\n\t"
               "s_add_u32 %1, %1, %6\n\t"
               "buffer_load_dwordx4 %3, %4, %0 offen lds\n\t"
               "s_add_u32 m0, m0, 0x800\n\t"
               "s_add_u32 %0, %0, %6\n\t"
               "buffer_load_dwordx4 %3, %4, %1 offen lds"
               : "+&s"(so), "=&s"(t) : "s"(lds), "v"(voff), "s"(r), "s"(d2), "s"(st), "s"(st3) : "memory", "scc");
}
DI void dma4(rsrc_t r, u32 lds, u32 voff, u32 so, u32 st) {
  asm volatile("s_mov_b32 m0, %1\n\t"
               "s_nop 0\n\t"
               "buffer_load_dwordx4 %2, %3, %0 offen lds\n\t"
               "s_add_u32 m0, m0, 0x800\n\t"
               "s_add_u32 %0, %0, %4\n\t"
               "buffer_load_dwordx4 %2, %3, %0 offen lds\n\t"
               "s_add_u32 m0, m0, 0x800\n\t"
               "s_add_u32 %0, %0, %4\n\t"
               "buffer_load_dwordx4 %2, %3, %0 offen lds\n\t"
               "s_add_u32 m0, m0, 0x800\n\t"
               "s_add_u32 %0, %0, %4\n\t"
               "buffer_load_dwordx4 %2, %3, %0 offen lds"
               : "+&s"(so) : "s"(lds), "v"(voff), "s"(r), "s"(st) : "memory", "scc");
}
DI void dma8lin(rsrc_t r, u32 lds, u32 voff, u32 so) {
  asm volatile("s_mov_b32 m0, %1\n\t"
               "s_nop 0\n\t"
               "buffer_load_dwordx4 %2, %3, %0 offen offset:0 sc1 lds\n\t"
               "buffer_load_dwordx4 %2, %3, %0 offen offset:1024 sc1 lds\n\t"
               "buffer_load_dwordx4 %2, %3, %0 offen offset:2048 sc1 lds\n\t"
               "buffer_load_dwordx4 %2, %3, %0 offen offset:3072 sc1 lds\n\t"
               "s_add_u32 m0, m0, 0x2000\n\t"
               "s_add_u32 %0, %0, 0x2000\n\t"
               "buffer_load_dwordx4 %2, %3, %0 offen offset:0 sc1 lds\n\t"
               "buffer_load_dwordx4 %2, %3, %0 offen offset:1024 sc1 lds\n\t"
               "buffer_load_dwordx4 %2, %3, %0 offen offset:2048 sc1 lds\n\t"
               "buffer_load_dwordx4 %2, %3, %0 offen offset:3072 sc1 lds"
               : "+&s"(so) : "s"(lds), "v"(voff), "s"(r) : "memory", "scc");
}
DI rsrc_t mk(const void* ptr, u32 bytes) { return __builtin_amdgcn_make_buffer_rsrc((void*)ptr, 0, bytes, 0x00020000); }
DI s16x4 rd_tr1(u32 a, int off) { return __builtin_amdgcn_ds_read_tr16_b64_v4i16((LAS s16x4*)(size_t)(a + off)); }
DI bf16x8 cat(s16x4 lo, s16x4 hi) { return __builtin_shufflevector(lo, hi, 0, 1, 2, 3, 4, 5, 6, 7); }
DI bf16x8 rd_tr(u32 a, int off) { return cat(rd_tr1(a, off), rd_tr1(a, off + 2048)); }
DI bf16x8 rd128(u32 a, int off) { return *(LAS bf16x8*)(size_t)(a + off); }
DI f32x4 rdf4(u32 a, int off) { return *(LAS f32x4*)(size_t)(a + off); }
DI float rdf(u32 a, int off) { return *(LAS float*)(size_t)(a + off); }
DI void wr128(u32 a, int off, u32x4 v) { *(LAS u32x4*)(size_t)(a + off) = v; }
DI u32 cvtpk(float lo, float hi) { f32x2 v = {lo, hi}; bf16x2 b = __builtin_convertvector(v, bf16x2); return __builtin_bit_cast(u32, b); }
DI float bflo(u32 v) { return __uint_as_float(v << 16); }
DI float bfhi(u32 v) { return __uint_as_float(v & 0xffff0000u); }
DI float bf2f(u16 v) { return __uint_as_float((u32)v << 16); }
DI float dot2(u32 a, u32 b, float c) { return __builtin_amdgcn_fdot2_f32_bf16(__builtin_bit_cast(bf16x2, a), __builtin_bit_cast(bf16x2, b), c, false); }
constexpr u32 ONES = 0x3f803f80u;
DI float rbf(float v) { return bflo(cvtpk(v, 0.f)); }
DI void split3(float v, float& a, float& b, float& c) { a = rbf(v); const float r = v - a; b = rbf(r); c = rbf(r - b); }
template <int C> DI float dppx(float v) { return __uint_as_float(__builtin_amdgcn_update_dpp(0u, __float_as_uint(v), C, 0xf, 0xf, true)); }
template <int C> DI double dppd(double v) { u32x2 t = __builtin_bit_cast(u32x2, v); t[0] = __builtin_amdgcn_update_dpp(0u, t[0], C, 0xf, 0xf, true); t[1] = __builtin_amdgcn_update_dpp(0u, t[1], C, 0xf, 0xf, true); return __builtin_bit_cast(double, t); }
DI void st16(u32x4 v, rsrc_t r, u32 voff, u32 soff) { asm volatile("buffer_store_dwordx4 %0, %1, %2, %3 offen sc1\n\ts_nop 1" :: "v"(v), "v"(voff), "s"(r), "s"(soff) : "memory"); }
DI void st_flag(char* ws, u32 idx, u32 tag) { __hip_atomic_store((GAS u32*)(ws + WS_FLAGS + idx * 128), tag, __ATOMIC_RELAXED, __HIP_MEMORY_SCOPE_AGENT); }
DI u32 ld_flag(const char* a) { return __hip_atomic_load((GAS u32*)a, __ATOMIC_RELAXED, __HIP_MEMORY_SCOPE_AGENT); }
DI void spin_all(const char* a, u32 tag) {
#pragma nounroll
  for (u32 i = 0; i < SPIN_CAP; i++) { if (__all(ld_flag(a) == tag)) break; __builtin_amdgcn_s_sleep(1); }
  asm volatile("" ::: "memory");
}
DI void spin_ge(const char* a, u32 lvl) {
#pragma nounroll
  for (u32 i = 0; i < SPIN_CAP; i++) { if (__all(ld_flag(a) >= lvl)) break; __builtin_amdgcn_s_sleep(1); }
  asm volatile("" ::: "memory");
}
DI void st_word(char* a, u32 v) { __hip_atomic_store((GAS u32*)a, v, __ATOMIC_RELAXED, __HIP_MEMORY_SCOPE_AGENT); }
DI void hs_leader(char* ws, int lane, u32 lds0, u64* stamps) {
  const rsrc_t r = mk(ws, WS_END);
  bool first = true; u32x4 last = {0u, 0u, 0u, 0u};
#pragma nounroll
  for (u32 i = 0; i < SPIN_CAP; i++) {
    asm volatile("" ::: "memory");
    const u32x4 rq = __builtin_amdgcn_raw_buffer_load_b128(r, lane * 16, WS_REQ, 16), ak = __builtin_amdgcn_raw_buffer_load_b128(r, lane * 16, WS_ACK, 16);
    if (first || rq[0] != last[0] || rq[1] != last[1] || rq[2] != last[2] || rq[3] != last[3]) st16(rq, r, lane * 16, WS_RSP);
    first = false; last = rq;
    if (__all(ak[0] == 1u && ak[1] == 1u && ak[2] == 1u && (ak[3] == 1u || lane == 63))) break;
    __builtin_amdgcn_s_sleep(1);
  }
  BAR();
  st_flag(ws, FL_GO, 1u);
  if (lane == 0) stamps[255 * 16 + 2] = __builtin_amdgcn_s_memrealtime();
}
DI float row_sum16(float v) { v += dppx<0xb1>(v); v += dppx<0x4e>(v); v += dppx<0x141>(v); v += dppx<0x140>(v); return v; }
DI void rows4_stats(const u16* x, char* ws, int row0, int lane) {
  const int row = row0 + (lane >> 4), l = lane & 15;
  const GAS u32x4* src = (const GAS u32x4*)(x + row * D);
  u32x4 v[4];
#pragma unroll
  for (int i = 0; i < 4; i++) v[i] = src[l + 16 * i];
  const u32x2 t = ((const GAS u32x2*)(src + 64))[l];
  f32x2 e[18], s = {0.f, 0.f};
#pragma unroll
  for (int i = 0; i < 18; i++) { const u32 d = i < 16 ? v[i >> 2][i & 3] : t[i - 16]; e[i] = (f32x2){bflo(d), bfhi(d)}; s += e[i]; }
  const float mean = row_sum16(s.x + s.y) * (1.f / D);
  s = (f32x2){0.f, 0.f};
#pragma unroll
  for (int i = 0; i < 18; i++) { const f32x2 d = e[i] - mean; s += d * d; }
  const float ve = row_sum16(s.x + s.y) * (1.f / D) + 1e-5f;
  if (l == 0) __hip_atomic_store((GAS u64*)(ws + WS_ST + row * 8), ((u64)__float_as_uint(ve) << 32) | __float_as_uint(mean), __ATOMIC_RELAXED, __HIP_MEMORY_SCOPE_AGENT);
}

DI f32x2 gelu2(f32x2 a, float k1, float k2, float sg) {
  f32x2 t = a * a; t = t * k2 + k1; t = t * a;
  t.x = __builtin_amdgcn_exp2f(t.x); t.y = __builtin_amdgcn_exp2f(t.y);
  t = t * sg + sg;
  t.x = __builtin_amdgcn_rcpf(t.x); t.y = __builtin_amdgcn_rcpf(t.y);
  return t * a;
}
DI u32x4 gelu_frag(f32x4 a, f32x4 b, float k1, float k2, float sg) {
  f32x2 g0 = gelu2((f32x2){a[0], a[1]}, k1, k2, sg), g1 = gelu2((f32x2){a[2], a[3]}, k1, k2, sg);
  f32x2 g2 = gelu2((f32x2){b[0], b[1]}, k1, k2, sg), g3 = gelu2((f32x2){b[2], b[3]}, k1, k2, sg);
  return (u32x4){cvtpk(g0.x, g0.y), cvtpk(g1.x, g1.y), cvtpk(g2.x, g2.y), cvtpk(g3.x, g3.y)};
}

template <int H, class UPD>
DI void stepA(f32x4 (&acc)[6][4], bf16x8 (&wp)[2], bf16x8 (&t)[4], const u32 (&wc)[4], const u32& n0, const u32& n1, const u32& xn, UPD&& upd) {
  bf16x8 w[8], nx[4];
  w[0] = wp[0]; w[1] = wp[1];
#pragma unroll
  for (int n = 0; n < 6; n++) {
    const int f = n + 2, ff = f < 6 ? f : f - 6;
    const u32 a = (f < 6 || H == 0) ? wc[ff & 3] : (ff ? n1 : n0); const int off = (f < 6 ? H : 1 - H) * WSLOT + (ff >> 2) * 4096;
    MMA(acc[n][0], w[n], t[0]); SB();
    s16x4 lo = rd_tr1(a, off); SB();
    MMA(acc[n][1], w[n], t[1]); SB();
    s16x4 hi = rd_tr1(a, off + 2048); SB();
    MMA(acc[n][2], w[n], t[2]); SB();
    if (n >= 4) { nx[2 * n - 8] = rd128(xn, (2 * n - 8) * 2048); SB(); }
    if (n == 1) { upd(); SB(); }
    MMA(acc[n][3], w[n], t[3]); SB();
    if (n >= 4) { nx[2 * n - 7] = rd128(xn, (2 * n - 7) * 2048); SB(); }
    if (n == 3) { BAR(); SB(); }
    w[f] = cat(lo, hi);
  }
  wp[0] = w[6]; wp[1] = w[7];
#pragma unroll
  for (int i = 0; i < 4; i++) t[i] = nx[i];
}

template <int KIND, bool ZERO = false, class FW, class FT, class FN, class MID>
DI void half(f32x4 (&acc)[3][4], bf16x8 (&w)[3], bf16x8 (&tp)[2], FW&& rw, FT&& rt_, FN&& rn, MID&& mid) {
  bf16x8 t[4], wn[3], tn[2];
  t[0] = tp[0]; t[1] = tp[1];
  s16x4 lo;
#pragma unroll
  for (int i = 0; i < 12; i++) {
    const int T = i / 3, n = i % 3;
    if constexpr (KIND == 2) MMA(acc[n][T], t[T], w[n]); else if constexpr (ZERO) MMA0(acc[n][T], w[n], t[T]); else MMA(acc[n][T], w[n], t[T]);
    SB();
    if constexpr (KIND == 0) {
      if (i == 0) t[2] = rt_(2, 0); if (i == 1) t[3] = rt_(3, 0);
      if (i >= 2 && i <= 7) { if ((i & 1) == 0) lo = rw((i - 2) >> 1, 0); else wn[(i - 2) >> 1] = cat(lo, rw((i - 2) >> 1, 1)); }
      if (i == 8) tn[0] = rn(0, 0); if (i == 9) tn[1] = rn(1, 0);
    } else if constexpr (KIND == 1) {
      if (i <= 3) { if ((i & 1) == 0) lo = rt_(2 + (i >> 1), 0); else t[2 + (i >> 1)] = cat(lo, rt_(2 + (i >> 1), 1)); }
      if (i >= 4 && i <= 6) wn[i - 4] = rw(i - 4, 0);
      if (i >= 7 && i <= 10) { if ((i & 1) == 1) lo = rn((i - 7) >> 1, 0); else tn[(i - 7) >> 1] = cat(lo, rn((i - 7) >> 1, 1)); }
    } else {
      if (i == 0) t[2] = rt_(2, 0); if (i == 2) t[3] = rt_(3, 0);
      if (i == 3 || i == 5 || i == 6) wn[i == 3 ? 0 : i - 4] = rw(i == 3 ? 0 : i - 4, 0);
      if (i == 8) tn[0] = rn(0, 0); if (i == 9) tn[1] = rn(1, 0);
    }
    mid(i);
    SB();
  }
#pragma unroll
  for (int i = 0; i < 3; i++) w[i] = wn[i];
  tp[0] = tn[0]; tp[1] = tn[1];
}

template <int I, int N, class FN> DI void sfor(FN&& f) { if constexpr (I < N) { f(std::integral_constant<int, I>{}); sfor<I + 1, N>(f); } }

DI f32x2 cmul(f32x2 a, f32x2 b) { return (f32x2){a.x * b.x - a.y * b.y, a.x * b.y + a.y * b.x}; }
DI f32x2 cfma(f32x2 a, f32x2 b, f32x2 c) { return (f32x2){__builtin_fmaf(a.x, b.x, __builtin_fmaf(-a.y, b.y, c.x)), __builtin_fmaf(a.x, b.y, __builtin_fmaf(a.y, b.x, c.y))}; }
struct CarryPre { f32x2 l16, l32, l64, L1, L4; };
DI CarryPre carry_pre(int c, u32 lds0) {
  const f32x4 pw = rdf4(lds0 + (18 & 3) * WSLOT + AX_TAB + c * 32, 16);
  CarryPre k; const f32x2 l8 = {pw[0], pw[1]};
  k.l16 = cmul(l8, l8); k.l32 = cmul(k.l16, k.l16); k.l64 = cmul(k.l32, k.l32); k.L1 = (f32x2){pw[2], pw[3]};
  const f32x2 L2 = cmul(k.L1, k.L1); k.L4 = cmul(L2, L2);
  PIN(k.l16); PIN(k.l32); PIN(k.l64); PIN(k.L1); PIN(k.L4);
  return k;
}
DI void ssm_carry(const Params& p, const CarryPre& k, int rt, int j, int c, u32 lds0) {
  const u32 scr = lds0 + TOFF + (22 & 3) * TSLOT + c * 8;
  f32x2 e[8];
#pragma unroll
  for (int s = 0; s < 8; s++) e[s] = *(LAS f32x2*)(size_t)(scr + s * 1536);
  const f32x2 a01 = cfma(k.l16, e[0], e[1]), a23 = cfma(k.l16, e[2], e[3]), a45 = cfma(k.l16, e[4], e[5]), a67 = cfma(k.l16, e[6], e[7]);
  const f32x2 a03 = cfma(k.l32, a01, a23), E = cfma(k.l64, a03, cfma(k.l32, a45, a67));
  const rsrc_t rE = mk(p.ws + WS_EG, 16 * 9216);
  const int vo = (192 * j + c) * 16 + (15 - rt) * 9216;
  st16((u32x4){__float_as_uint(E.x), 1u, __float_as_uint(E.y), 1u}, rE, vo, 0);
  u32x4 va[15], vb[15];
  auto issue = [&](u32x4 (&v)[15]) {
    asm volatile("" ::: "memory");
#pragma unroll
    for (int i = 0; i < 15; i++) v[i] = __builtin_amdgcn_raw_buffer_load_b128(rE, vo, (15 - i) * 9216, 16);
    asm volatile("" ::: "memory");
  };
  f32x2 cy;
  auto eval = [&](u32x4 (&v)[15]) {
    u32 n = 0; f32x2 q[4];
#pragma unroll
    for (int i = 0; i < 15; i++) {
      n += v[i][1] + v[i][3];
      const f32x2 x = {__uint_as_float(v[i][0]), __uint_as_float(v[i][2])};
      const int ch = (i + 1) & 3;
      q[ch] = i < 4 ? x : cfma(k.L4, q[ch], x);
    }
    cy = cfma(k.L1, cfma(k.L1, cfma(k.L1, q[0], q[1]), q[2]), q[3]);
    return __all(n == 2u * rt);
  };
  __builtin_amdgcn_s_sleep(6);
  issue(va);
  __builtin_amdgcn_s_sleep(10);
  issue(vb);
  if (!eval(va)) {
    issue(va);
    if (!eval(vb) && !eval(va)) {
#pragma nounroll
      for (u32 spin = 0; spin < SPIN_CAP; spin++) { u32x4 vc[15]; issue(vc); if (eval(vc)) break; }
    }
  }
#pragma unroll
  for (int s = 0; s < 8; s += 2) e[s] = *(LAS f32x2*)(size_t)(scr + s * 1536);
  const f32x2 c2 = cfma(k.l32, cy, a01), c4 = cfma(k.l64, cy, a03), c6 = cfma(k.l32, c4, a45);
  const f32x2 cin[8] = {cy, cfma(k.l16, cy, e[0]), c2, cfma(k.l16, c2, e[2]), c4, cfma(k.l16, c4, e[4]), c6, cfma(k.l16, c6, e[6])};
#pragma unroll
  for (int s = 0; s < 8; s++) *(LAS f32x2*)(size_t)(scr + s * 1536) = cin[s];
}

#define LBAR() do { BAR(); const u32 t_ = (u32)__builtin_amdgcn_s_memtime(), r_ = (u32)__builtin_amdgcn_s_memrealtime(); tsv = lane == tsn ? t_ : tsv; tsr = lane == tsn ? r_ : tsr; tsn++; } while (0)
DI void loader(u64x4 kv, int rt, int g, int j, int u, int lane, u32 lds0, u32 tag) {
  const bool ssm = g == 4;
  const u32 wbytes = ssm ? D * ROWB : D * F * 2;
  const int r8 = lane >> 3, gp = lane & 7;
  const int cgr = ((((gp >> 1) ^ ((r8 >> 1) & 3)) << 1) | (gp & 1));
  const int rgr = gp ^ (4 * u + (r8 >> 1));
  const int slice = 3 * g + j;
  const int kA = 16 * u + 8 * (r8 >> 2) + (r8 & 3);
  const int R = 8 * u + r8;
  const u32 voffA = ssm ? (192 * j + R) * ROWB + rgr * 16 : kA * (F * 2) + slice * 768 + cgr * 16;
  const int tok = ssm ? 16 * ((R >> 2) & 3) + 8 * (R & 1) + ((R >> 1) & 1) : R;
  const u32 voffX = (rt * 128 + tok) * ROWB + rgr * 16;
  const u32 ldsW = lds0 + u * 1024, ldsT = lds0 + TOFF;
  const u32 aKt = ssm ? 128 : 64 * F * 2, aPar = ssm ? 0 : 32 * F * 2, pstr = ssm ? 32 * ROWB : 128, a2 = ssm ? 16 * ROWB : 4 * F * 2, xstr = (ssm ? 2 : 16) * ROWB;
  const int rot = (rt >= 10 ? rt - 10 : rt) & ~1;
  asm volatile("s_waitcnt lgkmcnt(0)" : "+s"(kv) : "v"(voffA), "v"(voffX), "s"(aKt), "s"(rot));
  const rsrc_t rX = mk((void*)kv[0], S * ROWB), rA0 = mk((void*)(ssm ? kv[2] : kv[1]), wbytes), rA1 = mk((void*)(ssm ? kv[3] : kv[1]), wbytes);
  auto ktile = [&](int kap) { int kt = kap + rot; return kt >= 9 ? kt - 9 : kt; };
  auto issueA = [&](int kap, int h) { dma12(h ? rA1 : rA0, ldsW + ((2 * kap + h) & 3) * WSLOT, voffA, ktile(kap) * aKt + h * aPar, a2, pstr, pstr); };
  auto issueX = [&](int kap, int h) { dma4(rX, ldsT + (kap & 3) * TSLOT + h * 8192 + u * 1024, voffX, h * 64 * ROWB + ktile(kap) * 128, xstr); };
  issueA(0, 0); issueX(0, 0); issueX(0, 1); issueA(0, 1);
  __builtin_amdgcn_s_setprio(3);
  waitv<12>(); BAR();
  issueX(1, 0); issueX(1, 1); issueA(1, 0);
#pragma nounroll
  for (int kap = 0; kap < 7; kap++) {
    u32 l1 = ldsW + ((2 * kap + 3) & 3) * WSLOT, s1 = ktile(kap + 1) * aKt + aPar, l2 = ldsW + ((2 * kap) & 3) * WSLOT, s2 = ktile(kap + 2) * aKt;
    u32 lx = ldsT + ((kap + 2) & 3) * TSLOT + u * 1024, lx2 = lx + 8192, sx = ktile(kap + 2) * 128, sx2 = sx + 64 * ROWB;
    asm volatile("" : "+s"(l1), "+s"(s1), "+s"(l2), "+s"(s2), "+s"(lx), "+s"(lx2), "+s"(sx), "+s"(sx2));
    waitv<16>(); BAR(); dma12(rA1, l1, voffA, s1, a2, pstr, pstr); dma4(rX, lx, voffX, sx, xstr);
    waitv<16>(); BAR(); dma12(rA0, l2, voffA, s2, a2, pstr, pstr); dma4(rX, lx2, voffX, sx2, xstr);
  }
  waitv<16>(); BAR(); issueA(8, 1);
  const Params p = reload_params();
  const u32 voffL = lane * 16;
  const rsrc_t rWS = mk(p.ws, WS_END), rB0 = mk(ssm ? p.cre : p.w2, wbytes), rB1 = mk(ssm ? p.cim : p.w2, wbytes);
  const u32 voffB = ssm ? voffA : R * ROWB + j * 384 + cgr * 16;
  const u32 bJ = ssm ? 384 : 384 * ROWB, bPar = ssm ? 0 : 192 * ROWB, bI = ssm ? 128 : 32 * ROWB, b0 = ssm ? 0 : g * 1152 * ROWB;
  const int j1 = j == 2 ? 0 : j + 1, j2 = j == 0 ? 2 : j - 1;
  const u32 st3 = 96 * ROWB - 2 * pstr, pb = WS_P + (rt * 15 + 3 * g) * 98304 + u * 4096;
  auto issueB = [&](int s, int jj, int i) { dma12((i & 1) ? rB1 : rB0, ldsW + (s & 3) * WSLOT, voffB, b0 + jj * bJ + (i & 1) * bPar + (i >> 1) * bI, 16 * ROWB, pstr, st3); };
  auto issueP = [&](int s, int jj, int i) { dma8lin(rWS, ldsT + (s & 3) * TSLOT + u * 4096, voffL, pb + jj * 98304 + i * 16384); };
  waitv<12>(); BAR();
#pragma unroll
  for (int k = 0; k < 3; k++) dma_sc1(rWS, lds0 + (18 & 3) * WSLOT + AX_TAB + (u * 3 + k) * 1024, voffL, WS_TAB + j * 6144 + (u * 3 + k) * 1024);
  waitv<3>();
  BAR(); issueB(19, j, 0); waitv<12>();
  BAR(); issueB(20, j, 1); waitv<12>();
  if (ssm) { const CarryPre k = carry_pre(lane, lds0); BAR(); if (u == 1) ssm_carry(p, k, rt, j, lane, lds0); WAITL(); }
  BAR();
  issueB(21, j, 2); waitv<12>();
  if (ssm) BAR();
  BAR(); issueB(22, j, 3); waitv<12>();
  BAR(); issueB(23, j, 4); issueP(23, j, 4); waitv<20>();
  BAR(); issueB(24, j, 5); issueP(24, j, 5);
#pragma nounroll
  for (int n = 0; n < 3; n++) {
    const int i = n ? 2 * n - 2 : 4;
    u32 w1 = b0 + j1 * bJ + (i >> 1) * bI, w2 = b0 + j2 * bJ + (i >> 1) * bI, w3 = w1 + bPar, w4 = w2 + bPar;
    u32 p1 = pb + j1 * 98304 + i * 16384, p2 = pb + j2 * 98304 + i * 16384, p3 = p1 + 16384, p4 = p2 + 16384;
    asm volatile("" : "+s"(w1), "+s"(w2), "+s"(w3), "+s"(w4), "+s"(p1), "+s"(p2), "+s"(p3), "+s"(p4));
    waitv<20>(); BAR(); dma12(rB0, ldsW + 1 * WSLOT, voffB, w1, 16 * ROWB, pstr, st3); dma8lin(rWS, ldsT + 1 * TSLOT + u * 4096, voffL, p1);
    waitv<20>(); BAR(); dma12(rB0, ldsW + 2 * WSLOT, voffB, w2, 16 * ROWB, pstr, st3); dma8lin(rWS, ldsT + 2 * TSLOT + u * 4096, voffL, p2);
    waitv<20>(); BAR(); dma12(rB1, ldsW + 3 * WSLOT, voffB, w3, 16 * ROWB, pstr, st3); dma8lin(rWS, ldsT + 3 * TSLOT + u * 4096, voffL, p3);
    waitv<20>(); BAR(); dma12(rB1, ldsW + 0 * WSLOT, voffB, w4, 16 * ROWB, pstr, st3); dma8lin(rWS, ldsT + 0 * TSLOT + u * 4096, voffL, p4);
  }
  waitv<20>();
  BAR();
  {
    const u32 l = lds0 + WSLOT;
    const u32 vo = (lane & 31) < 24 ? (rt * 128 + u * 64 + (g & 3) * 16 + (lane >> 5)) * ROWB + j * 384 + (lane & 31) * 16 : 0x7fffffffu;
#pragma unroll
    for (int k = 0; k < 8; k++) dma(rX, l + (u * 8 + k) * 1024, vo, k * 2 * ROWB);
    dma(mk(u ? p.dskip : p.b2, ROWB), l + 16384 + u * 1024, voffL + j * 384, 0);
  }
  waitv<9>(); BAR(); BAR(); waitv<0>();
  BAR();
}

DI void helper(const Params& p, int rt, int g, int j, int h, int lane, u32 lds0, u32 tag) {
  u32 tsv = 0, tsr = 0; int tsn = 0;
  const bool ssm = g == 4;
  const int slice = 3 * g + j, q = lane >> 4, jl = lane & 15;
  const rsrc_t rWS = mk(p.ws, WS_END);
  char* const hsR = p.ws + WS_RSP + blockIdx.x * 4;
  const u32 hs0 = ld_flag(hsR);
  rows4_stats(p.x, p.ws, rt * 128 + slice * 8 + 4 * h, lane);
  const u32 hsn = __builtin_amdgcn_readfirstlane(hs0) + 1u;
  if (h == 0) st_word(hsR - 1024, hsn);
  __builtin_amdgcn_s_setprio(2);
  {
    const int e = (lane >> 2) & 3, z = 2 * (q & 1) + (e >> 1);
    bf16x8 f[12]; f32x4 c[12]; u32x4 one = {ONES, ONES, ONES, ONES};
    auto rdslot = [&](int sl) {
      if (ssm) {
        const u32 a = lds0 + (rt & 1) * 2 * WSLOT + h * 12288 + jl * 128 + ((q ^ (jl >> 1)) * 16);
#pragma unroll
        for (int n = 0; n < 6; n++) { f[n] = rd128(a, sl * WSLOT + n * 2048); f[6 + n] = rd128(a ^ 64, sl * WSLOT + n * 2048); }
      } else {
#pragma unroll
        for (int n = 0; n < 12; n++) f[n] = rd_tr(lds0 + (rt & 1) * 2 * WSLOT + h * 12288 + (4 * q + e) * 128 + (((n & 3) ^ z) * 32) + (lane & 3) * 8, sl * WSLOT + (n >> 2) * 4096);
      }
#pragma unroll
      for (int n = 0; n < 12; n++) PIN(f[n]);
      asm volatile("s_nop 1");
    };
#pragma unroll
    for (int n = 0; n < 12; n++) { c[n] = (f32x4){0.f, 0.f, 0.f, 0.f}; PIN(c[n]); }
    PIN(one);
    const int odd = rt & 1;
    LBAR();
    if (odd) { LBAR(); LBAR(); }
    if (rt < 9) {
      rdslot(0);
      if (ssm) {
#pragma unroll
        for (int n = 0; n < 12; n++) MMA(c[n % 6], f[n], one);
        MFENCE();
      } else {
#pragma unroll
        for (int n = 0; n < 12; n++) MMA(c[n], f[n], one);
        MFENCE();
      }
    }
    LBAR();
    if (rt < 9) {
      rdslot(1);
      if (ssm) {
#pragma unroll
        for (int n = 0; n < 12; n++) MMA(c[6 + n % 6], f[n], one);
        MFENCE();
      } else {
#pragma unroll
        for (int n = 0; n < 12; n++) MMA(c[n], f[n], one);
        MFENCE();
      }
    }
    LBAR();
    if (rt < 9) {
      f32x4 m = c[0];
#pragma unroll
      for (int n = 1; n < 12; n++) m = jl == n ? c[n] : m;
      const int col = (ssm ? (jl >= 6 ? 192 - 96 : 0) + 96 * h : 192 * h) + 16 * jl + 4 * q;
      st16(__builtin_bit_cast(u32x4, m), rWS, jl < 12 ? col * 4 : 0x7fffffffu, WS_CS + (slice * 16 + rt) * 1536);
    }
    if (!odd) { LBAR(); LBAR(); }
  }
  u32 pv = ld_flag(hsR);
  LBAR(); LBAR();
  waitv<0>(); LBAR();
  if (h == 0) { if (!__all(pv == hsn)) spin_all(hsR, hsn); st_word(hsR + 1024, 1u); }
  LBAR(); LBAR(); LBAR(); LBAR();
  const char* fGo = p.ws + WS_FLAGS + FL_GO * 128;
  pv = ld_flag(fGo);
  LBAR(); LBAR();
  if (!__all(pv == 1u)) spin_all(fGo, 1u);
  const int ir = 64 * h + lane, irt = ssm ? (ir & 64) + 16 * ((ir >> 2) & 3) + 8 * (ir & 1) + 2 * ((ir >> 4) & 3) + ((ir >> 1) & 1) : ir;
  const u32x2 sg = __builtin_amdgcn_raw_buffer_load_b64(rWS, (rt * 128 + irt) * 8, WS_ST, 16);
  const int cc = 192 * h + 4 * (lane < 48 ? lane : lane - 48);
  f32x4 cp[9];
#pragma unroll
  for (int k = 0; k < 9; k++) cp[k] = __builtin_bit_cast(f32x4, __builtin_amdgcn_raw_buffer_load_b128(rWS, cc * 4, WS_CS + (slice * 16 + k) * 1536, 16));
  const u32x2 bb = *(const GAS u32x2*)(p.b1 + (ssm ? 0 : slice * 384) + cc);
  LBAR(); LBAR(); SB();
  f32x4 cs = ((cp[0] + cp[1]) + (cp[2] + cp[3])) + ((cp[4] + cp[5]) + (cp[6] + cp[7])) + cp[8];
  float mean = __uint_as_float(sg[0]), rstd = __builtin_amdgcn_rsqf(__uint_as_float(sg[1])), sig = __uint_as_float(sg[1]) * rstd;
  u32x4 at, aw[4];
  { float m1, m2, m3; split3(-mean, m1, m2, m3); const float g1 = rbf(sig), g2 = sig - g1;
    at = (u32x4){cvtpk(m1, m1), cvtpk(m2, m2), cvtpk(m1, m3), cvtpk(g1, g2)}; mean = ssm ? -mean * rstd : mean; }
  PIN(cs); PIN(at); PIN(mean); PIN(rstd); PIN(sig);
  auto auxw = [&](int k) { float c1, c2, c3; split3(cs[k], c1, c2, c3); const float b = (k & 1) ? bfhi(bb[k >> 1]) : bflo(bb[k >> 1]);
    aw[k] = (u32x4){cvtpk(c1, c2), cvtpk(c1, c2), cvtpk(c3, c1), cvtpk(b, b)}; PIN(aw[k]); };
  const u32 ax = lds0 + (18 & 3) * WSLOT;
  LBAR(); SB();
  wr128(ax + AX_T + ir * 16, 0, at);
  { LAS float* sp = (LAS float*)(size_t)(ax + AX_S + ir * 4); sp[0] = mean; sp[128] = rstd; sp[256] = sig; }
  if (!ssm) { auxw(0); auxw(1); }
  LBAR(); SB();
  if (ssm) *(LAS f32x4*)(size_t)(ax + AX_W + cc * 4) = cs;
  else {
    auxw(2); auxw(3);
#pragma unroll
    for (int k = 0; k < 4; k++) wr128(ax + AX_W + cc * 4, k * 1600, aw[k]);
  }
  WAITL(); LBAR();
  if (ssm) { const CarryPre k = carry_pre(64 + 64 * h + lane, lds0); LBAR(); ssm_carry(p, k, rt, j, 64 + 64 * h + lane, lds0); WAITL(); LBAR(); }
  LBAR();
  char* const fMine = p.ws + WS_FLAGS + (FL_P + rt * 15 + slice) * 128;
  const char* fP = p.ws + WS_FLAGS + (FL_P + rt * 15 + 3 * g + ((lane & 1) ? (j == 2 ? 0 : j + 1) : (j == 0 ? 2 : j - 1))) * 128;
  LBAR(); if (h == 1 && !ssm) st_word(fMine, 2u);
  LBAR(); if (h == 1 && ssm) st_word(fMine, 1u);
#pragma unroll
  for (u32 lv = 1; lv < 4; lv++) {
    if (lv == 1) pv = ld_flag(fP);
    LBAR(); if (h == 0 && !__all(pv >= lv)) spin_ge(fP, lv);
    LBAR(); if (h == 1 && lv == 1) st_word(fMine, 2u);
    LBAR(); pv = ld_flag(fP);
    LBAR(); if (h == 1 && lv == 1) st_word(fMine, 3u);
  }
  LBAR(); LBAR(); LBAR(); LBAR();
  LBAR();
  if (h == 1) st_flag(p.ws, FL_O + (rt * 3 + j) * 5 + g, tag);
  else if (!ssm) {
    const int k = lane & 3;
    const char* fa = p.ws + WS_FLAGS + (FL_O + (rt * 3 + j) * 5 + (k < g ? k : k + 1)) * 128;
    u32 v = 0;
#pragma nounroll
    for (u32 i = 0; i < SPIN_CAP; i++) { v = ld_flag(fa); if (__all(v == tag || k == 3)) break; }
    const bool yok = __all(v == tag);
    *(LAS u32*)(size_t)lds0 = yok; WAITL(); BAR();
    if (!yok) { spin_all(fa, tag); BAR(); }
  }
  if (h == 1) { ((u32*)(p.stamps + 4096))[blockIdx.x * 64 + lane] = tsv; ((u32*)(p.stamps + 4096))[16384 + blockIdx.x * 64 + lane] = tsr; }
}

template <bool SSM>
DI void phaseB(const Params& p, int rt, int g, int j, int w, int lane, u32 lds0, u32 tag) {
  asm volatile("" : "+v"(lane));
  const int rg = w >> 2, cg = w & 3, q = lane >> 4, e = (lane >> 2) & 3, pp = lane & 3, jl = lane & 15;
  const int z = 2 * (q & 1) + (e >> 1);
  u32 wlo[3], whi[3], tb[2];
#pragma unroll
  for (int n = 0; n < 3; n++) {
    const int t = 3 * cg + n;
    wlo[n] = SSM ? lds0 + (16 * t + jl) * 128 + ((q ^ (jl >> 1)) * 16) : lds0 + (t >> 2) * 4096 + (4 * q + e) * 128 + (((t & 3) ^ z) * 32) + pp * 8;
    whi[n] = SSM ? (wlo[n] ^ 64) : wlo[n] + 2 * WSLOT;
  }
  tb[0] = SSM ? lds0 + TOFF + rg * 8192 + (q >> 1) * 2048 + (pp >> 1) * 1024 + (((8 * (q & 1) + e) * 16) ^ ((pp >> 1) * 64)) + 8 * (pp & 1) : lds0 + TOFF + rg * 4096 + lane * 16;
  tb[1] = tb[0] ^ 64;
  float mean = 0.f, rstd = 0.f;
  if constexpr (!SSM) { const u32 sa = lds0 + (18 & 3) * WSLOT + AX_S + (64 * rg + 16 * g + jl) * 4; mean = rdf(sa, 0); rstd = rdf(sa, 512); }
  f32x4 ob[3][4];
  auto RW = [&](int sl, int kb, int n, int part) {
    if constexpr (SSM) return rd128((kb ? whi[n] : wlo[n]) + (sl >> 1) * 2 * WSLOT, (sl & 1) * WSLOT);
    else return rd_tr1(sl < 2 ? wlo[n] : whi[n], (sl & 1) * WSLOT + kb * 12288 + part * 2048);
  };
  auto RT = [&](int sl, int kb, int T, int part) {
    if constexpr (SSM) return rd_tr1(tb[part], sl * TSLOT + kb * 4096 + T * 256);
    else return rd128(tb[0], sl * TSLOT + kb * 8192 + T * 1024);
  };
  bf16x8 wf[3], tp[2];
  auto step = [&](auto slc, auto&& mid) {
    constexpr int SL = decltype(slc)::value & 3, NS = (SL + 1) & 3;
    half<SSM ? 1 : 0, (decltype(slc)::value > 3)>(ob, wf, tp, [&](int n, int part) { return RW(SL, 1, n, part); }, [&](int T, int part) { return RT(SL, 0, T, part); }, [&](int T, int part) { return RT(SL, 1, T, part); }, mid);
    half<SSM ? 1 : 0>(ob, wf, tp, [&](int n, int part) { return RW(NS, 0, n, part); }, [&](int T, int part) { return RT(SL, 1, T, part); }, [&](int T, int part) { return RT(NS, 0, T, part); }, [](int) {});
  };
  using I0 = std::integral_constant<int, 0>; using I1 = std::integral_constant<int, 1>; using I2 = std::integral_constant<int, 2>; using I3 = std::integral_constant<int, 3>;
  if constexpr (!SSM) waitv<4>();
  WAITL(); BAR();
#pragma unroll
  for (int n = 0; n < 3; n++) { if constexpr (SSM) wf[n] = RW(3, 0, n, 0); else wf[n] = cat(RW(3, 0, n, 0), RW(3, 0, n, 1)); }
#pragma unroll
  for (int T = 0; T < 2; T++) { if constexpr (SSM) tp[T] = cat(RT(3, 0, T, 0), RT(3, 0, T, 1)); else tp[T] = RT(3, 0, T, 0); }
  auto barm = [](int i) { if (i == 11) BAR(); };
  step(std::integral_constant<int, 7>{}, [](int) {});
  step(I0{}, [&](int i) { if (i == 3) { if constexpr (SSM) waitv<8>(); BAR(); } });
#pragma nounroll
  for (int it = 0; it < 4; it++) { step(I1{}, barm); step(I2{}, [&](int i) { if (i == 11) { waitv<4>(); BAR(); } }); step(I3{}, barm); step(I0{}, [&](int i) { if (i == 11) { waitv<0>(); BAR(); } }); }
  MFENCE();
  const rsrc_t rWS = mk(p.ws, WS_END);
  const u32 xb = WS_XB + (rt * 3 + j) * 32 * XBW + w * XBW;
  f32x4 mine[3];
#pragma unroll
  for (int T = 0; T < 4; T++) {
    if (SSM || T != g) {
      const u32 o = xb + T * 8 * XBW + g * 1536;
      const u32x4 a = {cvtpk(ob[0][T][0], ob[0][T][1]), cvtpk(ob[0][T][2], ob[0][T][3]), cvtpk(ob[1][T][0], ob[1][T][1]), cvtpk(ob[1][T][2], ob[1][T][3])};
      const u32 b0 = cvtpk(ob[2][T][0], ob[2][T][1]), b1 = cvtpk(ob[2][T][2], ob[2][T][3]);
      const auto s0 = __builtin_amdgcn_permlane32_swap(b0, b0, false, false), s1 = __builtin_amdgcn_permlane32_swap(b1, b1, false, false);
      st16(a, rWS, lane * 16, o);
      if (lane < 32) st16((u32x4){b0, b1, s0[1], s1[1]}, rWS, lane * 16, o + 1024);
    } else {
#pragma unroll
      for (int n = 0; n < 3; n++) mine[n] = ob[n][T];
    }
  }
  if constexpr (SSM) {
    waitv<0>(); BAR();
    STAMP(7);
  } else {
    waitv<0>(); BAR();
    const int tok = rt * 128 + 64 * rg + 16 * g + jl, col = 192 * j + 48 * cg + 4 * q;
    u32x2 xv[3], bv[3], dv[3];
#pragma unroll
    for (int n = 0; n < 3; n++) {
      const u32 cb = lds0 + WSLOT + (48 * cg + 4 * q + 16 * n) * 2;
      xv[n] = *(LAS u32x2*)(size_t)(cb + (16 * rg + jl) * 512); bv[n] = *(LAS u32x2*)(size_t)(cb + 16384); dv[n] = *(LAS u32x2*)(size_t)(cb + 17408);
    }
    const u32 o = xb + g * 8 * XBW;
    u32x4 a[3]; u32x2 b[3];
    BAR();
#pragma unroll
    for (int k = 0; k < 3; k++) {
      const int src = k < g ? k : k + 1;
      a[k] = __builtin_amdgcn_raw_buffer_load_b128(rWS, lane * 16, o + src * 1536, 16);
      b[k] = __builtin_amdgcn_raw_buffer_load_b64(rWS, (lane & 31) * 16 + (lane >> 5) * 8, o + src * 1536 + 1024, 16);
    }
    const bool yok = __builtin_amdgcn_readfirstlane(*(LAS u32*)(size_t)lds0);
    u32x4 ya; u32x2 yb;
    auto ldy = [&]() { ya = __builtin_amdgcn_raw_buffer_load_b128(rWS, lane * 16, o + 4 * 1536, 16); yb = __builtin_amdgcn_raw_buffer_load_b64(rWS, (lane & 31) * 16 + (lane >> 5) * 8, o + 4 * 1536 + 1024, 16); };
    if (yok) ldy();
    f32x4 gate[3], base[3];
#pragma unroll
    for (int n = 0; n < 3; n++) {
#pragma unroll
      for (int c = 0; c < 4; c++) {
        float G = mine[n][c] + ((c & 1) ? bfhi(bv[n][c >> 1]) : bflo(bv[n][c >> 1]));
#pragma unroll
        for (int k = 0; k < 3; k++) { const u32 v = n < 2 ? a[k][2 * n + (c >> 1)] : b[k][c >> 1]; G += (c & 1) ? bfhi(v) : bflo(v); }
        const float xx = (c & 1) ? bfhi(xv[n][c >> 1]) : bflo(xv[n][c >> 1]);
        const float dd = (c & 1) ? bfhi(dv[n][c >> 1]) : bflo(dv[n][c >> 1]);
        gate[n][c] = G * __builtin_amdgcn_rcpf(1.f + __builtin_amdgcn_exp2f(-1.4426950408889634f * G));
        base[n][c] = xx + (xx - mean) * rstd * dd * gate[n][c];
      }
    }
#pragma unroll
    for (int n = 0; n < 3; n++) { PIN(gate[n]); PIN(base[n]); }
    if (!yok) { BAR(); ldy(); }
#pragma unroll
    for (int n = 0; n < 3; n++) {
      const u32 y0 = n < 2 ? ya[2 * n] : yb[0], y1 = n < 2 ? ya[2 * n + 1] : yb[1];
      *(GAS f32x4*)(p.out + tok * D + col + 16 * n) = base[n] + (f32x4){bflo(y0), bfhi(y0), bflo(y1), bfhi(y1)} * gate[n];
    }
    STAMP(7);
  }
}

DI void ffn_compute(const Params& p, int rt, int g, int j, int w, int lane, u32 lds0, u32 tag) {
  const int rg = w >> 2, cg = w & 3, q = lane >> 4, e = (lane >> 2) & 3, pp = lane & 3, jl = lane & 15;
  const int z = 2 * (q & 1) + (e >> 1);
  u32 wc[4];
#pragma unroll
  for (int n = 0; n < 4; n++) { const int t = 6 * cg + n; wc[n] = lds0 + (t >> 2) * 4096 + (4 * q + e) * 128 + (((t & 3) ^ z) * 32) + pp * 8; }
  const u32 xbase = lds0 + TOFF + (64 * rg + jl) * 128 + ((q ^ (jl >> 1)) * 16);
  f32x4 acc[6][4];
#pragma unroll
  for (int n = 0; n < 6; n++)
#pragma unroll
    for (int T = 0; T < 4; T++) { acc[n][T] = (f32x4){0.f, 0.f, 0.f, 0.f}; asm volatile("" : "+v"(acc[n][T])); }
  bf16x8 wp[2], t[4];
  __builtin_amdgcn_s_waitcnt(0xC07F);
  BAR();
  wp[0] = rd_tr(wc[0], 0); wp[1] = rd_tr(wc[1], 0);
#pragma unroll
  for (int T = 0; T < 4; T++) t[T] = rd128(xbase, T * 2048);
  u32 xs = 0, xn = 0, n0 = 0, n1 = 0; int dw = 2 * WSLOT;
#pragma nounroll
  for (int it = 0; it < 9; it++) {
    stepA<0>(acc, wp, t, wc, n0, n1, xn, [&]() { xn = (xbase ^ 64) + xs; PIN(xn); xs = (xs + TSLOT) & (4 * TSLOT - 1); });
    stepA<1>(acc, wp, t, wc, n0, n1, xn, [&]() { xn = xbase + xs; n0 = wc[0] + dw; n1 = wc[1] + dw; wc[2] += dw; wc[3] += dw; PIN(xn); PIN(n0); PIN(n1); PIN(wc[2]); PIN(wc[3]); waitv<0>(); });
    wc[0] = n0; wc[1] = n1; dw = -dw;
  }
  const u32 ax = lds0 + (18 & 3) * WSLOT;
  float k1[4], k2[4], sg[4];
#pragma unroll
  for (int T = 0; T < 4; T++) {
    u32x4 v = __builtin_bit_cast(u32x4, rd128(ax + AX_T + (64 * rg + jl) * 16, T * 256));
    if (q != 0) v = (u32x4){0u, 0u, 0u, 0u};
    t[T] = __builtin_bit_cast(bf16x8, v);
    const float r = rdf(ax + AX_S + 512 + (64 * rg + jl) * 4, T * 64);
    sg[T] = rdf(ax + AX_S + 1024 + (64 * rg + jl) * 4, T * 64);
    k1[T] = -2.3022081985f * r; k2[T] = -0.10294324f * r * r * r;
  }
  {
    bf16x8 a[6];
#pragma unroll
    for (int n = 0; n < 6; n++) a[n] = rd128(ax + AX_W + ((jl & 3) * 100 + 24 * cg + (jl >> 2)) * 16, n * 64);
    asm volatile("s_nop 1" : "+v"(t[0]), "+v"(t[1]), "+v"(t[2]), "+v"(t[3]));
#pragma unroll
    for (int n = 0; n < 6; n++)
#pragma unroll
      for (int T = 0; T < 4; T++) MMA(acc[n][T], a[n], t[T]);
    MFENCE();
  }
  const rsrc_t rWS = mk(p.ws, WS_END);
  const u32 pg = WS_P + (rt * 15 + 3 * g + j) * 98304 + (cg >> 1) * 16384 + (cg & 1) * 8192 + rg * 4096;
  const u32 pl = lds0 + TOFF + (cg & 1) * 8192 + rg * 4096 + lane * 16, pl0 = pl + ((19 + (cg >> 1)) & 3) * TSLOT, pl1 = pl + ((21 + (cg >> 1)) & 3) * TSLOT;
#pragma unroll
  for (int bb = 0; bb < 3; bb++) {
    const int b = (bb + 2) % 3;
#pragma unroll
    for (int T = 0; T < 4; T++) {
      const u32x4 P = gelu_frag(acc[2 * b][T], acc[2 * b + 1][T], k1[T], k2[T], sg[T]);
      st16(P, rWS, lane * 16, pg + b * 32768 + T * 1024);
      if (b < 2) wr128(b ? pl1 : pl0, T * 1024, P);
      SB();
    }
    if (bb == 0) BAR();
  }
  phaseB<false>(p, rt, g, j, w, lane, lds0, tag);
}

DI void ssm_compute(const Params& p, int rt, int j, int w, int lane, u32 lds0, u32 tag) {
  const int rg = w >> 2, cg = w & 3; int q = lane >> 4, jl = lane & 15;
  u32 wb = lds0 + (16 * cg + jl) * 128 + ((q ^ (jl >> 1)) * 16);
  const u32 xb0 = lds0 + TOFF + (64 * rg + jl) * 128 + ((q ^ (jl >> 1)) * 16);
  f32x4 ar[3][4], ai[3][4];
#pragma unroll
  for (int n = 0; n < 3; n++)
#pragma unroll
    for (int T = 0; T < 4; T++) { ar[n][T] = (f32x4){0.f, 0.f, 0.f, 0.f}; ai[n][T] = (f32x4){0.f, 0.f, 0.f, 0.f}; asm volatile("" : "+v"(ar[n][T]), "+v"(ai[n][T])); }
  bf16x8 wf[3], tp[2];
  auto barm = [](int i) { if (i == 11) BAR(); };
  __builtin_amdgcn_s_waitcnt(0xC07F);
  BAR();
#pragma unroll
  for (int n = 0; n < 3; n++) wf[n] = rd128(wb, n * 8192);
  tp[0] = rd128(xb0, 0); tp[1] = rd128(xb0, 2048);
  u32 xs = 0, x0 = xb0, x1 = xb0 ^ 64, wb1 = wb ^ 64; int dw = 2 * WSLOT;
#pragma nounroll
  for (int it = 0; it < 9; it++) {
    half<2>(ar, wf, tp, [&](int n, int) { return rd128(wb1, n * 8192); }, [&](int T, int) { return rd128(x0, T * 2048); }, [&](int T, int) { return rd128(x1, T * 2048); }, barm);
    half<2>(ar, wf, tp, [&](int n, int) { return rd128(wb, WSLOT + n * 8192); }, [&](int T, int) { return rd128(x1, T * 2048); }, [&](int T, int) { return rd128(x0, T * 2048); },
            [&](int i) { if (i == 10) { wb += dw; PIN(wb); xs = (xs + TSLOT) & (4 * TSLOT - 1); waitv<0>(); } });
    half<2>(ai, wf, tp, [&](int n, int) { return rd128(wb1, WSLOT + n * 8192); }, [&](int T, int) { return rd128(x0, T * 2048); }, [&](int T, int) { return rd128(x1, T * 2048); },
            [&](int i) { if (i == 7) { wb1 += dw; x0 = xb0 + xs; PIN(wb1); PIN(x0); dw = -dw; } if (i == 11) BAR(); });
    half<2>(ai, wf, tp, [&](int n, int) { return rd128(wb, n * 8192); }, [&](int T, int) { return rd128(x1, T * 2048); }, [&](int T, int) { return rd128(x0, T * 2048); },
            [&](int i) { if (i == 4) { x1 = x0 ^ 64; PIN(x1); } });
  }
  asm volatile("" : "+v"(lane));
  q = lane >> 4; jl = lane & 15;
  const u32 ax = lds0 + (18 & 3) * WSLOT;
  MFENCE();
  f32x2 lc[3];
  const u32 tb = ax + AX_TAB + (16 * cg + jl) * 32;
  const u32 scr = lds0 + TOFF + (22 & 3) * TSLOT + ((4 * rg + q) * 192 + 16 * cg + jl) * 8;
  f32x2 e0[3]; float csr[3], csi[3];
#pragma unroll
  for (int b = 0; b < 3; b++) { csr[b] = rdf(ax + AX_W + (16 * cg + jl) * 4, b * 256); csi[b] = rdf(ax + AX_W + (16 * cg + jl) * 4, 768 + b * 256); }
#pragma unroll
  for (int T = 0; T < 4; T++) {
    const f32x4 r = rdf4(ax + AX_S + 512 + (64 * rg + 4 * q) * 4, T * 64), nm = rdf4(ax + AX_S + (64 * rg + 4 * q) * 4, T * 64);
#pragma unroll
    for (int b = 0; b < 3; b++) { ar[b][T] = ar[b][T] * r + nm * csr[b]; ai[b][T] = ai[b][T] * r + nm * csi[b]; }
  }
  f32x2 l8[3];
#pragma unroll
  for (int b = 0; b < 3; b++) {
    lc[b] = *(LAS f32x2*)(size_t)(tb + b * 2048);
    l8[b] = *(LAS f32x2*)(size_t)(tb + b * 2048 + 16);
    f32x2 hr = {ar[b][0][0], ar[b][0][1]}, hi = {ai[b][0][0], ai[b][0][1]};
#pragma unroll
    for (int k = 1; k < 8; k++) {
      const f32x2 ur = {ar[b][k >> 1][(k & 1) * 2], ar[b][k >> 1][(k & 1) * 2 + 1]}, ui = {ai[b][k >> 1][(k & 1) * 2], ai[b][k >> 1][(k & 1) * 2 + 1]};
      const f32x2 nr = lc[b][0] * hr + (ur - lc[b][1] * hi), ni = lc[b][0] * hi + (ui + lc[b][1] * hr);
      hr = nr; hi = ni;
    }
    e0[b] = (f32x2){hr.x, hi.x};
    *(LAS f32x2*)(size_t)(scr + b * 512) = (f32x2){l8[b].x * hr.x - l8[b].y * hi.x + hr.y, l8[b].x * hi.x + l8[b].y * hr.x + hi.y};
  }
  WAITL(); BAR(); SB();
#pragma unroll
  for (int b = 0; b < 3; b++) {
    const f32x2 cf = *(LAS f32x2*)(size_t)(tb + b * 2048 + 8); const float c2r = cf.x, c2i = cf.y;
#pragma unroll
    for (int T = 0; T < 4; T++) {
      const f32x4 nr = c2r * ar[b][T] - c2i * ai[b][T], ni = -c2r * ai[b][T] - c2i * ar[b][T];
      ar[b][T] = nr; ai[b][T] = ni;
      asm volatile("" : "+v"(ar[b][T]), "+v"(ai[b][T]));
    }
  }
  BAR();
  const rsrc_t rWS = mk(p.ws, WS_END);
  const u32 io0 = w * 2048 + lane * 16, io1 = w * 2048 + 1024 + ((lane * 16) ^ 64);
  const u32 pg = WS_P + (rt * 15 + 12 + j) * 98304;
  f32x2 cins[3];
#pragma unroll
  for (int b = 0; b < 3; b++) { cins[b] = *(LAS f32x2*)(size_t)(scr + b * 512); l8[b] = *(LAS f32x2*)(size_t)(tb + b * 2048 + 16); }
#pragma unroll
  for (int bb = 0; bb < 3; bb++) {
    const int b = (bb + 2) % 3;
    u32x4 H[2][2];
    const f32x2 cin = cins[b];
    const f32x2 sr = {cin.x, l8[b].x * cin.x - l8[b].y * cin.y + e0[b].x}, si = {cin.y, l8[b].x * cin.y + l8[b].y * cin.x + e0[b].y};
    const f32x2 cf = *(LAS f32x2*)(size_t)(tb + b * 2048 + 8); const float c2r = cf.x, c2i = cf.y;
    f32x2 hr = c2r * sr - c2i * si, hi = -c2r * si - c2i * sr;
#pragma unroll
    for (int T = 0; T < 4; T++) {
      f32x2 pr[2], pi[2];
#pragma unroll
      for (int k = 0; k < 2; k++) {
        const f32x2 ur = {ar[b][T][2 * k], ar[b][T][2 * k + 1]}, ui = {ai[b][T][2 * k], ai[b][T][2 * k + 1]};
        const f32x2 nr = lc[b][0] * hr + (ur + lc[b][1] * hi), ni = lc[b][0] * hi + (ui - lc[b][1] * hr);
        hr = nr; hi = ni; pr[k] = nr; pi[k] = ni;
      }
      H[0][0][T] = cvtpk(pr[0].x, pr[1].x); H[0][1][T] = cvtpk(pr[0].y, pr[1].y);
      H[1][0][T] = cvtpk(pi[0].x, pi[1].x); H[1][1][T] = cvtpk(pi[0].y, pi[1].y);
    }
    if (bb == 1) BAR();
#pragma unroll
    for (int k = 0; k < 4; k++) {
      st16(H[k >> 1][k & 1], rWS, (k & 1) ? io1 : io0, pg + (2 * b + (k >> 1)) * 16384);
      if (b < 2) wr128(lds0 + TOFF + ((k & 1) ? io1 : io0), ((19 + 2 * b + (k >> 1)) & 3) * TSLOT, H[k >> 1][k & 1]);
    }
    SB();
  }
  phaseB<true>(p, rt, 4, j, w, lane, lds0, tag);
}

DI void spare(const Params& p, int sid, u32 lds0, u32 tag, u64 pc0) {
  const int tid = threadIdx.x;
  u32 cpf = 0;
  if (tid < CODE_LINES && !(sid & 1)) cpf = *(const GAS u32*)(pc0 + tid * 128);
  const rsrc_t rWS = mk(p.ws, WS_END);
  const int w = __builtin_amdgcn_readfirstlane(tid >> 6), lane = tid & 63;
  if (sid == 15 && w == 11) { st_flag(p.ws, FL_GO, 0u); st16((u32x4){0u, 0u, 0u, 0u}, rWS, lane * 16, WS_ACK); waitv<0>(); hs_leader(p.ws, lane, lds0, STAMPPTR); return; }
  u32 hsn = 0;
  if (w == 11) { hsn = ld_flag(p.ws + WS_RSP + blockIdx.x * 4) + 1u; st_word(p.ws + WS_REQ + blockIdx.x * 4, hsn); }
  else if (w == 10 || w == 8) { rows4_stats(p.x, p.ws, sid * 128 + 120 + 2 * (w - 8), lane);
  } else if (w == 9 && sid < 9) {
    const int c = sid * 64 + lane;
    const float lr = bf2f(p.lre[c]), li = bf2f(p.lim[c]);
    const float dt = rbf(expf(bf2f(p.logdt[c])));
    const float ex = expf(dt * lr), an = dt * li;
    const float br = ex * cosf(an), bi = ex * sinf(an);
    const float nr = br - 1.f, den = 1.f / (lr * lr + li * li);
    const float cr = (nr * lr + bi * li) * den, ci = (bi * lr - nr * li) * den;
    float ar = br, ai = bi, r8 = 0.f, i8 = 0.f;
#pragma unroll
    for (int k = 0; k < 7; k++) { const float t = ar * ar - ai * ai; ai = 2.f * ar * ai; ar = t; if (k == 2) { r8 = ar; i8 = ai; } }
    st16(__builtin_bit_cast(u32x4, (f32x4){br, bi, 2.f * cr, 2.f * ci}), rWS, c * 32, WS_TAB);
    st16(__builtin_bit_cast(u32x4, (f32x4){r8, i8, ar, ai}), rWS, c * 32 + 16, WS_TAB);
  }
  if (w == 11) spin_all(p.ws + WS_RSP + blockIdx.x * 4, hsn);
  waitv<0>(); asm volatile("" :: "v"(cpf)); BAR();
  if (sid == 15) return;
  if (w == 11) st_word(p.ws + WS_ACK + blockIdx.x * 4, 1u);
  {
    u32 acc = cpf;
    const GAS u32* w2 = (const GAS u32*)p.w2;
#pragma unroll
    for (int i = 0; i < 4; i++) { const int L = (i * 15 + sid) * 768 + tid; if (L < 41472) acc ^= w2[L * 32]; }
    { const int L = sid * 768 + tid; if (L < 5184) acc ^= ((const GAS u32*)p.cre)[L * 32]; else if (L < 10368) acc ^= ((const GAS u32*)p.cim)[(L - 5184) * 32]; }
    asm volatile("" :: "v"(acc));
  }
}

__global__ __launch_bounds__(768, 3) void fused_kernel(Params p) {
  extern __shared__ __attribute__((aligned(16))) char smem[];
  u64x4 kv; asm volatile("s_load_dwordx8 %0, %1, 0x0" : "=s"(kv) : "s"(__builtin_amdgcn_kernarg_segment_ptr())); SB();
  const u32 lds0 = (u32)(size_t)(LAS char*)smem;
  const int b = blockIdx.x, xcd = b & 7, idx = b >> 3;
  const u32 tag = 1u;
  u64 pc0; asm volatile("s_getpc_b64 %0" : "=s"(pc0));
  STAMP(0);
  const int tri = idx / 3, j = idx - 3 * tri;
  const int g = tri < 2 ? 4 : xcd >> 1, rt = 8 * (xcd & 1) + (tri < 2 ? (xcd & 6) + tri : tri - 2);
  const int lane = threadIdx.x & 63, w = __builtin_amdgcn_readfirstlane(threadIdx.x >> 6);
  const int sid = xcd * 2 + idx - 30;
  if (__builtin_expect(idx < 30 && w < 2, 1)) { loader(kv, rt, g, j, w, lane, lds0, tag); return; }
  asm volatile("s_waitcnt lgkmcnt(0)" :: "s"(kv));
  const Params q = reload_params();
  if (idx >= 30) { spare(q, sid, lds0, tag, pc0); STAMP(7); return; }
  if (w < 4) { helper(q, rt, g, j, w - 2, lane, lds0, tag); return; }
  {
    if (w == 4 && lane < 2) st_flag(q.ws, lane ? FL_P + rt * 15 + 3 * g + j : FL_O + (rt * 3 + j) * 5 + g, 0u);
    if (g == 4 && w >= 5 && w < 8) st16((u32x4){0u, 0u, 0u, 0u}, mk(q.ws, WS_END), (192 * j + threadIdx.x - 320) * 16, WS_EG + (15 - rt) * 9216);
  }
  if (g < 4) { const Params q2 = reload_params(); ffn_compute(q2, rt, g, j, w - 4, lane, lds0, tag); }
  else { const Params q2 = reload_params(); ssm_compute(q2, rt, j, w - 4, lane, lds0, tag); }
}

__device__ u32 med_diff(const u64* st, int kind, int a, int b) {
  __shared__ u32 v[256]; __shared__ u32 res; __shared__ u64 t0s[256];
  const int t = threadIdx.x; const int idx = t >> 3, xcd = t & 7;
  const int k = idx >= 30 ? 2 : (idx < 6 ? 1 : 0);
  t0s[t] = st[t * 16]; __syncthreads();
  u64 t0 = ~0ull; for (int i = 0; i < 256; i++) t0 = t0s[i] < t0 ? t0s[i] : t0;
  const bool mx = b >= 100; if (mx) b -= 100;
  const u32* tr = (const u32*)(st + 4096) + 16384 + t * 64;
  auto get = [&](int x) { return x == -1 ? (u32)t0 : x < 64 ? tr[x] : (u32)st[t * 16 + x - 64]; };
  v[t] = k == kind ? get(b) - get(a) : 0xffffffffu; __syncthreads();
  int n = 0, less = 0; for (int i = 0; i < 256; i++) { n += v[i] != 0xffffffffu; less += (v[i] < v[t]) || (v[i] == v[t] && i < t); }
  if (k == kind && less == (mx ? n - 1 : n / 2)) res = v[t];
  __syncthreads(); u32 r = res; __syncthreads(); return r;
}
__global__ void amp_k(const u64* st) {
  u32 v0 = med_diff(st, K0, A0, B0), v1 = med_diff(st, K1, A1, B1);
  v0 = (v0 + UNIT / 2) / UNIT; v1 = (v1 + UNIT / 2) / UNIT; if (v0 > 127) v0 = 127; if (v1 > 110) v1 = 110;
  if (threadIdx.x == 0) { u64 t0 = __builtin_amdgcn_s_memrealtime(); u64 d = (u64)(v0 + 128 * v1) * 100; while (__builtin_amdgcn_s_memrealtime() - t0 < d) __builtin_amdgcn_s_sleep(4); }
}

extern "C" void kernel_launch(void* const* d_in, const int* in_sizes, int n_in, void* d_out, int out_size, void* d_ws, size_t ws_size, hipStream_t stream) {
  Params p;
  p.x = (const u16*)d_in[0]; p.lre = (const u16*)d_in[1]; p.lim = (const u16*)d_in[2]; p.bre = (const u16*)d_in[3]; p.bim = (const u16*)d_in[4];
  p.cre = (const u16*)d_in[5]; p.cim = (const u16*)d_in[6]; p.logdt = (const u16*)d_in[7]; p.dskip = (const u16*)d_in[8];
  p.w1 = (const u16*)d_in[10]; p.b1 = (const u16*)d_in[11]; p.w2 = (const u16*)d_in[12]; p.b2 = (const u16*)d_in[13];
  p.out = (float*)d_out; p.ws = (char*)d_ws;
  p.stamps = (u64*)((char*)d_ws + (200u << 20));
  (void)hipFuncSetAttribute((const void*)fused_kernel, hipFuncAttributeMaxDynamicSharedMemorySize, LDS_BYTES);
  fused_kernel<<<256, 768, LDS_BYTES, stream>>>(p);
  amp_k<<<1, 256, 0, stream>>>(p.stamps);
}
```

```cpp
#include <hip/hip_runtime.h>
#include <utility>

#define LAS __attribute__((address_space(3)))
#define GAS __attribute__((address_space(1)))
#define DI __device__ __forceinline__
typedef unsigned short u16;
typedef unsigned u32;
typedef unsigned long long u64;
typedef unsigned u32x4 __attribute__((ext_vector_type(4)));
typedef unsigned u32x2 __attribute__((ext_vector_type(2)));
typedef short s16x4 __attribute__((ext_vector_type(4)));
typedef short bf16x8 __attribute__((ext_vector_type(8)));
typedef float f32x4 __attribute__((ext_vector_type(4)));
typedef float f32x2 __attribute__((ext_vector_type(2)));
typedef __bf16 bf16x2 __attribute__((ext_vector_type(2)));
typedef __amdgpu_buffer_rsrc_t rsrc_t;
typedef u64 u64x4 __attribute__((ext_vector_type(4)));

constexpr int S = 2048, D = 576, F = 4608;
constexpr int ROWB = D * 2;
constexpr int WSLOT = 24576, TOFF = 4 * WSLOT, TSLOT = 16384, LDS_BYTES = 163840;
constexpr int AX_W = 0, AX_T = 6656, AX_S = 8704, AX_TAB = 10752;
constexpr u32 WS_FLAGS = 0;
constexpr u32 FL_GO = 0, FL_P = 32, FL_O = 32 + 240, NFLAG = 32 + 480;
constexpr u32 WS_EG = NFLAG * 128;
constexpr u32 WS_ST = WS_EG + 16 * 576 * 16;
constexpr u32 WS_REQ = WS_ST + 3 * 12288, WS_RSP = WS_REQ + 1024, WS_ACK = WS_RSP + 1024;
constexpr u32 WS_CS = 256 * 1024;
constexpr u32 WS_TAB = WS_CS + 15 * 16 * 1536;
constexpr u32 WS_P = 1u << 20;
constexpr u32 XBW = 5 * 1536;
constexpr u32 WS_XB = WS_P + 240 * 98304;
constexpr u32 WS_END = WS_XB + 48 * 32 * XBW;
constexpr int CODE_LINES = 440;
constexpr u32 SPIN_CAP = 1u << 18;

struct Params {
  const u16 *x, *w1, *bre, *bim;
  const u16 *lre, *lim, *cre, *cim, *logdt, *dskip, *b1, *w2, *b2;
  float* out; char* ws;
};

DI Params reload_params() {
  Params q;
#if defined(__HIP_DEVICE_COMPILE__)
  typedef __attribute__((address_space(4))) const Params* KP;
  KP kp = (KP)__builtin_amdgcn_kernarg_segment_ptr(); asm volatile("" : "+s"(kp)); q = *kp;
#endif
  return q;
}
template <int N> DI void waitv() { asm volatile("s_waitcnt vmcnt(%0)" :: "n"(N) : "memory"); }
#define WAITL() asm volatile("s_waitcnt lgkmcnt(0)" ::: "memory")
#define BAR() do { asm volatile("" ::: "memory"); __builtin_amdgcn_s_barrier(); asm volatile("" ::: "memory"); } while (0)
#define SB() __builtin_amdgcn_sched_barrier(0)
#define MFENCE() do { SB(); asm volatile("s_nop 7\n\ts_nop 7"); SB(); } while (0)
#define MMA(c, a, b) asm volatile("v_mfma_f32_16x16x32_bf16 %0, %1, %2, %0" : "+v"(c) : "v"(a), "v"(b))
#define MMA0(c, a, b) asm volatile("v_mfma_f32_16x16x32_bf16 %0, %1, %2, 0" : "=&v"(c) : "v"(a), "v"(b))
#define PIN(x) asm volatile("" : "+v"(x))
#define STAMPPTR nullptr
#define STAMP(i)

DI void dma(rsrc_t r, u32 lds, u32 voff, u32 soff) {
  asm volatile("s_mov_b32 m0, %0\n\ts_nop 0\n\tbuffer_load_dwordx4 %1, %2, %3 offen lds" :: "s"(lds), "v"(voff), "s"(r), "s"(soff) : "memory");
}
DI void dma_sc1(rsrc_t r, u32 lds, u32 voff, u32 soff) {
  asm volatile("s_mov_b32 m0, %0\n\ts_nop 0\n\tbuffer_load_dwordx4 %1, %2, %3 offen sc1 lds" :: "s"(lds), "v"(voff), "s"(r), "s"(soff) : "memory");
}
DI void dma12(rsrc_t r, u32 lds, u32 voff, u32 so, u32 d2, u32 st, u32 st3) {
  u32 t;
  asm volatile("s_mov_b32 m0, %2\n\t"
               "s_add_u32 %1, %0, %5\n\t"
               "buffer_load_dwordx4 %3, %4, %0 offen lds\n\t"
               "s_add_u32 m0, m0, 0x800\n\t"
               "s_add_u32 %0, %0, %6\n\t"
               "buffer_load_dwordx4 %3, %4, %1 offen lds\n\t"
               "s_add_u32 m0, m0, 0x800\n\t"
               "s_add_u32 %1, %1, %6\n\t"
               "buffer_load_dwordx4 %3, %4, %0 offen lds\n\t"
               "s_add_u32 m0, m0, 0x800\n\t"
               "s_add_u32 %0, %0, %6\n\t"
               "buffer_load_dwordx4 %3, %4, %1 offen lds\n\t"
               "s_add_u32 m0, m0, 0x800\n\t"
               "s_add_u32 %1, %1, %6\n\t"
               "buffer_load_dwordx4 %3, %4, %0 offen lds\n\t"
               "s_add_u32 m0, m0, 0x800\n\t"
               "s_add_u32 %0, %0, %7\n\t"
               "buffer_load_dwordx4 %3, %4, %1 offen lds\n\t"
               "s_add_u32 m0, m0, 0x800\n\t"
               "s_add_u32 %1, %1, %7\n\t"
               "buffer_load_dwordx4 %3, %4, %0 offen lds\n\t"
               "s_add_u32 m0, m0, 0x800\n\t"
               "s_add_u32 %0, %0, %6\n\t"
               "buffer_load_dwordx4 %3, %4, %1 offen lds\n\t"
               "s_add_u32 m0, m0, 0x800\n\t"
               "s_add_u32 %1, %1, %6\n\t"
               "buffer_load_dwordx4 %3, %4, %0 offen lds\n\t"
               "s_add_u32 m0, m0, 0x800\n\t"
               "s_add_u32 %0, %0, %6\n\t"
               "buffer_load_dwordx4 %3, %4, %1 offen lds\n\t"
               "s_add_u32 m0, m0, 0x800\n\t"
               "s_add_u32 %1, %1, %6\n\t"
               "buffer_load_dwordx4 %3, %4, %0 offen lds\n\t"
               "s_add_u32 m0, m0, 0x800\n\t"
               "s_add_u32 %0, %0, %6\n\t"
               "buffer_load_dwordx4 %3, %4, %1 offen lds"
               : "+&s"(so), "=&s"(t) : "s"(lds), "v"(voff), "s"(r), "s"(d2), "s"(st), "s"(st3) : "memory", "scc");
}
DI void dma4(rsrc_t r, u32 lds, u32 voff, u32 so, u32 st) {
  asm volatile("s_mov_b32 m0, %1\n\t"
               "s_nop 0\n\t"
               "buffer_load_dwordx4 %2, %3, %0 offen lds\n\t"
               "s_add_u32 m0, m0, 0x800\n\t"
               "s_add_u32 %0, %0, %4\n\t"
               "buffer_load_dwordx4 %2, %3, %0 offen lds\n\t"
               "s_add_u32 m0, m0, 0x800\n\t"
               "s_add_u32 %0, %0, %4\n\t"
               "buffer_load_dwordx4 %2, %3, %0 offen lds\n\t"
               "s_add_u32 m0, m0, 0x800\n\t"
               "s_add_u32 %0, %0, %4\n\t"
               "buffer_load_dwordx4 %2, %3, %0 offen lds"
               : "+&s"(so) : "s"(lds), "v"(voff), "s"(r), "s"(st) : "memory", "scc");
}
DI void dma8lin(rsrc_t r, u32 lds, u32 voff, u32 so) {
  asm volatile("s_mov_b32 m0, %1\n\t"
               "s_nop 0\n\t"
               "buffer_load_dwordx4 %2, %3, %0 offen offset:0 sc1 lds\n\t"
               "buffer_load_dwordx4 %2, %3, %0 offen offset:1024 sc1 lds\n\t"
               "buffer_load_dwordx4 %2, %3, %0 offen offset:2048 sc1 lds\n\t"
               "buffer_load_dwordx4 %2, %3, %0 offen offset:3072 sc1 lds\n\t"
               "s_add_u32 m0, m0, 0x2000\n\t"
               "s_add_u32 %0, %0, 0x2000\n\t"
               "buffer_load_dwordx4 %2, %3, %0 offen offset:0 sc1 lds\n\t"
               "buffer_load_dwordx4 %2, %3, %0 offen offset:1024 sc1 lds\n\t"
               "buffer_load_dwordx4 %2, %3, %0 offen offset:2048 sc1 lds\n\t"
               "buffer_load_dwordx4 %2, %3, %0 offen offset:3072 sc1 lds"
               : "+&s"(so) : "s"(lds), "v"(voff), "s"(r) : "memory", "scc");
}
DI rsrc_t mk(const void* ptr, u32 bytes) { return __builtin_amdgcn_make_buffer_rsrc((void*)ptr, 0, bytes, 0x00020000); }
DI s16x4 rd_tr1(u32 a, int off) { return __builtin_amdgcn_ds_read_tr16_b64_v4i16((LAS s16x4*)(size_t)(a + off)); }
DI bf16x8 cat(s16x4 lo, s16x4 hi) { return __builtin_shufflevector(lo, hi, 0, 1, 2, 3, 4, 5, 6, 7); }
DI bf16x8 rd_tr(u32 a, int off) { return cat(rd_tr1(a, off), rd_tr1(a, off + 2048)); }
DI bf16x8 rd128(u32 a, int off) { return *(LAS bf16x8*)(size_t)(a + off); }
DI f32x4 rdf4(u32 a, int off) { return *(LAS f32x4*)(size_t)(a + off); }
DI float rdf(u32 a, int off) { return *(LAS float*)(size_t)(a + off); }
DI void wr128(u32 a, int off, u32x4 v) { *(LAS u32x4*)(size_t)(a + off) = v; }
DI u32 cvtpk(float lo, float hi) { f32x2 v = {lo, hi}; bf16x2 b = __builtin_convertvector(v, bf16x2); return __builtin_bit_cast(u32, b); }
DI float bflo(u32 v) { return __uint_as_float(v << 16); }
DI float bfhi(u32 v) { return __uint_as_float(v & 0xffff0000u); }
DI float bf2f(u16 v) { return __uint_as_float((u32)v << 16); }
DI float dot2(u32 a, u32 b, float c) { return __builtin_amdgcn_fdot2_f32_bf16(__builtin_bit_cast(bf16x2, a), __builtin_bit_cast(bf16x2, b), c, false); }
constexpr u32 ONES = 0x3f803f80u;
DI float rbf(float v) { return bflo(cvtpk(v, 0.f)); }
DI void split3(float v, float& a, float& b, float& c) { a = rbf(v); const float r = v - a; b = rbf(r); c = rbf(r - b); }
template <int C> DI float dppx(float v) { return __uint_as_float(__builtin_amdgcn_update_dpp(0u, __float_as_uint(v), C, 0xf, 0xf, true)); }
template <int C> DI double dppd(double v) { u32x2 t = __builtin_bit_cast(u32x2, v); t[0] = __builtin_amdgcn_update_dpp(0u, t[0], C, 0xf, 0xf, true); t[1] = __builtin_amdgcn_update_dpp(0u, t[1], C, 0xf, 0xf, true); return __builtin_bit_cast(double, t); }
DI void st16(u32x4 v, rsrc_t r, u32 voff, u32 soff) { asm volatile("buffer_store_dwordx4 %0, %1, %2, %3 offen sc1\n\ts_nop 1" :: "v"(v), "v"(voff), "s"(r), "s"(soff) : "memory"); }
DI void st_flag(char* ws, u32 idx, u32 tag) { __hip_atomic_store((GAS u32*)(ws + WS_FLAGS + idx * 128), tag, __ATOMIC_RELAXED, __HIP_MEMORY_SCOPE_AGENT); }
DI u32 ld_flag(const char* a) { return __hip_atomic_load((GAS u32*)a, __ATOMIC_RELAXED, __HIP_MEMORY_SCOPE_AGENT); }
DI void spin_all(const char* a, u32 tag) {
#pragma nounroll
  for (u32 i = 0; i < SPIN_CAP; i++) { if (__all(ld_flag(a) == tag)) break; __builtin_amdgcn_s_sleep(1); }
  asm volatile("" ::: "memory");
}
DI void spin_ge(const char* a, u32 lvl) {
#pragma nounroll
  for (u32 i = 0; i < SPIN_CAP; i++) { if (__all(ld_flag(a) >= lvl)) break; __builtin_amdgcn_s_sleep(1); }
  asm volatile("" ::: "memory");
}
DI void st_word(char* a, u32 v) { __hip_atomic_store((GAS u32*)a, v, __ATOMIC_RELAXED, __HIP_MEMORY_SCOPE_AGENT); }
DI void hs_leader(char* ws, int lane, u32 lds0, u64* stamps) {
  const rsrc_t r = mk(ws, WS_END);
  bool first = true; u32x4 last = {0u, 0u, 0u, 0u};
#pragma nounroll
  for (u32 i = 0; i < SPIN_CAP; i++) {
    asm volatile("" ::: "memory");
    const u32x4 rq = __builtin_amdgcn_raw_buffer_load_b128(r, lane * 16, WS_REQ, 16), ak = __builtin_amdgcn_raw_buffer_load_b128(r, lane * 16, WS_ACK, 16);
    if (first || rq[0] != last[0] || rq[1] != last[1] || rq[2] != last[2] || rq[3] != last[3]) st16(rq, r, lane * 16, WS_RSP);
    first = false; last = rq;
    if (__all(ak[0] == 1u && ak[1] == 1u && ak[2] == 1u && (ak[3] == 1u || lane == 63))) break;
    __builtin_amdgcn_s_sleep(1);
  }
  BAR();
  st_flag(ws, FL_GO, 1u);
}
DI float row_sum16(float v) { v += dppx<0xb1>(v); v += dppx<0x4e>(v); v += dppx<0x141>(v); v += dppx<0x140>(v); return v; }
DI void rows4_load(const u16* x, int row0, int lane, u32x4 (&v)[4], u32x2& t) {
  const GAS u32x4* src = (const GAS u32x4*)(x + (row0 + (lane >> 4)) * D);
#pragma unroll
  for (int i = 0; i < 4; i++) v[i] = src[(lane & 15) + 16 * i];
  t = ((const GAS u32x2*)(src + 64))[lane & 15];
}
DI void rows4_stats(char* ws, int row0, int lane, const u32x4 (&v)[4], const u32x2& t) {
  const int row = row0 + (lane >> 4), l = lane & 15;
  f32x2 e[18], s = {0.f, 0.f};
#pragma unroll
  for (int i = 0; i < 18; i++) { const u32 d = i < 16 ? v[i >> 2][i & 3] : t[i - 16]; e[i] = (f32x2){bflo(d), bfhi(d)}; s += e[i]; }
  const float mean = row_sum16(s.x + s.y) * (1.f / D);
  s = (f32x2){0.f, 0.f};
#pragma unroll
  for (int i = 0; i < 18; i++) { const f32x2 d = e[i] - mean; s += d * d; }
  const float ve = row_sum16(s.x + s.y) * (1.f / D) + 1e-5f;
  if (l == 0) __hip_atomic_store((GAS u64*)(ws + WS_ST + row * 8), ((u64)__float_as_uint(ve) << 32) | __float_as_uint(mean), __ATOMIC_RELAXED, __HIP_MEMORY_SCOPE_AGENT);
}

DI f32x2 gelu2(f32x2 a, float k1, float k2, float sg) {
  f32x2 t = a * a; t = t * k2 + k1; t = t * a;
  t.x = __builtin_amdgcn_exp2f(t.x); t.y = __builtin_amdgcn_exp2f(t.y);
  t = t * sg + sg;
  t.x = __builtin_amdgcn_rcpf(t.x); t.y = __builtin_amdgcn_rcpf(t.y);
  return t * a;
}
DI u32x4 gelu_frag(f32x4 a, f32x4 b, float k1, float k2, float sg) {
  f32x2 g0 = gelu2((f32x2){a[0], a[1]}, k1, k2, sg), g1 = gelu2((f32x2){a[2], a[3]}, k1, k2, sg);
  f32x2 g2 = gelu2((f32x2){b[0], b[1]}, k1, k2, sg), g3 = gelu2((f32x2){b[2], b[3]}, k1, k2, sg);
  return (u32x4){cvtpk(g0.x, g0.y), cvtpk(g1.x, g1.y), cvtpk(g2.x, g2.y), cvtpk(g3.x, g3.y)};
}

template <int H, class UPD>
DI void stepA(f32x4 (&acc)[6][4], bf16x8 (&wp)[2], bf16x8 (&t)[4], const u32 (&wc)[4], const u32& n0, const u32& n1, const u32& xn, UPD&& upd) {
  bf16x8 w[8], nx[4];
  w[0] = wp[0]; w[1] = wp[1];
#pragma unroll
  for (int n = 0; n < 6; n++) {
    const int f = n + 2, ff = f < 6 ? f : f - 6;
    const u32 a = (f < 6 || H == 0) ? wc[ff & 3] : (ff ? n1 : n0); const int off = (f < 6 ? H : 1 - H) * WSLOT + (ff >> 2) * 4096;
    MMA(acc[n][0], w[n], t[0]); SB();
    s16x4 lo = rd_tr1(a, off); SB();
    MMA(acc[n][1], w[n], t[1]); SB();
    s16x4 hi = rd_tr1(a, off + 2048); SB();
    MMA(acc[n][2], w[n], t[2]); SB();
    if (n >= 4) { nx[2 * n - 8] = rd128(xn, (2 * n - 8) * 2048); SB(); }
    if (n == 1) { upd(); SB(); }
    MMA(acc[n][3], w[n], t[3]); SB();
    if (n >= 4) { nx[2 * n - 7] = rd128(xn, (2 * n - 7) * 2048); SB(); }
    if (n == 3) { BAR(); SB(); }
    w[f] = cat(lo, hi);
  }
  wp[0] = w[6]; wp[1] = w[7];
#pragma unroll
  for (int i = 0; i < 4; i++) t[i] = nx[i];
}

template <int KIND, bool ZERO = false, class FW, class FT, class FN, class MID>
DI void half(f32x4 (&acc)[3][4], bf16x8 (&w)[3], bf16x8 (&tp)[2], FW&& rw, FT&& rt_, FN&& rn, MID&& mid) {
  bf16x8 t[4], wn[3], tn[2];
  t[0] = tp[0]; t[1] = tp[1];
  s16x4 lo;
#pragma unroll
  for (int i = 0; i < 12; i++) {
    const int T = i / 3, n = i % 3;
    if constexpr (KIND == 2) MMA(acc[n][T], t[T], w[n]); else if constexpr (ZERO) MMA0(acc[n][T], w[n], t[T]); else MMA(acc[n][T], w[n], t[T]);
    SB();
    if constexpr (KIND == 0) {
      if (i == 0) t[2] = rt_(2, 0); if (i == 1) t[3] = rt_(3, 0);
      if (i >= 2 && i <= 7) { if ((i & 1) == 0) lo = rw((i - 2) >> 1, 0); else wn[(i - 2) >> 1] = cat(lo, rw((i - 2) >> 1, 1)); }
      if (i == 8) tn[0] = rn(0, 0); if (i == 9) tn[1] = rn(1, 0);
    } else if constexpr (KIND == 1) {
      if (i <= 3) { if ((i & 1) == 0) lo = rt_(2 + (i >> 1), 0); else t[2 + (i >> 1)] = cat(lo, rt_(2 + (i >> 1), 1)); }
      if (i >= 4 && i <= 6) wn[i - 4] = rw(i - 4, 0);
      if (i >= 7 && i <= 10) { if ((i & 1) == 1) lo = rn((i - 7) >> 1, 0); else tn[(i - 7) >> 1] = cat(lo, rn((i - 7) >> 1, 1)); }
    } else {
      if (i == 0) t[2] = rt_(2, 0); if (i == 2) t[3] = rt_(3, 0);
      if (i == 3 || i == 5 || i == 6) wn[i == 3 ? 0 : i - 4] = rw(i == 3 ? 0 : i - 4, 0);
      if (i == 8) tn[0] = rn(0, 0); if (i == 9) tn[1] = rn(1, 0);
    }
    mid(i);
    SB();
  }
#pragma unroll
  for (int i = 0; i < 3; i++) w[i] = wn[i];
  tp[0] = tn[0]; tp[1] = tn[1];
}

template <int I, int N, class FN> DI void sfor(FN&& f) { if constexpr (I < N) { f(std::integral_constant<int, I>{}); sfor<I + 1, N>(f); } }

DI f32x2 cmul(f32x2 a, f32x2 b) { return (f32x2){a.x * b.x - a.y * b.y, a.x * b.y + a.y * b.x}; }
DI f32x2 cfma(f32x2 a, f32x2 b, f32x2 c) { return (f32x2){__builtin_fmaf(a.x, b.x, __builtin_fmaf(-a.y, b.y, c.x)), __builtin_fmaf(a.x, b.y, __builtin_fmaf(a.y, b.x, c.y))}; }
struct CarryPre { f32x2 l16, l32, l64, L1, L4; };
DI CarryPre carry_pre(int c, u32 lds0) {
  const f32x4 pw = rdf4(lds0 + (18 & 3) * WSLOT + AX_TAB + c * 32, 16);
  CarryPre k; const f32x2 l8 = {pw[0], pw[1]};
  k.l16 = cmul(l8, l8); k.l32 = cmul(k.l16, k.l16); k.l64 = cmul(k.l32, k.l32); k.L1 = (f32x2){pw[2], pw[3]};
  const f32x2 L2 = cmul(k.L1, k.L1); k.L4 = cmul(L2, L2);
  PIN(k.l16); PIN(k.l32); PIN(k.l64); PIN(k.L1); PIN(k.L4);
  return k;
}
DI void ssm_carry(const Params& p, const CarryPre& k, int rt, int j, int c, u32 lds0) {
  const u32 scr = lds0 + TOFF + (22 & 3) * TSLOT + c * 8;
  f32x2 e[8];
#pragma unroll
  for (int s = 0; s < 8; s++) e[s] = *(LAS f32x2*)(size_t)(scr + s * 1536);
  const f32x2 a01 = cfma(k.l16, e[0], e[1]), a23 = cfma(k.l16, e[2], e[3]), a45 = cfma(k.l16, e[4], e[5]), a67 = cfma(k.l16, e[6], e[7]);
  const f32x2 a03 = cfma(k.l32, a01, a23), E = cfma(k.l64, a03, cfma(k.l32, a45, a67));
  const rsrc_t rE = mk(p.ws + WS_EG, 16 * 9216);
  const int vo = (192 * j + c) * 16 + (15 - rt) * 9216;
  st16((u32x4){__float_as_uint(E.x), 1u, __float_as_uint(E.y), 1u}, rE, vo, 0);
  u32x4 va[15], vb[15];
  auto issue = [&](u32x4 (&v)[15]) {
    asm volatile("" ::: "memory");
#pragma unroll
    for (int i = 0; i < 15; i++) v[i] = __builtin_amdgcn_raw_buffer_load_b128(rE, vo, (15 - i) * 9216, 16);
    asm volatile("" ::: "memory");
  };
  f32x2 cy;
  auto eval = [&](u32x4 (&v)[15]) {
    u32 n = 0; f32x2 q[4];
#pragma unroll
    for (int i = 0; i < 15; i++) {
      n += v[i][1] + v[i][3];
      const f32x2 x = {__uint_as_float(v[i][0]), __uint_as_float(v[i][2])};
      const int ch = (i + 1) & 3;
      q[ch] = i < 4 ? x : cfma(k.L4, q[ch], x);
    }
    cy = cfma(k.L1, cfma(k.L1, cfma(k.L1, q[0], q[1]), q[2]), q[3]);
    return __all(n == 2u * rt);
  };
  __builtin_amdgcn_s_sleep(6);
  issue(va);
  __builtin_amdgcn_s_sleep(10);
  issue(vb);
  if (!eval(va)) {
    issue(va);
    if (!eval(vb) && !eval(va)) {
#pragma nounroll
      for (u32 spin = 0; spin < SPIN_CAP; spin++) { u32x4 vc[15]; issue(vc); if (eval(vc)) break; }
    }
  }
#pragma unroll
  for (int s = 0; s < 8; s += 2) e[s] = *(LAS f32x2*)(size_t)(scr + s * 1536);
  const f32x2 c2 = cfma(k.l32, cy, a01), c4 = cfma(k.l64, cy, a03), c6 = cfma(k.l32, c4, a45);
  const f32x2 cin[8] = {cy, cfma(k.l16, cy, e[0]), c2, cfma(k.l16, c2, e[2]), c4, cfma(k.l16, c4, e[4]), c6, cfma(k.l16, c6, e[6])};
#pragma unroll
  for (int s = 0; s < 8; s++) *(LAS f32x2*)(size_t)(scr + s * 1536) = cin[s];
}

#define LBAR() BAR()
DI void loader(u64x4 kv, int rt, int g, int j, int u, int lane, u32 lds0, u32 tag) {
  const bool ssm = g == 4;
  const u32 wbytes = ssm ? D * ROWB : D * F * 2;
  const int r8 = lane >> 3, gp = lane & 7;
  const int cgr = ((((gp >> 1) ^ ((r8 >> 1) & 3)) << 1) | (gp & 1));
  const int rgr = gp ^ (4 * u + (r8 >> 1));
  const int slice = 3 * g + j;
  const int kA = 16 * u + 8 * (r8 >> 2) + (r8 & 3);
  const int R = 8 * u + r8;
  const u32 voffA = ssm ? (192 * j + R) * ROWB + rgr * 16 : kA * (F * 2) + slice * 768 + cgr * 16;
  const int tok = ssm ? 16 * ((R >> 2) & 3) + 8 * (R & 1) + ((R >> 1) & 1) : R;
  const u32 voffX = (rt * 128 + tok) * ROWB + rgr * 16;
  const u32 ldsW = lds0 + u * 1024, ldsT = lds0 + TOFF;
  const u32 aKt = ssm ? 128 : 64 * F * 2, aPar = ssm ? 0 : 32 * F * 2, pstr = ssm ? 32 * ROWB : 128, a2 = ssm ? 16 * ROWB : 4 * F * 2, xstr = (ssm ? 2 : 16) * ROWB;
  const int rot = (rt >= 10 ? rt - 10 : rt) & ~1;
  asm volatile("s_waitcnt lgkmcnt(0)" : "+s"(kv) : "v"(voffA), "v"(voffX), "s"(aKt), "s"(rot));
  const rsrc_t rX = mk((void*)kv[0], S * ROWB), rA0 = mk((void*)(ssm ? kv[2] : kv[1]), wbytes), rA1 = mk((void*)(ssm ? kv[3] : kv[1]), wbytes);
  auto ktile = [&](int kap) { int kt = kap + rot; return kt >= 9 ? kt - 9 : kt; };
  auto issueA = [&](int kap, int h) { dma12(h ? rA1 : rA0, ldsW + ((2 * kap + h) & 3) * WSLOT, voffA, ktile(kap) * aKt + h * aPar, a2, pstr, pstr); };
  auto issueX = [&](int kap, int h) { dma4(rX, ldsT + (kap & 3) * TSLOT + h * 8192 + u * 1024, voffX, h * 64 * ROWB + ktile(kap) * 128, xstr); };
  issueA(0, 0); issueX(0, 0); issueX(0, 1); issueA(0, 1);
  __builtin_amdgcn_s_setprio(3);
  waitv<12>(); BAR();
  issueX(1, 0); issueX(1, 1); issueA(1, 0);
#pragma nounroll
  for (int kap = 0; kap < 7; kap++) {
    u32 l1 = ldsW + ((2 * kap + 3) & 3) * WSLOT, s1 = ktile(kap + 1) * aKt + aPar, l2 = ldsW + ((2 * kap) & 3) * WSLOT, s2 = ktile(kap + 2) * aKt;
    u32 lx = ldsT + ((kap + 2) & 3) * TSLOT + u * 1024, lx2 = lx + 8192, sx = ktile(kap + 2) * 128, sx2 = sx + 64 * ROWB;
    asm volatile("" : "+s"(l1), "+s"(s1), "+s"(l2), "+s"(s2), "+s"(lx), "+s"(lx2), "+s"(sx), "+s"(sx2));
    waitv<16>(); BAR(); dma12(rA1, l1, voffA, s1, a2, pstr, pstr); dma4(rX, lx, voffX, sx, xstr);
    waitv<16>(); BAR(); dma12(rA0, l2, voffA, s2, a2, pstr, pstr); dma4(rX, lx2, voffX, sx2, xstr);
  }
  waitv<16>(); BAR(); issueA(8, 1);
  const Params p = reload_params();
  const u32 voffL = lane * 16;
  const rsrc_t rWS = mk(p.ws, WS_END), rB0 = mk(ssm ? p.cre : p.w2, wbytes), rB1 = mk(ssm ? p.cim : p.w2, wbytes);
  const u32 voffB = ssm ? voffA : R * ROWB + j * 384 + cgr * 16;
  const u32 bJ = ssm ? 384 : 384 * ROWB, bPar = ssm ? 0 : 192 * ROWB, bI = ssm ? 128 : 32 * ROWB, b0 = ssm ? 0 : g * 1152 * ROWB;
  const int j1 = j == 2 ? 0 : j + 1, j2 = j == 0 ? 2 : j - 1;
  const u32 st3 = 96 * ROWB - 2 * pstr, pb = WS_P + (rt * 15 + 3 * g) * 98304 + u * 4096;
  auto issueB = [&](int s, int jj, int i) { dma12((i & 1) ? rB1 : rB0, ldsW + (s & 3) * WSLOT, voffB, b0 + jj * bJ + (i & 1) * bPar + (i >> 1) * bI, 16 * ROWB, pstr, st3); };
  auto issueP = [&](int s, int jj, int i) { dma8lin(rWS, ldsT + (s & 3) * TSLOT + u * 4096, voffL, pb + jj * 98304 + i * 16384); };
  waitv<12>(); BAR();
#pragma unroll
  for (int k = 0; k < 3; k++) dma_sc1(rWS, lds0 + (18 & 3) * WSLOT + AX_TAB + (u * 3 + k) * 1024, voffL, WS_TAB + j * 6144 + (u * 3 + k) * 1024);
  waitv<3>();
  BAR(); issueB(19, j, 0); waitv<12>();
  BAR(); issueB(20, j, 1); waitv<12>();
  if (ssm) { const CarryPre k = carry_pre(lane, lds0); BAR(); if (u == 1) ssm_carry(p, k, rt, j, lane, lds0); WAITL(); }
  BAR();
  issueB(21, j, 2); waitv<12>();
  if (ssm) BAR();
  BAR(); issueB(22, j, 3); waitv<12>();
  BAR(); issueB(23, j, 4); issueP(23, j, 4); waitv<20>();
  BAR(); issueB(24, j, 5); issueP(24, j, 5);
#pragma nounroll
  for (int n = 0; n < 3; n++) {
    const int i = n ? 2 * n - 2 : 4;
    u32 w1 = b0 + j1 * bJ + (i >> 1) * bI, w2 = b0 + j2 * bJ + (i >> 1) * bI, w3 = w1 + bPar, w4 = w2 + bPar;
    u32 p1 = pb + j1 * 98304 + i * 16384, p2 = pb + j2 * 98304 + i * 16384, p3 = p1 + 16384, p4 = p2 + 16384;
    asm volatile("" : "+s"(w1), "+s"(w2), "+s"(w3), "+s"(w4), "+s"(p1), "+s"(p2), "+s"(p3), "+s"(p4));
    waitv<20>(); BAR(); dma12(rB0, ldsW + 1 * WSLOT, voffB, w1, 16 * ROWB, pstr, st3); dma8lin(rWS, ldsT + 1 * TSLOT + u * 4096, voffL, p1);
    waitv<20>(); BAR(); dma12(rB0, ldsW + 2 * WSLOT, voffB, w2, 16 * ROWB, pstr, st3); dma8lin(rWS, ldsT + 2 * TSLOT + u * 4096, voffL, p2);
    waitv<20>(); BAR(); dma12(rB1, ldsW + 3 * WSLOT, voffB, w3, 16 * ROWB, pstr, st3); dma8lin(rWS, ldsT + 3 * TSLOT + u * 4096, voffL, p3);
    waitv<20>(); BAR(); dma12(rB1, ldsW + 0 * WSLOT, voffB, w4, 16 * ROWB, pstr, st3); dma8lin(rWS, ldsT + 0 * TSLOT + u * 4096, voffL, p4);
  }
  waitv<20>();
  BAR();
  {
    const u32 l = lds0 + WSLOT;
    const u32 vo = (lane & 31) < 24 ? (rt * 128 + u * 64 + (g & 3) * 16 + (lane >> 5)) * ROWB + j * 384 + (lane & 31) * 16 : 0x7fffffffu;
#pragma unroll
    for (int k = 0; k < 8; k++) dma(rX, l + (u * 8 + k) * 1024, vo, k * 2 * ROWB);
    dma(mk(u ? p.dskip : p.b2, ROWB), l + 16384 + u * 1024, voffL + j * 384, 0);
  }
  waitv<9>(); BAR(); BAR(); waitv<0>();
  BAR();
}

DI void helper(const Params& p, int rt, int g, int j, int h, int lane, u32 lds0, u32 tag) {
  const bool ssm = g == 4;
  const int slice = 3 * g + j, q = lane >> 4, jl = lane & 15;
  const rsrc_t rWS = mk(p.ws, WS_END);
  char* const hsR = p.ws + WS_RSP + blockIdx.x * 4;
  const u32 hs0 = ld_flag(hsR);
  u32x4 xr[4]; u32x2 xt; rows4_load(p.x, rt * 128 + slice * 8 + 4 * h, lane, xr, xt);
  u32 hsn = 0;
  auto startup = [&]() {
    hsn = __builtin_amdgcn_readfirstlane(hs0) + 1u;
    if (h == 0) st_word(hsR - 1024, hsn); else if (lane < 2) st_flag(p.ws, lane ? FL_P + rt * 15 + slice : FL_O + (rt * 3 + j) * 5 + g, 0u);
    if (ssm) { st16((u32x4){0u, 0u, 0u, 0u}, rWS, (192 * j + 64 * h + lane) * 16, WS_EG + (15 - rt) * 9216); st16((u32x4){0u, 0u, 0u, 0u}, rWS, (192 * j + 128 + lane) * 16, WS_EG + (15 - rt) * 9216); }
  };
  __builtin_amdgcn_s_setprio(2);
  {
    const int e = (lane >> 2) & 3, z = 2 * (q & 1) + (e >> 1);
    f32x4 c = {0.f, 0.f, 0.f, 0.f};
    auto pass = [&](int sl) {
      bf16x8 f[12];
      if (ssm) {
        const u32 a = lds0 + (rt & 1) * 2 * WSLOT + h * 12288 + jl * 128 + ((q ^ (jl >> 1)) * 16);
#pragma unroll
        for (int n = 0; n < 6; n++) { f[n] = rd128(a, sl * WSLOT + n * 2048); f[6 + n] = rd128(a ^ 64, sl * WSLOT + n * 2048); }
      } else {
#pragma unroll
        for (int n = 0; n < 12; n++) f[n] = rd_tr(lds0 + (rt & 1) * 2 * WSLOT + h * 12288 + (4 * q + e) * 128 + (((n & 3) ^ z) * 32) + (lane & 3) * 8, sl * WSLOT + (n >> 2) * 4096);
      }
      const int lo = ssm ? jl - 6 * sl : jl, hi = ssm ? jl - 6 * sl + 6 : jl;
#pragma unroll
      for (int n = 0; n < 12; n++) {
        const u32 o = (n < 6 ? lo : hi) == n ? ONES : 0u; const u32x4 sel = {o, o, o, o};
        asm volatile("s_nop 1\n\tv_mfma_f32_16x16x32_bf16 %0, %1, %2, %0" : "+v"(c) : "v"(f[n]), "v"(sel));
      }
      MFENCE();
    };
    const int odd = rt & 1;
    LBAR();
    if (odd) { LBAR(); startup(); rows4_stats(p.ws, rt * 128 + slice * 8 + 4 * h, lane, xr, xt); LBAR(); }
    if (rt < 9) pass(0);
    LBAR();
    if (!odd) startup();
    if (rt < 9) {
      pass(1);
      const int col = (ssm ? (jl >= 6 ? 192 - 96 : 0) + 96 * h : 192 * h) + 16 * jl + 4 * q;
      st16(__builtin_bit_cast(u32x4, c), rWS, jl < 12 ? col * 4 : 0x7fffffffu, WS_CS + (slice * 16 + rt) * 1536);
    }
    LBAR();
    if (!odd) { rows4_stats(p.ws, rt * 128 + slice * 8 + 4 * h, lane, xr, xt); LBAR(); LBAR(); }
  }
  u32 pv = ld_flag(hsR);
  LBAR();
  waitv<0>(); LBAR();
  if (h == 0) { if (!__all(pv == hsn)) spin_all(hsR, hsn); st_word(hsR + 1024, 1u); }
  LBAR(); LBAR(); LBAR(); LBAR();
  const char* fGo = p.ws + WS_FLAGS + FL_GO * 128;
  pv = ld_flag(fGo);
  LBAR(); LBAR();
  if (!__all(pv == 1u)) spin_all(fGo, 1u);
  const int ir = 64 * h + lane, irt = ssm ? (ir & 64) + 16 * ((ir >> 2) & 3) + 8 * (ir & 1) + 2 * ((ir >> 4) & 3) + ((ir >> 1) & 1) : ir;
  const u32x2 sg = __builtin_amdgcn_raw_buffer_load_b64(rWS, (rt * 128 + irt) * 8, WS_ST, 16);
  const int cc = 192 * h + 4 * (lane < 48 ? lane : lane - 48);
  f32x4 cp[9];
#pragma unroll
  for (int k = 0; k < 9; k++) cp[k] = __builtin_bit_cast(f32x4, __builtin_amdgcn_raw_buffer_load_b128(rWS, cc * 4, WS_CS + (slice * 16 + k) * 1536, 16));
  const u32x2 bb = *(const GAS u32x2*)(p.b1 + (ssm ? 0 : slice * 384) + cc);
  LBAR(); LBAR(); SB();
  f32x4 cs = ((cp[0] + cp[1]) + (cp[2] + cp[3])) + ((cp[4] + cp[5]) + (cp[6] + cp[7])) + cp[8];
  float mean = __uint_as_float(sg[0]), rstd = __builtin_amdgcn_rsqf(__uint_as_float(sg[1])), sig = __uint_as_float(sg[1]) * rstd;
  u32x4 at, aw[4];
  { float m1, m2, m3; split3(-mean, m1, m2, m3); const float g1 = rbf(sig), g2 = sig - g1;
    at = (u32x4){cvtpk(m1, m1), cvtpk(m2, m2), cvtpk(m1, m3), cvtpk(g1, g2)}; mean = ssm ? -mean * rstd : mean; }
  PIN(cs); PIN(at); PIN(mean); PIN(rstd); PIN(sig);
  auto auxw = [&](int k) { float c1, c2, c3; split3(cs[k], c1, c2, c3); const float b = (k & 1) ? bfhi(bb[k >> 1]) : bflo(bb[k >> 1]);
    aw[k] = (u32x4){cvtpk(c1, c2), cvtpk(c1, c2), cvtpk(c3, c1), cvtpk(b, b)}; PIN(aw[k]); };
  const u32 ax = lds0 + (18 & 3) * WSLOT;
  LBAR(); SB();
  if (!ssm) { auxw(0); auxw(1); }
  LBAR(); SB();
  wr128(ax + AX_T + ir * 16, 0, at);
  { LAS float* sp = (LAS float*)(size_t)(ax + AX_S + ir * 4); sp[0] = mean; sp[128] = rstd; sp[256] = sig; }
  if (!ssm) { auxw(2); auxw(3); }
  LBAR(); SB();
  if (ssm) *(LAS f32x4*)(size_t)(ax + AX_W + cc * 4) = cs;
  else {
#pragma unroll
    for (int k = 0; k < 4; k++) wr128(ax + AX_W + cc * 4, k * 1600, aw[k]);
  }
  WAITL(); LBAR();
  if (ssm) { const CarryPre k = carry_pre(64 + 64 * h + lane, lds0); LBAR(); ssm_carry(p, k, rt, j, 64 + 64 * h + lane, lds0); WAITL(); LBAR(); }
  LBAR();
  char* const fMine = p.ws + WS_FLAGS + (FL_P + rt * 15 + slice) * 128;
  const char* fP = p.ws + WS_FLAGS + (FL_P + rt * 15 + 3 * g + ((lane & 1) ? (j == 2 ? 0 : j + 1) : (j == 0 ? 2 : j - 1))) * 128;
  LBAR(); if (h == 1 && !ssm) st_word(fMine, 2u);
  LBAR(); if (h == 1 && ssm) st_word(fMine, 1u);
#pragma unroll
  for (u32 lv = 1; lv < 4; lv++) {
    if (lv == 1) pv = ld_flag(fP);
    LBAR(); if (h == 0 && !__all(pv >= lv)) spin_ge(fP, lv);
    LBAR(); if (h == 1 && lv == 1) st_word(fMine, 2u);
    LBAR(); pv = ld_flag(fP);
    LBAR(); if (h == 1 && lv == 1) st_word(fMine, 3u);
  }
  LBAR(); LBAR(); LBAR(); LBAR();
  LBAR();
  if (h == 1) st_flag(p.ws, FL_O + (rt * 3 + j) * 5 + g, tag);
  else if (!ssm) {
    const int k = lane & 3;
    const char* fa = p.ws + WS_FLAGS + (FL_O + (rt * 3 + j) * 5 + (k < g ? k : k + 1)) * 128;
    u32 v = 0;
#pragma nounroll
    for (u32 i = 0; i < SPIN_CAP; i++) { v = ld_flag(fa); if (__all(v == tag || k == 3)) break; }
    const bool yok = __all(v == tag);
    *(LAS u32*)(size_t)lds0 = yok; WAITL(); BAR();
    if (!yok) { spin_all(fa, tag); BAR(); }
  }
}

template <bool SSM>
DI void phaseB(const Params& p, int rt, int g, int j, int w, int lane, u32 lds0, u32 tag) {
  asm volatile("" : "+v"(lane));
  const int rg = w >> 2, cg = w & 3, q = lane >> 4, e = (lane >> 2) & 3, pp = lane & 3, jl = lane & 15;
  const int z = 2 * (q & 1) + (e >> 1);
  u32 wlo[3], whi[3], tb[2];
#pragma unroll
  for (int n = 0; n < 3; n++) {
    const int t = 3 * cg + n;
    wlo[n] = SSM ? lds0 + (16 * t + jl) * 128 + ((q ^ (jl >> 1)) * 16) : lds0 + (t >> 2) * 4096 + (4 * q + e) * 128 + (((t & 3) ^ z) * 32) + pp * 8;
    whi[n] = SSM ? (wlo[n] ^ 64) : wlo[n] + 2 * WSLOT;
  }
  tb[0] = SSM ? lds0 + TOFF + rg * 8192 + (q >> 1) * 2048 + (pp >> 1) * 1024 + (((8 * (q & 1) + e) * 16) ^ ((pp >> 1) * 64)) + 8 * (pp & 1) : lds0 + TOFF + rg * 4096 + lane * 16;
  tb[1] = tb[0] ^ 64;
  float mean = 0.f, rstd = 0.f;
  if constexpr (!SSM) { const u32 sa = lds0 + (18 & 3) * WSLOT + AX_S + (64 * rg + 16 * g + jl) * 4; mean = rdf(sa, 0); rstd = rdf(sa, 512); }
  f32x4 ob[3][4];
  auto RW = [&](int sl, int kb, int n, int part) {
    if constexpr (SSM) return rd128((kb ? whi[n] : wlo[n]) + (sl >> 1) * 2 * WSLOT, (sl & 1) * WSLOT);
    else return rd_tr1(sl < 2 ? wlo[n] : whi[n], (sl & 1) * WSLOT + kb * 12288 + part * 2048);
  };
  auto RT = [&](int sl, int kb, int T, int part) {
    if constexpr (SSM) return rd_tr1(tb[part], sl * TSLOT + kb * 4096 + T * 256);
    else return rd128(tb[0], sl * TSLOT + kb * 8192 + T * 1024);
  };
  bf16x8 wf[3], tp[2];
  auto step = [&](auto slc, auto&& mid) {
    constexpr int SL = decltype(slc)::value & 3, NS = (SL + 1) & 3;
    half<SSM ? 1 : 0, (decltype(slc)::value > 3)>(ob, wf, tp, [&](int n, int part) { return RW(SL, 1, n, part); }, [&](int T, int part) { return RT(SL, 0, T, part); }, [&](int T, int part) { return RT(SL, 1, T, part); }, mid);
    half<SSM ? 1 : 0>(ob, wf, tp, [&](int n, int part) { return RW(NS, 0, n, part); }, [&](int T, int part) { return RT(SL, 1, T, part); }, [&](int T, int part) { return RT(NS, 0, T, part); }, [](int) {});
  };
  using I0 = std::integral_constant<int, 0>; using I1 = std::integral_constant<int, 1>; using I2 = std::integral_constant<int, 2>; using I3 = std::integral_constant<int, 3>;
  if constexpr (!SSM) waitv<4>();
  __builtin_amdgcn_s_waitcnt(0xC07F); WAITL(); BAR();
#pragma unroll
  for (int n = 0; n < 3; n++) { if constexpr (SSM) wf[n] = RW(3, 0, n, 0); else wf[n] = cat(RW(3, 0, n, 0), RW(3, 0, n, 1)); }
#pragma unroll
  for (int T = 0; T < 2; T++) { if constexpr (SSM) tp[T] = cat(RT(3, 0, T, 0), RT(3, 0, T, 1)); else tp[T] = RT(3, 0, T, 0); }
  auto barm = [](int i) { if (i == 11) BAR(); };
  step(std::integral_constant<int, 7>{}, [](int) {});
  step(I0{}, [&](int i) { if (i == 3) { if constexpr (SSM) waitv<8>(); BAR(); } });
#pragma nounroll
  for (int it = 0; it < 4; it++) { step(I1{}, barm); step(I2{}, [&](int i) { if (i == 11) { waitv<4>(); BAR(); } }); step(I3{}, barm); step(I0{}, [&](int i) { if (i == 11) { waitv<0>(); BAR(); } }); }
  MFENCE();
  const rsrc_t rWS = mk(p.ws, WS_END);
  const u32 xb = WS_XB + (rt * 3 + j) * 32 * XBW + w * XBW;
  f32x4 mine[3];
#pragma unroll
  for (int T = 0; T < 4; T++) {
    if (SSM || T != g) {
      const u32 o = xb + T * 8 * XBW + g * 1536;
      const u32x4 a = {cvtpk(ob[0][T][0], ob[0][T][1]), cvtpk(ob[0][T][2], ob[0][T][3]), cvtpk(ob[1][T][0], ob[1][T][1]), cvtpk(ob[1][T][2], ob[1][T][3])};
      const u32 b0 = cvtpk(ob[2][T][0], ob[2][T][1]), b1 = cvtpk(ob[2][T][2], ob[2][T][3]);
      const auto s0 = __builtin_amdgcn_permlane32_swap(b0, b0, false, false), s1 = __builtin_amdgcn_permlane32_swap(b1, b1, false, false);
      st16(a, rWS, lane * 16, o);
      if (lane < 32) st16((u32x4){b0, b1, s0[1], s1[1]}, rWS, lane * 16, o + 1024);
    } else {
#pragma unroll
      for (int n = 0; n < 3; n++) mine[n] = ob[n][T];
    }
  }
  if constexpr (SSM) {
    waitv<0>(); BAR();
    STAMP(7);
  } else {
    waitv<0>(); BAR();
    const int tok = rt * 128 + 64 * rg + 16 * g + jl, col = 192 * j + 48 * cg + 4 * q;
    u32x2 xv[3], bv[3], dv[3];
#pragma unroll
    for (int n = 0; n < 3; n++) {
      const u32 cb = lds0 + WSLOT + (48 * cg + 4 * q + 16 * n) * 2;
      xv[n] = *(LAS u32x2*)(size_t)(cb + (16 * rg + jl) * 512); bv[n] = *(LAS u32x2*)(size_t)(cb + 16384); dv[n] = *(LAS u32x2*)(size_t)(cb + 17408);
    }
    const u32 o = xb + g * 8 * XBW;
    u32x4 a[3]; u32x2 b[3];
    BAR();
#pragma unroll
    for (int k = 0; k < 3; k++) {
      const int src = k < g ? k : k + 1;
      a[k] = __builtin_amdgcn_raw_buffer_load_b128(rWS, lane * 16, o + src * 1536, 16);
      b[k] = __builtin_amdgcn_raw_buffer_load_b64(rWS, (lane & 31) * 16 + (lane >> 5) * 8, o + src * 1536 + 1024, 16);
    }
    const bool yok = __builtin_amdgcn_readfirstlane(*(LAS u32*)(size_t)lds0);
    u32x4 ya; u32x2 yb;
    auto ldy = [&]() { ya = __builtin_amdgcn_raw_buffer_load_b128(rWS, lane * 16, o + 4 * 1536, 16); yb = __builtin_amdgcn_raw_buffer_load_b64(rWS, (lane & 31) * 16 + (lane >> 5) * 8, o + 4 * 1536 + 1024, 16); };
    if (yok) ldy();
    f32x4 gate[3], base[3];
#pragma unroll
    for (int n = 0; n < 3; n++) {
#pragma unroll
      for (int c = 0; c < 4; c++) {
        float G = mine[n][c] + ((c & 1) ? bfhi(bv[n][c >> 1]) : bflo(bv[n][c >> 1]));
#pragma unroll
        for (int k = 0; k < 3; k++) { const u32 v = n < 2 ? a[k][2 * n + (c >> 1)] : b[k][c >> 1]; G += (c & 1) ? bfhi(v) : bflo(v); }
        const float xx = (c & 1) ? bfhi(xv[n][c >> 1]) : bflo(xv[n][c >> 1]);
        const float dd = (c & 1) ? bfhi(dv[n][c >> 1]) : bflo(dv[n][c >> 1]);
        gate[n][c] = G * __builtin_amdgcn_rcpf(1.f + __builtin_amdgcn_exp2f(-1.4426950408889634f * G));
        base[n][c] = xx + (xx - mean) * rstd * dd * gate[n][c];
      }
    }
#pragma unroll
    for (int n = 0; n < 3; n++) { PIN(gate[n]); PIN(base[n]); }
    if (!yok) { BAR(); ldy(); }
#pragma unroll
    for (int n = 0; n < 3; n++) {
      const u32 y0 = n < 2 ? ya[2 * n] : yb[0], y1 = n < 2 ? ya[2 * n + 1] : yb[1];
      *(GAS f32x4*)(p.out + tok * D + col + 16 * n) = base[n] + (f32x4){bflo(y0), bfhi(y0), bflo(y1), bfhi(y1)} * gate[n];
    }
    STAMP(7);
  }
}

DI void ffn_compute(int rt, int g, int j, int w, int lane, u32 lds0, u32 tag) {
  const int rg = w >> 2, cg = w & 3, q = lane >> 4, e = (lane >> 2) & 3, pp = lane & 3, jl = lane & 15;
  const int z = 2 * (q & 1) + (e >> 1);
  u32 wc[4];
#pragma unroll
  for (int n = 0; n < 4; n++) { const int t = 6 * cg + n; wc[n] = lds0 + (t >> 2) * 4096 + (4 * q + e) * 128 + (((t & 3) ^ z) * 32) + pp * 8; }
  const u32 xbase = lds0 + TOFF + (64 * rg + jl) * 128 + ((q ^ (jl >> 1)) * 16);
  f32x4 acc[6][4];
#pragma unroll
  for (int n = 0; n < 6; n++)
#pragma unroll
    for (int T = 0; T < 4; T++) { acc[n][T] = (f32x4){0.f, 0.f, 0.f, 0.f}; asm volatile("" : "+v"(acc[n][T])); }
  bf16x8 wp[2], t[4];
  BAR();
  wp[0] = rd_tr(wc[0], 0); wp[1] = rd_tr(wc[1], 0);
#pragma unroll
  for (int T = 0; T < 4; T++) t[T] = rd128(xbase, T * 2048);
  u32 xs = 0, xn = 0, n0 = 0, n1 = 0; int dw = 2 * WSLOT;
#pragma nounroll
  for (int it = 0; it < 9; it++) {
    stepA<0>(acc, wp, t, wc, n0, n1, xn, [&]() { xn = (xbase ^ 64) + xs; PIN(xn); xs = (xs + TSLOT) & (4 * TSLOT - 1); });
    stepA<1>(acc, wp, t, wc, n0, n1, xn, [&]() { xn = xbase + xs; n0 = wc[0] + dw; n1 = wc[1] + dw; wc[2] += dw; wc[3] += dw; PIN(xn); PIN(n0); PIN(n1); PIN(wc[2]); PIN(wc[3]); });
    wc[0] = n0; wc[1] = n1; dw = -dw;
  }
  const Params p = reload_params();
  const u32 ax = lds0 + (18 & 3) * WSLOT;
  float k1[4], k2[4], sg[4];
#pragma unroll
  for (int T = 0; T < 4; T++) {
    u32x4 v = __builtin_bit_cast(u32x4, rd128(ax + AX_T + (64 * rg + jl) * 16, T * 256));
    if (q != 0) v = (u32x4){0u, 0u, 0u, 0u};
    t[T] = __builtin_bit_cast(bf16x8, v);
    const float r = rdf(ax + AX_S + 512 + (64 * rg + jl) * 4, T * 64);
    sg[T] = rdf(ax + AX_S + 1024 + (64 * rg + jl) * 4, T * 64);
    k1[T] = -2.3022081985f * r; k2[T] = -0.10294324f * r * r * r;
  }
  {
    bf16x8 a[6];
#pragma unroll
    for (int n = 0; n < 6; n++) a[n] = rd128(ax + AX_W + ((jl & 3) * 100 + 24 * cg + (jl >> 2)) * 16, n * 64);
    asm volatile("s_nop 1" : "+v"(t[0]), "+v"(t[1]), "+v"(t[2]), "+v"(t[3]));
#pragma unroll
    for (int n = 0; n < 6; n++)
#pragma unroll
      for (int T = 0; T < 4; T++) MMA(acc[n][T], a[n], t[T]);
    MFENCE();
  }
  const rsrc_t rWS = mk(p.ws, WS_END);
  const u32 pg = WS_P + (rt * 15 + 3 * g + j) * 98304 + (cg >> 1) * 16384 + (cg & 1) * 8192 + rg * 4096;
  const u32 pl = lds0 + TOFF + (cg & 1) * 8192 + rg * 4096 + lane * 16, pl0 = pl + ((19 + (cg >> 1)) & 3) * TSLOT, pl1 = pl + ((21 + (cg >> 1)) & 3) * TSLOT;
#pragma unroll
  for (int bb = 0; bb < 3; bb++) {
    const int b = (bb + 2) % 3;
#pragma unroll
    for (int T = 0; T < 4; T++) {
      const u32x4 P = gelu_frag(acc[2 * b][T], acc[2 * b + 1][T], k1[T], k2[T], sg[T]);
      st16(P, rWS, lane * 16, pg + b * 32768 + T * 1024);
      if (b < 2) wr128(b ? pl1 : pl0, T * 1024, P);
      SB();
    }
    if (bb == 0) BAR();
  }
  phaseB<false>(p, rt, g, j, w, lane, lds0, tag);
}

DI void ssm_compute(int rt, int j, int w, int lane, u32 lds0, u32 tag) {
  const int rg = w >> 2, cg = w & 3; int q = lane >> 4, jl = lane & 15;
  u32 wb = lds0 + (16 * cg + jl) * 128 + ((q ^ (jl >> 1)) * 16);
  const u32 xb0 = lds0 + TOFF + (64 * rg + jl) * 128 + ((q ^ (jl >> 1)) * 16);
  f32x4 ar[3][4], ai[3][4];
#pragma unroll
  for (int n = 0; n < 3; n++)
#pragma unroll
    for (int T = 0; T < 4; T++) { ar[n][T] = (f32x4){0.f, 0.f, 0.f, 0.f}; ai[n][T] = (f32x4){0.f, 0.f, 0.f, 0.f}; asm volatile("" : "+v"(ar[n][T]), "+v"(ai[n][T])); }
  bf16x8 wf[3], tp[2];
  auto barm = [](int i) { if (i == 11) BAR(); };
  __builtin_amdgcn_s_waitcnt(0xC07F);
  BAR();
#pragma unroll
  for (int n = 0; n < 3; n++) wf[n] = rd128(wb, n * 8192);
  tp[0] = rd128(xb0, 0); tp[1] = rd128(xb0, 2048);
  u32 xs = 0, x0 = xb0, x1 = xb0 ^ 64, wb1 = wb ^ 64; int dw = 2 * WSLOT;
#pragma nounroll
  for (int it = 0; it < 9; it++) {
    half<2>(ar, wf, tp, [&](int n, int) { return rd128(wb1, n * 8192); }, [&](int T, int) { return rd128(x0, T * 2048); }, [&](int T, int) { return rd128(x1, T * 2048); }, barm);
    half<2>(ar, wf, tp, [&](int n, int) { return rd128(wb, WSLOT + n * 8192); }, [&](int T, int) { return rd128(x1, T * 2048); }, [&](int T, int) { return rd128(x0, T * 2048); },
            [&](int i) { if (i == 10) { wb += dw; PIN(wb); xs = (xs + TSLOT) & (4 * TSLOT - 1); } });
    half<2>(ai, wf, tp, [&](int n, int) { return rd128(wb1, WSLOT + n * 8192); }, [&](int T, int) { return rd128(x0, T * 2048); }, [&](int T, int) { return rd128(x1, T * 2048); },
            [&](int i) { if (i == 7) { wb1 += dw; x0 = xb0 + xs; PIN(wb1); PIN(x0); dw = -dw; } if (i == 11) BAR(); });
    half<2>(ai, wf, tp, [&](int n, int) { return rd128(wb, n * 8192); }, [&](int T, int) { return rd128(x1, T * 2048); }, [&](int T, int) { return rd128(x0, T * 2048); },
            [&](int i) { if (i == 4) { x1 = x0 ^ 64; PIN(x1); } });
  }
  const Params p = reload_params();
  asm volatile("" : "+v"(lane));
  q = lane >> 4; jl = lane & 15;
  const u32 ax = lds0 + (18 & 3) * WSLOT;
  MFENCE();
  f32x2 lc[3];
  const u32 tb = ax + AX_TAB + (16 * cg + jl) * 32;
  const u32 scr = lds0 + TOFF + (22 & 3) * TSLOT + ((4 * rg + q) * 192 + 16 * cg + jl) * 8;
  f32x2 e0[3]; float csr[3], csi[3];
#pragma unroll
  for (int b = 0; b < 3; b++) { csr[b] = rdf(ax + AX_W + (16 * cg + jl) * 4, b * 256); csi[b] = rdf(ax + AX_W + (16 * cg + jl) * 4, 768 + b * 256); }
#pragma unroll
  for (int T = 0; T < 4; T++) {
    const f32x4 r = rdf4(ax + AX_S + 512 + (64 * rg + 4 * q) * 4, T * 64), nm = rdf4(ax + AX_S + (64 * rg + 4 * q) * 4, T * 64);
#pragma unroll
    for (int b = 0; b < 3; b++) { ar[b][T] = ar[b][T] * r + nm * csr[b]; ai[b][T] = ai[b][T] * r + nm * csi[b]; }
  }
  f32x2 l8[3];
#pragma unroll
  for (int b = 0; b < 3; b++) {
    lc[b] = *(LAS f32x2*)(size_t)(tb + b * 2048);
    l8[b] = *(LAS f32x2*)(size_t)(tb + b * 2048 + 16);
    f32x2 hr = {ar[b][0][0], ar[b][0][1]}, hi = {ai[b][0][0], ai[b][0][1]};
#pragma unroll
    for (int k = 1; k < 8; k++) {
      const f32x2 ur = {ar[b][k >> 1][(k & 1) * 2], ar[b][k >> 1][(k & 1) * 2 + 1]}, ui = {ai[b][k >> 1][(k & 1) * 2], ai[b][k >> 1][(k & 1) * 2 + 1]};
      const f32x2 nr = lc[b][0] * hr + (ur - lc[b][1] * hi), ni = lc[b][0] * hi + (ui + lc[b][1] * hr);
      hr = nr; hi = ni;
    }
    e0[b] = (f32x2){hr.x, hi.x};
    *(LAS f32x2*)(size_t)(scr + b * 512) = (f32x2){l8[b].x * hr.x - l8[b].y * hi.x + hr.y, l8[b].x * hi.x + l8[b].y * hr.x + hi.y};
  }
  WAITL(); BAR(); SB();
#pragma unroll
  for (int b = 0; b < 3; b++) {
    const f32x2 cf = *(LAS f32x2*)(size_t)(tb + b * 2048 + 8); const float c2r = cf.x, c2i = cf.y;
#pragma unroll
    for (int T = 0; T < 4; T++) {
      const f32x4 nr = c2r * ar[b][T] - c2i * ai[b][T], ni = -c2r * ai[b][T] - c2i * ar[b][T];
      ar[b][T] = nr; ai[b][T] = ni;
      asm volatile("" : "+v"(ar[b][T]), "+v"(ai[b][T]));
    }
  }
  BAR();
  const rsrc_t rWS = mk(p.ws, WS_END);
  const u32 io0 = w * 2048 + lane * 16, io1 = w * 2048 + 1024 + ((lane * 16) ^ 64);
  const u32 pg = WS_P + (rt * 15 + 12 + j) * 98304;
  f32x2 cins[3];
#pragma unroll
  for (int b = 0; b < 3; b++) { cins[b] = *(LAS f32x2*)(size_t)(scr + b * 512); l8[b] = *(LAS f32x2*)(size_t)(tb + b * 2048 + 16); }
#pragma unroll
  for (int bb = 0; bb < 3; bb++) {
    const int b = (bb + 2) % 3;
    u32x4 H[2][2];
    const f32x2 cin = cins[b];
    const f32x2 sr = {cin.x, l8[b].x * cin.x - l8[b].y * cin.y + e0[b].x}, si = {cin.y, l8[b].x * cin.y + l8[b].y * cin.x + e0[b].y};
    const f32x2 cf = *(LAS f32x2*)(size_t)(tb + b * 2048 + 8); const float c2r = cf.x, c2i = cf.y;
    f32x2 hr = c2r * sr - c2i * si, hi = -c2r * si - c2i * sr;
#pragma unroll
    for (int T = 0; T < 4; T++) {
      f32x2 pr[2], pi[2];
#pragma unroll
      for (int k = 0; k < 2; k++) {
        const f32x2 ur = {ar[b][T][2 * k], ar[b][T][2 * k + 1]}, ui = {ai[b][T][2 * k], ai[b][T][2 * k + 1]};
        const f32x2 nr = lc[b][0] * hr + (ur + lc[b][1] * hi), ni = lc[b][0] * hi + (ui - lc[b][1] * hr);
        hr = nr; hi = ni; pr[k] = nr; pi[k] = ni;
      }
      H[0][0][T] = cvtpk(pr[0].x, pr[1].x); H[0][1][T] = cvtpk(pr[0].y, pr[1].y);
      H[1][0][T] = cvtpk(pi[0].x, pi[1].x); H[1][1][T] = cvtpk(pi[0].y, pi[1].y);
    }
    if (bb == 1) BAR();
#pragma unroll
    for (int k = 0; k < 4; k++) {
      st16(H[k >> 1][k & 1], rWS, (k & 1) ? io1 : io0, pg + (2 * b + (k >> 1)) * 16384);
      if (b < 2) wr128(lds0 + TOFF + ((k & 1) ? io1 : io0), ((19 + 2 * b + (k >> 1)) & 3) * TSLOT, H[k >> 1][k & 1]);
    }
    SB();
  }
  phaseB<true>(p, rt, 4, j, w, lane, lds0, tag);
}

DI void spare(const Params& p, int sid, u32 lds0, u32 tag) {
  const int tid = threadIdx.x;
  const rsrc_t rWS = mk(p.ws, WS_END);
  const int w = __builtin_amdgcn_readfirstlane(tid >> 6), lane = tid & 63;
  if (sid == 15 && w == 11) { st_flag(p.ws, FL_GO, 0u); st16((u32x4){0u, 0u, 0u, 0u}, rWS, lane * 16, WS_ACK); waitv<0>(); hs_leader(p.ws, lane, lds0, STAMPPTR); return; }
  u32 hsn = 0;
  if (w == 11) { hsn = ld_flag(p.ws + WS_RSP + blockIdx.x * 4) + 1u; st_word(p.ws + WS_REQ + blockIdx.x * 4, hsn); }
  else if (w == 10 || w == 8) { u32x4 v[4]; u32x2 t; rows4_load(p.x, sid * 128 + 120 + 2 * (w - 8), lane, v, t); rows4_stats(p.ws, sid * 128 + 120 + 2 * (w - 8), lane, v, t);
  } else if (w == 9 && sid < 9) {
    const int c = sid * 64 + lane;
    const float lr = bf2f(p.lre[c]), li = bf2f(p.lim[c]);
    const float dt = rbf(expf(bf2f(p.logdt[c])));
    const float ex = expf(dt * lr), an = dt * li;
    const float br = ex * cosf(an), bi = ex * sinf(an);
    const float nr = br - 1.f, den = 1.f / (lr * lr + li * li);
    const float cr = (nr * lr + bi * li) * den, ci = (bi * lr - nr * li) * den;
    float ar = br, ai = bi, r8 = 0.f, i8 = 0.f;
#pragma unroll
    for (int k = 0; k < 7; k++) { const float t = ar * ar - ai * ai; ai = 2.f * ar * ai; ar = t; if (k == 2) { r8 = ar; i8 = ai; } }
    st16(__builtin_bit_cast(u32x4, (f32x4){br, bi, 2.f * cr, 2.f * ci}), rWS, c * 32, WS_TAB);
    st16(__builtin_bit_cast(u32x4, (f32x4){r8, i8, ar, ai}), rWS, c * 32 + 16, WS_TAB);
  }
  if (w == 11) spin_all(p.ws + WS_RSP + blockIdx.x * 4, hsn);
  waitv<0>(); BAR();
  if (sid == 15) return;
  if (w == 11) st_word(p.ws + WS_ACK + blockIdx.x * 4, 1u);
  {
    u32 acc = 0;
    const GAS u32* w2 = (const GAS u32*)p.w2;
#pragma unroll
    for (int i = 0; i < 4; i++) { const int L = (i * 15 + sid) * 768 + tid; if (L < 41472) acc ^= w2[L * 32]; }
    { const int L = sid * 768 + tid; if (L < 5184) acc ^= ((const GAS u32*)p.cre)[L * 32]; else if (L < 10368) acc ^= ((const GAS u32*)p.cim)[(L - 5184) * 32]; }
    asm volatile("" :: "v"(acc));
  }
}

__global__ __launch_bounds__(768, 3) void fused_kernel(Params p) {
  extern __shared__ __attribute__((aligned(16))) char smem[];
  u64x4 kv; asm volatile("s_load_dwordx8 %0, %1, 0x0" : "=s"(kv) : "s"(__builtin_amdgcn_kernarg_segment_ptr())); SB();
  const u32 lds0 = (u32)(size_t)(LAS char*)smem;
  const int b = blockIdx.x, xcd = b & 7, idx = b >> 3;
  const u32 tag = 1u;
  if (b < 32 && (threadIdx.x >> 7) == 1) {
    u64 pc0; asm volatile("s_getpc_b64 %0" : "=s"(pc0));
    const u32 ln = min((b >> 3) * 128 + (threadIdx.x & 127), (u32)CODE_LINES);
    asm volatile("s_mov_b32 m0, %0\n\ts_nop 0\n\tglobal_load_lds_dword %1, %2" :: "s"(lds0 + TOFF + 3 * TSLOT), "v"(ln * 128), "s"(pc0) : "memory");
  }
  STAMP(0);
  const int tri = idx / 3, j = idx - 3 * tri;
  const int g = tri < 2 ? 4 : xcd >> 1, rt = 8 * (xcd & 1) + (tri < 2 ? (xcd & 6) + tri : tri - 2);
  const int lane = threadIdx.x & 63, w = __builtin_amdgcn_readfirstlane(threadIdx.x >> 6);
  const int sid = xcd * 2 + idx - 30;
  if (__builtin_expect(idx < 30 && w < 2, 1)) { loader(kv, rt, g, j, w, lane, lds0, tag); return; }
  asm volatile("s_waitcnt lgkmcnt(0)" :: "s"(kv));
  if (idx < 30 && w >= 4) {
    if (g < 4) ffn_compute(rt, g, j, w - 4, lane, lds0, tag); else ssm_compute(rt, j, w - 4, lane, lds0, tag);
    return;
  }
  const Params q = reload_params();
  if (idx < 30) helper(q, rt, g, j, w - 2, lane, lds0, tag); else { spare(q, sid, lds0, tag); STAMP(7); }
}

extern "C" void kernel_launch(void* const* d_in, const int* in_sizes, int n_in, void* d_out, int out_size, void* d_ws, size_t ws_size, hipStream_t stream) {
  Params p;
  p.x = (const u16*)d_in[0]; p.lre = (const u16*)d_in[1]; p.lim = (const u16*)d_in[2]; p.bre = (const u16*)d_in[3]; p.bim = (const u16*)d_in[4];
  p.cre = (const u16*)d_in[5]; p.cim = (const u16*)d_in[6]; p.logdt = (const u16*)d_in[7]; p.dskip = (const u16*)d_in[8];
  p.w1 = (const u16*)d_in[10]; p.b1 = (const u16*)d_in[11]; p.w2 = (const u16*)d_in[12]; p.b2 = (const u16*)d_in[13];
  p.out = (float*)d_out; p.ws = (char*)d_ws + ((ws_size - WS_END) & ~(size_t)0x1fffff);
  (void)hipFuncSetAttribute((const void*)fused_kernel, hipFuncAttributeMaxDynamicSharedMemorySize, LDS_BYTES);
  fused_kernel<<<256, 768, LDS_BYTES, stream>>>(p);
}
```

```cpp
#include <hip/hip_runtime.h>
#include <utility>

#define LAS __attribute__((address_space(3)))
#define GAS __attribute__((address_space(1)))
#define DI __device__ __forceinline__
typedef unsigned short u16;
typedef unsigned u32;
typedef unsigned long long u64;
typedef unsigned u32x4 __attribute__((ext_vector_type(4)));
typedef unsigned u32x2 __attribute__((ext_vector_type(2)));
typedef short s16x4 __attribute__((ext_vector_type(4)));
typedef short bf16x8 __attribute__((ext_vector_type(8)));
typedef float f32x4 __attribute__((ext_vector_type(4)));
typedef float f32x2 __attribute__((ext_vector_type(2)));
typedef __bf16 bf16x2 __attribute__((ext_vector_type(2)));
typedef __amdgpu_buffer_rsrc_t rsrc_t;
typedef u64 u64x4 __attribute__((ext_vector_type(4)));

constexpr int S = 2048, D = 576, F = 4608;
constexpr int ROWB = D * 2;
constexpr int WSLOT = 24576, TOFF = 4 * WSLOT, TSLOT = 16384, LDS_BYTES = 163840;
constexpr int AX_W = 0, AX_T = 6656, AX_S = 8704, AX_TAB = 10752;
constexpr u32 WS_FLAGS = 0;
constexpr u32 FL_GO = 0, FL_P = 32, FL_O = 32 + 240, NFLAG = 32 + 480;
constexpr u32 WS_EG = NFLAG * 128;
constexpr u32 WS_REQ = WS_EG + 16 * 576 * 16, WS_RSP = WS_REQ + 1024, WS_ACK = WS_RSP + 1024;
constexpr u32 WS_CS = 256 * 1024;
constexpr u32 WS_LN = WS_CS + 15 * 16 * 1536;
constexpr u32 WS_TAB = WS_LN + 16 * 5120;
constexpr u32 WS_P = 1u << 20;
constexpr u32 XBW = 5 * 1536;
constexpr u32 WS_XB = WS_P + 240 * 98304;
constexpr u32 WS_END = WS_XB + 48 * 32 * XBW;
constexpr int CODE_LINES = 440;
constexpr u32 SPIN_CAP = 1u << 18;

struct Params {
  const u16 *x, *w1, *bre, *bim;
  const u16 *lre, *lim, *cre, *cim, *logdt, *dskip, *b1, *w2, *b2;
  float* out; char* ws;
};

DI Params reload_params() {
  Params q;
#if defined(__HIP_DEVICE_COMPILE__)
  typedef __attribute__((address_space(4))) const Params* KP;
  KP kp = (KP)__builtin_amdgcn_kernarg_segment_ptr(); asm volatile("" : "+s"(kp)); q = *kp;
#endif
  return q;
}
template <int N> DI void waitv() { asm volatile("s_waitcnt vmcnt(%0)" :: "n"(N) : "memory"); }
#define WAITL() asm volatile("s_waitcnt lgkmcnt(0)" ::: "memory")
#define BAR() do { asm volatile("" ::: "memory"); __builtin_amdgcn_s_barrier(); asm volatile("" ::: "memory"); } while (0)
#define SB() __builtin_amdgcn_sched_barrier(0)
#define MFENCE() do { SB(); asm volatile("s_nop 7\n\ts_nop 7"); SB(); } while (0)
#define MMA(c, a, b) asm volatile("v_mfma_f32_16x16x32_bf16 %0, %1, %2, %0" : "+v"(c) : "v"(a), "v"(b))
#define MMA0(c, a, b) asm volatile("v_mfma_f32_16x16x32_bf16 %0, %1, %2, 0" : "=&v"(c) : "v"(a), "v"(b))
#define MMAN(c, a, b) asm volatile("s_nop 1\n\tv_mfma_f32_16x16x32_bf16 %0, %1, %2, %0" : "+v"(c) : "v"(a), "v"(b))
#define PIN(x) asm volatile("" : "+v"(x))
#define STAMPPTR nullptr
#define STAMP(i)

DI void dma(rsrc_t r, u32 lds, u32 voff, u32 soff) {
  asm volatile("s_mov_b32 m0, %0\n\ts_nop 0\n\tbuffer_load_dwordx4 %1, %2, %3 offen lds" :: "s"(lds), "v"(voff), "s"(r), "s"(soff) : "memory");
}
DI void dma_sc1(rsrc_t r, u32 lds, u32 voff, u32 soff) {
  asm volatile("s_mov_b32 m0, %0\n\ts_nop 0\n\tbuffer_load_dwordx4 %1, %2, %3 offen sc1 lds" :: "s"(lds), "v"(voff), "s"(r), "s"(soff) : "memory");
}
DI void dma12(rsrc_t r, u32 lds, u32 voff, u32 so, u32 d2, u32 st, u32 st3) {
  u32 t;
  asm volatile("s_mov_b32 m0, %2\n\t"
               "s_add_u32 %1, %0, %5\n\t"
               "buffer_load_dwordx4 %3, %4, %0 offen lds\n\t"
               "s_add_u32 m0, m0, 0x800\n\t"
               "s_add_u32 %0, %0, %6\n\t"
               "buffer_load_dwordx4 %3, %4, %1 offen lds\n\t"
               "s_add_u32 m0, m0, 0x800\n\t"
               "s_add_u32 %1, %1, %6\n\t"
               "buffer_load_dwordx4 %3, %4, %0 offen lds\n\t"
               "s_add_u32 m0, m0, 0x800\n\t"
               "s_add_u32 %0, %0, %6\n\t"
               "buffer_load_dwordx4 %3, %4, %1 offen lds\n\t"
               "s_add_u32 m0, m0, 0x800\n\t"
               "s_add_u32 %1, %1, %6\n\t"
               "buffer_load_dwordx4 %3, %4, %0 offen lds\n\t"
               "s_add_u32 m0, m0, 0x800\n\t"
               "s_add_u32 %0, %0, %7\n\t"
               "buffer_load_dwordx4 %3, %4, %1 offen lds\n\t"
               "s_add_u32 m0, m0, 0x800\n\t"
               "s_add_u32 %1, %1, %7\n\t"
               "buffer_load_dwordx4 %3, %4, %0 offen lds\n\t"
               "s_add_u32 m0, m0, 0x800\n\t"
               "s_add_u32 %0, %0, %6\n\t"
               "buffer_load_dwordx4 %3, %4, %1 offen lds\n\t"
               "s_add_u32 m0, m0, 0x800\n\t"
               "s_add_u32 %1, %1, %6\n\t"
               "buffer_load_dwordx4 %3, %4, %0 offen lds\n\t"
               "s_add_u32 m0, m0, 0x800\n\t"
               "s_add_u32 %0, %0, %6\n\t"
               "buffer_load_dwordx4 %3, %4, %1 offen lds\n\t"
               "s_add_u32 m0, m0, 0x800\n\t"
               "s_add_u32 %1, %1, %6\n\t"
               "buffer_load_dwordx4 %3, %4, %0 offen lds\n\t"
               "s_add_u32 m0, m0, 0x800\n\t"
               "s_add_u32 %0, %0, %6\n\t"
               "buffer_load_dwordx4 %3, %4, %1 offen lds"
               : "+&s"(so), "=&s"(t) : "s"(lds), "v"(voff), "s"(r), "s"(d2), "s"(st), "s"(st3) : "memory", "scc");
}
DI void dma4(rsrc_t r, u32 lds, u32 voff, u32 so, u32 st) {
  asm volatile("s_mov_b32 m0, %1\n\t"
               "s_nop 0\n\t"
               "buffer_load_dwordx4 %2, %3, %0 offen lds\n\t"
               "s_add_u32 m0, m0, 0x800\n\t"
               "s_add_u32 %0, %0, %4\n\t"
               "buffer_load_dwordx4 %2, %3, %0 offen lds\n\t"
               "s_add_u32 m0, m0, 0x800\n\t"
               "s_add_u32 %0, %0, %4\n\t"
               "buffer_load_dwordx4 %2, %3, %0 offen lds\n\t"
               "s_add_u32 m0, m0, 0x800\n\t"
               "s_add_u32 %0, %0, %4\n\t"
               "buffer_load_dwordx4 %2, %3, %0 offen lds"
               : "+&s"(so) : "s"(lds), "v"(voff), "s"(r), "s"(st) : "memory", "scc");
}
DI void dma8lin(rsrc_t r, u32 lds, u32 voff, u32 so) {
  asm volatile("s_mov_b32 m0, %1\n\t"
               "s_nop 0\n\t"
               "buffer_load_dwordx4 %2, %3, %0 offen offset:0 sc1 lds\n\t"
               "buffer_load_dwordx4 %2, %3, %0 offen offset:1024 sc1 lds\n\t"
               "buffer_load_dwordx4 %2, %3, %0 offen offset:2048 sc1 lds\n\t"
               "buffer_load_dwordx4 %2, %3, %0 offen offset:3072 sc1 lds\n\t"
               "s_add_u32 m0, m0, 0x2000\n\t"
               "s_add_u32 %0, %0, 0x2000\n\t"
               "buffer_load_dwordx4 %2, %3, %0 offen offset:0 sc1 lds\n\t"
               "buffer_load_dwordx4 %2, %3, %0 offen offset:1024 sc1 lds\n\t"
               "buffer_load_dwordx4 %2, %3, %0 offen offset:2048 sc1 lds\n\t"
               "buffer_load_dwordx4 %2, %3, %0 offen offset:3072 sc1 lds"
               : "+&s"(so) : "s"(lds), "v"(voff), "s"(r) : "memory", "scc");
}
DI rsrc_t mk(const void* ptr, u32 bytes) { return __builtin_amdgcn_make_buffer_rsrc((void*)ptr, 0, bytes, 0x00020000); }
DI s16x4 rd_tr1(u32 a, int off) { return __builtin_amdgcn_ds_read_tr16_b64_v4i16((LAS s16x4*)(size_t)(a + off)); }
DI bf16x8 cat(s16x4 lo, s16x4 hi) { return __builtin_shufflevector(lo, hi, 0, 1, 2, 3, 4, 5, 6, 7); }
DI bf16x8 rd_tr(u32 a, int off) { return cat(rd_tr1(a, off), rd_tr1(a, off + 2048)); }
DI bf16x8 rd128(u32 a, int off) { return *(LAS bf16x8*)(size_t)(a + off); }
DI f32x4 rdf4(u32 a, int off) { return *(LAS f32x4*)(size_t)(a + off); }
DI float rdf(u32 a, int off) { return *(LAS float*)(size_t)(a + off); }
DI void wr128(u32 a, int off, u32x4 v) { *(LAS u32x4*)(size_t)(a + off) = v; }
DI u32 cvtpk(float lo, float hi) { f32x2 v = {lo, hi}; bf16x2 b = __builtin_convertvector(v, bf16x2); return __builtin_bit_cast(u32, b); }
DI float bflo(u32 v) { return __uint_as_float(v << 16); }
DI float bfhi(u32 v) { return __uint_as_float(v & 0xffff0000u); }
DI float bf2f(u16 v) { return __uint_as_float((u32)v << 16); }
DI float dot2(u32 a, u32 b, float c) { return __builtin_amdgcn_fdot2_f32_bf16(__builtin_bit_cast(bf16x2, a), __builtin_bit_cast(bf16x2, b), c, false); }
constexpr u32 ONES = 0x3f803f80u;
DI float rbf(float v) { return bflo(cvtpk(v, 0.f)); }
DI void split3(float v, float& a, float& b, float& c) { a = rbf(v); const float r = v - a; b = rbf(r); c = rbf(r - b); }
template <int C> DI float dppx(float v) { return __uint_as_float(__builtin_amdgcn_update_dpp(0u, __float_as_uint(v), C, 0xf, 0xf, true)); }
template <int C> DI double dppd(double v) { u32x2 t = __builtin_bit_cast(u32x2, v); t[0] = __builtin_amdgcn_update_dpp(0u, t[0], C, 0xf, 0xf, true); t[1] = __builtin_amdgcn_update_dpp(0u, t[1], C, 0xf, 0xf, true); return __builtin_bit_cast(double, t); }
DI void st16(u32x4 v, rsrc_t r, u32 voff, u32 soff) { asm volatile("buffer_store_dwordx4 %0, %1, %2, %3 offen sc1\n\ts_nop 1" :: "v"(v), "v"(voff), "s"(r), "s"(soff) : "memory"); }
DI void st_flag(char* ws, u32 idx, u32 tag) { __hip_atomic_store((GAS u32*)(ws + WS_FLAGS + idx * 128), tag, __ATOMIC_RELAXED, __HIP_MEMORY_SCOPE_AGENT); }
DI u32 ld_flag(const char* a) { return __hip_atomic_load((GAS u32*)a, __ATOMIC_RELAXED, __HIP_MEMORY_SCOPE_AGENT); }
DI void spin_all(const char* a, u32 tag) {
#pragma nounroll
  for (u32 i = 0; i < SPIN_CAP; i++) { if (__all(ld_flag(a) == tag)) break; __builtin_amdgcn_s_sleep(1); }
  asm volatile("" ::: "memory");
}
DI void spin_ge(const char* a, u32 lvl) {
#pragma nounroll
  for (u32 i = 0; i < SPIN_CAP; i++) { if (__all(ld_flag(a) >= lvl)) break; __builtin_amdgcn_s_sleep(1); }
  asm volatile("" ::: "memory");
}
DI void st_word(char* a, u32 v) { __hip_atomic_store((GAS u32*)a, v, __ATOMIC_RELAXED, __HIP_MEMORY_SCOPE_AGENT); }
DI void hs_leader(char* ws, int lane, u32 lds0, u64* stamps) {
  const rsrc_t r = mk(ws, WS_END);
  bool first = true, joined = false; u32x4 last = {0u, 0u, 0u, 0u};
#pragma nounroll
  for (u32 i = 0; i < SPIN_CAP; i++) {
    asm volatile("" ::: "memory");
    const u32x4 rq = __builtin_amdgcn_raw_buffer_load_b128(r, lane * 16, WS_REQ, 16), ak = __builtin_amdgcn_raw_buffer_load_b128(r, lane * 16, WS_ACK, 16);
    if (first || rq[0] != last[0] || rq[1] != last[1] || rq[2] != last[2] || rq[3] != last[3]) st16(rq, r, lane * 16, WS_RSP);
    first = false; last = rq;
    if (__all(ak[0] == 1u && ak[1] == 1u && ak[2] == 1u && (ak[3] == 1u || lane == 63))) break;
    if (i == 2) { BAR(); joined = true; }
    __builtin_amdgcn_s_sleep(1);
  }
  if (!joined) BAR();
  BAR();
  st_flag(ws, FL_GO, 1u);
}
DI float row_sum16(float v) { v += dppx<0xb1>(v); v += dppx<0x4e>(v); v += dppx<0x141>(v); v += dppx<0x140>(v); return v; }

DI f32x2 gelu2(f32x2 a, float k1, float k2, float sg) {
  f32x2 t = a * a; t = t * k2 + k1; t = t * a;
  t.x = __builtin_amdgcn_exp2f(t.x); t.y = __builtin_amdgcn_exp2f(t.y);
  t = t * sg + sg;
  t.x = __builtin_amdgcn_rcpf(t.x); t.y = __builtin_amdgcn_rcpf(t.y);
  return t * a;
}
DI u32x4 gelu_frag(f32x4 a, f32x4 b, float k1, float k2, float sg) {
  f32x2 g0 = gelu2((f32x2){a[0], a[1]}, k1, k2, sg), g1 = gelu2((f32x2){a[2], a[3]}, k1, k2, sg);
  f32x2 g2 = gelu2((f32x2){b[0], b[1]}, k1, k2, sg), g3 = gelu2((f32x2){b[2], b[3]}, k1, k2, sg);
  return (u32x4){cvtpk(g0.x, g0.y), cvtpk(g1.x, g1.y), cvtpk(g2.x, g2.y), cvtpk(g3.x, g3.y)};
}

template <int H, class UPD>
DI void stepA(f32x4 (&acc)[6][4], bf16x8 (&wp)[2], bf16x8 (&t)[4], const u32 (&wc)[4], const u32& n0, const u32& n1, const u32& xn, UPD&& upd) {
  bf16x8 w[8], nx[4];
  w[0] = wp[0]; w[1] = wp[1];
#pragma unroll
  for (int n = 0; n < 6; n++) {
    const int f = n + 2, ff = f < 6 ? f : f - 6;
    const u32 a = (f < 6 || H == 0) ? wc[ff & 3] : (ff ? n1 : n0); const int off = (f < 6 ? H : 1 - H) * WSLOT + (ff >> 2) * 4096;
    MMA(acc[n][0], w[n], t[0]); SB();
    s16x4 lo = rd_tr1(a, off); SB();
    MMA(acc[n][1], w[n], t[1]); SB();
    s16x4 hi = rd_tr1(a, off + 2048); SB();
    MMA(acc[n][2], w[n], t[2]); SB();
    if (n >= 4) { nx[2 * n - 8] = rd128(xn, (2 * n - 8) * 2048); SB(); }
    if (n == 1) { upd(); SB(); }
    MMA(acc[n][3], w[n], t[3]); SB();
    if (n >= 4) { nx[2 * n - 7] = rd128(xn, (2 * n - 7) * 2048); SB(); }
    if (n == 3) { BAR(); SB(); }
    w[f] = cat(lo, hi);
  }
  wp[0] = w[6]; wp[1] = w[7];
#pragma unroll
  for (int i = 0; i < 4; i++) t[i] = nx[i];
}

template <int KIND, bool ZERO = false, class FW, class FT, class FN, class MID>
DI void half(f32x4 (&acc)[3][4], bf16x8 (&w)[3], bf16x8 (&tp)[2], FW&& rw, FT&& rt_, FN&& rn, MID&& mid) {
  bf16x8 t[4], wn[3], tn[2];
  t[0] = tp[0]; t[1] = tp[1];
  s16x4 lo;
#pragma unroll
  for (int i = 0; i < 12; i++) {
    const int T = i / 3, n = i % 3;
    if constexpr (KIND == 2) MMA(acc[n][T], t[T], w[n]); else if constexpr (ZERO) MMA0(acc[n][T], w[n], t[T]); else MMA(acc[n][T], w[n], t[T]);
    SB();
    if constexpr (KIND == 0) {
      if (i == 0) t[2] = rt_(2, 0); if (i == 1) t[3] = rt_(3, 0);
      if (i >= 2 && i <= 7) { if ((i & 1) == 0) lo = rw((i - 2) >> 1, 0); else wn[(i - 2) >> 1] = cat(lo, rw((i - 2) >> 1, 1)); }
      if (i == 8) tn[0] = rn(0, 0); if (i == 9) tn[1] = rn(1, 0);
    } else if constexpr (KIND == 1) {
      if (i <= 3) { if ((i & 1) == 0) lo = rt_(2 + (i >> 1), 0); else t[2 + (i >> 1)] = cat(lo, rt_(2 + (i >> 1), 1)); }
      if (i >= 4 && i <= 6) wn[i - 4] = rw(i - 4, 0);
      if (i >= 7 && i <= 10) { if ((i & 1) == 1) lo = rn((i - 7) >> 1, 0); else tn[(i - 7) >> 1] = cat(lo, rn((i - 7) >> 1, 1)); }
    } else {
      if (i == 0) t[2] = rt_(2, 0); if (i == 2) t[3] = rt_(3, 0);
      if (i == 3 || i == 5 || i == 6) wn[i == 3 ? 0 : i - 4] = rw(i == 3 ? 0 : i - 4, 0);
      if (i == 8) tn[0] = rn(0, 0); if (i == 9) tn[1] = rn(1, 0);
    }
    mid(i);
    SB();
  }
#pragma unroll
  for (int i = 0; i < 3; i++) w[i] = wn[i];
  tp[0] = tn[0]; tp[1] = tn[1];
}

template <int I, int N, class FN> DI void sfor(FN&& f) { if constexpr (I < N) { f(std::integral_constant<int, I>{}); sfor<I + 1, N>(f); } }

DI f32x2 cmul(f32x2 a, f32x2 b) { return (f32x2){a.x * b.x - a.y * b.y, a.x * b.y + a.y * b.x}; }
DI f32x2 cfma(f32x2 a, f32x2 b, f32x2 c) { return (f32x2){__builtin_fmaf(a.x, b.x, __builtin_fmaf(-a.y, b.y, c.x)), __builtin_fmaf(a.x, b.y, __builtin_fmaf(a.y, b.x, c.y))}; }
struct CarryPre { f32x2 l16, l32, l64, L1, L4; };
DI CarryPre carry_pre(int c, u32 lds0) {
  const f32x4 pw = rdf4(lds0 + (18 & 3) * WSLOT + AX_TAB + c * 32, 16);
  CarryPre k; const f32x2 l8 = {pw[0], pw[1]};
  k.l16 = cmul(l8, l8); k.l32 = cmul(k.l16, k.l16); k.l64 = cmul(k.l32, k.l32); k.L1 = (f32x2){pw[2], pw[3]};
  const f32x2 L2 = cmul(k.L1, k.L1); k.L4 = cmul(L2, L2);
  PIN(k.l16); PIN(k.l32); PIN(k.l64); PIN(k.L1); PIN(k.L4);
  return k;
}
DI void ssm_carry(const Params& p, const CarryPre& k, int rt, int j, int c, u32 lds0) {
  const u32 scr = lds0 + TOFF + (22 & 3) * TSLOT + c * 8;
  f32x2 e[8];
#pragma unroll
  for (int s = 0; s < 8; s++) e[s] = *(LAS f32x2*)(size_t)(scr + s * 1536);
  const f32x2 a01 = cfma(k.l16, e[0], e[1]), a23 = cfma(k.l16, e[2], e[3]), a45 = cfma(k.l16, e[4], e[5]), a67 = cfma(k.l16, e[6], e[7]);
  const f32x2 a03 = cfma(k.l32, a01, a23), E = cfma(k.l64, a03, cfma(k.l32, a45, a67));
  const rsrc_t rE = mk(p.ws + WS_EG, 16 * 9216);
  const int vo = (192 * j + c) * 16 + (15 - rt) * 9216;
  st16((u32x4){__float_as_uint(E.x), 1u, __float_as_uint(E.y), 1u}, rE, vo, 0);
  u32x4 va[15], vb[15];
  auto issue = [&](u32x4 (&v)[15]) {
    asm volatile("" ::: "memory");
#pragma unroll
    for (int i = 0; i < 15; i++) v[i] = __builtin_amdgcn_raw_buffer_load_b128(rE, vo, (15 - i) * 9216, 16);
    asm volatile("" ::: "memory");
  };
  f32x2 cy;
  auto eval = [&](u32x4 (&v)[15]) {
    u32 n = 0; f32x2 q[4];
#pragma unroll
    for (int i = 0; i < 15; i++) {
      n += v[i][1] + v[i][3];
      const f32x2 x = {__uint_as_float(v[i][0]), __uint_as_float(v[i][2])};
      const int ch = (i + 1) & 3;
      q[ch] = i < 4 ? x : cfma(k.L4, q[ch], x);
    }
    cy = cfma(k.L1, cfma(k.L1, cfma(k.L1, q[0], q[1]), q[2]), q[3]);
    return __all(n == 2u * rt);
  };
  __builtin_amdgcn_s_sleep(6);
  issue(va);
  __builtin_amdgcn_s_sleep(10);
  issue(vb);
  if (!eval(va)) {
    issue(va);
    if (!eval(vb) && !eval(va)) {
#pragma nounroll
      for (u32 spin = 0; spin < SPIN_CAP; spin++) { u32x4 vc[15]; issue(vc); if (eval(vc)) break; }
    }
  }
#pragma unroll
  for (int s = 0; s < 8; s += 2) e[s] = *(LAS f32x2*)(size_t)(scr + s * 1536);
  const f32x2 c2 = cfma(k.l32, cy, a01), c4 = cfma(k.l64, cy, a03), c6 = cfma(k.l32, c4, a45);
  const f32x2 cin[8] = {cy, cfma(k.l16, cy, e[0]), c2, cfma(k.l16, c2, e[2]), c4, cfma(k.l16, c4, e[4]), c6, cfma(k.l16, c6, e[6])};
#pragma unroll
  for (int s = 0; s < 8; s++) *(LAS f32x2*)(size_t)(scr + s * 1536) = cin[s];
}

#define LBAR() BAR()
DI void loader(u64x4 kv, int rt, int g, int j, int u, int lane, u32 lds0, u32 tag) {
  const bool ssm = g == 4;
  const u32 wbytes = ssm ? D * ROWB : D * F * 2;
  const int r8 = lane >> 3, gp = lane & 7;
  const int cgr = ((((gp >> 1) ^ ((r8 >> 1) & 3)) << 1) | (gp & 1));
  const int rgr = gp ^ (4 * u + (r8 >> 1));
  const int slice = 3 * g + j;
  const int kA = 16 * u + 8 * (r8 >> 2) + (r8 & 3);
  const int R = 8 * u + r8;
  const u32 voffA = ssm ? (192 * j + R) * ROWB + rgr * 16 : kA * (F * 2) + slice * 768 + cgr * 16;
  const int tok = ssm ? 16 * ((R >> 2) & 3) + 8 * (R & 1) + ((R >> 1) & 1) : R;
  const u32 voffX = (rt * 128 + tok) * ROWB + rgr * 16;
  const u32 ldsW = lds0 + u * 1024, ldsT = lds0 + TOFF;
  const u32 aKt = ssm ? 128 : 64 * F * 2, aPar = ssm ? 0 : 32 * F * 2, pstr = ssm ? 32 * ROWB : 128, a2 = ssm ? 16 * ROWB : 4 * F * 2, xstr = (ssm ? 2 : 16) * ROWB;
  const int rot = (rt >= 10 ? rt - 10 : rt) & ~1;
  asm volatile("s_waitcnt lgkmcnt(0)" : "+s"(kv) : "v"(voffA), "v"(voffX), "s"(aKt), "s"(rot));
  const rsrc_t rX = mk((void*)kv[0], S * ROWB), rA0 = mk((void*)(ssm ? kv[2] : kv[1]), wbytes), rA1 = mk((void*)(ssm ? kv[3] : kv[1]), wbytes);
  auto ktile = [&](int kap) { int kt = kap + rot; return kt >= 9 ? kt - 9 : kt; };
  auto issueA = [&](int kap, int h) { dma12(h ? rA1 : rA0, ldsW + ((2 * kap + h) & 3) * WSLOT, voffA, ktile(kap) * aKt + h * aPar, a2, pstr, pstr); };
  auto issueX = [&](int kap, int h) { dma4(rX, ldsT + (kap & 3) * TSLOT + h * 8192 + u * 1024, voffX, h * 64 * ROWB + ktile(kap) * 128, xstr); };
  issueA(0, 0); issueX(0, 0); issueX(0, 1); issueA(0, 1);
  __builtin_amdgcn_s_setprio(3);
  waitv<12>(); BAR();
  issueX(1, 0); issueX(1, 1); issueA(1, 0);
#pragma nounroll
  for (int kap = 0; kap < 7; kap++) {
    u32 l1 = ldsW + ((2 * kap + 3) & 3) * WSLOT, s1 = ktile(kap + 1) * aKt + aPar, l2 = ldsW + ((2 * kap) & 3) * WSLOT, s2 = ktile(kap + 2) * aKt;
    u32 lx = ldsT + ((kap + 2) & 3) * TSLOT + u * 1024, lx2 = lx + 8192, sx = ktile(kap + 2) * 128, sx2 = sx + 64 * ROWB;
    asm volatile("" : "+s"(l1), "+s"(s1), "+s"(l2), "+s"(s2), "+s"(lx), "+s"(lx2), "+s"(sx), "+s"(sx2));
    waitv<16>(); BAR(); dma12(rA1, l1, voffA, s1, a2, pstr, pstr); dma4(rX, lx, voffX, sx, xstr);
    waitv<16>(); BAR(); dma12(rA0, l2, voffA, s2, a2, pstr, pstr); dma4(rX, lx2, voffX, sx2, xstr);
  }
  waitv<16>(); BAR(); issueA(8, 1);
  const Params p = reload_params();
  const u32 voffL = lane * 16;
  const rsrc_t rWS = mk(p.ws, WS_END), rB0 = mk(ssm ? p.cre : p.w2, wbytes), rB1 = mk(ssm ? p.cim : p.w2, wbytes);
  const u32 voffB = ssm ? voffA : R * ROWB + j * 384 + cgr * 16;
  const u32 bJ = ssm ? 384 : 384 * ROWB, bPar = ssm ? 0 : 192 * ROWB, bI = ssm ? 128 : 32 * ROWB, b0 = ssm ? 0 : g * 1152 * ROWB;
  const int j1 = j == 2 ? 0 : j + 1, j2 = j == 0 ? 2 : j - 1;
  const u32 st3 = 96 * ROWB - 2 * pstr, pb = WS_P + (rt * 15 + 3 * g) * 98304 + u * 4096;
  auto issueB = [&](int s, int jj, int i) { dma12((i & 1) ? rB1 : rB0, ldsW + (s & 3) * WSLOT, voffB, b0 + jj * bJ + (i & 1) * bPar + (i >> 1) * bI, 16 * ROWB, pstr, st3); };
  auto issueP = [&](int s, int jj, int i) { dma8lin(rWS, ldsT + (s & 3) * TSLOT + u * 4096, voffL, pb + jj * 98304 + i * 16384); };
  waitv<12>(); BAR();
#pragma unroll
  for (int k = 0; k < 4; k++) {
    const u32 dst = ssm ? (k < 3 ? AX_TAB + (u * 3 + k) * 1024 : AX_S) : AX_T + k * 1024;
    const u32 src = ssm ? (k < 3 ? WS_TAB + j * 6144 + (u * 3 + k) * 1024 : WS_LN + rt * 5120 + 4096) : WS_LN + rt * 5120 + k * 1024;
    dma_sc1(rWS, lds0 + (18 & 3) * WSLOT + dst, voffL, src);
  }
  waitv<4>();
  BAR(); issueB(19, j, 0); waitv<12>();
  BAR(); issueB(20, j, 1); waitv<12>();
  if (ssm) { const CarryPre k = carry_pre(lane, lds0); BAR(); if (u == 1) ssm_carry(p, k, rt, j, lane, lds0); WAITL(); }
  BAR();
  issueB(21, j, 2); waitv<12>();
  if (ssm) BAR();
  BAR(); issueB(22, j, 3); waitv<12>();
  BAR(); issueB(23, j, 4); issueP(23, j, 4); waitv<20>();
  BAR(); issueB(24, j, 5); issueP(24, j, 5);
#pragma nounroll
  for (int n = 0; n < 3; n++) {
    const int i = n ? 2 * n - 2 : 4;
    u32 w1 = b0 + j1 * bJ + (i >> 1) * bI, w2 = b0 + j2 * bJ + (i >> 1) * bI, w3 = w1 + bPar, w4 = w2 + bPar;
    u32 p1 = pb + j1 * 98304 + i * 16384, p2 = pb + j2 * 98304 + i * 16384, p3 = p1 + 16384, p4 = p2 + 16384;
    asm volatile("" : "+s"(w1), "+s"(w2), "+s"(w3), "+s"(w4), "+s"(p1), "+s"(p2), "+s"(p3), "+s"(p4));
    waitv<20>(); BAR(); dma12(rB0, ldsW + 1 * WSLOT, voffB, w1, 16 * ROWB, pstr, st3); dma8lin(rWS, ldsT + 1 * TSLOT + u * 4096, voffL, p1);
    waitv<20>(); BAR(); dma12(rB0, ldsW + 2 * WSLOT, voffB, w2, 16 * ROWB, pstr, st3); dma8lin(rWS, ldsT + 2 * TSLOT + u * 4096, voffL, p2);
    waitv<20>(); BAR(); dma12(rB1, ldsW + 3 * WSLOT, voffB, w3, 16 * ROWB, pstr, st3); dma8lin(rWS, ldsT + 3 * TSLOT + u * 4096, voffL, p3);
    waitv<20>(); BAR(); dma12(rB1, ldsW + 0 * WSLOT, voffB, w4, 16 * ROWB, pstr, st3); dma8lin(rWS, ldsT + 0 * TSLOT + u * 4096, voffL, p4);
  }
  waitv<20>();
  BAR();
  {
    const u32 l = lds0 + WSLOT;
    const u32 vo = (lane & 31) < 24 ? (rt * 128 + u * 64 + (g & 3) * 16 + (lane >> 5)) * ROWB + j * 384 + (lane & 31) * 16 : 0x7fffffffu;
#pragma unroll
    for (int k = 0; k < 8; k++) dma(rX, l + (u * 8 + k) * 1024, vo, k * 2 * ROWB);
    dma(mk(u ? p.dskip : p.b2, ROWB), l + 16384 + u * 1024, voffL + j * 384, 0);
  }
  waitv<9>(); BAR(); BAR(); waitv<0>();
  BAR();
}

DI void helper(const Params& p, int rt, int g, int j, int h, int lane, u32 lds0, u32 tag) {
  const bool ssm = g == 4;
  const int slice = 3 * g + j, q = lane >> 4, jl = lane & 15;
  const rsrc_t rWS = mk(p.ws, WS_END);
  char* const hsR = p.ws + WS_RSP + blockIdx.x * 4;
  const u32 hs0 = ld_flag(hsR);
  u32 hsn = 0;
  auto startup = [&]() {
    hsn = __builtin_amdgcn_readfirstlane(hs0) + 1u;
    if (h == 0) st_word(hsR - 1024, hsn); else if (lane < 2) st_flag(p.ws, lane ? FL_P + rt * 15 + slice : FL_O + (rt * 3 + j) * 5 + g, 0u);
    if (ssm) { st16((u32x4){0u, 0u, 0u, 0u}, rWS, (192 * j + 64 * h + lane) * 16, WS_EG + (15 - rt) * 9216); st16((u32x4){0u, 0u, 0u, 0u}, rWS, (192 * j + 128 + lane) * 16, WS_EG + (15 - rt) * 9216); }
  };
  __builtin_amdgcn_s_setprio(2);
  {
    const int e = (lane >> 2) & 3, z = 2 * (q & 1) + (e >> 1);
    f32x4 c = {0.f, 0.f, 0.f, 0.f};
    u32x4 sel[12];
#pragma unroll
    for (int n = 0; n < 12; n++) { const u32 o = jl == n ? ONES : 0u; sel[n] = (u32x4){o, o, o, o}; PIN(sel[n]); }
    PIN(c);
    auto pass = [&](int sl) {
      bf16x8 f[12];
      if (ssm) {
        const u32 a = lds0 + (rt & 1) * 2 * WSLOT + h * 12288 + jl * 128 + ((q ^ (jl >> 1)) * 16);
#pragma unroll
        for (int n = 0; n < 6; n++) { f[n] = rd128(a, sl * WSLOT + n * 2048); f[6 + n] = rd128(a ^ 64, sl * WSLOT + n * 2048); }
      } else {
#pragma unroll
        for (int n = 0; n < 12; n++) f[n] = rd_tr(lds0 + (rt & 1) * 2 * WSLOT + h * 12288 + (4 * q + e) * 128 + (((n & 3) ^ z) * 32) + (lane & 3) * 8, sl * WSLOT + (n >> 2) * 4096);
      }
#pragma unroll
      for (int n = 0; n < 12; n++) PIN(f[n]);
      asm volatile("s_nop 1");
      if (ssm) {
#pragma unroll
        for (int n = 0; n < 12; n++) MMAN(c, f[n], sel[6 * sl + n % 6]);
        MFENCE();
      } else {
#pragma unroll
        for (int n = 0; n < 12; n++) MMAN(c, f[n], sel[n]);
        MFENCE();
      }
    };
    const int odd = rt & 1;
    const u32 cso = jl < 12 ? ((ssm ? (jl >= 6 ? 192 - 96 : 0) + 96 * h : 192 * h) + 16 * jl + 4 * q) * 4 : 0x7fffffffu;
    LBAR();
    if (odd) { LBAR(); startup(); LBAR(); }
    if (rt < 9) pass(0);
    LBAR();
    if (rt < 9) { pass(1); st16(__builtin_bit_cast(u32x4, c), rWS, cso, WS_CS + (slice * 16 + rt) * 1536); }
    LBAR();
    if (!odd) { startup(); LBAR(); LBAR(); }
  }
  LBAR();
  u32 pv = ld_flag(hsR);
  waitv<0>(); LBAR();
  if (h == 0) { if (!__all(pv == hsn)) spin_all(hsR, hsn); st_word(hsR + 1024, 1u); }
  LBAR(); LBAR(); LBAR(); LBAR();
  const char* fGo = p.ws + WS_FLAGS + FL_GO * 128;
  pv = ld_flag(fGo);
  LBAR(); LBAR();
  if (!__all(pv == 1u)) spin_all(fGo, 1u);
  const int cc = 192 * h + 4 * (lane < 48 ? lane : lane - 48);
  f32x4 cp[9];
#pragma unroll
  for (int k = 0; k < 9; k++) cp[k] = __builtin_bit_cast(f32x4, __builtin_amdgcn_raw_buffer_load_b128(rWS, cc * 4, WS_CS + (slice * 16 + k) * 1536, 16));
  const u32x2 bb = *(const GAS u32x2*)(p.b1 + (ssm ? 0 : slice * 384) + cc);
  LBAR(); LBAR(); SB();
  f32x4 cs = ((cp[0] + cp[1]) + (cp[2] + cp[3])) + ((cp[4] + cp[5]) + (cp[6] + cp[7])) + cp[8];
  u32x4 aw[4];
  PIN(cs);
  auto auxw = [&](int k) { float c1, c2, c3; split3(cs[k], c1, c2, c3); const float b = (k & 1) ? bfhi(bb[k >> 1]) : bflo(bb[k >> 1]);
    aw[k] = (u32x4){cvtpk(c1, c2), cvtpk(c1, c2), cvtpk(c3, c1), cvtpk(b, b)}; PIN(aw[k]); };
  const u32 ax = lds0 + (18 & 3) * WSLOT;
  LBAR(); SB();
  if (!ssm) { auxw(0); auxw(1); }
  LBAR(); SB();
  if (!ssm) { auxw(2); auxw(3); }
  LBAR(); SB();
  if (ssm) *(LAS f32x4*)(size_t)(ax + AX_W + cc * 4) = cs;
  else {
#pragma unroll
    for (int k = 0; k < 4; k++) wr128(ax + AX_W + cc * 4, k * 1600, aw[k]);
  }
  WAITL(); LBAR();
  if (ssm) { const CarryPre k = carry_pre(64 + 64 * h + lane, lds0); LBAR(); ssm_carry(p, k, rt, j, 64 + 64 * h + lane, lds0); WAITL(); LBAR(); }
  LBAR();
  char* const fMine = p.ws + WS_FLAGS + (FL_P + rt * 15 + slice) * 128;
  const char* fP = p.ws + WS_FLAGS + (FL_P + rt * 15 + 3 * g + ((lane & 1) ? (j == 2 ? 0 : j + 1) : (j == 0 ? 2 : j - 1))) * 128;
  LBAR(); if (h == 1 && !ssm) st_word(fMine, 2u);
  LBAR(); if (h == 1 && ssm) st_word(fMine, 1u);
#pragma unroll
  for (u32 lv = 1; lv < 4; lv++) {
    if (lv == 1) pv = ld_flag(fP);
    LBAR(); if (h == 0 && !__all(pv >= lv)) spin_ge(fP, lv);
    LBAR(); if (h == 1 && lv == 1) st_word(fMine, 2u);
    LBAR(); pv = ld_flag(fP);
    LBAR(); if (h == 1 && lv == 1) st_word(fMine, 3u);
  }
  LBAR(); LBAR(); LBAR(); LBAR();
  LBAR();
  if (h == 1) st_flag(p.ws, FL_O + (rt * 3 + j) * 5 + g, tag);
  else if (!ssm) {
    const int k = lane & 3;
    const char* fa = p.ws + WS_FLAGS + (FL_O + (rt * 3 + j) * 5 + (k < g ? k : k + 1)) * 128;
    u32 v = 0;
#pragma nounroll
    for (u32 i = 0; i < SPIN_CAP; i++) { v = ld_flag(fa); if (__all(v == tag || k == 3)) break; }
    const bool yok = __all(v == tag);
    *(LAS u32*)(size_t)lds0 = yok; WAITL(); BAR();
    if (!yok) { spin_all(fa, tag); BAR(); }
  }
}

template <bool SSM>
DI void phaseB(const Params& p, int rt, int g, int j, int w, int lane, u32 lds0, u32 tag) {
  asm volatile("" : "+v"(lane));
  const int rg = w >> 2, cg = w & 3, q = lane >> 4, e = (lane >> 2) & 3, pp = lane & 3, jl = lane & 15;
  const int z = 2 * (q & 1) + (e >> 1);
  u32 wlo[3], whi[3], tb[2];
#pragma unroll
  for (int n = 0; n < 3; n++) {
    const int t = 3 * cg + n;
    wlo[n] = SSM ? lds0 + (16 * t + jl) * 128 + ((q ^ (jl >> 1)) * 16) : lds0 + (t >> 2) * 4096 + (4 * q + e) * 128 + (((t & 3) ^ z) * 32) + pp * 8;
    whi[n] = SSM ? (wlo[n] ^ 64) : wlo[n] + 2 * WSLOT;
  }
  tb[0] = SSM ? lds0 + TOFF + rg * 8192 + (q >> 1) * 2048 + (pp >> 1) * 1024 + (((8 * (q & 1) + e) * 16) ^ ((pp >> 1) * 64)) + 8 * (pp & 1) : lds0 + TOFF + rg * 4096 + lane * 16;
  tb[1] = tb[0] ^ 64;
  float mean = 0.f, rstd = 0.f;
  if constexpr (!SSM) { const u32 sa = lds0 + (18 & 3) * WSLOT + AX_S + (64 * rg + 16 * g + jl) * 4; mean = rdf(sa, 0); rstd = rdf(sa, 512); }
  f32x4 ob[3][4];
  auto RW = [&](int sl, int kb, int n, int part) {
    if constexpr (SSM) return rd128((kb ? whi[n] : wlo[n]) + (sl >> 1) * 2 * WSLOT, (sl & 1) * WSLOT);
    else return rd_tr1(sl < 2 ? wlo[n] : whi[n], (sl & 1) * WSLOT + kb * 12288 + part * 2048);
  };
  auto RT = [&](int sl, int kb, int T, int part) {
    if constexpr (SSM) return rd_tr1(tb[part], sl * TSLOT + kb * 4096 + T * 256);
    else return rd128(tb[0], sl * TSLOT + kb * 8192 + T * 1024);
  };
  bf16x8 wf[3], tp[2];
  auto step = [&](auto slc, auto&& mid) {
    constexpr int SL = decltype(slc)::value & 3, NS = (SL + 1) & 3;
    half<SSM ? 1 : 0, (decltype(slc)::value > 3)>(ob, wf, tp, [&](int n, int part) { return RW(SL, 1, n, part); }, [&](int T, int part) { return RT(SL, 0, T, part); }, [&](int T, int part) { return RT(SL, 1, T, part); }, mid);
    half<SSM ? 1 : 0>(ob, wf, tp, [&](int n, int part) { return RW(NS, 0, n, part); }, [&](int T, int part) { return RT(SL, 1, T, part); }, [&](int T, int part) { return RT(NS, 0, T, part); }, [](int) {});
  };
  using I0 = std::integral_constant<int, 0>; using I1 = std::integral_constant<int, 1>; using I2 = std::integral_constant<int, 2>; using I3 = std::integral_constant<int, 3>;
  if constexpr (!SSM) waitv<4>();
  __builtin_amdgcn_s_waitcnt(0xC07F); WAITL(); BAR();
#pragma unroll
  for (int n = 0; n < 3; n++) { if constexpr (SSM) wf[n] = RW(3, 0, n, 0); else wf[n] = cat(RW(3, 0, n, 0), RW(3, 0, n, 1)); }
#pragma unroll
  for (int T = 0; T < 2; T++) { if constexpr (SSM) tp[T] = cat(RT(3, 0, T, 0), RT(3, 0, T, 1)); else tp[T] = RT(3, 0, T, 0); }
  auto barm = [](int i) { if (i == 11) BAR(); };
  step(std::integral_constant<int, 7>{}, [](int) {});
  step(I0{}, [&](int i) { if (i == 3) { if constexpr (SSM) waitv<8>(); BAR(); } });
#pragma nounroll
  for (int it = 0; it < 4; it++) { step(I1{}, barm); step(I2{}, [&](int i) { if (i == 11) { waitv<4>(); BAR(); } }); step(I3{}, barm); step(I0{}, [&](int i) { if (i == 11) { waitv<0>(); BAR(); } }); }
  MFENCE();
  const rsrc_t rWS = mk(p.ws, WS_END);
  const u32 xb = WS_XB + (rt * 3 + j) * 32 * XBW + w * XBW;
  f32x4 mine[3];
#pragma unroll
  for (int T = 0; T < 4; T++) {
    if (SSM || T != g) {
      const u32 o = xb + T * 8 * XBW + g * 1536;
      const u32x4 a = {cvtpk(ob[0][T][0], ob[0][T][1]), cvtpk(ob[0][T][2], ob[0][T][3]), cvtpk(ob[1][T][0], ob[1][T][1]), cvtpk(ob[1][T][2], ob[1][T][3])};
      const u32 b0 = cvtpk(ob[2][T][0], ob[2][T][1]), b1 = cvtpk(ob[2][T][2], ob[2][T][3]);
      const auto s0 = __builtin_amdgcn_permlane32_swap(b0, b0, false, false), s1 = __builtin_amdgcn_permlane32_swap(b1, b1, false, false);
      st16(a, rWS, lane * 16, o);
      if (lane < 32) st16((u32x4){b0, b1, s0[1], s1[1]}, rWS, lane * 16, o + 1024);
    } else {
#pragma unroll
      for (int n = 0; n < 3; n++) mine[n] = ob[n][T];
    }
  }
  if constexpr (SSM) {
    waitv<0>(); BAR();
    STAMP(7);
  } else {
    waitv<0>(); BAR();
    const int tok = rt * 128 + 64 * rg + 16 * g + jl, col = 192 * j + 48 * cg + 4 * q;
    u32x2 xv[3], bv[3], dv[3];
#pragma unroll
    for (int n = 0; n < 3; n++) {
      const u32 cb = lds0 + WSLOT + (48 * cg + 4 * q + 16 * n) * 2;
      xv[n] = *(LAS u32x2*)(size_t)(cb + (16 * rg + jl) * 512); bv[n] = *(LAS u32x2*)(size_t)(cb + 16384); dv[n] = *(LAS u32x2*)(size_t)(cb + 17408);
    }
    const u32 o = xb + g * 8 * XBW;
    u32x4 a[3]; u32x2 b[3];
    BAR();
#pragma unroll
    for (int k = 0; k < 3; k++) {
      const int src = k < g ? k : k + 1;
      a[k] = __builtin_amdgcn_raw_buffer_load_b128(rWS, lane * 16, o + src * 1536, 16);
      b[k] = __builtin_amdgcn_raw_buffer_load_b64(rWS, (lane & 31) * 16 + (lane >> 5) * 8, o + src * 1536 + 1024, 16);
    }
    const bool yok = __builtin_amdgcn_readfirstlane(*(LAS u32*)(size_t)lds0);
    u32x4 ya; u32x2 yb;
    auto ldy = [&]() { ya = __builtin_amdgcn_raw_buffer_load_b128(rWS, lane * 16, o + 4 * 1536, 16); yb = __builtin_amdgcn_raw_buffer_load_b64(rWS, (lane & 31) * 16 + (lane >> 5) * 8, o + 4 * 1536 + 1024, 16); };
    if (yok) ldy();
    f32x4 gate[3], base[3];
#pragma unroll
    for (int n = 0; n < 3; n++) {
#pragma unroll
      for (int c = 0; c < 4; c++) {
        float G = mine[n][c] + ((c & 1) ? bfhi(bv[n][c >> 1]) : bflo(bv[n][c >> 1]));
#pragma unroll
        for (int k = 0; k < 3; k++) { const u32 v = n < 2 ? a[k][2 * n + (c >> 1)] : b[k][c >> 1]; G += (c & 1) ? bfhi(v) : bflo(v); }
        const float xx = (c & 1) ? bfhi(xv[n][c >> 1]) : bflo(xv[n][c >> 1]);
        const float dd = (c & 1) ? bfhi(dv[n][c >> 1]) : bflo(dv[n][c >> 1]);
        gate[n][c] = G * __builtin_amdgcn_rcpf(1.f + __builtin_amdgcn_exp2f(-1.4426950408889634f * G));
        base[n][c] = xx + (xx - mean) * rstd * dd * gate[n][c];
      }
    }
#pragma unroll
    for (int n = 0; n < 3; n++) { PIN(gate[n]); PIN(base[n]); }
    if (!yok) { BAR(); ldy(); }
#pragma unroll
    for (int n = 0; n < 3; n++) {
      const u32 y0 = n < 2 ? ya[2 * n] : yb[0], y1 = n < 2 ? ya[2 * n + 1] : yb[1];
      *(GAS f32x4*)(p.out + tok * D + col + 16 * n) = base[n] + (f32x4){bflo(y0), bfhi(y0), bflo(y1), bfhi(y1)} * gate[n];
    }
    STAMP(7);
  }
}

DI void ffn_compute(int rt, int g, int j, int w, int lane, u32 lds0, u32 tag) {
  const int rg = w >> 2, cg = w & 3, q = lane >> 4, e = (lane >> 2) & 3, pp = lane & 3, jl = lane & 15;
  const int z = 2 * (q & 1) + (e >> 1);
  u32 wc[4];
#pragma unroll
  for (int n = 0; n < 4; n++) { const int t = 6 * cg + n; wc[n] = lds0 + (t >> 2) * 4096 + (4 * q + e) * 128 + (((t & 3) ^ z) * 32) + pp * 8; }
  const u32 xbase = lds0 + TOFF + (64 * rg + jl) * 128 + ((q ^ (jl >> 1)) * 16);
  f32x4 acc[6][4];
#pragma unroll
  for (int n = 0; n < 6; n++)
#pragma unroll
    for (int T = 0; T < 4; T++) { acc[n][T] = (f32x4){0.f, 0.f, 0.f, 0.f}; asm volatile("" : "+v"(acc[n][T])); }
  bf16x8 wp[2], t[4];
  BAR();
  wp[0] = rd_tr(wc[0], 0); wp[1] = rd_tr(wc[1], 0);
#pragma unroll
  for (int T = 0; T < 4; T++) t[T] = rd128(xbase, T * 2048);
  u32 xs = 0, xn = 0, n0 = 0, n1 = 0; int dw = 2 * WSLOT;
#pragma nounroll
  for (int it = 0; it < 9; it++) {
    stepA<0>(acc, wp, t, wc, n0, n1, xn, [&]() { xn = (xbase ^ 64) + xs; PIN(xn); xs = (xs + TSLOT) & (4 * TSLOT - 1); });
    stepA<1>(acc, wp, t, wc, n0, n1, xn, [&]() { xn = xbase + xs; n0 = wc[0] + dw; n1 = wc[1] + dw; wc[2] += dw; wc[3] += dw; PIN(xn); PIN(n0); PIN(n1); PIN(wc[2]); PIN(wc[3]); });
    wc[0] = n0; wc[1] = n1; dw = -dw;
  }
  const Params p = reload_params();
  const u32 ax = lds0 + (18 & 3) * WSLOT;
  float k1[4], k2[4], sg[4];
#pragma unroll
  for (int T = 0; T < 4; T++) {
    u32x4 v = __builtin_bit_cast(u32x4, rd128(ax + AX_T + (64 * rg + jl) * 16, T * 256));
    if (q != 0) v = (u32x4){0u, 0u, 0u, 0u};
    t[T] = __builtin_bit_cast(bf16x8, v);
    const float r = rdf(ax + AX_S + 512 + (64 * rg + jl) * 4, T * 64);
    sg[T] = rdf(ax + AX_S + 1024 + (64 * rg + jl) * 4, T * 64);
    k1[T] = -2.3022081985f * r; k2[T] = -0.10294324f * r * r * r;
  }
  {
    bf16x8 a[6];
#pragma unroll
    for (int n = 0; n < 6; n++) a[n] = rd128(ax + AX_W + ((jl & 3) * 100 + 24 * cg + (jl >> 2)) * 16, n * 64);
    asm volatile("s_nop 1" : "+v"(t[0]), "+v"(t[1]), "+v"(t[2]), "+v"(t[3]));
#pragma unroll
    for (int n = 0; n < 6; n++)
#pragma unroll
      for (int T = 0; T < 4; T++) MMA(acc[n][T], a[n], t[T]);
    MFENCE();
  }
  const rsrc_t rWS = mk(p.ws, WS_END);
  const u32 pg = WS_P + (rt * 15 + 3 * g + j) * 98304 + (cg >> 1) * 16384 + (cg & 1) * 8192 + rg * 4096;
  const u32 pl = lds0 + TOFF + (cg & 1) * 8192 + rg * 4096 + lane * 16, pl0 = pl + ((19 + (cg >> 1)) & 3) * TSLOT, pl1 = pl + ((21 + (cg >> 1)) & 3) * TSLOT;
#pragma unroll
  for (int bb = 0; bb < 3; bb++) {
    const int b = (bb + 2) % 3;
#pragma unroll
    for (int T = 0; T < 4; T++) {
      const u32x4 P = gelu_frag(acc[2 * b][T], acc[2 * b + 1][T], k1[T], k2[T], sg[T]);
      st16(P, rWS, lane * 16, pg + b * 32768 + T * 1024);
      if (b < 2) wr128(b ? pl1 : pl0, T * 1024, P);
      SB();
    }
    if (bb == 0) BAR();
  }
  phaseB<false>(p, rt, g, j, w, lane, lds0, tag);
}

DI void ssm_compute(int rt, int j, int w, int lane, u32 lds0, u32 tag) {
  const int rg = w >> 2, cg = w & 3; int q = lane >> 4, jl = lane & 15;
  u32 wb = lds0 + (16 * cg + jl) * 128 + ((q ^ (jl >> 1)) * 16);
  const u32 xb0 = lds0 + TOFF + (64 * rg + jl) * 128 + ((q ^ (jl >> 1)) * 16);
  f32x4 ar[3][4], ai[3][4];
#pragma unroll
  for (int n = 0; n < 3; n++)
#pragma unroll
    for (int T = 0; T < 4; T++) { ar[n][T] = (f32x4){0.f, 0.f, 0.f, 0.f}; ai[n][T] = (f32x4){0.f, 0.f, 0.f, 0.f}; asm volatile("" : "+v"(ar[n][T]), "+v"(ai[n][T])); }
  bf16x8 wf[3], tp[2];
  auto barm = [](int i) { if (i == 11) BAR(); };
  __builtin_amdgcn_s_waitcnt(0xC07F);
  BAR();
#pragma unroll
  for (int n = 0; n < 3; n++) wf[n] = rd128(wb, n * 8192);
  tp[0] = rd128(xb0, 0); tp[1] = rd128(xb0, 2048);
  u32 xs = 0, x0 = xb0, x1 = xb0 ^ 64, wb1 = wb ^ 64; int dw = 2 * WSLOT;
#pragma nounroll
  for (int it = 0; it < 9; it++) {
    half<2>(ar, wf, tp, [&](int n, int) { return rd128(wb1, n * 8192); }, [&](int T, int) { return rd128(x0, T * 2048); }, [&](int T, int) { return rd128(x1, T * 2048); }, barm);
    half<2>(ar, wf, tp, [&](int n, int) { return rd128(wb, WSLOT + n * 8192); }, [&](int T, int) { return rd128(x1, T * 2048); }, [&](int T, int) { return rd128(x0, T * 2048); },
            [&](int i) { if (i == 10) { wb += dw; PIN(wb); xs = (xs + TSLOT) & (4 * TSLOT - 1); } });
    half<2>(ai, wf, tp, [&](int n, int) { return rd128(wb1, WSLOT + n * 8192); }, [&](int T, int) { return rd128(x0, T * 2048); }, [&](int T, int) { return rd128(x1, T * 2048); },
            [&](int i) { if (i == 7) { wb1 += dw; x0 = xb0 + xs; PIN(wb1); PIN(x0); dw = -dw; } if (i == 11) BAR(); });
    half<2>(ai, wf, tp, [&](int n, int) { return rd128(wb, n * 8192); }, [&](int T, int) { return rd128(x1, T * 2048); }, [&](int T, int) { return rd128(x0, T * 2048); },
            [&](int i) { if (i == 4) { x1 = x0 ^ 64; PIN(x1); } });
  }
  const Params p = reload_params();
  asm volatile("" : "+v"(lane));
  q = lane >> 4; jl = lane & 15;
  const u32 ax = lds0 + (18 & 3) * WSLOT;
  MFENCE();
  f32x2 lc[3];
  const u32 tb = ax + AX_TAB + (16 * cg + jl) * 32;
  const u32 scr = lds0 + TOFF + (22 & 3) * TSLOT + ((4 * rg + q) * 192 + 16 * cg + jl) * 8;
  f32x2 e0[3]; float csr[3], csi[3];
#pragma unroll
  for (int b = 0; b < 3; b++) { csr[b] = rdf(ax + AX_W + (16 * cg + jl) * 4, b * 256); csi[b] = rdf(ax + AX_W + (16 * cg + jl) * 4, 768 + b * 256); }
#pragma unroll
  for (int T = 0; T < 4; T++) {
    const f32x4 r = rdf4(ax + AX_S + 512 + (64 * rg + 4 * q) * 4, T * 64), nm = rdf4(ax + AX_S + (64 * rg + 4 * q) * 4, T * 64);
#pragma unroll
    for (int b = 0; b < 3; b++) { ar[b][T] = ar[b][T] * r + nm * csr[b]; ai[b][T] = ai[b][T] * r + nm * csi[b]; }
  }
  f32x2 l8[3];
#pragma unroll
  for (int b = 0; b < 3; b++) {
    lc[b] = *(LAS f32x2*)(size_t)(tb + b * 2048);
    l8[b] = *(LAS f32x2*)(size_t)(tb + b * 2048 + 16);
    f32x2 hr = {ar[b][0][0], ar[b][0][1]}, hi = {ai[b][0][0], ai[b][0][1]};
#pragma unroll
    for (int k = 1; k < 8; k++) {
      const f32x2 ur = {ar[b][k >> 1][(k & 1) * 2], ar[b][k >> 1][(k & 1) * 2 + 1]}, ui = {ai[b][k >> 1][(k & 1) * 2], ai[b][k >> 1][(k & 1) * 2 + 1]};
      const f32x2 nr = lc[b][0] * hr + (ur - lc[b][1] * hi), ni = lc[b][0] * hi + (ui + lc[b][1] * hr);
      hr = nr; hi = ni;
    }
    e0[b] = (f32x2){hr.x, hi.x};
    *(LAS f32x2*)(size_t)(scr + b * 512) = (f32x2){l8[b].x * hr.x - l8[b].y * hi.x + hr.y, l8[b].x * hi.x + l8[b].y * hr.x + hi.y};
  }
  WAITL(); BAR(); SB();
#pragma unroll
  for (int b = 0; b < 3; b++) {
    const f32x2 cf = *(LAS f32x2*)(size_t)(tb + b * 2048 + 8); const float c2r = cf.x, c2i = cf.y;
#pragma unroll
    for (int T = 0; T < 4; T++) {
      const f32x4 nr = c2r * ar[b][T] - c2i * ai[b][T], ni = -c2r * ai[b][T] - c2i * ar[b][T];
      ar[b][T] = nr; ai[b][T] = ni;
      asm volatile("" : "+v"(ar[b][T]), "+v"(ai[b][T]));
    }
  }
  BAR();
  const rsrc_t rWS = mk(p.ws, WS_END);
  const u32 io0 = w * 2048 + lane * 16, io1 = w * 2048 + 1024 + ((lane * 16) ^ 64);
  const u32 pg = WS_P + (rt * 15 + 12 + j) * 98304;
  f32x2 cins[3];
#pragma unroll
  for (int b = 0; b < 3; b++) { cins[b] = *(LAS f32x2*)(size_t)(scr + b * 512); l8[b] = *(LAS f32x2*)(size_t)(tb + b * 2048 + 16); }
#pragma unroll
  for (int bb = 0; bb < 3; bb++) {
    const int b = (bb + 2) % 3;
    u32x4 H[2][2];
    const f32x2 cin = cins[b];
    const f32x2 sr = {cin.x, l8[b].x * cin.x - l8[b].y * cin.y + e0[b].x}, si = {cin.y, l8[b].x * cin.y + l8[b].y * cin.x + e0[b].y};
    const f32x2 cf = *(LAS f32x2*)(size_t)(tb + b * 2048 + 8); const float c2r = cf.x, c2i = cf.y;
    f32x2 hr = c2r * sr - c2i * si, hi = -c2r * si - c2i * sr;
#pragma unroll
    for (int T = 0; T < 4; T++) {
      f32x2 pr[2], pi[2];
#pragma unroll
      for (int k = 0; k < 2; k++) {
        const f32x2 ur = {ar[b][T][2 * k], ar[b][T][2 * k + 1]}, ui = {ai[b][T][2 * k], ai[b][T][2 * k + 1]};
        const f32x2 nr = lc[b][0] * hr + (ur + lc[b][1] * hi), ni = lc[b][0] * hi + (ui - lc[b][1] * hr);
        hr = nr; hi = ni; pr[k] = nr; pi[k] = ni;
      }
      H[0][0][T] = cvtpk(pr[0].x, pr[1].x); H[0][1][T] = cvtpk(pr[0].y, pr[1].y);
      H[1][0][T] = cvtpk(pi[0].x, pi[1].x); H[1][1][T] = cvtpk(pi[0].y, pi[1].y);
    }
    if (bb == 1) BAR();
#pragma unroll
    for (int k = 0; k < 4; k++) {
      st16(H[k >> 1][k & 1], rWS, (k & 1) ? io1 : io0, pg + (2 * b + (k >> 1)) * 16384);
      if (b < 2) wr128(lds0 + TOFF + ((k & 1) ? io1 : io0), ((19 + 2 * b + (k >> 1)) & 3) * TSLOT, H[k >> 1][k & 1]);
    }
    SB();
  }
  phaseB<true>(p, rt, 4, j, w, lane, lds0, tag);
}

DI void spare(const Params& p, int sid, u32 lds0, u32 tag) {
  const int tid = threadIdx.x;
  const rsrc_t rWS = mk(p.ws, WS_END);
  const int w = __builtin_amdgcn_readfirstlane(tid >> 6), lane = tid & 63;
  if (sid == 15 && w == 11) { st_flag(p.ws, FL_GO, 0u); st16((u32x4){0u, 0u, 0u, 0u}, rWS, lane * 16, WS_ACK); waitv<0>(); hs_leader(p.ws, lane, lds0, STAMPPTR); return; }
  u32 hsn = 0;
  if (w == 11) { hsn = ld_flag(p.ws + WS_RSP + blockIdx.x * 4) + 1u; st_word(p.ws + WS_REQ + blockIdx.x * 4, hsn); }
  else if (w < 8) {
    const int row = tid >> 2, part = tid & 3;
    const GAS u32x4* src = (const GAS u32x4*)(p.x + (sid * 128 + row) * D) + part;
    u32x4 v[18];
#pragma unroll
    for (int i = 0; i < 18; i++) v[i] = src[4 * i];
    float s = 0.f, s2 = 0.f;
#pragma unroll
    for (int i = 0; i < 18; i++) { s = dot2(v[i][0], ONES, s); s2 = dot2(v[i][1], ONES, s2); s = dot2(v[i][2], ONES, s); s2 = dot2(v[i][3], ONES, s2); }
    s += s2; s += dppx<0xb1>(s); s += dppx<0x4e>(s);
    const float mean = s * (1.f / D);
    f32x2 dv = {0.f, 0.f};
#pragma unroll
    for (int i = 0; i < 72; i++) { const u32 wd = v[i >> 2][i & 3]; const f32x2 d = (f32x2){bflo(wd), bfhi(wd)} - mean; dv += d * d; }
    s = dv.x + dv.y; s += dppx<0xb1>(s); s += dppx<0x4e>(s);
    const float ve = s * (1.f / D) + 1e-5f, rstd = __builtin_amdgcn_rsqf(ve), sig = ve * rstd;
    float m1, m2, m3; split3(-mean, m1, m2, m3);
    const float g1 = rbf(sig), g2 = sig - g1;
    const int ir = (row & 64) + 16 * ((row >> 1) & 3) + 4 * ((row >> 4) & 3) + 2 * (row & 1) + ((row >> 3) & 1);
    if (part == 0) wr128(lds0 + row * 16, 0, (u32x4){cvtpk(m1, m1), cvtpk(m2, m2), cvtpk(m1, m3), cvtpk(g1, g2)});
    else *(LAS float*)(size_t)(lds0 + 2048 + (part - 1) * 512 + row * 4) = part == 1 ? mean : (part == 2 ? rstd : sig);
    if (part >= 2) *(LAS float*)(size_t)(lds0 + 4096 + (part - 2) * 512 + ir * 4) = part == 2 ? -mean * rstd : rstd;
  } else if (w == 9 && sid < 9) {
    const int c = sid * 64 + lane;
    const float lr = bf2f(p.lre[c]), li = bf2f(p.lim[c]);
    const float dt = rbf(expf(bf2f(p.logdt[c])));
    const float ex = expf(dt * lr), an = dt * li;
    const float br = ex * cosf(an), bi = ex * sinf(an);
    const float nr = br - 1.f, den = 1.f / (lr * lr + li * li);
    const float cr = (nr * lr + bi * li) * den, ci = (bi * lr - nr * li) * den;
    float ar = br, ai = bi, r8 = 0.f, i8 = 0.f;
#pragma unroll
    for (int k = 0; k < 7; k++) { const float t = ar * ar - ai * ai; ai = 2.f * ar * ai; ar = t; if (k == 2) { r8 = ar; i8 = ai; } }
    st16(__builtin_bit_cast(u32x4, (f32x4){br, bi, 2.f * cr, 2.f * ci}), rWS, c * 32, WS_TAB);
    st16(__builtin_bit_cast(u32x4, (f32x4){r8, i8, ar, ai}), rWS, c * 32 + 16, WS_TAB);
  }
  WAITL(); BAR();
  if (w < 5) st16(__builtin_bit_cast(u32x4, rd128(lds0 + tid * 16, 0)), rWS, tid * 16, WS_LN + sid * 5120);
  if (w == 11) spin_all(p.ws + WS_RSP + blockIdx.x * 4, hsn);
  waitv<0>(); BAR();
  if (sid == 15) return;
  if (w == 11) st_word(p.ws + WS_ACK + blockIdx.x * 4, 1u);
  {
    u32 acc = 0;
    const GAS u32* w2 = (const GAS u32*)p.w2;
#pragma unroll
    for (int i = 0; i < 4; i++) {
      const int L = (i * 15 + sid) * 768 + tid, r = L / 9, kb = r / 1536, rr = r - kb * 1536;
      if (L < 41472) acc ^= w2[((rr >> 5) * 96 + kb * 32 + (rr & 31)) * (ROWB / 4) + (L - r * 9) * 32];
    }
    { const int L = sid * 768 + tid; if (L < 5184) acc ^= ((const GAS u32*)p.cre)[L * 32]; else if (L < 10368) acc ^= ((const GAS u32*)p.cim)[(L - 5184) * 32]; }
    asm volatile("" :: "v"(acc));
  }
}

__global__ __launch_bounds__(768, 3) void fused_kernel(Params p) {
  extern __shared__ __attribute__((aligned(16))) char smem[];
  u64x4 kv; asm volatile("s_load_dwordx8 %0, %1, 0x0" : "=s"(kv) : "s"(__builtin_amdgcn_kernarg_segment_ptr())); SB();
  const u32 lds0 = (u32)(size_t)(LAS char*)smem;
  const int b = blockIdx.x, xcd = b & 7, idx = b >> 3;
  const u32 tag = 1u;
  if (b < 32 && (threadIdx.x >> 7) == 1) {
    u64 pc0; asm volatile("s_getpc_b64 %0" : "=s"(pc0));
    const u32 ln = min((b >> 3) * 128 + (threadIdx.x & 127), (u32)CODE_LINES);
    asm volatile("s_mov_b32 m0, %0\n\ts_nop 0\n\tglobal_load_lds_dword %1, %2" :: "s"(lds0 + TOFF + 3 * TSLOT), "v"(ln * 128), "s"(pc0) : "memory");
  }
  STAMP(0);
  const int tri = idx / 3, j = idx - 3 * tri;
  const int g = tri < 2 ? 4 : xcd >> 1, rt = 8 * (xcd & 1) + (tri < 2 ? (xcd & 6) + tri : tri - 2);
  const int lane = threadIdx.x & 63, w = __builtin_amdgcn_readfirstlane(threadIdx.x >> 6);
  const int sid = xcd * 2 + idx - 30;
  if (__builtin_expect(idx < 30 && w < 2, 1)) { loader(kv, rt, g, j, w, lane, lds0, tag); return; }
  asm volatile("s_waitcnt lgkmcnt(0)" :: "s"(kv));
  if (idx < 30 && w >= 4) {
    if (g < 4) ffn_compute(rt, g, j, w - 4, lane, lds0, tag); else ssm_compute(rt, j, w - 4, lane, lds0, tag);
    return;
  }
  const Params q = reload_params();
  if (idx < 30) helper(q, rt, g, j, w - 2, lane, lds0, tag); else { spare(q, sid, lds0, tag); STAMP(7); }
}

extern "C" void kernel_launch(void* const* d_in, const int* in_sizes, int n_in, void* d_out, int out_size, void* d_ws, size_t ws_size, hipStream_t stream) {
  Params p;
  p.x = (const u16*)d_in[0]; p.lre = (const u16*)d_in[1]; p.lim = (const u16*)d_in[2]; p.bre = (const u16*)d_in[3]; p.bim = (const u16*)d_in[4];
  p.cre = (const u16*)d_in[5]; p.cim = (const u16*)d_in[6]; p.logdt = (const u16*)d_in[7]; p.dskip = (const u16*)d_in[8];
  p.w1 = (const u16*)d_in[10]; p.b1 = (const u16*)d_in[11]; p.w2 = (const u16*)d_in[12]; p.b2 = (const u16*)d_in[13];
  p.out = (float*)d_out; p.ws = (char*)d_ws + ((ws_size - WS_END) & ~(size_t)0x1fffff);
  (void)hipFuncSetAttribute((const void*)fused_kernel, hipFuncAttributeMaxDynamicSharedMemorySize, LDS_BYTES);
  fused_kernel<<<256, 768, LDS_BYTES, stream>>>(p);
}
```

```cpp
#define STAMPS
#define UNIT 25
#define K0 0
#define A0 -1
#define B0 -1
#define K1 0
#define A1 -1
#define B1 100
#include <hip/hip_runtime.h>
#include <utility>

#define LAS __attribute__((address_space(3)))
#define GAS __attribute__((address_space(1)))
#define DI __device__ __forceinline__
typedef unsigned short u16;
typedef unsigned u32;
typedef unsigned long long u64;
typedef unsigned u32x4 __attribute__((ext_vector_type(4)));
typedef unsigned u32x2 __attribute__((ext_vector_type(2)));
typedef short s16x4 __attribute__((ext_vector_type(4)));
typedef short bf16x8 __attribute__((ext_vector_type(8)));
typedef float f32x4 __attribute__((ext_vector_type(4)));
typedef float f32x2 __attribute__((ext_vector_type(2)));
typedef __bf16 bf16x2 __attribute__((ext_vector_type(2)));
typedef __amdgpu_buffer_rsrc_t rsrc_t;
typedef u64 u64x4 __attribute__((ext_vector_type(4)));

constexpr int S = 2048, D = 576, F = 4608;
constexpr int ROWB = D * 2;
constexpr int WSLOT = 24576, TOFF = 4 * WSLOT, TSLOT = 16384, LDS_BYTES = 163840;
constexpr int AX_W = 0, AX_T = 6656, AX_S = 8704, AX_TAB = 10752;
constexpr u32 WS_FLAGS = 0;
constexpr u32 FL_GO = 0, FL_P = 32, FL_O = 32 + 240, NFLAG = 32 + 480;
constexpr u32 WS_EG = NFLAG * 128;
constexpr u32 WS_REQ = WS_EG + 16 * 576 * 16, WS_RSP = WS_REQ + 1024, WS_ACK = WS_RSP + 1024;
constexpr u32 WS_CS = 256 * 1024;
constexpr u32 WS_LN = WS_CS + 15 * 16 * 1536;
constexpr u32 WS_TAB = WS_LN + 16 * 5120;
constexpr u32 WS_P = 1u << 20;
constexpr u32 XBW = 5 * 1536;
constexpr u32 WS_XB = WS_P + 240 * 98304;
constexpr u32 WS_END = WS_XB + 48 * 32 * XBW;
constexpr int CODE_LINES = 440;
constexpr u32 SPIN_CAP = 1u << 18;

struct Params {
  const u16 *x, *w1, *bre, *bim;
  const u16 *lre, *lim, *cre, *cim, *logdt, *dskip, *b1, *w2, *b2;
  float* out; char* ws;
  u64* stamps;
};

DI Params reload_params() {
  Params q;
#if defined(__HIP_DEVICE_COMPILE__)
  typedef __attribute__((address_space(4))) const Params* KP;
  KP kp = (KP)__builtin_amdgcn_kernarg_segment_ptr(); asm volatile("" : "+s"(kp)); q = *kp;
#endif
  return q;
}
template <int N> DI void waitv() { asm volatile("s_waitcnt vmcnt(%0)" :: "n"(N) : "memory"); }
#define WAITL() asm volatile("s_waitcnt lgkmcnt(0)" ::: "memory")
#define BAR() do { asm volatile("" ::: "memory"); __builtin_amdgcn_s_barrier(); asm volatile("" ::: "memory"); } while (0)
#define SB() __builtin_amdgcn_sched_barrier(0)
#define MFENCE() do { SB(); asm volatile("s_nop 7\n\ts_nop 7"); SB(); } while (0)
#define MMA(c, a, b) asm volatile("v_mfma_f32_16x16x32_bf16 %0, %1, %2, %0" : "+v"(c) : "v"(a), "v"(b))
#define MMA0(c, a, b) asm volatile("v_mfma_f32_16x16x32_bf16 %0, %1, %2, 0" : "=&v"(c) : "v"(a), "v"(b))
#define MMAN(c, a, b) asm volatile("s_nop 1\n\tv_mfma_f32_16x16x32_bf16 %0, %1, %2, %0" : "+v"(c) : "v"(a), "v"(b))
#define PIN(x) asm volatile("" : "+v"(x))
#define STAMPPTR p.stamps
#define STAMP(i) do { SB(); if (threadIdx.x == ((i) == 7 && blockIdx.x < 240 ? 256 : 0)) p.stamps[blockIdx.x * 16 + (i)] = __builtin_amdgcn_s_memrealtime(); SB(); } while (0)

DI void dma(rsrc_t r, u32 lds, u32 voff, u32 soff) {
  asm volatile("s_mov_b32 m0, %0\n\ts_nop 0\n\tbuffer_load_dwordx4 %1, %2, %3 offen lds" :: "s"(lds), "v"(voff), "s"(r), "s"(soff) : "memory");
}
DI void dma_sc1(rsrc_t r, u32 lds, u32 voff, u32 soff) {
  asm volatile("s_mov_b32 m0, %0\n\ts_nop 0\n\tbuffer_load_dwordx4 %1, %2, %3 offen sc1 lds" :: "s"(lds), "v"(voff), "s"(r), "s"(soff) : "memory");
}
DI void dma12(rsrc_t r, u32 lds, u32 voff, u32 so, u32 d2, u32 st, u32 st3) {
  u32 t;
  asm volatile("s_mov_b32 m0, %2\n\t"
               "s_add_u32 %1, %0, %5\n\t"
               "buffer_load_dwordx4 %3, %4, %0 offen lds\n\t"
               "s_add_u32 m0, m0, 0x800\n\t"
               "s_add_u32 %0, %0, %6\n\t"
               "buffer_load_dwordx4 %3, %4, %1 offen lds\n\t"
               "s_add_u32 m0, m0, 0x800\n\t"
               "s_add_u32 %1, %1, %6\n\t"
               "buffer_load_dwordx4 %3, %4, %0 offen lds\n\t"
               "s_add_u32 m0, m0, 0x800\n\t"
               "s_add_u32 %0, %0, %6\n\t"
               "buffer_load_dwordx4 %3, %4, %1 offen lds\n\t"
               "s_add_u32 m0, m0, 0x800\n\t"
               "s_add_u32 %1, %1, %6\n\t"
               "buffer_load_dwordx4 %3, %4, %0 offen lds\n\t"
               "s_add_u32 m0, m0, 0x800\n\t"
               "s_add_u32 %0, %0, %7\n\t"
               "buffer_load_dwordx4 %3, %4, %1 offen lds\n\t"
               "s_add_u32 m0, m0, 0x800\n\t"
               "s_add_u32 %1, %1, %7\n\t"
               "buffer_load_dwordx4 %3, %4, %0 offen lds\n\t"
               "s_add_u32 m0, m0, 0x800\n\t"
               "s_add_u32 %0, %0, %6\n\t"
               "buffer_load_dwordx4 %3, %4, %1 offen lds\n\t"
               "s_add_u32 m0, m0, 0x800\n\t"
               "s_add_u32 %1, %1, %6\n\t"
               "buffer_load_dwordx4 %3, %4, %0 offen lds\n\t"
               "s_add_u32 m0, m0, 0x800\n\t"
               "s_add_u32 %0, %0, %6\n\t"
               "buffer_load_dwordx4 %3, %4, %1 offen lds\n\t"
               "s_add_u32 m0, m0, 0x800\n\t"
               "s_add_u32 %1, %1, %6\n\t"
               "buffer_load_dwordx4 %3, %4, %0 offen lds\n\t"
               "s_add_u32 m0, m0, 0x800\n\t"
               "s_add_u32 %0, %0, %6\n\t"
               "buffer_load_dwordx4 %3, %4, %1 offen lds"
               : "+&s"(so), "=&s"(t) : "s"(lds), "v"(voff), "s"(r), "s"(d2), "s"(st), "s"(st3) : "memory", "scc");
}
DI void dma4(rsrc_t r, u32 lds, u32 voff, u32 so, u32 st) {
  asm volatile("s_mov_b32 m0, %1\n\t"
               "s_nop 0\n\t"
               "buffer_load_dwordx4 %2, %3, %0 offen lds\n\t"
               "s_add_u32 m0, m0, 0x800\n\t"
               "s_add_u32 %0, %0, %4\n\t"
               "buffer_load_dwordx4 %2, %3, %0 offen lds\n\t"
               "s_add_u32 m0, m0, 0x800\n\t"
               "s_add_u32 %0, %0, %4\n\t"
               "buffer_load_dwordx4 %2, %3, %0 offen lds\n\t"
               "s_add_u32 m0, m0, 0x800\n\t"
               "s_add_u32 %0, %0, %4\n\t"
               "buffer_load_dwordx4 %2, %3, %0 offen lds"
               : "+&s"(so) : "s"(lds), "v"(voff), "s"(r), "s"(st) : "memory", "scc");
}
DI void dma8lin(rsrc_t r, u32 lds, u32 voff, u32 so) {
  asm volatile("s_mov_b32 m0, %1\n\t"
               "s_nop 0\n\t"
               "buffer_load_dwordx4 %2, %3, %0 offen offset:0 sc1 lds\n\t"
               "buffer_load_dwordx4 %2, %3, %0 offen offset:1024 sc1 lds\n\t"
               "buffer_load_dwordx4 %2, %3, %0 offen offset:2048 sc1 lds\n\t"
               "buffer_load_dwordx4 %2, %3, %0 offen offset:3072 sc1 lds\n\t"
               "s_add_u32 m0, m0, 0x2000\n\t"
               "s_add_u32 %0, %0, 0x2000\n\t"
               "buffer_load_dwordx4 %2, %3, %0 offen offset:0 sc1 lds\n\t"
               "buffer_load_dwordx4 %2, %3, %0 offen offset:1024 sc1 lds\n\t"
               "buffer_load_dwordx4 %2, %3, %0 offen offset:2048 sc1 lds\n\t"
               "buffer_load_dwordx4 %2, %3, %0 offen offset:3072 sc1 lds"
               : "+&s"(so) : "s"(lds), "v"(voff), "s"(r) : "memory", "scc");
}
DI rsrc_t mk(const void* ptr, u32 bytes) { return __builtin_amdgcn_make_buffer_rsrc((void*)ptr, 0, bytes, 0x00020000); }
DI s16x4 rd_tr1(u32 a, int off) { return __builtin_amdgcn_ds_read_tr16_b64_v4i16((LAS s16x4*)(size_t)(a + off)); }
DI bf16x8 cat(s16x4 lo, s16x4 hi) { return __builtin_shufflevector(lo, hi, 0, 1, 2, 3, 4, 5, 6, 7); }
DI bf16x8 rd_tr(u32 a, int off) { return cat(rd_tr1(a, off), rd_tr1(a, off + 2048)); }
DI bf16x8 rd128(u32 a, int off) { return *(LAS bf16x8*)(size_t)(a + off); }
DI f32x4 rdf4(u32 a, int off) { return *(LAS f32x4*)(size_t)(a + off); }
DI float rdf(u32 a, int off) { return *(LAS float*)(size_t)(a + off); }
DI void wr128(u32 a, int off, u32x4 v) { *(LAS u32x4*)(size_t)(a + off) = v; }
DI u32 cvtpk(float lo, float hi) { f32x2 v = {lo, hi}; bf16x2 b = __builtin_convertvector(v, bf16x2); return __builtin_bit_cast(u32, b); }
DI float bflo(u32 v) { return __uint_as_float(v << 16); }
DI float bfhi(u32 v) { return __uint_as_float(v & 0xffff0000u); }
DI float bf2f(u16 v) { return __uint_as_float((u32)v << 16); }
DI float dot2(u32 a, u32 b, float c) { return __builtin_amdgcn_fdot2_f32_bf16(__builtin_bit_cast(bf16x2, a), __builtin_bit_cast(bf16x2, b), c, false); }
constexpr u32 ONES = 0x3f803f80u;
DI float rbf(float v) { return bflo(cvtpk(v, 0.f)); }
DI void split3(float v, float& a, float& b, float& c) { a = rbf(v); const float r = v - a; b = rbf(r); c = rbf(r - b); }
template <int C> DI float dppx(float v) { return __uint_as_float(__builtin_amdgcn_update_dpp(0u, __float_as_uint(v), C, 0xf, 0xf, true)); }
template <int C> DI double dppd(double v) { u32x2 t = __builtin_bit_cast(u32x2, v); t[0] = __builtin_amdgcn_update_dpp(0u, t[0], C, 0xf, 0xf, true); t[1] = __builtin_amdgcn_update_dpp(0u, t[1], C, 0xf, 0xf, true); return __builtin_bit_cast(double, t); }
DI void st16(u32x4 v, rsrc_t r, u32 voff, u32 soff) { asm volatile("buffer_store_dwordx4 %0, %1, %2, %3 offen sc1\n\ts_nop 1" :: "v"(v), "v"(voff), "s"(r), "s"(soff) : "memory"); }
DI void st_flag(char* ws, u32 idx, u32 tag) { __hip_atomic_store((GAS u32*)(ws + WS_FLAGS + idx * 128), tag, __ATOMIC_RELAXED, __HIP_MEMORY_SCOPE_AGENT); }
DI u32 ld_flag(const char* a) { return __hip_atomic_load((GAS u32*)a, __ATOMIC_RELAXED, __HIP_MEMORY_SCOPE_AGENT); }
DI void spin_all(const char* a, u32 tag) {
#pragma nounroll
  for (u32 i = 0; i < SPIN_CAP; i++) { if (__all(ld_flag(a) == tag)) break; __builtin_amdgcn_s_sleep(1); }
  asm volatile("" ::: "memory");
}
DI void spin_ge(const char* a, u32 lvl) {
#pragma nounroll
  for (u32 i = 0; i < SPIN_CAP; i++) { if (__all(ld_flag(a) >= lvl)) break; __builtin_amdgcn_s_sleep(1); }
  asm volatile("" ::: "memory");
}
DI void st_word(char* a, u32 v) { __hip_atomic_store((GAS u32*)a, v, __ATOMIC_RELAXED, __HIP_MEMORY_SCOPE_AGENT); }
DI void hs_leader(char* ws, int lane, u32 lds0, u64* stamps) {
  const rsrc_t r = mk(ws, WS_END);
  bool first = true, joined = false; u32x4 last = {0u, 0u, 0u, 0u};
#pragma nounroll
  for (u32 i = 0; i < SPIN_CAP; i++) {
    asm volatile("" ::: "memory");
    const u32x4 rq = __builtin_amdgcn_raw_buffer_load_b128(r, lane * 16, WS_REQ, 16), ak = __builtin_amdgcn_raw_buffer_load_b128(r, lane * 16, WS_ACK, 16);
    if (first || rq[0] != last[0] || rq[1] != last[1] || rq[2] != last[2] || rq[3] != last[3]) st16(rq, r, lane * 16, WS_RSP);
    first = false; last = rq;
    if (__all(ak[0] == 1u && ak[1] == 1u && ak[2] == 1u && (ak[3] == 1u || lane == 63))) break;
    if (i == 2) { BAR(); joined = true; }
    __builtin_amdgcn_s_sleep(1);
  }
  if (!joined) BAR();
  BAR();
  st_flag(ws, FL_GO, 1u);
  if (lane == 0) stamps[255 * 16 + 2] = __builtin_amdgcn_s_memrealtime();
}
DI float row_sum16(float v) { v += dppx<0xb1>(v); v += dppx<0x4e>(v); v += dppx<0x141>(v); v += dppx<0x140>(v); return v; }

DI f32x2 gelu2(f32x2 a, float k1, float k2, float sg) {
  f32x2 t = a * a; t = t * k2 + k1; t = t * a;
  t.x = __builtin_amdgcn_exp2f(t.x); t.y = __builtin_amdgcn_exp2f(t.y);
  t = t * sg + sg;
  t.x = __builtin_amdgcn_rcpf(t.x); t.y = __builtin_amdgcn_rcpf(t.y);
  return t * a;
}
DI u32x4 gelu_frag(f32x4 a, f32x4 b, float k1, float k2, float sg) {
  f32x2 g0 = gelu2((f32x2){a[0], a[1]}, k1, k2, sg), g1 = gelu2((f32x2){a[2], a[3]}, k1, k2, sg);
  f32x2 g2 = gelu2((f32x2){b[0], b[1]}, k1, k2, sg), g3 = gelu2((f32x2){b[2], b[3]}, k1, k2, sg);
  return (u32x4){cvtpk(g0.x, g0.y), cvtpk(g1.x, g1.y), cvtpk(g2.x, g2.y), cvtpk(g3.x, g3.y)};
}

template <int H, class UPD>
DI void stepA(f32x4 (&acc)[6][4], bf16x8 (&wp)[2], bf16x8 (&t)[4], const u32 (&wc)[4], const u32& n0, const u32& n1, const u32& xn, UPD&& upd) {
  bf16x8 w[8], nx[4];
  w[0] = wp[0]; w[1] = wp[1];
#pragma unroll
  for (int n = 0; n < 6; n++) {
    const int f = n + 2, ff = f < 6 ? f : f - 6;
    const u32 a = (f < 6 || H == 0) ? wc[ff & 3] : (ff ? n1 : n0); const int off = (f < 6 ? H : 1 - H) * WSLOT + (ff >> 2) * 4096;
    MMA(acc[n][0], w[n], t[0]); SB();
    s16x4 lo = rd_tr1(a, off); SB();
    MMA(acc[n][1], w[n], t[1]); SB();
    s16x4 hi = rd_tr1(a, off + 2048); SB();
    MMA(acc[n][2], w[n], t[2]); SB();
    if (n >= 4) { nx[2 * n - 8] = rd128(xn, (2 * n - 8) * 2048); SB(); }
    if (n == 1) { upd(); SB(); }
    MMA(acc[n][3], w[n], t[3]); SB();
    if (n >= 4) { nx[2 * n - 7] = rd128(xn, (2 * n - 7) * 2048); SB(); }
    if (n == 3) { BAR(); SB(); }
    w[f] = cat(lo, hi);
  }
  wp[0] = w[6]; wp[1] = w[7];
#pragma unroll
  for (int i = 0; i < 4; i++) t[i] = nx[i];
}

template <int KIND, bool ZERO = false, class FW, class FT, class FN, class MID>
DI void half(f32x4 (&acc)[3][4], bf16x8 (&w)[3], bf16x8 (&tp)[2], FW&& rw, FT&& rt_, FN&& rn, MID&& mid) {
  bf16x8 t[4], wn[3], tn[2];
  t[0] = tp[0]; t[1] = tp[1];
  s16x4 lo;
#pragma unroll
  for (int i = 0; i < 12; i++) {
    const int T = i / 3, n = i % 3;
    if constexpr (KIND == 2) MMA(acc[n][T], t[T], w[n]); else if constexpr (ZERO) MMA0(acc[n][T], w[n], t[T]); else MMA(acc[n][T], w[n], t[T]);
    SB();
    if constexpr (KIND == 0) {
      if (i == 0) t[2] = rt_(2, 0); if (i == 1) t[3] = rt_(3, 0);
      if (i >= 2 && i <= 7) { if ((i & 1) == 0) lo = rw((i - 2) >> 1, 0); else wn[(i - 2) >> 1] = cat(lo, rw((i - 2) >> 1, 1)); }
      if (i == 8) tn[0] = rn(0, 0); if (i == 9) tn[1] = rn(1, 0);
    } else if constexpr (KIND == 1) {
      if (i <= 3) { if ((i & 1) == 0) lo = rt_(2 + (i >> 1), 0); else t[2 + (i >> 1)] = cat(lo, rt_(2 + (i >> 1), 1)); }
      if (i >= 4 && i <= 6) wn[i - 4] = rw(i - 4, 0);
      if (i >= 7 && i <= 10) { if ((i & 1) == 1) lo = rn((i - 7) >> 1, 0); else tn[(i - 7) >> 1] = cat(lo, rn((i - 7) >> 1, 1)); }
    } else {
      if (i == 0) t[2] = rt_(2, 0); if (i == 2) t[3] = rt_(3, 0);
      if (i == 3 || i == 5 || i == 6) wn[i == 3 ? 0 : i - 4] = rw(i == 3 ? 0 : i - 4, 0);
      if (i == 8) tn[0] = rn(0, 0); if (i == 9) tn[1] = rn(1, 0);
    }
    mid(i);
    SB();
  }
#pragma unroll
  for (int i = 0; i < 3; i++) w[i] = wn[i];
  tp[0] = tn[0]; tp[1] = tn[1];
}

template <int I, int N, class FN> DI void sfor(FN&& f) { if constexpr (I < N) { f(std::integral_constant<int, I>{}); sfor<I + 1, N>(f); } }

DI f32x2 cmul(f32x2 a, f32x2 b) { return (f32x2){a.x * b.x - a.y * b.y, a.x * b.y + a.y * b.x}; }
DI f32x2 cfma(f32x2 a, f32x2 b, f32x2 c) { return (f32x2){__builtin_fmaf(a.x, b.x, __builtin_fmaf(-a.y, b.y, c.x)), __builtin_fmaf(a.x, b.y, __builtin_fmaf(a.y, b.x, c.y))}; }
struct CarryPre { f32x2 l16, l32, l64, L1, L4; };
DI CarryPre carry_pre(int c, u32 lds0) {
  const f32x4 pw = rdf4(lds0 + (18 & 3) * WSLOT + AX_TAB + c * 32, 16);
  CarryPre k; const f32x2 l8 = {pw[0], pw[1]};
  k.l16 = cmul(l8, l8); k.l32 = cmul(k.l16, k.l16); k.l64 = cmul(k.l32, k.l32); k.L1 = (f32x2){pw[2], pw[3]};
  const f32x2 L2 = cmul(k.L1, k.L1); k.L4 = cmul(L2, L2);
  PIN(k.l16); PIN(k.l32); PIN(k.l64); PIN(k.L1); PIN(k.L4);
  return k;
}
DI void ssm_carry(const Params& p, const CarryPre& k, int rt, int j, int c, u32 lds0) {
  const u32 scr = lds0 + TOFF + (22 & 3) * TSLOT + c * 8;
  f32x2 e[8];
#pragma unroll
  for (int s = 0; s < 8; s++) e[s] = *(LAS f32x2*)(size_t)(scr + s * 1536);
  const f32x2 a01 = cfma(k.l16, e[0], e[1]), a23 = cfma(k.l16, e[2], e[3]), a45 = cfma(k.l16, e[4], e[5]), a67 = cfma(k.l16, e[6], e[7]);
  const f32x2 a03 = cfma(k.l32, a01, a23), E = cfma(k.l64, a03, cfma(k.l32, a45, a67));
  const rsrc_t rE = mk(p.ws + WS_EG, 16 * 9216);
  const int vo = (192 * j + c) * 16 + (15 - rt) * 9216;
  st16((u32x4){__float_as_uint(E.x), 1u, __float_as_uint(E.y), 1u}, rE, vo, 0);
  u32x4 va[15], vb[15];
  auto issue = [&](u32x4 (&v)[15]) {
    asm volatile("" ::: "memory");
#pragma unroll
    for (int i = 0; i < 15; i++) v[i] = __builtin_amdgcn_raw_buffer_load_b128(rE, vo, (15 - i) * 9216, 16);
    asm volatile("" ::: "memory");
  };
  f32x2 cy;
  auto eval = [&](u32x4 (&v)[15]) {
    u32 n = 0; f32x2 q[4];
#pragma unroll
    for (int i = 0; i < 15; i++) {
      n += v[i][1] + v[i][3];
      const f32x2 x = {__uint_as_float(v[i][0]), __uint_as_float(v[i][2])};
      const int ch = (i + 1) & 3;
      q[ch] = i < 4 ? x : cfma(k.L4, q[ch], x);
    }
    cy = cfma(k.L1, cfma(k.L1, cfma(k.L1, q[0], q[1]), q[2]), q[3]);
    return __all(n == 2u * rt);
  };
  __builtin_amdgcn_s_sleep(6);
  issue(va);
  __builtin_amdgcn_s_sleep(10);
  issue(vb);
  if (!eval(va)) {
    issue(va);
    if (!eval(vb) && !eval(va)) {
#pragma nounroll
      for (u32 spin = 0; spin < SPIN_CAP; spin++) { u32x4 vc[15]; issue(vc); if (eval(vc)) break; }
    }
  }
#pragma unroll
  for (int s = 0; s < 8; s += 2) e[s] = *(LAS f32x2*)(size_t)(scr + s * 1536);
  const f32x2 c2 = cfma(k.l32, cy, a01), c4 = cfma(k.l64, cy, a03), c6 = cfma(k.l32, c4, a45);
  const f32x2 cin[8] = {cy, cfma(k.l16, cy, e[0]), c2, cfma(k.l16, c2, e[2]), c4, cfma(k.l16, c4, e[4]), c6, cfma(k.l16, c6, e[6])};
#pragma unroll
  for (int s = 0; s < 8; s++) *(LAS f32x2*)(size_t)(scr + s * 1536) = cin[s];
}

#define LBAR() do { BAR(); const u32 t_ = (u32)__builtin_amdgcn_s_memtime(), r_ = (u32)__builtin_amdgcn_s_memrealtime(); tsv = lane == tsn ? t_ : tsv; tsr = lane == tsn ? r_ : tsr; tsn++; } while (0)
DI void loader(u64x4 kv, int rt, int g, int j, int u, int lane, u32 lds0, u32 tag) {
  const bool ssm = g == 4;
  const u32 wbytes = ssm ? D * ROWB : D * F * 2;
  const int r8 = lane >> 3, gp = lane & 7;
  const int cgr = ((((gp >> 1) ^ ((r8 >> 1) & 3)) << 1) | (gp & 1));
  const int rgr = gp ^ (4 * u + (r8 >> 1));
  const int slice = 3 * g + j;
  const int kA = 16 * u + 8 * (r8 >> 2) + (r8 & 3);
  const int R = 8 * u + r8;
  const u32 voffA = ssm ? (192 * j + R) * ROWB + rgr * 16 : kA * (F * 2) + slice * 768 + cgr * 16;
  const int tok = ssm ? 16 * ((R >> 2) & 3) + 8 * (R & 1) + ((R >> 1) & 1) : R;
  const u32 voffX = (rt * 128 + tok) * ROWB + rgr * 16;
  const u32 ldsW = lds0 + u * 1024, ldsT = lds0 + TOFF;
  const u32 aKt = ssm ? 128 : 64 * F * 2, aPar = ssm ? 0 : 32 * F * 2, pstr = ssm ? 32 * ROWB : 128, a2 = ssm ? 16 * ROWB : 4 * F * 2, xstr = (ssm ? 2 : 16) * ROWB;
  const int rot = (rt >= 10 ? rt - 10 : rt) & ~1;
  asm volatile("s_waitcnt lgkmcnt(0)" : "+s"(kv) : "v"(voffA), "v"(voffX), "s"(aKt), "s"(rot));
  const rsrc_t rX = mk((void*)kv[0], S * ROWB), rA0 = mk((void*)(ssm ? kv[2] : kv[1]), wbytes), rA1 = mk((void*)(ssm ? kv[3] : kv[1]), wbytes);
  auto ktile = [&](int kap) { int kt = kap + rot; return kt >= 9 ? kt - 9 : kt; };
  auto issueA = [&](int kap, int h) { dma12(h ? rA1 : rA0, ldsW + ((2 * kap + h) & 3) * WSLOT, voffA, ktile(kap) * aKt + h * aPar, a2, pstr, pstr); };
  auto issueX = [&](int kap, int h) { dma4(rX, ldsT + (kap & 3) * TSLOT + h * 8192 + u * 1024, voffX, h * 64 * ROWB + ktile(kap) * 128, xstr); };
  issueA(0, 0); issueX(0, 0); issueX(0, 1); issueA(0, 1);
  __builtin_amdgcn_s_setprio(3);
  waitv<12>(); BAR();
  issueX(1, 0); issueX(1, 1); issueA(1, 0);
#pragma nounroll
  for (int kap = 0; kap < 7; kap++) {
    u32 l1 = ldsW + ((2 * kap + 3) & 3) * WSLOT, s1 = ktile(kap + 1) * aKt + aPar, l2 = ldsW + ((2 * kap) & 3) * WSLOT, s2 = ktile(kap + 2) * aKt;
    u32 lx = ldsT + ((kap + 2) & 3) * TSLOT + u * 1024, lx2 = lx + 8192, sx = ktile(kap + 2) * 128, sx2 = sx + 64 * ROWB;
    asm volatile("" : "+s"(l1), "+s"(s1), "+s"(l2), "+s"(s2), "+s"(lx), "+s"(lx2), "+s"(sx), "+s"(sx2));
    waitv<16>(); BAR(); dma12(rA1, l1, voffA, s1, a2, pstr, pstr); dma4(rX, lx, voffX, sx, xstr);
    waitv<16>(); BAR(); dma12(rA0, l2, voffA, s2, a2, pstr, pstr); dma4(rX, lx2, voffX, sx2, xstr);
  }
  waitv<16>(); BAR(); issueA(8, 1);
  const Params p = reload_params();
  const u32 voffL = lane * 16;
  const rsrc_t rWS = mk(p.ws, WS_END), rB0 = mk(ssm ? p.cre : p.w2, wbytes), rB1 = mk(ssm ? p.cim : p.w2, wbytes);
  const u32 voffB = ssm ? voffA : R * ROWB + j * 384 + cgr * 16;
  const u32 bJ = ssm ? 384 : 384 * ROWB, bPar = ssm ? 0 : 192 * ROWB, bI = ssm ? 128 : 32 * ROWB, b0 = ssm ? 0 : g * 1152 * ROWB;
  const int j1 = j == 2 ? 0 : j + 1, j2 = j == 0 ? 2 : j - 1;
  const u32 st3 = 96 * ROWB - 2 * pstr, pb = WS_P + (rt * 15 + 3 * g) * 98304 + u * 4096;
  auto issueB = [&](int s, int jj, int i) { dma12((i & 1) ? rB1 : rB0, ldsW + (s & 3) * WSLOT, voffB, b0 + jj * bJ + (i & 1) * bPar + (i >> 1) * bI, 16 * ROWB, pstr, st3); };
  auto issueP = [&](int s, int jj, int i) { dma8lin(rWS, ldsT + (s & 3) * TSLOT + u * 4096, voffL, pb + jj * 98304 + i * 16384); };
  waitv<12>(); BAR();
#pragma unroll
  for (int k = 0; k < 4; k++) {
    const u32 dst = ssm ? (k < 3 ? AX_TAB + (u * 3 + k) * 1024 : AX_S) : AX_T + k * 1024;
    const u32 src = ssm ? (k < 3 ? WS_TAB + j * 6144 + (u * 3 + k) * 1024 : WS_LN + rt * 5120 + 4096) : WS_LN + rt * 5120 + k * 1024;
    dma_sc1(rWS, lds0 + (18 & 3) * WSLOT + dst, voffL, src);
  }
  waitv<4>();
  BAR(); issueB(19, j, 0); waitv<12>();
  BAR(); issueB(20, j, 1); waitv<12>();
  if (ssm) { const CarryPre k = carry_pre(lane, lds0); BAR(); if (u == 1) ssm_carry(p, k, rt, j, lane, lds0); WAITL(); }
  BAR();
  issueB(21, j, 2); waitv<12>();
  if (ssm) BAR();
  BAR(); issueB(22, j, 3); waitv<12>();
  BAR(); issueB(23, j, 4); issueP(23, j, 4); waitv<20>();
  BAR(); issueB(24, j, 5); issueP(24, j, 5);
#pragma nounroll
  for (int n = 0; n < 3; n++) {
    const int i = n ? 2 * n - 2 : 4;
    u32 w1 = b0 + j1 * bJ + (i >> 1) * bI, w2 = b0 + j2 * bJ + (i >> 1) * bI, w3 = w1 + bPar, w4 = w2 + bPar;
    u32 p1 = pb + j1 * 98304 + i * 16384, p2 = pb + j2 * 98304 + i * 16384, p3 = p1 + 16384, p4 = p2 + 16384;
    asm volatile("" : "+s"(w1), "+s"(w2), "+s"(w3), "+s"(w4), "+s"(p1), "+s"(p2), "+s"(p3), "+s"(p4));
    waitv<20>(); BAR(); dma12(rB0, ldsW + 1 * WSLOT, voffB, w1, 16 * ROWB, pstr, st3); dma8lin(rWS, ldsT + 1 * TSLOT + u * 4096, voffL, p1);
    waitv<20>(); BAR(); dma12(rB0, ldsW + 2 * WSLOT, voffB, w2, 16 * ROWB, pstr, st3); dma8lin(rWS, ldsT + 2 * TSLOT + u * 4096, voffL, p2);
    waitv<20>(); BAR(); dma12(rB1, ldsW + 3 * WSLOT, voffB, w3, 16 * ROWB, pstr, st3); dma8lin(rWS, ldsT + 3 * TSLOT + u * 4096, voffL, p3);
    waitv<20>(); BAR(); dma12(rB1, ldsW + 0 * WSLOT, voffB, w4, 16 * ROWB, pstr, st3); dma8lin(rWS, ldsT + 0 * TSLOT + u * 4096, voffL, p4);
  }
  waitv<20>();
  BAR();
  {
    const u32 l = lds0 + WSLOT;
    const u32 vo = (lane & 31) < 24 ? (rt * 128 + u * 64 + (g & 3) * 16 + (lane >> 5)) * ROWB + j * 384 + (lane & 31) * 16 : 0x7fffffffu;
#pragma unroll
    for (int k = 0; k < 8; k++) dma(rX, l + (u * 8 + k) * 1024, vo, k * 2 * ROWB);
    dma(mk(u ? p.dskip : p.b2, ROWB), l + 16384 + u * 1024, voffL + j * 384, 0);
  }
  waitv<9>(); BAR(); BAR(); waitv<0>();
  BAR();
}

DI void helper(const Params& p, int rt, int g, int j, int h, int lane, u32 lds0, u32 tag) {
  u32 tsv = 0, tsr = 0; int tsn = 0;
  const bool ssm = g == 4;
  const int slice = 3 * g + j, q = lane >> 4, jl = lane & 15;
  const rsrc_t rWS = mk(p.ws, WS_END);
  char* const hsR = p.ws + WS_RSP + blockIdx.x * 4;
  const u32 hs0 = ld_flag(hsR);
  u32 hsn = 0;
  auto startup = [&]() {
    hsn = __builtin_amdgcn_readfirstlane(hs0) + 1u;
    if (h == 0) st_word(hsR - 1024, hsn); else if (lane < 2) st_flag(p.ws, lane ? FL_P + rt * 15 + slice : FL_O + (rt * 3 + j) * 5 + g, 0u);
    if (ssm) { st16((u32x4){0u, 0u, 0u, 0u}, rWS, (192 * j + 64 * h + lane) * 16, WS_EG + (15 - rt) * 9216); st16((u32x4){0u, 0u, 0u, 0u}, rWS, (192 * j + 128 + lane) * 16, WS_EG + (15 - rt) * 9216); }
  };
  __builtin_amdgcn_s_setprio(2);
  {
    const int e = (lane >> 2) & 3, z = 2 * (q & 1) + (e >> 1);
    f32x4 c = {0.f, 0.f, 0.f, 0.f};
    u32x4 sel[12];
#pragma unroll
    for (int n = 0; n < 12; n++) { const u32 o = jl == n ? ONES : 0u; sel[n] = (u32x4){o, o, o, o}; PIN(sel[n]); }
    PIN(c);
    auto pass = [&](int sl) {
      bf16x8 f[12];
      if (ssm) {
        const u32 a = lds0 + (rt & 1) * 2 * WSLOT + h * 12288 + jl * 128 + ((q ^ (jl >> 1)) * 16);
#pragma unroll
        for (int n = 0; n < 6; n++) { f[n] = rd128(a, sl * WSLOT + n * 2048); f[6 + n] = rd128(a ^ 64, sl * WSLOT + n * 2048); }
      } else {
#pragma unroll
        for (int n = 0; n < 12; n++) f[n] = rd_tr(lds0 + (rt & 1) * 2 * WSLOT + h * 12288 + (4 * q + e) * 128 + (((n & 3) ^ z) * 32) + (lane & 3) * 8, sl * WSLOT + (n >> 2) * 4096);
      }
#pragma unroll
      for (int n = 0; n < 12; n++) PIN(f[n]);
      asm volatile("s_nop 1");
      if (ssm) {
#pragma unroll
        for (int n = 0; n < 12; n++) MMAN(c, f[n], sel[6 * sl + n % 6]);
        MFENCE();
      } else {
#pragma unroll
        for (int n = 0; n < 12; n++) MMAN(c, f[n], sel[n]);
        MFENCE();
      }
    };
    const int odd = rt & 1;
    const u32 cso = jl < 12 ? ((ssm ? (jl >= 6 ? 192 - 96 : 0) + 96 * h : 192 * h) + 16 * jl + 4 * q) * 4 : 0x7fffffffu;
    LBAR();
    if (odd) { LBAR(); startup(); LBAR(); }
    if (rt < 9) pass(0);
    LBAR();
    if (rt < 9) { pass(1); st16(__builtin_bit_cast(u32x4, c), rWS, cso, WS_CS + (slice * 16 + rt) * 1536); }
    LBAR();
    if (!odd) { startup(); LBAR(); LBAR(); }
  }
  LBAR();
  u32 pv = ld_flag(hsR);
  waitv<0>(); LBAR();
  if (h == 0) { if (!__all(pv == hsn)) spin_all(hsR, hsn); st_word(hsR + 1024, 1u); }
  LBAR(); LBAR(); LBAR(); LBAR();
  const char* fGo = p.ws + WS_FLAGS + FL_GO * 128;
  pv = ld_flag(fGo);
  LBAR(); LBAR();
  if (!__all(pv == 1u)) spin_all(fGo, 1u);
  const int cc = 192 * h + 4 * (lane < 48 ? lane : lane - 48);
  f32x4 cp[9];
#pragma unroll
  for (int k = 0; k < 9; k++) cp[k] = __builtin_bit_cast(f32x4, __builtin_amdgcn_raw_buffer_load_b128(rWS, cc * 4, WS_CS + (slice * 16 + k) * 1536, 16));
  const u32x2 bb = *(const GAS u32x2*)(p.b1 + (ssm ? 0 : slice * 384) + cc);
  LBAR(); LBAR(); SB();
  f32x4 cs = ((cp[0] + cp[1]) + (cp[2] + cp[3])) + ((cp[4] + cp[5]) + (cp[6] + cp[7])) + cp[8];
  u32x4 aw[4];
  PIN(cs);
  auto auxw = [&](int k) { float c1, c2, c3; split3(cs[k], c1, c2, c3); const float b = (k & 1) ? bfhi(bb[k >> 1]) : bflo(bb[k >> 1]);
    aw[k] = (u32x4){cvtpk(c1, c2), cvtpk(c1, c2), cvtpk(c3, c1), cvtpk(b, b)}; PIN(aw[k]); };
  const u32 ax = lds0 + (18 & 3) * WSLOT;
  LBAR(); SB();
  if (!ssm) { auxw(0); auxw(1); }
  LBAR(); SB();
  if (!ssm) { auxw(2); auxw(3); }
  LBAR(); SB();
  if (ssm) *(LAS f32x4*)(size_t)(ax + AX_W + cc * 4) = cs;
  else {
#pragma unroll
    for (int k = 0; k < 4; k++) wr128(ax + AX_W + cc * 4, k * 1600, aw[k]);
  }
  WAITL(); LBAR();
  if (ssm) { const CarryPre k = carry_pre(64 + 64 * h + lane, lds0); LBAR(); ssm_carry(p, k, rt, j, 64 + 64 * h + lane, lds0); WAITL(); LBAR(); }
  LBAR();
  char* const fMine = p.ws + WS_FLAGS + (FL_P + rt * 15 + slice) * 128;
  const char* fP = p.ws + WS_FLAGS + (FL_P + rt * 15 + 3 * g + ((lane & 1) ? (j == 2 ? 0 : j + 1) : (j == 0 ? 2 : j - 1))) * 128;
  LBAR(); if (h == 1 && !ssm) st_word(fMine, 2u);
  LBAR(); if (h == 1 && ssm) st_word(fMine, 1u);
#pragma unroll
  for (u32 lv = 1; lv < 4; lv++) {
    if (lv == 1) pv = ld_flag(fP);
    LBAR(); if (h == 0 && !__all(pv >= lv)) spin_ge(fP, lv);
    LBAR(); if (h == 1 && lv == 1) st_word(fMine, 2u);
    LBAR(); pv = ld_flag(fP);
    LBAR(); if (h == 1 && lv == 1) st_word(fMine, 3u);
  }
  LBAR(); LBAR(); LBAR(); LBAR();
  LBAR();
  if (h == 1) st_flag(p.ws, FL_O + (rt * 3 + j) * 5 + g, tag);
  else if (!ssm) {
    const int k = lane & 3;
    const char* fa = p.ws + WS_FLAGS + (FL_O + (rt * 3 + j) * 5 + (k < g ? k : k + 1)) * 128;
    u32 v = 0;
#pragma nounroll
    for (u32 i = 0; i < SPIN_CAP; i++) { v = ld_flag(fa); if (__all(v == tag || k == 3)) break; }
    const bool yok = __all(v == tag);
    *(LAS u32*)(size_t)lds0 = yok; WAITL(); BAR();
    if (!yok) { spin_all(fa, tag); BAR(); }
  }
  if (h == 1) { ((u32*)(p.stamps + 4096))[blockIdx.x * 64 + lane] = tsv; ((u32*)(p.stamps + 4096))[16384 + blockIdx.x * 64 + lane] = tsr; }
}

template <bool SSM>
DI void phaseB(const Params& p, int rt, int g, int j, int w, int lane, u32 lds0, u32 tag) {
  asm volatile("" : "+v"(lane));
  const int rg = w >> 2, cg = w & 3, q = lane >> 4, e = (lane >> 2) & 3, pp = lane & 3, jl = lane & 15;
  const int z = 2 * (q & 1) + (e >> 1);
  u32 wlo[3], whi[3], tb[2];
#pragma unroll
  for (int n = 0; n < 3; n++) {
    const int t = 3 * cg + n;
    wlo[n] = SSM ? lds0 + (16 * t + jl) * 128 + ((q ^ (jl >> 1)) * 16) : lds0 + (t >> 2) * 4096 + (4 * q + e) * 128 + (((t & 3) ^ z) * 32) + pp * 8;
    whi[n] = SSM ? (wlo[n] ^ 64) : wlo[n] + 2 * WSLOT;
  }
  tb[0] = SSM ? lds0 + TOFF + rg * 8192 + (q >> 1) * 2048 + (pp >> 1) * 1024 + (((8 * (q & 1) + e) * 16) ^ ((pp >> 1) * 64)) + 8 * (pp & 1) : lds0 + TOFF + rg * 4096 + lane * 16;
  tb[1] = tb[0] ^ 64;
  float mean = 0.f, rstd = 0.f;
  if constexpr (!SSM) { const u32 sa = lds0 + (18 & 3) * WSLOT + AX_S + (64 * rg + 16 * g + jl) * 4; mean = rdf(sa, 0); rstd = rdf(sa, 512); }
  f32x4 ob[3][4];
  auto RW = [&](int sl, int kb, int n, int part) {
    if constexpr (SSM) return rd128((kb ? whi[n] : wlo[n]) + (sl >> 1) * 2 * WSLOT, (sl & 1) * WSLOT);
    else return rd_tr1(sl < 2 ? wlo[n] : whi[n], (sl & 1) * WSLOT + kb * 12288 + part * 2048);
  };
  auto RT = [&](int sl, int kb, int T, int part) {
    if constexpr (SSM) return rd_tr1(tb[part], sl * TSLOT + kb * 4096 + T * 256);
    else return rd128(tb[0], sl * TSLOT + kb * 8192 + T * 1024);
  };
  bf16x8 wf[3], tp[2];
  auto step = [&](auto slc, auto&& mid) {
    constexpr int SL = decltype(slc)::value & 3, NS = (SL + 1) & 3;
    half<SSM ? 1 : 0, (decltype(slc)::value > 3)>(ob, wf, tp, [&](int n, int part) { return RW(SL, 1, n, part); }, [&](int T, int part) { return RT(SL, 0, T, part); }, [&](int T, int part) { return RT(SL, 1, T, part); }, mid);
    half<SSM ? 1 : 0>(ob, wf, tp, [&](int n, int part) { return RW(NS, 0, n, part); }, [&](int T, int part) { return RT(SL, 1, T, part); }, [&](int T, int part) { return RT(NS, 0, T, part); }, [](int) {});
  };
  using I0 = std::integral_constant<int, 0>; using I1 = std::integral_constant<int, 1>; using I2 = std::integral_constant<int, 2>; using I3 = std::integral_constant<int, 3>;
  if constexpr (!SSM) waitv<4>();
  __builtin_amdgcn_s_waitcnt(0xC07F); WAITL(); BAR();
#pragma unroll
  for (int n = 0; n < 3; n++) { if constexpr (SSM) wf[n] = RW(3, 0, n, 0); else wf[n] = cat(RW(3, 0, n, 0), RW(3, 0, n, 1)); }
#pragma unroll
  for (int T = 0; T < 2; T++) { if constexpr (SSM) tp[T] = cat(RT(3, 0, T, 0), RT(3, 0, T, 1)); else tp[T] = RT(3, 0, T, 0); }
  auto barm = [](int i) { if (i == 11) BAR(); };
  step(std::integral_constant<int, 7>{}, [](int) {});
  step(I0{}, [&](int i) { if (i == 3) { if constexpr (SSM) waitv<8>(); BAR(); } });
#pragma nounroll
  for (int it = 0; it < 4; it++) { step(I1{}, barm); step(I2{}, [&](int i) { if (i == 11) { waitv<4>(); BAR(); } }); step(I3{}, barm); step(I0{}, [&](int i) { if (i == 11) { waitv<0>(); BAR(); } }); }
  MFENCE();
  const rsrc_t rWS = mk(p.ws, WS_END);
  const u32 xb = WS_XB + (rt * 3 + j) * 32 * XBW + w * XBW;
  f32x4 mine[3];
#pragma unroll
  for (int T = 0; T < 4; T++) {
    if (SSM || T != g) {
      const u32 o = xb + T * 8 * XBW + g * 1536;
      const u32x4 a = {cvtpk(ob[0][T][0], ob[0][T][1]), cvtpk(ob[0][T][2], ob[0][T][3]), cvtpk(ob[1][T][0], ob[1][T][1]), cvtpk(ob[1][T][2], ob[1][T][3])};
      const u32 b0 = cvtpk(ob[2][T][0], ob[2][T][1]), b1 = cvtpk(ob[2][T][2], ob[2][T][3]);
      const auto s0 = __builtin_amdgcn_permlane32_swap(b0, b0, false, false), s1 = __builtin_amdgcn_permlane32_swap(b1, b1, false, false);
      st16(a, rWS, lane * 16, o);
      if (lane < 32) st16((u32x4){b0, b1, s0[1], s1[1]}, rWS, lane * 16, o + 1024);
    } else {
#pragma unroll
      for (int n = 0; n < 3; n++) mine[n] = ob[n][T];
    }
  }
  if constexpr (SSM) {
    waitv<0>(); BAR();
    STAMP(7);
  } else {
    waitv<0>(); BAR();
    const int tok = rt * 128 + 64 * rg + 16 * g + jl, col = 192 * j + 48 * cg + 4 * q;
    u32x2 xv[3], bv[3], dv[3];
#pragma unroll
    for (int n = 0; n < 3; n++) {
      const u32 cb = lds0 + WSLOT + (48 * cg + 4 * q + 16 * n) * 2;
      xv[n] = *(LAS u32x2*)(size_t)(cb + (16 * rg + jl) * 512); bv[n] = *(LAS u32x2*)(size_t)(cb + 16384); dv[n] = *(LAS u32x2*)(size_t)(cb + 17408);
    }
    const u32 o = xb + g * 8 * XBW;
    u32x4 a[3]; u32x2 b[3];
    BAR();
#pragma unroll
    for (int k = 0; k < 3; k++) {
      const int src = k < g ? k : k + 1;
      a[k] = __builtin_amdgcn_raw_buffer_load_b128(rWS, lane * 16, o + src * 1536, 16);
      b[k] = __builtin_amdgcn_raw_buffer_load_b64(rWS, (lane & 31) * 16 + (lane >> 5) * 8, o + src * 1536 + 1024, 16);
    }
    const bool yok = __builtin_amdgcn_readfirstlane(*(LAS u32*)(size_t)lds0);
    u32x4 ya; u32x2 yb;
    auto ldy = [&]() { ya = __builtin_amdgcn_raw_buffer_load_b128(rWS, lane * 16, o + 4 * 1536, 16); yb = __builtin_amdgcn_raw_buffer_load_b64(rWS, (lane & 31) * 16 + (lane >> 5) * 8, o + 4 * 1536 + 1024, 16); };
    if (yok) ldy();
    f32x4 gate[3], base[3];
#pragma unroll
    for (int n = 0; n < 3; n++) {
#pragma unroll
      for (int c = 0; c < 4; c++) {
        float G = mine[n][c] + ((c & 1) ? bfhi(bv[n][c >> 1]) : bflo(bv[n][c >> 1]));
#pragma unroll
        for (int k = 0; k < 3; k++) { const u32 v = n < 2 ? a[k][2 * n + (c >> 1)] : b[k][c >> 1]; G += (c & 1) ? bfhi(v) : bflo(v); }
        const float xx = (c & 1) ? bfhi(xv[n][c >> 1]) : bflo(xv[n][c >> 1]);
        const float dd = (c & 1) ? bfhi(dv[n][c >> 1]) : bflo(dv[n][c >> 1]);
        gate[n][c] = G * __builtin_amdgcn_rcpf(1.f + __builtin_amdgcn_exp2f(-1.4426950408889634f * G));
        base[n][c] = xx + (xx - mean) * rstd * dd * gate[n][c];
      }
    }
#pragma unroll
    for (int n = 0; n < 3; n++) { PIN(gate[n]); PIN(base[n]); }
    if (!yok) { BAR(); ldy(); }
#pragma unroll
    for (int n = 0; n < 3; n++) {
      const u32 y0 = n < 2 ? ya[2 * n] : yb[0], y1 = n < 2 ? ya[2 * n + 1] : yb[1];
      *(GAS f32x4*)(p.out + tok * D + col + 16 * n) = base[n] + (f32x4){bflo(y0), bfhi(y0), bflo(y1), bfhi(y1)} * gate[n];
    }
    STAMP(7);
  }
}

DI void ffn_compute(int rt, int g, int j, int w, int lane, u32 lds0, u32 tag) {
  const int rg = w >> 2, cg = w & 3, q = lane >> 4, e = (lane >> 2) & 3, pp = lane & 3, jl = lane & 15;
  const int z = 2 * (q & 1) + (e >> 1);
  u32 wc[4];
#pragma unroll
  for (int n = 0; n < 4; n++) { const int t = 6 * cg + n; wc[n] = lds0 + (t >> 2) * 4096 + (4 * q + e) * 128 + (((t & 3) ^ z) * 32) + pp * 8; }
  const u32 xbase = lds0 + TOFF + (64 * rg + jl) * 128 + ((q ^ (jl >> 1)) * 16);
  f32x4 acc[6][4];
#pragma unroll
  for (int n = 0; n < 6; n++)
#pragma unroll
    for (int T = 0; T < 4; T++) { acc[n][T] = (f32x4){0.f, 0.f, 0.f, 0.f}; asm volatile("" : "+v"(acc[n][T])); }
  bf16x8 wp[2], t[4];
  BAR();
  wp[0] = rd_tr(wc[0], 0); wp[1] = rd_tr(wc[1], 0);
#pragma unroll
  for (int T = 0; T < 4; T++) t[T] = rd128(xbase, T * 2048);
  u32 xs = 0, xn = 0, n0 = 0, n1 = 0; int dw = 2 * WSLOT;
#pragma nounroll
  for (int it = 0; it < 9; it++) {
    stepA<0>(acc, wp, t, wc, n0, n1, xn, [&]() { xn = (xbase ^ 64) + xs; PIN(xn); xs = (xs + TSLOT) & (4 * TSLOT - 1); });
    stepA<1>(acc, wp, t, wc, n0, n1, xn, [&]() { xn = xbase + xs; n0 = wc[0] + dw; n1 = wc[1] + dw; wc[2] += dw; wc[3] += dw; PIN(xn); PIN(n0); PIN(n1); PIN(wc[2]); PIN(wc[3]); });
    wc[0] = n0; wc[1] = n1; dw = -dw;
  }
  const Params p = reload_params();
  const u32 ax = lds0 + (18 & 3) * WSLOT;
  float k1[4], k2[4], sg[4];
#pragma unroll
  for (int T = 0; T < 4; T++) {
    u32x4 v = __builtin_bit_cast(u32x4, rd128(ax + AX_T + (64 * rg + jl) * 16, T * 256));
    if (q != 0) v = (u32x4){0u, 0u, 0u, 0u};
    t[T] = __builtin_bit_cast(bf16x8, v);
    const float r = rdf(ax + AX_S + 512 + (64 * rg + jl) * 4, T * 64);
    sg[T] = rdf(ax + AX_S + 1024 + (64 * rg + jl) * 4, T * 64);
    k1[T] = -2.3022081985f * r; k2[T] = -0.10294324f * r * r * r;
  }
  {
    bf16x8 a[6];
#pragma unroll
    for (int n = 0; n < 6; n++) a[n] = rd128(ax + AX_W + ((jl & 3) * 100 + 24 * cg + (jl >> 2)) * 16, n * 64);
    asm volatile("s_nop 1" : "+v"(t[0]), "+v"(t[1]), "+v"(t[2]), "+v"(t[3]));
#pragma unroll
    for (int n = 0; n < 6; n++)
#pragma unroll
      for (int T = 0; T < 4; T++) MMA(acc[n][T], a[n], t[T]);
    MFENCE();
  }
  const rsrc_t rWS = mk(p.ws, WS_END);
  const u32 pg = WS_P + (rt * 15 + 3 * g + j) * 98304 + (cg >> 1) * 16384 + (cg & 1) * 8192 + rg * 4096;
  const u32 pl = lds0 + TOFF + (cg & 1) * 8192 + rg * 4096 + lane * 16, pl0 = pl + ((19 + (cg >> 1)) & 3) * TSLOT, pl1 = pl + ((21 + (cg >> 1)) & 3) * TSLOT;
#pragma unroll
  for (int bb = 0; bb < 3; bb++) {
    const int b = (bb + 2) % 3;
#pragma unroll
    for (int T = 0; T < 4; T++) {
      const u32x4 P = gelu_frag(acc[2 * b][T], acc[2 * b + 1][T], k1[T], k2[T], sg[T]);
      st16(P, rWS, lane * 16, pg + b * 32768 + T * 1024);
      if (b < 2) wr128(b ? pl1 : pl0, T * 1024, P);
      SB();
    }
    if (bb == 0) BAR();
  }
  phaseB<false>(p, rt, g, j, w, lane, lds0, tag);
}

DI void ssm_compute(int rt, int j, int w, int lane, u32 lds0, u32 tag) {
  const int rg = w >> 2, cg = w & 3; int q = lane >> 4, jl = lane & 15;
  u32 wb = lds0 + (16 * cg + jl) * 128 + ((q ^ (jl >> 1)) * 16);
  const u32 xb0 = lds0 + TOFF + (64 * rg + jl) * 128 + ((q ^ (jl >> 1)) * 16);
  f32x4 ar[3][4], ai[3][4];
#pragma unroll
  for (int n = 0; n < 3; n++)
#pragma unroll
    for (int T = 0; T < 4; T++) { ar[n][T] = (f32x4){0.f, 0.f, 0.f, 0.f}; ai[n][T] = (f32x4){0.f, 0.f, 0.f, 0.f}; asm volatile("" : "+v"(ar[n][T]), "+v"(ai[n][T])); }
  bf16x8 wf[3], tp[2];
  auto barm = [](int i) { if (i == 11) BAR(); };
  __builtin_amdgcn_s_waitcnt(0xC07F);
  BAR();
#pragma unroll
  for (int n = 0; n < 3; n++) wf[n] = rd128(wb, n * 8192);
  tp[0] = rd128(xb0, 0); tp[1] = rd128(xb0, 2048);
  u32 xs = 0, x0 = xb0, x1 = xb0 ^ 64, wb1 = wb ^ 64; int dw = 2 * WSLOT;
#pragma nounroll
  for (int it = 0; it < 9; it++) {
    half<2>(ar, wf, tp, [&](int n, int) { return rd128(wb1, n * 8192); }, [&](int T, int) { return rd128(x0, T * 2048); }, [&](int T, int) { return rd128(x1, T * 2048); }, barm);
    half<2>(ar, wf, tp, [&](int n, int) { return rd128(wb, WSLOT + n * 8192); }, [&](int T, int) { return rd128(x1, T * 2048); }, [&](int T, int) { return rd128(x0, T * 2048); },
            [&](int i) { if (i == 10) { wb += dw; PIN(wb); xs = (xs + TSLOT) & (4 * TSLOT - 1); } });
    half<2>(ai, wf, tp, [&](int n, int) { return rd128(wb1, WSLOT + n * 8192); }, [&](int T, int) { return rd128(x0, T * 2048); }, [&](int T, int) { return rd128(x1, T * 2048); },
            [&](int i) { if (i == 7) { wb1 += dw; x0 = xb0 + xs; PIN(wb1); PIN(x0); dw = -dw; } if (i == 11) BAR(); });
    half<2>(ai, wf, tp, [&](int n, int) { return rd128(wb, n * 8192); }, [&](int T, int) { return rd128(x1, T * 2048); }, [&](int T, int) { return rd128(x0, T * 2048); },
            [&](int i) { if (i == 4) { x1 = x0 ^ 64; PIN(x1); } });
  }
  const Params p = reload_params();
  asm volatile("" : "+v"(lane));
  q = lane >> 4; jl = lane & 15;
  const u32 ax = lds0 + (18 & 3) * WSLOT;
  MFENCE();
  f32x2 lc[3];
  const u32 tb = ax + AX_TAB + (16 * cg + jl) * 32;
  const u32 scr = lds0 + TOFF + (22 & 3) * TSLOT + ((4 * rg + q) * 192 + 16 * cg + jl) * 8;
  f32x2 e0[3]; float csr[3], csi[3];
#pragma unroll
  for (int b = 0; b < 3; b++) { csr[b] = rdf(ax + AX_W + (16 * cg + jl) * 4, b * 256); csi[b] = rdf(ax + AX_W + (16 * cg + jl) * 4, 768 + b * 256); }
#pragma unroll
  for (int T = 0; T < 4; T++) {
    const f32x4 r = rdf4(ax + AX_S + 512 + (64 * rg + 4 * q) * 4, T * 64), nm = rdf4(ax + AX_S + (64 * rg + 4 * q) * 4, T * 64);
#pragma unroll
    for (int b = 0; b < 3; b++) { ar[b][T] = ar[b][T] * r + nm * csr[b]; ai[b][T] = ai[b][T] * r + nm * csi[b]; }
  }
  f32x2 l8[3];
#pragma unroll
  for (int b = 0; b < 3; b++) {
    lc[b] = *(LAS f32x2*)(size_t)(tb + b * 2048);
    l8[b] = *(LAS f32x2*)(size_t)(tb + b * 2048 + 16);
    f32x2 hr = {ar[b][0][0], ar[b][0][1]}, hi = {ai[b][0][0], ai[b][0][1]};
#pragma unroll
    for (int k = 1; k < 8; k++) {
      const f32x2 ur = {ar[b][k >> 1][(k & 1) * 2], ar[b][k >> 1][(k & 1) * 2 + 1]}, ui = {ai[b][k >> 1][(k & 1) * 2], ai[b][k >> 1][(k & 1) * 2 + 1]};
      const f32x2 nr = lc[b][0] * hr + (ur - lc[b][1] * hi), ni = lc[b][0] * hi + (ui + lc[b][1] * hr);
      hr = nr; hi = ni;
    }
    e0[b] = (f32x2){hr.x, hi.x};
    *(LAS f32x2*)(size_t)(scr + b * 512) = (f32x2){l8[b].x * hr.x - l8[b].y * hi.x + hr.y, l8[b].x * hi.x + l8[b].y * hr.x + hi.y};
  }
  WAITL(); BAR(); SB();
#pragma unroll
  for (int b = 0; b < 3; b++) {
    const f32x2 cf = *(LAS f32x2*)(size_t)(tb + b * 2048 + 8); const float c2r = cf.x, c2i = cf.y;
#pragma unroll
    for (int T = 0; T < 4; T++) {
      const f32x4 nr = c2r * ar[b][T] - c2i * ai[b][T], ni = -c2r * ai[b][T] - c2i * ar[b][T];
      ar[b][T] = nr; ai[b][T] = ni;
      asm volatile("" : "+v"(ar[b][T]), "+v"(ai[b][T]));
    }
  }
  BAR();
  const rsrc_t rWS = mk(p.ws, WS_END);
  const u32 io0 = w * 2048 + lane * 16, io1 = w * 2048 + 1024 + ((lane * 16) ^ 64);
  const u32 pg = WS_P + (rt * 15 + 12 + j) * 98304;
  f32x2 cins[3];
#pragma unroll
  for (int b = 0; b < 3; b++) { cins[b] = *(LAS f32x2*)(size_t)(scr + b * 512); l8[b] = *(LAS f32x2*)(size_t)(tb + b * 2048 + 16); }
#pragma unroll
  for (int bb = 0; bb < 3; bb++) {
    const int b = (bb + 2) % 3;
    u32x4 H[2][2];
    const f32x2 cin = cins[b];
    const f32x2 sr = {cin.x, l8[b].x * cin.x - l8[b].y * cin.y + e0[b].x}, si = {cin.y, l8[b].x * cin.y + l8[b].y * cin.x + e0[b].y};
    const f32x2 cf = *(LAS f32x2*)(size_t)(tb + b * 2048 + 8); const float c2r = cf.x, c2i = cf.y;
    f32x2 hr = c2r * sr - c2i * si, hi = -c2r * si - c2i * sr;
#pragma unroll
    for (int T = 0; T < 4; T++) {
      f32x2 pr[2], pi[2];
#pragma unroll
      for (int k = 0; k < 2; k++) {
        const f32x2 ur = {ar[b][T][2 * k], ar[b][T][2 * k + 1]}, ui = {ai[b][T][2 * k], ai[b][T][2 * k + 1]};
        const f32x2 nr = lc[b][0] * hr + (ur + lc[b][1] * hi), ni = lc[b][0] * hi + (ui - lc[b][1] * hr);
        hr = nr; hi = ni; pr[k] = nr; pi[k] = ni;
      }
      H[0][0][T] = cvtpk(pr[0].x, pr[1].x); H[0][1][T] = cvtpk(pr[0].y, pr[1].y);
      H[1][0][T] = cvtpk(pi[0].x, pi[1].x); H[1][1][T] = cvtpk(pi[0].y, pi[1].y);
    }
    if (bb == 1) BAR();
#pragma unroll
    for (int k = 0; k < 4; k++) {
      st16(H[k >> 1][k & 1], rWS, (k & 1) ? io1 : io0, pg + (2 * b + (k >> 1)) * 16384);
      if (b < 2) wr128(lds0 + TOFF + ((k & 1) ? io1 : io0), ((19 + 2 * b + (k >> 1)) & 3) * TSLOT, H[k >> 1][k & 1]);
    }
    SB();
  }
  phaseB<true>(p, rt, 4, j, w, lane, lds0, tag);
}

DI void spare(const Params& p, int sid, u32 lds0, u32 tag) {
  const int tid = threadIdx.x;
  const rsrc_t rWS = mk(p.ws, WS_END);
  const int w = __builtin_amdgcn_readfirstlane(tid >> 6), lane = tid & 63;
  if (sid == 15 && w == 11) { st_flag(p.ws, FL_GO, 0u); st16((u32x4){0u, 0u, 0u, 0u}, rWS, lane * 16, WS_ACK); waitv<0>(); hs_leader(p.ws, lane, lds0, STAMPPTR); return; }
  u32 hsn = 0;
  if (w == 11) { hsn = ld_flag(p.ws + WS_RSP + blockIdx.x * 4) + 1u; st_word(p.ws + WS_REQ + blockIdx.x * 4, hsn); }
  else if (w < 8) {
    const int row = tid >> 2, part = tid & 3;
    const GAS u32x4* src = (const GAS u32x4*)(p.x + (sid * 128 + row) * D) + part;
    u32x4 v[18];
#pragma unroll
    for (int i = 0; i < 18; i++) v[i] = src[4 * i];
    float s = 0.f, s2 = 0.f;
#pragma unroll
    for (int i = 0; i < 18; i++) { s = dot2(v[i][0], ONES, s); s2 = dot2(v[i][1], ONES, s2); s = dot2(v[i][2], ONES, s); s2 = dot2(v[i][3], ONES, s2); }
    s += s2; s += dppx<0xb1>(s); s += dppx<0x4e>(s);
    const float mean = s * (1.f / D);
    f32x2 dv = {0.f, 0.f};
#pragma unroll
    for (int i = 0; i < 72; i++) { const u32 wd = v[i >> 2][i & 3]; const f32x2 d = (f32x2){bflo(wd), bfhi(wd)} - mean; dv += d * d; }
    s = dv.x + dv.y; s += dppx<0xb1>(s); s += dppx<0x4e>(s);
    const float ve = s * (1.f / D) + 1e-5f, rstd = __builtin_amdgcn_rsqf(ve), sig = ve * rstd;
    float m1, m2, m3; split3(-mean, m1, m2, m3);
    const float g1 = rbf(sig), g2 = sig - g1;
    const int ir = (row & 64) + 16 * ((row >> 1) & 3) + 4 * ((row >> 4) & 3) + 2 * (row & 1) + ((row >> 3) & 1);
    if (part == 0) wr128(lds0 + row * 16, 0, (u32x4){cvtpk(m1, m1), cvtpk(m2, m2), cvtpk(m1, m3), cvtpk(g1, g2)});
    else *(LAS float*)(size_t)(lds0 + 2048 + (part - 1) * 512 + row * 4) = part == 1 ? mean : (part == 2 ? rstd : sig);
    if (part >= 2) *(LAS float*)(size_t)(lds0 + 4096 + (part - 2) * 512 + ir * 4) = part == 2 ? -mean * rstd : rstd;
  } else if (w == 9 && sid < 9) {
    const int c = sid * 64 + lane;
    const float lr = bf2f(p.lre[c]), li = bf2f(p.lim[c]);
    const float dt = rbf(expf(bf2f(p.logdt[c])));
    const float ex = expf(dt * lr), an = dt * li;
    const float br = ex * cosf(an), bi = ex * sinf(an);
    const float nr = br - 1.f, den = 1.f / (lr * lr + li * li);
    const float cr = (nr * lr + bi * li) * den, ci = (bi * lr - nr * li) * den;
    float ar = br, ai = bi, r8 = 0.f, i8 = 0.f;
#pragma unroll
    for (int k = 0; k < 7; k++) { const float t = ar * ar - ai * ai; ai = 2.f * ar * ai; ar = t; if (k == 2) { r8 = ar; i8 = ai; } }
    st16(__builtin_bit_cast(u32x4, (f32x4){br, bi, 2.f * cr, 2.f * ci}), rWS, c * 32, WS_TAB);
    st16(__builtin_bit_cast(u32x4, (f32x4){r8, i8, ar, ai}), rWS, c * 32 + 16, WS_TAB);
  }
  WAITL(); BAR();
  if (w < 5) st16(__builtin_bit_cast(u32x4, rd128(lds0 + tid * 16, 0)), rWS, tid * 16, WS_LN + sid * 5120);
  if (w == 11) spin_all(p.ws + WS_RSP + blockIdx.x * 4, hsn);
  waitv<0>(); BAR();
  if (sid == 15) return;
  if (w == 11) st_word(p.ws + WS_ACK + blockIdx.x * 4, 1u);
  {
    u32 acc = 0;
    const GAS u32* w2 = (const GAS u32*)p.w2;
#pragma unroll
    for (int i = 0; i < 4; i++) {
      const int L = (i * 15 + sid) * 768 + tid, r = L / 9, kb = r / 1536, rr = r - kb * 1536;
      if (L < 41472) acc ^= w2[((rr >> 5) * 96 + kb * 32 + (rr & 31)) * (ROWB / 4) + (L - r * 9) * 32];
    }
    { const int L = sid * 768 + tid; if (L < 5184) acc ^= ((const GAS u32*)p.cre)[L * 32]; else if (L < 10368) acc ^= ((const GAS u32*)p.cim)[(L - 5184) * 32]; }
    asm volatile("" :: "v"(acc));
  }
}

__global__ __launch_bounds__(768, 3) void fused_kernel(Params p) {
  extern __shared__ __attribute__((aligned(16))) char smem[];
  u64x4 kv; asm volatile("s_load_dwordx8 %0, %1, 0x0" : "=s"(kv) : "s"(__builtin_amdgcn_kernarg_segment_ptr())); SB();
  const u32 lds0 = (u32)(size_t)(LAS char*)smem;
  const int b = blockIdx.x, xcd = b & 7, idx = b >> 3;
  const u32 tag = 1u;
  if (b < 32 && (threadIdx.x >> 7) == 1) {
    u64 pc0; asm volatile("s_getpc_b64 %0" : "=s"(pc0));
    const u32 ln = min((b >> 3) * 128 + (threadIdx.x & 127), (u32)CODE_LINES);
    asm volatile("s_mov_b32 m0, %0\n\ts_nop 0\n\tglobal_load_lds_dword %1, %2" :: "s"(lds0 + TOFF + 3 * TSLOT), "v"(ln * 128), "s"(pc0) : "memory");
  }
  STAMP(0);
  const int tri = idx / 3, j = idx - 3 * tri;
  const int g = tri < 2 ? 4 : xcd >> 1, rt = 8 * (xcd & 1) + (tri < 2 ? (xcd & 6) + tri : tri - 2);
  const int lane = threadIdx.x & 63, w = __builtin_amdgcn_readfirstlane(threadIdx.x >> 6);
  const int sid = xcd * 2 + idx - 30;
  if (__builtin_expect(idx < 30 && w < 2, 1)) { loader(kv, rt, g, j, w, lane, lds0, tag); return; }
  asm volatile("s_waitcnt lgkmcnt(0)" :: "s"(kv));
  if (idx < 30 && w >= 4) {
    if (g < 4) ffn_compute(rt, g, j, w - 4, lane, lds0, tag); else ssm_compute(rt, j, w - 4, lane, lds0, tag);
    return;
  }
  const Params q = reload_params();
  if (idx < 30) helper(q, rt, g, j, w - 2, lane, lds0, tag); else { spare(q, sid, lds0, tag); STAMP(7); }
}

__device__ u32 med_diff(const u64* st, int kind, int a, int b) {
  __shared__ u32 v[256]; __shared__ u32 res; __shared__ u64 t0s[256];
  const int t = threadIdx.x; const int idx = t >> 3, xcd = t & 7;
  const int k = idx >= 30 ? 2 : (idx < 6 ? 1 : 0);
  t0s[t] = st[t * 16]; __syncthreads();
  u64 t0 = ~0ull; for (int i = 0; i < 256; i++) t0 = t0s[i] < t0 ? t0s[i] : t0;
  const bool mx = b >= 100; if (mx) b -= 100;
  const u32* tr = (const u32*)(st + 4096) + 16384 + t * 64;
  auto get = [&](int x) { return x == -1 ? (u32)t0 : x < 64 ? tr[x] : (u32)st[t * 16 + x - 64]; };
  v[t] = k == kind ? get(b) - get(a) : 0xffffffffu; __syncthreads();
  int n = 0, less = 0; for (int i = 0; i < 256; i++) { n += v[i] != 0xffffffffu; less += (v[i] < v[t]) || (v[i] == v[t] && i < t); }
  if (k == kind && less == (mx ? n - 1 : n / 2)) res = v[t];
  __syncthreads(); u32 r = res; __syncthreads(); return r;
}
__global__ void amp_k(const u64* st) {
  u32 v0 = med_diff(st, K0, A0, B0), v1 = med_diff(st, K1, A1, B1);
  v0 = (v0 + UNIT / 2) / UNIT; v1 = (v1 + UNIT / 2) / UNIT; if (v0 > 127) v0 = 127; if (v1 > 110) v1 = 110;
  if (threadIdx.x == 0) { u64 t0 = __builtin_amdgcn_s_memrealtime(); u64 d = (u64)(v0 + 128 * v1) * 100; while (__builtin_amdgcn_s_memrealtime() - t0 < d) __builtin_amdgcn_s_sleep(4); }
}

extern "C" void kernel_launch(void* const* d_in, const int* in_sizes, int n_in, void* d_out, int out_size, void* d_ws, size_t ws_size, hipStream_t stream) {
  Params p;
  p.x = (const u16*)d_in[0]; p.lre = (const u16*)d_in[1]; p.lim = (const u16*)d_in[2]; p.bre = (const u16*)d_in[3]; p.bim = (const u16*)d_in[4];
  p.cre = (const u16*)d_in[5]; p.cim = (const u16*)d_in[6]; p.logdt = (const u16*)d_in[7]; p.dskip = (const u16*)d_in[8];
  p.w1 = (const u16*)d_in[10]; p.b1 = (const u16*)d_in[11]; p.w2 = (const u16*)d_in[12]; p.b2 = (const u16*)d_in[13];
  p.out = (float*)d_out; p.ws = (char*)d_ws;
  p.stamps = (u64*)((char*)d_ws + (200u << 20));
  (void)hipFuncSetAttribute((const void*)fused_kernel, hipFuncAttributeMaxDynamicSharedMemorySize, LDS_BYTES);
  fused_kernel<<<256, 768, LDS_BYTES, stream>>>(p);
  amp_k<<<1, 256, 0, stream>>>(p.stamps);
}
```

```cpp
#define RAMP_0 780
#define RAMP_D 0
#define RAMP_E 780
#include <hip/hip_runtime.h>
#include <utility>

#define LAS __attribute__((address_space(3)))
#define GAS __attribute__((address_space(1)))
#define DI __device__ __forceinline__
typedef unsigned short u16;
typedef unsigned u32;
typedef unsigned long long u64;
typedef unsigned u32x4 __attribute__((ext_vector_type(4)));
typedef unsigned u32x2 __attribute__((ext_vector_type(2)));
typedef short s16x4 __attribute__((ext_vector_type(4)));
typedef short bf16x8 __attribute__((ext_vector_type(8)));
typedef float f32x4 __attribute__((ext_vector_type(4)));
typedef float f32x2 __attribute__((ext_vector_type(2)));
typedef __bf16 bf16x2 __attribute__((ext_vector_type(2)));
typedef __amdgpu_buffer_rsrc_t rsrc_t;
typedef u64 u64x4 __attribute__((ext_vector_type(4)));

constexpr int S = 2048, D = 576, F = 4608;
constexpr int ROWB = D * 2;
constexpr int WSLOT = 24576, TOFF = 4 * WSLOT, TSLOT = 16384, LDS_BYTES = 163840;
constexpr int AX_W = 0, AX_T = 6656, AX_S = 8704, AX_TAB = 10752;
constexpr u32 WS_FLAGS = 0;
constexpr u32 FL_GO = 0, FL_P = 32, FL_O = 32 + 240, NFLAG = 32 + 480;
constexpr u32 WS_EG = NFLAG * 128;
constexpr u32 WS_REQ = WS_EG + 16 * 576 * 16, WS_RSP = WS_REQ + 1024, WS_ACK = WS_RSP + 1024;
constexpr u32 WS_CS = 256 * 1024;
constexpr u32 WS_LN = WS_CS + 15 * 16 * 1536;
constexpr u32 WS_TAB = WS_LN + 16 * 5120;
constexpr u32 WS_P = 1u << 20;
constexpr u32 XBW = 5 * 1536;
constexpr u32 WS_XB = WS_P + 240 * 98304;
constexpr u32 WS_END = WS_XB + 48 * 32 * XBW;
constexpr int CODE_LINES = 470;
constexpr u32 SPIN_CAP = 1u << 18;

struct Params {
  const u16 *x, *w1, *bre, *bim;
  const u16 *lre, *lim, *cre, *cim, *logdt, *dskip, *b1, *w2, *b2;
  float* out; char* ws;
};

DI Params reload_params() {
  Params q;
#if defined(__HIP_DEVICE_COMPILE__)
  typedef __attribute__((address_space(4))) const Params* KP;
  KP kp = (KP)__builtin_amdgcn_kernarg_segment_ptr(); asm volatile("" : "+s"(kp)); q = *kp;
#endif
  return q;
}
template <int N> DI void waitv() { asm volatile("s_waitcnt vmcnt(%0)" :: "n"(N) : "memory"); }
#define WAITL() asm volatile("s_waitcnt lgkmcnt(0)" ::: "memory")
#define BAR() do { asm volatile("" ::: "memory"); __builtin_amdgcn_s_barrier(); asm volatile("" ::: "memory"); } while (0)
#define SB() __builtin_amdgcn_sched_barrier(0)
#define MFENCE() do { SB(); asm volatile("s_nop 7\n\ts_nop 7"); SB(); } while (0)
#define MMA(c, a, b) asm volatile("v_mfma_f32_16x16x32_bf16 %0, %1, %2, %0" : "+v"(c) : "v"(a), "v"(b))
#define MMA0(c, a, b) asm volatile("v_mfma_f32_16x16x32_bf16 %0, %1, %2, 0" : "=&v"(c) : "v"(a), "v"(b))
#define MMAN(c, a, b) asm volatile("s_nop 1\n\tv_mfma_f32_16x16x32_bf16 %0, %1, %2, %0" : "+v"(c) : "v"(a), "v"(b))
#define PIN(x) asm volatile("" : "+v"(x))
#define STAMPPTR nullptr
#define STAMP(i)

DI void dma(rsrc_t r, u32 lds, u32 voff, u32 soff) {
  asm volatile("s_mov_b32 m0, %0\n\ts_nop 0\n\tbuffer_load_dwordx4 %1, %2, %3 offen lds" :: "s"(lds), "v"(voff), "s"(r), "s"(soff) : "memory");
}
DI void dma_sc1(rsrc_t r, u32 lds, u32 voff, u32 soff) {
  asm volatile("s_mov_b32 m0, %0\n\ts_nop 0\n\tbuffer_load_dwordx4 %1, %2, %3 offen sc1 lds" :: "s"(lds), "v"(voff), "s"(r), "s"(soff) : "memory");
}
DI void dma12(rsrc_t r, u32 lds, u32 voff, u32 so, u32 d2, u32 st, u32 st3) {
  u32 t;
  asm volatile("s_mov_b32 m0, %2\n\t"
               "s_add_u32 %1, %0, %5\n\t"
               "buffer_load_dwordx4 %3, %4, %0 offen lds\n\t"
               "s_add_u32 m0, m0, 0x800\n\t"
               "s_add_u32 %0, %0, %6\n\t"
               "buffer_load_dwordx4 %3, %4, %1 offen lds\n\t"
               "s_add_u32 m0, m0, 0x800\n\t"
               "s_add_u32 %1, %1, %6\n\t"
               "buffer_load_dwordx4 %3, %4, %0 offen lds\n\t"
               "s_add_u32 m0, m0, 0x800\n\t"
               "s_add_u32 %0, %0, %6\n\t"
               "buffer_load_dwordx4 %3, %4, %1 offen lds\n\t"
               "s_add_u32 m0, m0, 0x800\n\t"
               "s_add_u32 %1, %1, %6\n\t"
               "buffer_load_dwordx4 %3, %4, %0 offen lds\n\t"
               "s_add_u32 m0, m0, 0x800\n\t"
               "s_add_u32 %0, %0, %7\n\t"
               "buffer_load_dwordx4 %3, %4, %1 offen lds\n\t"
               "s_add_u32 m0, m0, 0x800\n\t"
               "s_add_u32 %1, %1, %7\n\t"
               "buffer_load_dwordx4 %3, %4, %0 offen lds\n\t"
               "s_add_u32 m0, m0, 0x800\n\t"
               "s_add_u32 %0, %0, %6\n\t"
               "buffer_load_dwordx4 %3, %4, %1 offen lds\n\t"
               "s_add_u32 m0, m0, 0x800\n\t"
               "s_add_u32 %1, %1, %6\n\t"
               "buffer_load_dwordx4 %3, %4, %0 offen lds\n\t"
               "s_add_u32 m0, m0, 0x800\n\t"
               "s_add_u32 %0, %0, %6\n\t"
               "buffer_load_dwordx4 %3, %4, %1 offen lds\n\t"
               "s_add_u32 m0, m0, 0x800\n\t"
               "s_add_u32 %1, %1, %6\n\t"
               "buffer_load_dwordx4 %3, %4, %0 offen lds\n\t"
               "s_add_u32 m0, m0, 0x800\n\t"
               "s_add_u32 %0, %0, %6\n\t"
               "buffer_load_dwordx4 %3, %4, %1 offen lds"
               : "+&s"(so), "=&s"(t) : "s"(lds), "v"(voff), "s"(r), "s"(d2), "s"(st), "s"(st3) : "memory", "scc");
}
DI void dma4(rsrc_t r, u32 lds, u32 voff, u32 so, u32 st) {
  asm volatile("s_mov_b32 m0, %1\n\t"
               "s_nop 0\n\t"
               "buffer_load_dwordx4 %2, %3, %0 offen lds\n\t"
               "s_add_u32 m0, m0, 0x800\n\t"
               "s_add_u32 %0, %0, %4\n\t"
               "buffer_load_dwordx4 %2, %3, %0 offen lds\n\t"
               "s_add_u32 m0, m0, 0x800\n\t"
               "s_add_u32 %0, %0, %4\n\t"
               "buffer_load_dwordx4 %2, %3, %0 offen lds\n\t"
               "s_add_u32 m0, m0, 0x800\n\t"
               "s_add_u32 %0, %0, %4\n\t"
               "buffer_load_dwordx4 %2, %3, %0 offen lds"
               : "+&s"(so) : "s"(lds), "v"(voff), "s"(r), "s"(st) : "memory", "scc");
}
DI void dma8lin(rsrc_t r, u32 lds, u32 voff, u32 so) {
  asm volatile("s_mov_b32 m0, %1\n\t"
               "s_nop 0\n\t"
               "buffer_load_dwordx4 %2, %3, %0 offen offset:0 sc1 lds\n\t"
               "buffer_load_dwordx4 %2, %3, %0 offen offset:1024 sc1 lds\n\t"
               "buffer_load_dwordx4 %2, %3, %0 offen offset:2048 sc1 lds\n\t"
               "buffer_load_dwordx4 %2, %3, %0 offen offset:3072 sc1 lds\n\t"
               "s_add_u32 m0, m0, 0x2000\n\t"
               "s_add_u32 %0, %0, 0x2000\n\t"
               "buffer_load_dwordx4 %2, %3, %0 offen offset:0 sc1 lds\n\t"
               "buffer_load_dwordx4 %2, %3, %0 offen offset:1024 sc1 lds\n\t"
               "buffer_load_dwordx4 %2, %3, %0 offen offset:2048 sc1 lds\n\t"
               "buffer_load_dwordx4 %2, %3, %0 offen offset:3072 sc1 lds"
               : "+&s"(so) : "s"(lds), "v"(voff), "s"(r) : "memory", "scc");
}
DI rsrc_t mk(const void* ptr, u32 bytes) { return __builtin_amdgcn_make_buffer_rsrc((void*)ptr, 0, bytes, 0x00020000); }
DI s16x4 rd_tr1(u32 a, int off) { return __builtin_amdgcn_ds_read_tr16_b64_v4i16((LAS s16x4*)(size_t)(a + off)); }
DI bf16x8 cat(s16x4 lo, s16x4 hi) { return __builtin_shufflevector(lo, hi, 0, 1, 2, 3, 4, 5, 6, 7); }
DI bf16x8 rd_tr(u32 a, int off) { return cat(rd_tr1(a, off), rd_tr1(a, off + 2048)); }
DI bf16x8 rd128(u32 a, int off) { return *(LAS bf16x8*)(size_t)(a + off); }
DI f32x4 rdf4(u32 a, int off) { return *(LAS f32x4*)(size_t)(a + off); }
DI float rdf(u32 a, int off) { return *(LAS float*)(size_t)(a + off); }
DI void wr128(u32 a, int off, u32x4 v) { *(LAS u32x4*)(size_t)(a + off) = v; }
DI u32 cvtpk(float lo, float hi) { f32x2 v = {lo, hi}; bf16x2 b = __builtin_convertvector(v, bf16x2); return __builtin_bit_cast(u32, b); }
DI float bflo(u32 v) { return __uint_as_float(v << 16); }
DI float bfhi(u32 v) { return __uint_as_float(v & 0xffff0000u); }
DI float bf2f(u16 v) { return __uint_as_float((u32)v << 16); }
DI float dot2(u32 a, u32 b, float c) { return __builtin_amdgcn_fdot2_f32_bf16(__builtin_bit_cast(bf16x2, a), __builtin_bit_cast(bf16x2, b), c, false); }
constexpr u32 ONES = 0x3f803f80u;
DI float rbf(float v) { return bflo(cvtpk(v, 0.f)); }
DI void split3(float v, float& a, float& b, float& c) { a = rbf(v); const float r = v - a; b = rbf(r); c = rbf(r - b); }
template <int C> DI float dppx(float v) { return __uint_as_float(__builtin_amdgcn_update_dpp(0u, __float_as_uint(v), C, 0xf, 0xf, true)); }
template <int C> DI double dppd(double v) { u32x2 t = __builtin_bit_cast(u32x2, v); t[0] = __builtin_amdgcn_update_dpp(0u, t[0], C, 0xf, 0xf, true); t[1] = __builtin_amdgcn_update_dpp(0u, t[1], C, 0xf, 0xf, true); return __builtin_bit_cast(double, t); }
DI void st16(u32x4 v, rsrc_t r, u32 voff, u32 soff) { asm volatile("buffer_store_dwordx4 %0, %1, %2, %3 offen sc1\n\ts_nop 1" :: "v"(v), "v"(voff), "s"(r), "s"(soff) : "memory"); }
DI void st_flag(char* ws, u32 idx, u32 tag) { __hip_atomic_store((GAS u32*)(ws + WS_FLAGS + idx * 128), tag, __ATOMIC_RELAXED, __HIP_MEMORY_SCOPE_AGENT); }
DI u32 ld_flag(const char* a) { return __hip_atomic_load((GAS u32*)a, __ATOMIC_RELAXED, __HIP_MEMORY_SCOPE_AGENT); }
DI void spin_all(const char* a, u32 tag) {
#pragma nounroll
  for (u32 i = 0; i < SPIN_CAP; i++) { if (__all(ld_flag(a) == tag)) break; __builtin_amdgcn_s_sleep(1); }
  asm volatile("" ::: "memory");
}
DI void spin_ge(const char* a, u32 lvl) {
#pragma nounroll
  for (u32 i = 0; i < SPIN_CAP; i++) { if (__all(ld_flag(a) >= lvl)) break; __builtin_amdgcn_s_sleep(1); }
  asm volatile("" ::: "memory");
}
DI void st_word(char* a, u32 v) { __hip_atomic_store((GAS u32*)a, v, __ATOMIC_RELAXED, __HIP_MEMORY_SCOPE_AGENT); }
DI void hs_leader(char* ws, int lane, u32 lds0, u64* stamps) {
  const rsrc_t r = mk(ws, WS_END);
  bool first = true, joined = false; u32x4 last = {0u, 0u, 0u, 0u};
#pragma nounroll
  for (u32 i = 0; i < SPIN_CAP; i++) {
    asm volatile("" ::: "memory");
    const u32x4 rq = __builtin_amdgcn_raw_buffer_load_b128(r, lane * 16, WS_REQ, 16), ak = __builtin_amdgcn_raw_buffer_load_b128(r, lane * 16, WS_ACK, 16);
    if (first || rq[0] != last[0] || rq[1] != last[1] || rq[2] != last[2] || rq[3] != last[3]) st16(rq, r, lane * 16, WS_RSP);
    first = false; last = rq;
    if (__all(ak[0] == 1u && ak[1] == 1u && ak[2] == 1u && (ak[3] == 1u || lane == 63))) break;
    if (i == 2) { BAR(); joined = true; }
    __builtin_amdgcn_s_sleep(1);
  }
  if (!joined) BAR();
  BAR();
  st_flag(ws, FL_GO, 1u);
}
DI float row_sum16(float v) { v += dppx<0xb1>(v); v += dppx<0x4e>(v); v += dppx<0x141>(v); v += dppx<0x140>(v); return v; }

DI f32x2 gelu2(f32x2 a, float k1, float k2, float sg) {
  f32x2 t = a * a; t = t * k2 + k1; t = t * a;
  t.x = __builtin_amdgcn_exp2f(t.x); t.y = __builtin_amdgcn_exp2f(t.y);
  t = t * sg + sg;
  t.x = __builtin_amdgcn_rcpf(t.x); t.y = __builtin_amdgcn_rcpf(t.y);
  return t * a;
}
DI u32x4 gelu_frag(f32x4 a, f32x4 b, float k1, float k2, float sg) {
  f32x2 g0 = gelu2((f32x2){a[0], a[1]}, k1, k2, sg), g1 = gelu2((f32x2){a[2], a[3]}, k1, k2, sg);
  f32x2 g2 = gelu2((f32x2){b[0], b[1]}, k1, k2, sg), g3 = gelu2((f32x2){b[2], b[3]}, k1, k2, sg);
  return (u32x4){cvtpk(g0.x, g0.y), cvtpk(g1.x, g1.y), cvtpk(g2.x, g2.y), cvtpk(g3.x, g3.y)};
}

template <int H, class UPD>
DI void stepA(f32x4 (&acc)[6][4], bf16x8 (&wp)[2], bf16x8 (&t)[4], const u32 (&wc)[4], const u32& n0, const u32& n1, const u32& xn, UPD&& upd) {
  bf16x8 w[8], nx[4];
  w[0] = wp[0]; w[1] = wp[1];
#pragma unroll
  for (int n = 0; n < 6; n++) {
    const int f = n + 2, ff = f < 6 ? f : f - 6;
    const u32 a = (f < 6 || H == 0) ? wc[ff & 3] : (ff ? n1 : n0); const int off = (f < 6 ? H : 1 - H) * WSLOT + (ff >> 2) * 4096;
    MMA(acc[n][0], w[n], t[0]); SB();
    s16x4 lo = rd_tr1(a, off); SB();
    MMA(acc[n][1], w[n], t[1]); SB();
    s16x4 hi = rd_tr1(a, off + 2048); SB();
    MMA(acc[n][2], w[n], t[2]); SB();
    if (n >= 4) { nx[2 * n - 8] = rd128(xn, (2 * n - 8) * 2048); SB(); }
    if (n == 1) { upd(); SB(); }
    MMA(acc[n][3], w[n], t[3]); SB();
    if (n >= 4) { nx[2 * n - 7] = rd128(xn, (2 * n - 7) * 2048); SB(); }
    if (n == 3) { BAR(); SB(); }
    w[f] = cat(lo, hi);
  }
  wp[0] = w[6]; wp[1] = w[7];
#pragma unroll
  for (int i = 0; i < 4; i++) t[i] = nx[i];
}

template <int KIND, bool ZERO = false, class FW, class FT, class FN, class MID>
DI void half(f32x4 (&acc)[3][4], bf16x8 (&w)[3], bf16x8 (&tp)[2], FW&& rw, FT&& rt_, FN&& rn, MID&& mid) {
  bf16x8 t[4], wn[3], tn[2];
  t[0] = tp[0]; t[1] = tp[1];
  s16x4 lo;
#pragma unroll
  for (int i = 0; i < 12; i++) {
    const int T = i / 3, n = i % 3;
    if constexpr (KIND == 2) MMA(acc[n][T], t[T], w[n]); else if constexpr (ZERO) MMA0(acc[n][T], w[n], t[T]); else MMA(acc[n][T], w[n], t[T]);
    SB();
    if constexpr (KIND == 0) {
      if (i == 0) t[2] = rt_(2, 0); if (i == 1) t[3] = rt_(3, 0);
      if (i >= 2 && i <= 7) { if ((i & 1) == 0) lo = rw((i - 2) >> 1, 0); else wn[(i - 2) >> 1] = cat(lo, rw((i - 2) >> 1, 1)); }
      if (i == 8) tn[0] = rn(0, 0); if (i == 9) tn[1] = rn(1, 0);
    } else if constexpr (KIND == 1) {
      if (i <= 3) { if ((i & 1) == 0) lo = rt_(2 + (i >> 1), 0); else t[2 + (i >> 1)] = cat(lo, rt_(2 + (i >> 1), 1)); }
      if (i >= 4 && i <= 6) wn[i - 4] = rw(i - 4, 0);
      if (i >= 7 && i <= 10) { if ((i & 1) == 1) lo = rn((i - 7) >> 1, 0); else tn[(i - 7) >> 1] = cat(lo, rn((i - 7) >> 1, 1)); }
    } else {
      if (i == 0) t[2] = rt_(2, 0); if (i == 2) t[3] = rt_(3, 0);
      if (i == 3 || i == 5 || i == 6) wn[i == 3 ? 0 : i - 4] = rw(i == 3 ? 0 : i - 4, 0);
      if (i == 8) tn[0] = rn(0, 0); if (i == 9) tn[1] = rn(1, 0);
    }
    mid(i);
    SB();
  }
#pragma unroll
  for (int i = 0; i < 3; i++) w[i] = wn[i];
  tp[0] = tn[0]; tp[1] = tn[1];
}

template <int I, int N, class FN> DI void sfor(FN&& f) { if constexpr (I < N) { f(std::integral_constant<int, I>{}); sfor<I + 1, N>(f); } }

DI f32x2 cmul(f32x2 a, f32x2 b) { return (f32x2){a.x * b.x - a.y * b.y, a.x * b.y + a.y * b.x}; }
DI f32x2 cfma(f32x2 a, f32x2 b, f32x2 c) { return (f32x2){__builtin_fmaf(a.x, b.x, __builtin_fmaf(-a.y, b.y, c.x)), __builtin_fmaf(a.x, b.y, __builtin_fmaf(a.y, b.x, c.y))}; }
struct CarryPre { f32x2 l16, l32, l64, L1, L4; };
DI CarryPre carry_pre(int c, u32 lds0) {
  const f32x4 pw = rdf4(lds0 + (18 & 3) * WSLOT + AX_TAB + c * 32, 16);
  CarryPre k; const f32x2 l8 = {pw[0], pw[1]};
  k.l16 = cmul(l8, l8); k.l32 = cmul(k.l16, k.l16); k.l64 = cmul(k.l32, k.l32); k.L1 = (f32x2){pw[2], pw[3]};
  const f32x2 L2 = cmul(k.L1, k.L1); k.L4 = cmul(L2, L2);
  PIN(k.l16); PIN(k.l32); PIN(k.l64); PIN(k.L1); PIN(k.L4);
  return k;
}
DI void ssm_carry(const Params& p, const CarryPre& k, int rt, int j, int c, u32 lds0) {
  const u32 scr = lds0 + TOFF + (22 & 3) * TSLOT + c * 8;
  f32x2 e[8];
#pragma unroll
  for (int s = 0; s < 8; s++) e[s] = *(LAS f32x2*)(size_t)(scr + s * 1536);
  const f32x2 a01 = cfma(k.l16, e[0], e[1]), a23 = cfma(k.l16, e[2], e[3]), a45 = cfma(k.l16, e[4], e[5]), a67 = cfma(k.l16, e[6], e[7]);
  const f32x2 a03 = cfma(k.l32, a01, a23), E = cfma(k.l64, a03, cfma(k.l32, a45, a67));
  const rsrc_t rE = mk(p.ws + WS_EG, 16 * 9216);
  const int vo = (192 * j + c) * 16 + (15 - rt) * 9216;
  st16((u32x4){__float_as_uint(E.x), 1u, __float_as_uint(E.y), 1u}, rE, vo, 0);
  u32x4 va[15], vb[15];
  auto issue = [&](u32x4 (&v)[15]) {
    asm volatile("" ::: "memory");
#pragma unroll
    for (int i = 0; i < 15; i++) v[i] = __builtin_amdgcn_raw_buffer_load_b128(rE, vo, (15 - i) * 9216, 16);
    asm volatile("" ::: "memory");
  };
  f32x2 cy;
  auto eval = [&](u32x4 (&v)[15]) {
    u32 n = 0; f32x2 q[4];
#pragma unroll
    for (int i = 0; i < 15; i++) {
      n += v[i][1] + v[i][3];
      const f32x2 x = {__uint_as_float(v[i][0]), __uint_as_float(v[i][2])};
      const int ch = (i + 1) & 3;
      q[ch] = i < 4 ? x : cfma(k.L4, q[ch], x);
    }
    cy = cfma(k.L1, cfma(k.L1, cfma(k.L1, q[0], q[1]), q[2]), q[3]);
    return __all(n == 2u * rt);
  };
  __builtin_amdgcn_s_sleep(6);
  issue(va);
  __builtin_amdgcn_s_sleep(10);
  issue(vb);
  if (!eval(va)) {
    issue(va);
    if (!eval(vb) && !eval(va)) {
#pragma nounroll
      for (u32 spin = 0; spin < SPIN_CAP; spin++) { u32x4 vc[15]; issue(vc); if (eval(vc)) break; }
    }
  }
#pragma unroll
  for (int s = 0; s < 8; s += 2) e[s] = *(LAS f32x2*)(size_t)(scr + s * 1536);
  const f32x2 c2 = cfma(k.l32, cy, a01), c4 = cfma(k.l64, cy, a03), c6 = cfma(k.l32, c4, a45);
  const f32x2 cin[8] = {cy, cfma(k.l16, cy, e[0]), c2, cfma(k.l16, c2, e[2]), c4, cfma(k.l16, c4, e[4]), c6, cfma(k.l16, c6, e[6])};
#pragma unroll
  for (int s = 0; s < 8; s++) *(LAS f32x2*)(size_t)(scr + s * 1536) = cin[s];
}

#define LBAR() PBAR()
#define PBAR() do { if (RAMP_0) { while ((int)((u32)__builtin_amdgcn_s_memtime() - tnext) < 0) {} } BAR(); \
    if (RAMP_0) { tnext = (u32)__builtin_amdgcn_s_memtime() + pace; pace = max(pace - RAMP_D, pmin); } } while (0)
DI void loader(u64x4 kv, int rt, int g, int j, int u, int lane, u32 lds0, u32 tag) {
  const bool ssm = g == 4;
  const u32 wbytes = ssm ? D * ROWB : D * F * 2;
  const int r8 = lane >> 3, gp = lane & 7;
  const int cgr = ((((gp >> 1) ^ ((r8 >> 1) & 3)) << 1) | (gp & 1));
  const int rgr = gp ^ (4 * u + (r8 >> 1));
  const int slice = 3 * g + j;
  const int kA = 16 * u + 8 * (r8 >> 2) + (r8 & 3);
  const int R = 8 * u + r8;
  const u32 voffA = ssm ? (192 * j + R) * ROWB + rgr * 16 : kA * (F * 2) + slice * 768 + cgr * 16;
  const int tok = ssm ? 16 * ((R >> 2) & 3) + 8 * (R & 1) + ((R >> 1) & 1) : R;
  const u32 voffX = (rt * 128 + tok) * ROWB + rgr * 16;
  const u32 ldsW = lds0 + u * 1024, ldsT = lds0 + TOFF;
  const u32 aKt = ssm ? 128 : 64 * F * 2, aPar = ssm ? 0 : 32 * F * 2, pstr = ssm ? 32 * ROWB : 128, a2 = ssm ? 16 * ROWB : 4 * F * 2, xstr = (ssm ? 2 : 16) * ROWB;
  const int rot = (rt >= 10 ? rt - 10 : rt) & ~1;
  asm volatile("s_waitcnt lgkmcnt(0)" : "+s"(kv) : "v"(voffA), "v"(voffX), "s"(aKt), "s"(rot));
  const rsrc_t rX = mk((void*)kv[0], S * ROWB), rA0 = mk((void*)(ssm ? kv[2] : kv[1]), wbytes), rA1 = mk((void*)(ssm ? kv[3] : kv[1]), wbytes);
  auto ktile = [&](int kap) { int kt = kap + rot; return kt >= 9 ? kt - 9 : kt; };
  auto issueA = [&](int kap, int h) { dma12(h ? rA1 : rA0, ldsW + ((2 * kap + h) & 3) * WSLOT, voffA, ktile(kap) * aKt + h * aPar, a2, pstr, pstr); };
  auto issueX = [&](int kap, int h) { dma4(rX, ldsT + (kap & 3) * TSLOT + h * 8192 + u * 1024, voffX, h * 64 * ROWB + ktile(kap) * 128, xstr); };
  issueA(0, 0); issueX(0, 0); issueX(0, 1); issueA(0, 1);
  __builtin_amdgcn_s_setprio(3);
  waitv<12>(); BAR();
  issueX(1, 0); issueX(1, 1); issueA(1, 0);
#pragma nounroll
  for (int kap = 0; kap < 7; kap++) {
    u32 l1 = ldsW + ((2 * kap + 3) & 3) * WSLOT, s1 = ktile(kap + 1) * aKt + aPar, l2 = ldsW + ((2 * kap) & 3) * WSLOT, s2 = ktile(kap + 2) * aKt;
    u32 lx = ldsT + ((kap + 2) & 3) * TSLOT + u * 1024, lx2 = lx + 8192, sx = ktile(kap + 2) * 128, sx2 = sx + 64 * ROWB;
    asm volatile("" : "+s"(l1), "+s"(s1), "+s"(l2), "+s"(s2), "+s"(lx), "+s"(lx2), "+s"(sx), "+s"(sx2));
    waitv<16>(); BAR(); dma12(rA1, l1, voffA, s1, a2, pstr, pstr); dma4(rX, lx, voffX, sx, xstr);
    waitv<16>(); BAR(); dma12(rA0, l2, voffA, s2, a2, pstr, pstr); dma4(rX, lx2, voffX, sx2, xstr);
  }
  waitv<16>(); BAR(); issueA(8, 1);
  const Params p = reload_params();
  const u32 voffL = lane * 16;
  const rsrc_t rWS = mk(p.ws, WS_END), rB0 = mk(ssm ? p.cre : p.w2, wbytes), rB1 = mk(ssm ? p.cim : p.w2, wbytes);
  const u32 voffB = ssm ? voffA : R * ROWB + j * 384 + cgr * 16;
  const u32 bJ = ssm ? 384 : 384 * ROWB, bPar = ssm ? 0 : 192 * ROWB, bI = ssm ? 128 : 32 * ROWB, b0 = ssm ? 0 : g * 1152 * ROWB;
  const int j1 = j == 2 ? 0 : j + 1, j2 = j == 0 ? 2 : j - 1;
  const u32 st3 = 96 * ROWB - 2 * pstr, pb = WS_P + (rt * 15 + 3 * g) * 98304 + u * 4096;
  auto issueB = [&](int s, int jj, int i) { dma12((i & 1) ? rB1 : rB0, ldsW + (s & 3) * WSLOT, voffB, b0 + jj * bJ + (i & 1) * bPar + (i >> 1) * bI, 16 * ROWB, pstr, st3); };
  auto issueP = [&](int s, int jj, int i) { dma8lin(rWS, ldsT + (s & 3) * TSLOT + u * 4096, voffL, pb + jj * 98304 + i * 16384); };
  waitv<12>(); BAR();
#pragma unroll
  for (int k = 0; k < 4; k++) {
    const u32 dst = ssm ? (k < 3 ? AX_TAB + (u * 3 + k) * 1024 : AX_S) : AX_T + k * 1024;
    const u32 src = ssm ? (k < 3 ? WS_TAB + j * 6144 + (u * 3 + k) * 1024 : WS_LN + rt * 5120 + 4096) : WS_LN + rt * 5120 + k * 1024;
    dma_sc1(rWS, lds0 + (18 & 3) * WSLOT + dst, voffL, src);
  }
  waitv<4>();
  BAR(); issueB(19, j, 0); waitv<12>();
  BAR(); issueB(20, j, 1); waitv<12>();
  if (ssm) { const CarryPre k = carry_pre(lane, lds0); BAR(); if (u == 1) ssm_carry(p, k, rt, j, lane, lds0); WAITL(); }
  BAR();
  issueB(21, j, 2); waitv<12>();
  if (ssm) BAR();
  BAR(); issueB(22, j, 3); waitv<12>();
  BAR(); issueB(23, j, 4); issueP(23, j, 4); waitv<20>();
  BAR(); issueB(24, j, 5); issueP(24, j, 5);
#pragma nounroll
  for (int n = 0; n < 3; n++) {
    const int i = n ? 2 * n - 2 : 4;
    u32 w1 = b0 + j1 * bJ + (i >> 1) * bI, w2 = b0 + j2 * bJ + (i >> 1) * bI, w3 = w1 + bPar, w4 = w2 + bPar;
    u32 p1 = pb + j1 * 98304 + i * 16384, p2 = pb + j2 * 98304 + i * 16384, p3 = p1 + 16384, p4 = p2 + 16384;
    asm volatile("" : "+s"(w1), "+s"(w2), "+s"(w3), "+s"(w4), "+s"(p1), "+s"(p2), "+s"(p3), "+s"(p4));
    waitv<20>(); BAR(); dma12(rB0, ldsW + 1 * WSLOT, voffB, w1, 16 * ROWB, pstr, st3); dma8lin(rWS, ldsT + 1 * TSLOT + u * 4096, voffL, p1);
    waitv<20>(); BAR(); dma12(rB0, ldsW + 2 * WSLOT, voffB, w2, 16 * ROWB, pstr, st3); dma8lin(rWS, ldsT + 2 * TSLOT + u * 4096, voffL, p2);
    waitv<20>(); BAR(); dma12(rB1, ldsW + 3 * WSLOT, voffB, w3, 16 * ROWB, pstr, st3); dma8lin(rWS, ldsT + 3 * TSLOT + u * 4096, voffL, p3);
    waitv<20>(); BAR(); dma12(rB1, ldsW + 0 * WSLOT, voffB, w4, 16 * ROWB, pstr, st3); dma8lin(rWS, ldsT + 0 * TSLOT + u * 4096, voffL, p4);
  }
  waitv<20>();
  BAR();
  {
    const u32 l = lds0 + WSLOT;
    const u32 vo = (lane & 31) < 24 ? (rt * 128 + u * 64 + (g & 3) * 16 + (lane >> 5)) * ROWB + j * 384 + (lane & 31) * 16 : 0x7fffffffu;
#pragma unroll
    for (int k = 0; k < 8; k++) dma(rX, l + (u * 8 + k) * 1024, vo, k * 2 * ROWB);
    dma(mk(u ? p.dskip : p.b2, ROWB), l + 16384 + u * 1024, voffL + j * 384, 0);
  }
  waitv<9>(); BAR(); BAR(); waitv<0>();
  BAR();
}

DI void helper(const Params& p, int rt, int g, int j, int h, int lane, u32 lds0, u32 tag) {
  const bool ssm = g == 4;
  int pace = (h == 1 && !ssm) ? RAMP_0 : 0, pmin = (h == 1 && !ssm) ? RAMP_E : 0; u32 tnext = (u32)__builtin_amdgcn_s_memtime(); (void)pace; (void)pmin; (void)tnext;
  const int slice = 3 * g + j, q = lane >> 4, jl = lane & 15;
  const rsrc_t rWS = mk(p.ws, WS_END);
  char* const hsR = p.ws + WS_RSP + blockIdx.x * 4;
  const u32 hs0 = ld_flag(hsR);
  u32 hsn = 0;
  auto startup = [&]() {
    hsn = __builtin_amdgcn_readfirstlane(hs0) + 1u;
    if (h == 0) st_word(hsR - 1024, hsn); else if (lane < 2) st_flag(p.ws, lane ? FL_P + rt * 15 + slice : FL_O + (rt * 3 + j) * 5 + g, 0u);
    if (ssm) { st16((u32x4){0u, 0u, 0u, 0u}, rWS, (192 * j + 64 * h + lane) * 16, WS_EG + (15 - rt) * 9216); st16((u32x4){0u, 0u, 0u, 0u}, rWS, (192 * j + 128 + lane) * 16, WS_EG + (15 - rt) * 9216); }
  };
  __builtin_amdgcn_s_setprio(2);
  {
    const int e = (lane >> 2) & 3, z = 2 * (q & 1) + (e >> 1);
    f32x4 c = {0.f, 0.f, 0.f, 0.f};
    u32x4 sel[12];
#pragma unroll
    for (int n = 0; n < 12; n++) { const u32 o = jl == n ? ONES : 0u; sel[n] = (u32x4){o, o, o, o}; PIN(sel[n]); }
    PIN(c);
    auto pass = [&](int sl) {
      bf16x8 f[12];
      if (ssm) {
        const u32 a = lds0 + (rt & 1) * 2 * WSLOT + h * 12288 + jl * 128 + ((q ^ (jl >> 1)) * 16);
#pragma unroll
        for (int n = 0; n < 6; n++) { f[n] = rd128(a, sl * WSLOT + n * 2048); f[6 + n] = rd128(a ^ 64, sl * WSLOT + n * 2048); }
      } else {
#pragma unroll
        for (int n = 0; n < 12; n++) f[n] = rd_tr(lds0 + (rt & 1) * 2 * WSLOT + h * 12288 + (4 * q + e) * 128 + (((n & 3) ^ z) * 32) + (lane & 3) * 8, sl * WSLOT + (n >> 2) * 4096);
      }
#pragma unroll
      for (int n = 0; n < 12; n++) PIN(f[n]);
      asm volatile("s_nop 1");
      if (ssm) {
#pragma unroll
        for (int n = 0; n < 12; n++) MMAN(c, f[n], sel[6 * sl + n % 6]);
        MFENCE();
      } else {
#pragma unroll
        for (int n = 0; n < 12; n++) MMAN(c, f[n], sel[n]);
        MFENCE();
      }
    };
    const int odd = rt & 1;
    const u32 cso = jl < 12 ? ((ssm ? (jl >= 6 ? 192 - 96 : 0) + 96 * h : 192 * h) + 16 * jl + 4 * q) * 4 : 0x7fffffffu;
    LBAR();
    if (odd) { LBAR(); startup(); LBAR(); }
    if (rt < 9) pass(0);
    LBAR();
    if (rt < 9) { pass(1); st16(__builtin_bit_cast(u32x4, c), rWS, cso, WS_CS + (slice * 16 + rt) * 1536); }
    LBAR();
    if (!odd) { startup(); LBAR(); LBAR(); }
  }
  LBAR();
  u32 pv = ld_flag(hsR);
  waitv<0>(); LBAR();
  if (h == 0) { if (!__all(pv == hsn)) spin_all(hsR, hsn); st_word(hsR + 1024, 1u); }
  LBAR(); LBAR(); LBAR(); LBAR();
  const char* fGo = p.ws + WS_FLAGS + FL_GO * 128;
  pv = ld_flag(fGo);
  LBAR(); LBAR();
  if (!__all(pv == 1u)) spin_all(fGo, 1u);
  const int cc = 192 * h + 4 * (lane < 48 ? lane : lane - 48);
  f32x4 cp[9];
#pragma unroll
  for (int k = 0; k < 9; k++) cp[k] = __builtin_bit_cast(f32x4, __builtin_amdgcn_raw_buffer_load_b128(rWS, cc * 4, WS_CS + (slice * 16 + k) * 1536, 16));
  const u32x2 bb = *(const GAS u32x2*)(p.b1 + (ssm ? 0 : slice * 384) + cc);
  LBAR(); LBAR(); SB();
  f32x4 cs = ((cp[0] + cp[1]) + (cp[2] + cp[3])) + ((cp[4] + cp[5]) + (cp[6] + cp[7])) + cp[8];
  u32x4 aw[4];
  PIN(cs);
  auto auxw = [&](int k) { float c1, c2, c3; split3(cs[k], c1, c2, c3); const float b = (k & 1) ? bfhi(bb[k >> 1]) : bflo(bb[k >> 1]);
    aw[k] = (u32x4){cvtpk(c1, c2), cvtpk(c1, c2), cvtpk(c3, c1), cvtpk(b, b)}; PIN(aw[k]); };
  const u32 ax = lds0 + (18 & 3) * WSLOT;
  LBAR(); SB();
  if (!ssm) { auxw(0); auxw(1); }
  LBAR(); SB();
  if (!ssm) { auxw(2); auxw(3); }
  LBAR(); SB();
  if (ssm) *(LAS f32x4*)(size_t)(ax + AX_W + cc * 4) = cs;
  else {
#pragma unroll
    for (int k = 0; k < 4; k++) wr128(ax + AX_W + cc * 4, k * 1600, aw[k]);
  }
  WAITL(); LBAR();
  pace = pmin = 0;
  if (ssm) { const CarryPre k = carry_pre(64 + 64 * h + lane, lds0); LBAR(); ssm_carry(p, k, rt, j, 64 + 64 * h + lane, lds0); WAITL(); LBAR(); }
  LBAR();
  char* const fMine = p.ws + WS_FLAGS + (FL_P + rt * 15 + slice) * 128;
  const char* fP = p.ws + WS_FLAGS + (FL_P + rt * 15 + 3 * g + ((lane & 1) ? (j == 2 ? 0 : j + 1) : (j == 0 ? 2 : j - 1))) * 128;
  LBAR(); if (h == 1 && !ssm) st_word(fMine, 2u);
  LBAR(); if (h == 1 && ssm) st_word(fMine, 1u);
#pragma unroll
  for (u32 lv = 1; lv < 4; lv++) {
    if (lv == 1) pv = ld_flag(fP);
    LBAR(); if (h == 0 && !__all(pv >= lv)) spin_ge(fP, lv);
    LBAR(); if (h == 1 && lv == 1) st_word(fMine, 2u);
    LBAR(); pv = ld_flag(fP);
    LBAR(); if (h == 1 && lv == 1) st_word(fMine, 3u);
  }
  LBAR(); LBAR(); LBAR(); LBAR();
  LBAR();
  if (h == 1) st_flag(p.ws, FL_O + (rt * 3 + j) * 5 + g, tag);
  else if (!ssm) {
    const int k = lane & 3;
    const char* fa = p.ws + WS_FLAGS + (FL_O + (rt * 3 + j) * 5 + (k < g ? k : k + 1)) * 128;
    u32 v = 0;
#pragma nounroll
    for (u32 i = 0; i < SPIN_CAP; i++) { v = ld_flag(fa); if (__all(v == tag || k == 3)) break; }
    const bool yok = __all(v == tag);
    *(LAS u32*)(size_t)lds0 = yok; WAITL(); BAR();
    if (!yok) { spin_all(fa, tag); BAR(); }
  }
}

template <bool SSM>
DI void phaseB(const Params& p, int rt, int g, int j, int w, int lane, u32 lds0, u32 tag) {
  asm volatile("" : "+v"(lane));
  const int rg = w >> 2, cg = w & 3, q = lane >> 4, e = (lane >> 2) & 3, pp = lane & 3, jl = lane & 15;
  const int z = 2 * (q & 1) + (e >> 1);
  u32 wlo[3], whi[3], tb[2];
#pragma unroll
  for (int n = 0; n < 3; n++) {
    const int t = 3 * cg + n;
    wlo[n] = SSM ? lds0 + (16 * t + jl) * 128 + ((q ^ (jl >> 1)) * 16) : lds0 + (t >> 2) * 4096 + (4 * q + e) * 128 + (((t & 3) ^ z) * 32) + pp * 8;
    whi[n] = SSM ? (wlo[n] ^ 64) : wlo[n] + 2 * WSLOT;
  }
  tb[0] = SSM ? lds0 + TOFF + rg * 8192 + (q >> 1) * 2048 + (pp >> 1) * 1024 + (((8 * (q & 1) + e) * 16) ^ ((pp >> 1) * 64)) + 8 * (pp & 1) : lds0 + TOFF + rg * 4096 + lane * 16;
  tb[1] = tb[0] ^ 64;
  float mean = 0.f, rstd = 0.f;
  if constexpr (!SSM) { const u32 sa = lds0 + (18 & 3) * WSLOT + AX_S + (64 * rg + 16 * g + jl) * 4; mean = rdf(sa, 0); rstd = rdf(sa, 512); }
  f32x4 ob[3][4];
  auto RW = [&](int sl, int kb, int n, int part) {
    if constexpr (SSM) return rd128((kb ? whi[n] : wlo[n]) + (sl >> 1) * 2 * WSLOT, (sl & 1) * WSLOT);
    else return rd_tr1(sl < 2 ? wlo[n] : whi[n], (sl & 1) * WSLOT + kb * 12288 + part * 2048);
  };
  auto RT = [&](int sl, int kb, int T, int part) {
    if constexpr (SSM) return rd_tr1(tb[part], sl * TSLOT + kb * 4096 + T * 256);
    else return rd128(tb[0], sl * TSLOT + kb * 8192 + T * 1024);
  };
  bf16x8 wf[3], tp[2];
  auto step = [&](auto slc, auto&& mid) {
    constexpr int SL = decltype(slc)::value & 3, NS = (SL + 1) & 3;
    half<SSM ? 1 : 0, (decltype(slc)::value > 3)>(ob, wf, tp, [&](int n, int part) { return RW(SL, 1, n, part); }, [&](int T, int part) { return RT(SL, 0, T, part); }, [&](int T, int part) { return RT(SL, 1, T, part); }, mid);
    half<SSM ? 1 : 0>(ob, wf, tp, [&](int n, int part) { return RW(NS, 0, n, part); }, [&](int T, int part) { return RT(SL, 1, T, part); }, [&](int T, int part) { return RT(NS, 0, T, part); }, [](int) {});
  };
  using I0 = std::integral_constant<int, 0>; using I1 = std::integral_constant<int, 1>; using I2 = std::integral_constant<int, 2>; using I3 = std::integral_constant<int, 3>;
  if constexpr (!SSM) waitv<4>();
  __builtin_amdgcn_s_waitcnt(0xC07F); WAITL(); BAR();
#pragma unroll
  for (int n = 0; n < 3; n++) { if constexpr (SSM) wf[n] = RW(3, 0, n, 0); else wf[n] = cat(RW(3, 0, n, 0), RW(3, 0, n, 1)); }
#pragma unroll
  for (int T = 0; T < 2; T++) { if constexpr (SSM) tp[T] = cat(RT(3, 0, T, 0), RT(3, 0, T, 1)); else tp[T] = RT(3, 0, T, 0); }
  auto barm = [](int i) { if (i == 11) BAR(); };
  step(std::integral_constant<int, 7>{}, [](int) {});
  step(I0{}, [&](int i) { if (i == 3) { if constexpr (SSM) waitv<8>(); BAR(); } });
#pragma nounroll
  for (int it = 0; it < 4; it++) { step(I1{}, barm); step(I2{}, [&](int i) { if (i == 11) { waitv<4>(); BAR(); } }); step(I3{}, barm); step(I0{}, [&](int i) { if (i == 11) { waitv<0>(); BAR(); } }); }
  MFENCE();
  const rsrc_t rWS = mk(p.ws, WS_END);
  const u32 xb = WS_XB + (rt * 3 + j) * 32 * XBW + w * XBW;
  f32x4 mine[3];
#pragma unroll
  for (int T = 0; T < 4; T++) {
    if (SSM || T != g) {
      const u32 o = xb + T * 8 * XBW + g * 1536;
      const u32x4 a = {cvtpk(ob[0][T][0], ob[0][T][1]), cvtpk(ob[0][T][2], ob[0][T][3]), cvtpk(ob[1][T][0], ob[1][T][1]), cvtpk(ob[1][T][2], ob[1][T][3])};
      const u32 b0 = cvtpk(ob[2][T][0], ob[2][T][1]), b1 = cvtpk(ob[2][T][2], ob[2][T][3]);
      const auto s0 = __builtin_amdgcn_permlane32_swap(b0, b0, false, false), s1 = __builtin_amdgcn_permlane32_swap(b1, b1, false, false);
      st16(a, rWS, lane * 16, o);
      if (lane < 32) st16((u32x4){b0, b1, s0[1], s1[1]}, rWS, lane * 16, o + 1024);
    } else {
#pragma unroll
      for (int n = 0; n < 3; n++) mine[n] = ob[n][T];
    }
  }
  if constexpr (SSM) {
    waitv<0>(); BAR();
    STAMP(7);
  } else {
    waitv<0>(); BAR();
    const int tok = rt * 128 + 64 * rg + 16 * g + jl, col = 192 * j + 48 * cg + 4 * q;
    u32x2 xv[3], bv[3], dv[3];
#pragma unroll
    for (int n = 0; n < 3; n++) {
      const u32 cb = lds0 + WSLOT + (48 * cg + 4 * q + 16 * n) * 2;
      xv[n] = *(LAS u32x2*)(size_t)(cb + (16 * rg + jl) * 512); bv[n] = *(LAS u32x2*)(size_t)(cb + 16384); dv[n] = *(LAS u32x2*)(size_t)(cb + 17408);
    }
    const u32 o = xb + g * 8 * XBW;
    u32x4 a[3]; u32x2 b[3];
    BAR();
#pragma unroll
    for (int k = 0; k < 3; k++) {
      const int src = k < g ? k : k + 1;
      a[k] = __builtin_amdgcn_raw_buffer_load_b128(rWS, lane * 16, o + src * 1536, 16);
      b[k] = __builtin_amdgcn_raw_buffer_load_b64(rWS, (lane & 31) * 16 + (lane >> 5) * 8, o + src * 1536 + 1024, 16);
    }
    const bool yok = __builtin_amdgcn_readfirstlane(*(LAS u32*)(size_t)lds0);
    u32x4 ya; u32x2 yb;
    auto ldy = [&]() { ya = __builtin_amdgcn_raw_buffer_load_b128(rWS, lane * 16, o + 4 * 1536, 16); yb = __builtin_amdgcn_raw_buffer_load_b64(rWS, (lane & 31) * 16 + (lane >> 5) * 8, o + 4 * 1536 + 1024, 16); };
    if (yok) ldy();
    f32x4 gate[3], base[3];
#pragma unroll
    for (int n = 0; n < 3; n++) {
#pragma unroll
      for (int c = 0; c < 4; c++) {
        float G = mine[n][c] + ((c & 1) ? bfhi(bv[n][c >> 1]) : bflo(bv[n][c >> 1]));
#pragma unroll
        for (int k = 0; k < 3; k++) { const u32 v = n < 2 ? a[k][2 * n + (c >> 1)] : b[k][c >> 1]; G += (c & 1) ? bfhi(v) : bflo(v); }
        const float xx = (c & 1) ? bfhi(xv[n][c >> 1]) : bflo(xv[n][c >> 1]);
        const float dd = (c & 1) ? bfhi(dv[n][c >> 1]) : bflo(dv[n][c >> 1]);
        gate[n][c] = G * __builtin_amdgcn_rcpf(1.f + __builtin_amdgcn_exp2f(-1.4426950408889634f * G));
        base[n][c] = xx + (xx - mean) * rstd * dd * gate[n][c];
      }
    }
#pragma unroll
    for (int n = 0; n < 3; n++) { PIN(gate[n]); PIN(base[n]); }
    if (!yok) { BAR(); ldy(); }
#pragma unroll
    for (int n = 0; n < 3; n++) {
      const u32 y0 = n < 2 ? ya[2 * n] : yb[0], y1 = n < 2 ? ya[2 * n + 1] : yb[1];
      *(GAS f32x4*)(p.out + tok * D + col + 16 * n) = base[n] + (f32x4){bflo(y0), bfhi(y0), bflo(y1), bfhi(y1)} * gate[n];
    }
    STAMP(7);
  }
}

DI void ffn_compute(int rt, int g, int j, int w, int lane, u32 lds0, u32 tag) {
  const int rg = w >> 2, cg = w & 3, q = lane >> 4, e = (lane >> 2) & 3, pp = lane & 3, jl = lane & 15;
  const int z = 2 * (q & 1) + (e >> 1);
  u32 wc[4];
#pragma unroll
  for (int n = 0; n < 4; n++) { const int t = 6 * cg + n; wc[n] = lds0 + (t >> 2) * 4096 + (4 * q + e) * 128 + (((t & 3) ^ z) * 32) + pp * 8; }
  const u32 xbase = lds0 + TOFF + (64 * rg + jl) * 128 + ((q ^ (jl >> 1)) * 16);
  f32x4 acc[6][4];
#pragma unroll
  for (int n = 0; n < 6; n++)
#pragma unroll
    for (int T = 0; T < 4; T++) { acc[n][T] = (f32x4){0.f, 0.f, 0.f, 0.f}; asm volatile("" : "+v"(acc[n][T])); }
  bf16x8 wp[2], t[4];
  BAR();
  wp[0] = rd_tr(wc[0], 0); wp[1] = rd_tr(wc[1], 0);
#pragma unroll
  for (int T = 0; T < 4; T++) t[T] = rd128(xbase, T * 2048);
  u32 xs = 0, xn = 0, n0 = 0, n1 = 0; int dw = 2 * WSLOT;
#pragma nounroll
  for (int it = 0; it < 9; it++) {
    stepA<0>(acc, wp, t, wc, n0, n1, xn, [&]() { xn = (xbase ^ 64) + xs; PIN(xn); xs = (xs + TSLOT) & (4 * TSLOT - 1); });
    stepA<1>(acc, wp, t, wc, n0, n1, xn, [&]() { xn = xbase + xs; n0 = wc[0] + dw; n1 = wc[1] + dw; wc[2] += dw; wc[3] += dw; PIN(xn); PIN(n0); PIN(n1); PIN(wc[2]); PIN(wc[3]); });
    wc[0] = n0; wc[1] = n1; dw = -dw;
  }
  const Params p = reload_params();
  const u32 ax = lds0 + (18 & 3) * WSLOT;
  float k1[4], k2[4], sg[4];
#pragma unroll
  for (int T = 0; T < 4; T++) {
    u32x4 v = __builtin_bit_cast(u32x4, rd128(ax + AX_T + (64 * rg + jl) * 16, T * 256));
    if (q != 0) v = (u32x4){0u, 0u, 0u, 0u};
    t[T] = __builtin_bit_cast(bf16x8, v);
    const float r = rdf(ax + AX_S + 512 + (64 * rg + jl) * 4, T * 64);
    sg[T] = rdf(ax + AX_S + 1024 + (64 * rg + jl) * 4, T * 64);
    k1[T] = -2.3022081985f * r; k2[T] = -0.10294324f * r * r * r;
  }
  {
    bf16x8 a[6];
#pragma unroll
    for (int n = 0; n < 6; n++) a[n] = rd128(ax + AX_W + ((jl & 3) * 100 + 24 * cg + (jl >> 2)) * 16, n * 64);
    asm volatile("s_nop 1" : "+v"(t[0]), "+v"(t[1]), "+v"(t[2]), "+v"(t[3]));
#pragma unroll
    for (int n = 0; n < 6; n++)
#pragma unroll
      for (int T = 0; T < 4; T++) MMA(acc[n][T], a[n], t[T]);
    MFENCE();
  }
  const rsrc_t rWS = mk(p.ws, WS_END);
  const u32 pg = WS_P + (rt * 15 + 3 * g + j) * 98304 + (cg >> 1) * 16384 + (cg & 1) * 8192 + rg * 4096;
  const u32 pl = lds0 + TOFF + (cg & 1) * 8192 + rg * 4096 + lane * 16, pl0 = pl + ((19 + (cg >> 1)) & 3) * TSLOT, pl1 = pl + ((21 + (cg >> 1)) & 3) * TSLOT;
#pragma unroll
  for (int bb = 0; bb < 3; bb++) {
    const int b = (bb + 2) % 3;
#pragma unroll
    for (int T = 0; T < 4; T++) {
      const u32x4 P = gelu_frag(acc[2 * b][T], acc[2 * b + 1][T], k1[T], k2[T], sg[T]);
      st16(P, rWS, lane * 16, pg + b * 32768 + T * 1024);
      if (b < 2) wr128(b ? pl1 : pl0, T * 1024, P);
      SB();
    }
    if (bb == 0) BAR();
  }
  phaseB<false>(p, rt, g, j, w, lane, lds0, tag);
}

DI void ssm_compute(int rt, int j, int w, int lane, u32 lds0, u32 tag) {
  const int rg = w >> 2, cg = w & 3; int q = lane >> 4, jl = lane & 15;
  u32 wb = lds0 + (16 * cg + jl) * 128 + ((q ^ (jl >> 1)) * 16);
  const u32 xb0 = lds0 + TOFF + (64 * rg + jl) * 128 + ((q ^ (jl >> 1)) * 16);
  f32x4 ar[3][4], ai[3][4];
#pragma unroll
  for (int n = 0; n < 3; n++)
#pragma unroll
    for (int T = 0; T < 4; T++) { ar[n][T] = (f32x4){0.f, 0.f, 0.f, 0.f}; ai[n][T] = (f32x4){0.f, 0.f, 0.f, 0.f}; asm volatile("" : "+v"(ar[n][T]), "+v"(ai[n][T])); }
  bf16x8 wf[3], tp[2];
  auto barm = [](int i) { if (i == 11) BAR(); };
  __builtin_amdgcn_s_waitcnt(0xC07F);
  BAR();
#pragma unroll
  for (int n = 0; n < 3; n++) wf[n] = rd128(wb, n * 8192);
  tp[0] = rd128(xb0, 0); tp[1] = rd128(xb0, 2048);
  u32 xs = 0, x0 = xb0, x1 = xb0 ^ 64, wb1 = wb ^ 64; int dw = 2 * WSLOT;
#pragma nounroll
  for (int it = 0; it < 9; it++) {
    half<2>(ar, wf, tp, [&](int n, int) { return rd128(wb1, n * 8192); }, [&](int T, int) { return rd128(x0, T * 2048); }, [&](int T, int) { return rd128(x1, T * 2048); }, barm);
    half<2>(ar, wf, tp, [&](int n, int) { return rd128(wb, WSLOT + n * 8192); }, [&](int T, int) { return rd128(x1, T * 2048); }, [&](int T, int) { return rd128(x0, T * 2048); },
            [&](int i) { if (i == 10) { wb += dw; PIN(wb); xs = (xs + TSLOT) & (4 * TSLOT - 1); } });
    half<2>(ai, wf, tp, [&](int n, int) { return rd128(wb1, WSLOT + n * 8192); }, [&](int T, int) { return rd128(x0, T * 2048); }, [&](int T, int) { return rd128(x1, T * 2048); },
            [&](int i) { if (i == 7) { wb1 += dw; x0 = xb0 + xs; PIN(wb1); PIN(x0); dw = -dw; } if (i == 11) BAR(); });
    half<2>(ai, wf, tp, [&](int n, int) { return rd128(wb, n * 8192); }, [&](int T, int) { return rd128(x1, T * 2048); }, [&](int T, int) { return rd128(x0, T * 2048); },
            [&](int i) { if (i == 4) { x1 = x0 ^ 64; PIN(x1); } });
  }
  const Params p = reload_params();
  asm volatile("" : "+v"(lane));
  q = lane >> 4; jl = lane & 15;
  const u32 ax = lds0 + (18 & 3) * WSLOT;
  MFENCE();
  f32x2 lc[3];
  const u32 tb = ax + AX_TAB + (16 * cg + jl) * 32;
  const u32 scr = lds0 + TOFF + (22 & 3) * TSLOT + ((4 * rg + q) * 192 + 16 * cg + jl) * 8;
  f32x2 e0[3]; float csr[3], csi[3];
#pragma unroll
  for (int b = 0; b < 3; b++) { csr[b] = rdf(ax + AX_W + (16 * cg + jl) * 4, b * 256); csi[b] = rdf(ax + AX_W + (16 * cg + jl) * 4, 768 + b * 256); }
#pragma unroll
  for (int T = 0; T < 4; T++) {
    const f32x4 r = rdf4(ax + AX_S + 512 + (64 * rg + 4 * q) * 4, T * 64), nm = rdf4(ax + AX_S + (64 * rg + 4 * q) * 4, T * 64);
#pragma unroll
    for (int b = 0; b < 3; b++) { ar[b][T] = ar[b][T] * r + nm * csr[b]; ai[b][T] = ai[b][T] * r + nm * csi[b]; }
  }
  f32x2 l8[3];
#pragma unroll
  for (int b = 0; b < 3; b++) {
    lc[b] = *(LAS f32x2*)(size_t)(tb + b * 2048);
    l8[b] = *(LAS f32x2*)(size_t)(tb + b * 2048 + 16);
    f32x2 hr = {ar[b][0][0], ar[b][0][1]}, hi = {ai[b][0][0], ai[b][0][1]};
#pragma unroll
    for (int k = 1; k < 8; k++) {
      const f32x2 ur = {ar[b][k >> 1][(k & 1) * 2], ar[b][k >> 1][(k & 1) * 2 + 1]}, ui = {ai[b][k >> 1][(k & 1) * 2], ai[b][k >> 1][(k & 1) * 2 + 1]};
      const f32x2 nr = lc[b][0] * hr + (ur - lc[b][1] * hi), ni = lc[b][0] * hi + (ui + lc[b][1] * hr);
      hr = nr; hi = ni;
    }
    e0[b] = (f32x2){hr.x, hi.x};
    *(LAS f32x2*)(size_t)(scr + b * 512) = (f32x2){l8[b].x * hr.x - l8[b].y * hi.x + hr.y, l8[b].x * hi.x + l8[b].y * hr.x + hi.y};
  }
  WAITL(); BAR(); SB();
#pragma unroll
  for (int b = 0; b < 3; b++) {
    const f32x2 cf = *(LAS f32x2*)(size_t)(tb + b * 2048 + 8); const float c2r = cf.x, c2i = cf.y;
#pragma unroll
    for (int T = 0; T < 4; T++) {
      const f32x4 nr = c2r * ar[b][T] - c2i * ai[b][T], ni = -c2r * ai[b][T] - c2i * ar[b][T];
      ar[b][T] = nr; ai[b][T] = ni;
      asm volatile("" : "+v"(ar[b][T]), "+v"(ai[b][T]));
    }
  }
  BAR();
  const rsrc_t rWS = mk(p.ws, WS_END);
  const u32 io0 = w * 2048 + lane * 16, io1 = w * 2048 + 1024 + ((lane * 16) ^ 64);
  const u32 pg = WS_P + (rt * 15 + 12 + j) * 98304;
  f32x2 cins[3];
#pragma unroll
  for (int b = 0; b < 3; b++) { cins[b] = *(LAS f32x2*)(size_t)(scr + b * 512); l8[b] = *(LAS f32x2*)(size_t)(tb + b * 2048 + 16); }
#pragma unroll
  for (int bb = 0; bb < 3; bb++) {
    const int b = (bb + 2) % 3;
    u32x4 H[2][2];
    const f32x2 cin = cins[b];
    const f32x2 sr = {cin.x, l8[b].x * cin.x - l8[b].y * cin.y + e0[b].x}, si = {cin.y, l8[b].x * cin.y + l8[b].y * cin.x + e0[b].y};
    const f32x2 cf = *(LAS f32x2*)(size_t)(tb + b * 2048 + 8); const float c2r = cf.x, c2i = cf.y;
    f32x2 hr = c2r * sr - c2i * si, hi = -c2r * si - c2i * sr;
#pragma unroll
    for (int T = 0; T < 4; T++) {
      f32x2 pr[2], pi[2];
#pragma unroll
      for (int k = 0; k < 2; k++) {
        const f32x2 ur = {ar[b][T][2 * k], ar[b][T][2 * k + 1]}, ui = {ai[b][T][2 * k], ai[b][T][2 * k + 1]};
        const f32x2 nr = lc[b][0] * hr + (ur + lc[b][1] * hi), ni = lc[b][0] * hi + (ui - lc[b][1] * hr);
        hr = nr; hi = ni; pr[k] = nr; pi[k] = ni;
      }
      H[0][0][T] = cvtpk(pr[0].x, pr[1].x); H[0][1][T] = cvtpk(pr[0].y, pr[1].y);
      H[1][0][T] = cvtpk(pi[0].x, pi[1].x); H[1][1][T] = cvtpk(pi[0].y, pi[1].y);
    }
    if (bb == 1) BAR();
#pragma unroll
    for (int k = 0; k < 4; k++) {
      st16(H[k >> 1][k & 1], rWS, (k & 1) ? io1 : io0, pg + (2 * b + (k >> 1)) * 16384);
      if (b < 2) wr128(lds0 + TOFF + ((k & 1) ? io1 : io0), ((19 + 2 * b + (k >> 1)) & 3) * TSLOT, H[k >> 1][k & 1]);
    }
    SB();
  }
  phaseB<true>(p, rt, 4, j, w, lane, lds0, tag);
}

DI void spare(const Params& p, int sid, u32 lds0, u32 tag) {
  const int tid = threadIdx.x;
  const rsrc_t rWS = mk(p.ws, WS_END);
  const int w = __builtin_amdgcn_readfirstlane(tid >> 6), lane = tid & 63;
  if (sid == 15 && w == 11) { st_flag(p.ws, FL_GO, 0u); st16((u32x4){0u, 0u, 0u, 0u}, rWS, lane * 16, WS_ACK); waitv<0>(); hs_leader(p.ws, lane, lds0, STAMPPTR); return; }
  u32 hsn = 0;
  if (w == 11) { hsn = ld_flag(p.ws + WS_RSP + blockIdx.x * 4) + 1u; st_word(p.ws + WS_REQ + blockIdx.x * 4, hsn); }
  else if (w < 8) {
    const int row = tid >> 2, part = tid & 3;
    const GAS u32x4* src = (const GAS u32x4*)(p.x + (sid * 128 + row) * D) + part;
    u32x4 v[18];
#pragma unroll
    for (int i = 0; i < 18; i++) v[i] = src[4 * i];
    float s = 0.f, s2 = 0.f;
#pragma unroll
    for (int i = 0; i < 18; i++) { s = dot2(v[i][0], ONES, s); s2 = dot2(v[i][1], ONES, s2); s = dot2(v[i][2], ONES, s); s2 = dot2(v[i][3], ONES, s2); }
    s += s2; s += dppx<0xb1>(s); s += dppx<0x4e>(s);
    const float mean = s * (1.f / D);
    f32x2 dv = {0.f, 0.f};
#pragma unroll
    for (int i = 0; i < 72; i++) { const u32 wd = v[i >> 2][i & 3]; const f32x2 d = (f32x2){bflo(wd), bfhi(wd)} - mean; dv += d * d; }
    s = dv.x + dv.y; s += dppx<0xb1>(s); s += dppx<0x4e>(s);
    const float ve = s * (1.f / D) + 1e-5f, rstd = __builtin_amdgcn_rsqf(ve), sig = ve * rstd;
    float m1, m2, m3; split3(-mean, m1, m2, m3);
    const float g1 = rbf(sig), g2 = sig - g1;
    const int ir = (row & 64) + 16 * ((row >> 1) & 3) + 4 * ((row >> 4) & 3) + 2 * (row & 1) + ((row >> 3) & 1);
    if (part == 0) wr128(lds0 + row * 16, 0, (u32x4){cvtpk(m1, m1), cvtpk(m2, m2), cvtpk(m1, m3), cvtpk(g1, g2)});
    else *(LAS float*)(size_t)(lds0 + 2048 + (part - 1) * 512 + row * 4) = part == 1 ? mean : (part == 2 ? rstd : sig);
    if (part >= 2) *(LAS float*)(size_t)(lds0 + 4096 + (part - 2) * 512 + ir * 4) = part == 2 ? -mean * rstd : rstd;
  } else if ((w == 8 || w == 10) && sid < 15) {
    __builtin_amdgcn_s_sleep(10);
#pragma unroll
    for (int i = 0; i < 6; i++) {
      const u32 L = min(sid * 346 + i * 64 + lane, 5183);
      asm volatile("s_mov_b32 m0, %0\n\ts_nop 0\n\tglobal_load_lds_dword %1, %2" :: "s"(lds0 + 8192), "v"(L * 128), "s"(w == 8 ? p.cre : p.cim) : "memory");
    }
  } else if (w == 9 && sid < 9) {
    const int c = sid * 64 + lane;
    const float lr = bf2f(p.lre[c]), li = bf2f(p.lim[c]);
    const float dt = rbf(expf(bf2f(p.logdt[c])));
    const float ex = expf(dt * lr), an = dt * li;
    const float br = ex * cosf(an), bi = ex * sinf(an);
    const float nr = br - 1.f, den = 1.f / (lr * lr + li * li);
    const float cr = (nr * lr + bi * li) * den, ci = (bi * lr - nr * li) * den;
    float ar = br, ai = bi, r8 = 0.f, i8 = 0.f;
#pragma unroll
    for (int k = 0; k < 7; k++) { const float t = ar * ar - ai * ai; ai = 2.f * ar * ai; ar = t; if (k == 2) { r8 = ar; i8 = ai; } }
    st16(__builtin_bit_cast(u32x4, (f32x4){br, bi, 2.f * cr, 2.f * ci}), rWS, c * 32, WS_TAB);
    st16(__builtin_bit_cast(u32x4, (f32x4){r8, i8, ar, ai}), rWS, c * 32 + 16, WS_TAB);
  }
  WAITL(); BAR();
  if (w < 5) st16(__builtin_bit_cast(u32x4, rd128(lds0 + tid * 16, 0)), rWS, tid * 16, WS_LN + sid * 5120);
  if (w == 11) spin_all(p.ws + WS_RSP + blockIdx.x * 4, hsn);
  waitv<0>(); BAR();
  if (sid == 15) return;
  if (w == 11) st_word(p.ws + WS_ACK + blockIdx.x * 4, 1u);
  {
    u32 acc = 0;
    const GAS u32* w2 = (const GAS u32*)p.w2;
#pragma unroll
    for (int i = 0; i < 4; i++) {
      const int L = (i * 15 + sid) * 768 + tid, r = L / 9, kb = r / 1536, rr = r - kb * 1536;
      if (L < 41472) acc ^= w2[((rr >> 5) * 96 + kb * 32 + (rr & 31)) * (ROWB / 4) + (L - r * 9) * 32];
    }
    asm volatile("" :: "v"(acc));
  }
}

__global__ __launch_bounds__(768, 3) void fused_kernel(Params p) {
  extern __shared__ __attribute__((aligned(16))) char smem[];
  u64x4 kv; asm volatile("s_load_dwordx8 %0, %1, 0x0" : "=s"(kv) : "s"(__builtin_amdgcn_kernarg_segment_ptr())); SB();
  const u32 lds0 = (u32)(size_t)(LAS char*)smem;
  const int b = blockIdx.x, xcd = b & 7, idx = b >> 3;
  const u32 tag = 1u;
  if (b < 32 && (threadIdx.x >> 7) == 1) {
    u64 pc0; asm volatile("s_getpc_b64 %0" : "=s"(pc0));
    const u32 ln = min((b >> 3) * 128 + (threadIdx.x & 127), (u32)CODE_LINES);
    asm volatile("s_mov_b32 m0, %0\n\ts_nop 0\n\tglobal_load_lds_dword %1, %2" :: "s"(lds0 + TOFF + 3 * TSLOT), "v"(ln * 128), "s"(pc0) : "memory");
  }
  STAMP(0);
  const int tri = idx / 3, j = idx - 3 * tri;
  const int g = tri < 2 ? 4 : xcd >> 1, rt = 8 * (xcd & 1) + (tri < 2 ? (xcd & 6) + tri : tri - 2);
  const int lane = threadIdx.x & 63, w = __builtin_amdgcn_readfirstlane(threadIdx.x >> 6);
  const int sid = xcd * 2 + idx - 30;
  if (__builtin_expect(idx < 30 && w < 2, 1)) { loader(kv, rt, g, j, w, lane, lds0, tag); return; }
  asm volatile("s_waitcnt lgkmcnt(0)" :: "s"(kv));
  if (idx < 30 && w >= 4) {
    if (g < 4) ffn_compute(rt, g, j, w - 4, lane, lds0, tag); else ssm_compute(rt, j, w - 4, lane, lds0, tag);
    return;
  }
  const Params q = reload_params();
  if (idx < 30) helper(q, rt, g, j, w - 2, lane, lds0, tag); else { spare(q, sid, lds0, tag); STAMP(7); }
}

extern "C" void kernel_launch(void* const* d_in, const int* in_sizes, int n_in, void* d_out, int out_size, void* d_ws, size_t ws_size, hipStream_t stream) {
  Params p;
  p.x = (const u16*)d_in[0]; p.lre = (const u16*)d_in[1]; p.lim = (const u16*)d_in[2]; p.bre = (const u16*)d_in[3]; p.bim = (const u16*)d_in[4];
  p.cre = (const u16*)d_in[5]; p.cim = (const u16*)d_in[6]; p.logdt = (const u16*)d_in[7]; p.dskip = (const u16*)d_in[8];
  p.w1 = (const u16*)d_in[10]; p.b1 = (const u16*)d_in[11]; p.w2 = (const u16*)d_in[12]; p.b2 = (const u16*)d_in[13];
  p.out = (float*)d_out; p.ws = (char*)d_ws + ((ws_size - WS_END) & ~(size_t)0x1fffff);
  (void)hipFuncSetAttribute((const void*)fused_kernel, hipFuncAttributeMaxDynamicSharedMemorySize, LDS_BYTES);
  fused_kernel<<<256, 768, LDS_BYTES, stream>>>(p);
}
```

```cpp
#include <hip/hip_runtime.h>
#include <utility>

#define LAS __attribute__((address_space(3)))
#define GAS __attribute__((address_space(1)))
#define DI __device__ __forceinline__
typedef unsigned short u16;
typedef unsigned u32;
typedef unsigned long long u64;
typedef unsigned u32x4 __attribute__((ext_vector_type(4)));
typedef unsigned u32x2 __attribute__((ext_vector_type(2)));
typedef short s16x4 __attribute__((ext_vector_type(4)));
typedef short bf16x8 __attribute__((ext_vector_type(8)));
typedef float f32x4 __attribute__((ext_vector_type(4)));
typedef float f32x2 __attribute__((ext_vector_type(2)));
typedef __bf16 bf16x2 __attribute__((ext_vector_type(2)));
typedef __amdgpu_buffer_rsrc_t rsrc_t;
typedef u64 u64x4 __attribute__((ext_vector_type(4)));

#define RAMP_0 1250
#define RAMP_D 100
#define RAMP_E 0
constexpr int S = 2048, D = 576, F = 4608;
constexpr int ROWB = D * 2;
constexpr int WSLOT = 24576, TOFF = 4 * WSLOT, TSLOT = 16384, LDS_BYTES = 163840;
constexpr int AX_W = 0, AX_T = 6656, AX_S = 8704, AX_TAB = 10752;
constexpr u32 WS_FLAGS = 0;
constexpr u32 FL_GO = 0, FL_P = 32, FL_O = 32 + 240, NFLAG = 32 + 480;
constexpr u32 WS_EG = NFLAG * 128;
constexpr u32 WS_REQ = WS_EG + 16 * 576 * 16, WS_RSP = WS_REQ + 1024, WS_ACK = WS_RSP + 1024;
constexpr u32 WS_CS = 256 * 1024;
constexpr u32 WS_LN = WS_CS + 15 * 16 * 1536;
constexpr u32 WS_TAB = WS_LN + 16 * 5120;
constexpr u32 WS_P = 1u << 20;
constexpr u32 XBW = 5 * 1536;
constexpr u32 WS_XB = WS_P + 240 * 98304;
constexpr u32 WS_END = WS_XB + 48 * 32 * XBW;
constexpr int CODE_LINES = 470;
constexpr u32 SPIN_CAP = 1u << 18;

struct Params {
  const u16 *x, *w1, *bre, *bim;
  const u16 *lre, *lim, *cre, *cim, *logdt, *dskip, *b1, *w2, *b2;
  float* out; char* ws;
};

DI Params reload_params() {
  Params q;
#if defined(__HIP_DEVICE_COMPILE__)
  typedef __attribute__((address_space(4))) const Params* KP;
  KP kp = (KP)__builtin_amdgcn_kernarg_segment_ptr(); asm volatile("" : "+s"(kp)); q = *kp;
#endif
  return q;
}
template <int N> DI void waitv() { asm volatile("s_waitcnt vmcnt(%0)" :: "n"(N) : "memory"); }
#define WAITL() asm volatile("s_waitcnt lgkmcnt(0)" ::: "memory")
#define BAR() do { asm volatile("" ::: "memory"); __builtin_amdgcn_s_barrier(); asm volatile("" ::: "memory"); } while (0)
#define SB() __builtin_amdgcn_sched_barrier(0)
#define MFENCE() do { SB(); asm volatile("s_nop 7\n\ts_nop 7"); SB(); } while (0)
#define MMA(c, a, b) asm volatile("v_mfma_f32_16x16x32_bf16 %0, %1, %2, %0" : "+v"(c) : "v"(a), "v"(b))
#define MMA0(c, a, b) asm volatile("v_mfma_f32_16x16x32_bf16 %0, %1, %2, 0" : "=&v"(c) : "v"(a), "v"(b))
#define MMAN(c, a, b) asm volatile("s_nop 1\n\tv_mfma_f32_16x16x32_bf16 %0, %1, %2, %0" : "+v"(c) : "v"(a), "v"(b))
#define PIN(x) asm volatile("" : "+v"(x))
#define STAMPPTR nullptr
#define STAMP(i)

DI void dma(rsrc_t r, u32 lds, u32 voff, u32 soff) {
  asm volatile("s_mov_b32 m0, %0\n\ts_nop 0\n\tbuffer_load_dwordx4 %1, %2, %3 offen lds" :: "s"(lds), "v"(voff), "s"(r), "s"(soff) : "memory");
}
DI void dma_sc1(rsrc_t r, u32 lds, u32 voff, u32 soff) {
  asm volatile("s_mov_b32 m0, %0\n\ts_nop 0\n\tbuffer_load_dwordx4 %1, %2, %3 offen sc1 lds" :: "s"(lds), "v"(voff), "s"(r), "s"(soff) : "memory");
}
DI void dma12(rsrc_t r, u32 lds, u32 voff, u32 so, u32 d2, u32 st, u32 st3) {
  u32 t;
  asm volatile("s_mov_b32 m0, %2\n\t"
               "s_add_u32 %1, %0, %5\n\t"
               "buffer_load_dwordx4 %3, %4, %0 offen lds\n\t"
               "s_add_u32 m0, m0, 0x800\n\t"
               "s_add_u32 %0, %0, %6\n\t"
               "buffer_load_dwordx4 %3, %4, %1 offen lds\n\t"
               "s_add_u32 m0, m0, 0x800\n\t"
               "s_add_u32 %1, %1, %6\n\t"
               "buffer_load_dwordx4 %3, %4, %0 offen lds\n\t"
               "s_add_u32 m0, m0, 0x800\n\t"
               "s_add_u32 %0, %0, %6\n\t"
               "buffer_load_dwordx4 %3, %4, %1 offen lds\n\t"
               "s_add_u32 m0, m0, 0x800\n\t"
               "s_add_u32 %1, %1, %6\n\t"
               "buffer_load_dwordx4 %3, %4, %0 offen lds\n\t"
               "s_add_u32 m0, m0, 0x800\n\t"
               "s_add_u32 %0, %0, %7\n\t"
               "buffer_load_dwordx4 %3, %4, %1 offen lds\n\t"
               "s_add_u32 m0, m0, 0x800\n\t"
               "s_add_u32 %1, %1, %7\n\t"
               "buffer_load_dwordx4 %3, %4, %0 offen lds\n\t"
               "s_add_u32 m0, m0, 0x800\n\t"
               "s_add_u32 %0, %0, %6\n\t"
               "buffer_load_dwordx4 %3, %4, %1 offen lds\n\t"
               "s_add_u32 m0, m0, 0x800\n\t"
               "s_add_u32 %1, %1, %6\n\t"
               "buffer_load_dwordx4 %3, %4, %0 offen lds\n\t"
               "s_add_u32 m0, m0, 0x800\n\t"
               "s_add_u32 %0, %0, %6\n\t"
               "buffer_load_dwordx4 %3, %4, %1 offen lds\n\t"
               "s_add_u32 m0, m0, 0x800\n\t"
               "s_add_u32 %1, %1, %6\n\t"
               "buffer_load_dwordx4 %3, %4, %0 offen lds\n\t"
               "s_add_u32 m0, m0, 0x800\n\t"
               "s_add_u32 %0, %0, %6\n\t"
               "buffer_load_dwordx4 %3, %4, %1 offen lds"
               : "+&s"(so), "=&s"(t) : "s"(lds), "v"(voff), "s"(r), "s"(d2), "s"(st), "s"(st3) : "memory", "scc");
}
DI void dma4(rsrc_t r, u32 lds, u32 voff, u32 so, u32 st) {
  asm volatile("s_mov_b32 m0, %1\n\t"
               "s_nop 0\n\t"
               "buffer_load_dwordx4 %2, %3, %0 offen lds\n\t"
               "s_add_u32 m0, m0, 0x800\n\t"
               "s_add_u32 %0, %0, %4\n\t"
               "buffer_load_dwordx4 %2, %3, %0 offen lds\n\t"
               "s_add_u32 m0, m0, 0x800\n\t"
               "s_add_u32 %0, %0, %4\n\t"
               "buffer_load_dwordx4 %2, %3, %0 offen lds\n\t"
               "s_add_u32 m0, m0, 0x800\n\t"
               "s_add_u32 %0, %0, %4\n\t"
               "buffer_load_dwordx4 %2, %3, %0 offen lds"
               : "+&s"(so) : "s"(lds), "v"(voff), "s"(r), "s"(st) : "memory", "scc");
}
DI void dma8lin(rsrc_t r, u32 lds, u32 voff, u32 so) {
  asm volatile("s_mov_b32 m0, %1\n\t"
               "s_nop 0\n\t"
               "buffer_load_dwordx4 %2, %3, %0 offen offset:0 sc1 lds\n\t"
               "buffer_load_dwordx4 %2, %3, %0 offen offset:1024 sc1 lds\n\t"
               "buffer_load_dwordx4 %2, %3, %0 offen offset:2048 sc1 lds\n\t"
               "buffer_load_dwordx4 %2, %3, %0 offen offset:3072 sc1 lds\n\t"
               "s_add_u32 m0, m0, 0x2000\n\t"
               "s_add_u32 %0, %0, 0x2000\n\t"
               "buffer_load_dwordx4 %2, %3, %0 offen offset:0 sc1 lds\n\t"
               "buffer_load_dwordx4 %2, %3, %0 offen offset:1024 sc1 lds\n\t"
               "buffer_load_dwordx4 %2, %3, %0 offen offset:2048 sc1 lds\n\t"
               "buffer_load_dwordx4 %2, %3, %0 offen offset:3072 sc1 lds"
               : "+&s"(so) : "s"(lds), "v"(voff), "s"(r) : "memory", "scc");
}
DI rsrc_t mk(const void* ptr, u32 bytes) { return __builtin_amdgcn_make_buffer_rsrc((void*)ptr, 0, bytes, 0x00020000); }
DI s16x4 rd_tr1(u32 a, int off) { return __builtin_amdgcn_ds_read_tr16_b64_v4i16((LAS s16x4*)(size_t)(a + off)); }
DI bf16x8 cat(s16x4 lo, s16x4 hi) { return __builtin_shufflevector(lo, hi, 0, 1, 2, 3, 4, 5, 6, 7); }
DI bf16x8 rd_tr(u32 a, int off) { return cat(rd_tr1(a, off), rd_tr1(a, off + 2048)); }
DI bf16x8 rd128(u32 a, int off) { return *(LAS bf16x8*)(size_t)(a + off); }
DI f32x4 rdf4(u32 a, int off) { return *(LAS f32x4*)(size_t)(a + off); }
DI float rdf(u32 a, int off) { return *(LAS float*)(size_t)(a + off); }
DI void wr128(u32 a, int off, u32x4 v) { *(LAS u32x4*)(size_t)(a + off) = v; }
DI u32 cvtpk(float lo, float hi) { f32x2 v = {lo, hi}; bf16x2 b = __builtin_convertvector(v, bf16x2); return __builtin_bit_cast(u32, b); }
DI float bflo(u32 v) { return __uint_as_float(v << 16); }
DI float bfhi(u32 v) { return __uint_as_float(v & 0xffff0000u); }
DI float bf2f(u16 v) { return __uint_as_float((u32)v << 16); }
DI float dot2(u32 a, u32 b, float c) { return __builtin_amdgcn_fdot2_f32_bf16(__builtin_bit_cast(bf16x2, a), __builtin_bit_cast(bf16x2, b), c, false); }
constexpr u32 ONES = 0x3f803f80u;
DI float rbf(float v) { return bflo(cvtpk(v, 0.f)); }
DI void split3(float v, float& a, float& b, float& c) { a = rbf(v); const float r = v - a; b = rbf(r); c = rbf(r - b); }
template <int C> DI float dppx(float v) { return __uint_as_float(__builtin_amdgcn_update_dpp(0u, __float_as_uint(v), C, 0xf, 0xf, true)); }
template <int C> DI double dppd(double v) { u32x2 t = __builtin_bit_cast(u32x2, v); t[0] = __builtin_amdgcn_update_dpp(0u, t[0], C, 0xf, 0xf, true); t[1] = __builtin_amdgcn_update_dpp(0u, t[1], C, 0xf, 0xf, true); return __builtin_bit_cast(double, t); }
DI void st16(u32x4 v, rsrc_t r, u32 voff, u32 soff) { asm volatile("buffer_store_dwordx4 %0, %1, %2, %3 offen sc1\n\ts_nop 1" :: "v"(v), "v"(voff), "s"(r), "s"(soff) : "memory"); }
DI void st_flag(char* ws, u32 idx, u32 tag) { __hip_atomic_store((GAS u32*)(ws + WS_FLAGS + idx * 128), tag, __ATOMIC_RELAXED, __HIP_MEMORY_SCOPE_AGENT); }
DI u32 ld_flag(const char* a) { return __hip_atomic_load((GAS u32*)a, __ATOMIC_RELAXED, __HIP_MEMORY_SCOPE_AGENT); }
DI void spin_all(const char* a, u32 tag) {
#pragma nounroll
  for (u32 i = 0; i < SPIN_CAP; i++) { if (__all(ld_flag(a) == tag)) break; __builtin_amdgcn_s_sleep(1); }
  asm volatile("" ::: "memory");
}
DI void spin_ge(const char* a, u32 lvl) {
#pragma nounroll
  for (u32 i = 0; i < SPIN_CAP; i++) { if (__all(ld_flag(a) >= lvl)) break; __builtin_amdgcn_s_sleep(1); }
  asm volatile("" ::: "memory");
}
DI void st_word(char* a, u32 v) { __hip_atomic_store((GAS u32*)a, v, __ATOMIC_RELAXED, __HIP_MEMORY_SCOPE_AGENT); }
DI void hs_leader(char* ws, int lane, u32 lds0, u64* stamps) {
  const rsrc_t r = mk(ws, WS_END);
  bool first = true; u32x4 last = {0u, 0u, 0u, 0u};
#pragma nounroll
  for (u32 i = 0; i < SPIN_CAP; i++) {
    asm volatile("" ::: "memory");
    const u32x4 rq = __builtin_amdgcn_raw_buffer_load_b128(r, lane * 16, WS_REQ, 16), ak = __builtin_amdgcn_raw_buffer_load_b128(r, lane * 16, WS_ACK, 16);
    if (first || rq[0] != last[0] || rq[1] != last[1] || rq[2] != last[2] || rq[3] != last[3]) st16(rq, r, lane * 16, WS_RSP);
    first = false; last = rq;
    if (__all(ak[0] == 1u && ak[1] == 1u && ak[2] == 1u && (ak[3] == 1u || lane == 63))) break;
    __builtin_amdgcn_s_sleep(1);
  }
  BAR();
  st_flag(ws, FL_GO, 1u);
}
DI float row_sum16(float v) { v += dppx<0xb1>(v); v += dppx<0x4e>(v); v += dppx<0x141>(v); v += dppx<0x140>(v); return v; }

DI f32x2 gelu2(f32x2 a, float k1, float k2, float sg) {
  f32x2 t = a * a; t = t * k2 + k1; t = t * a;
  t.x = __builtin_amdgcn_exp2f(t.x); t.y = __builtin_amdgcn_exp2f(t.y);
  t = t * sg + sg;
  t.x = __builtin_amdgcn_rcpf(t.x); t.y = __builtin_amdgcn_rcpf(t.y);
  return t * a;
}
DI u32x4 gelu_frag(f32x4 a, f32x4 b, float k1, float k2, float sg) {
  f32x2 g0 = gelu2((f32x2){a[0], a[1]}, k1, k2, sg), g1 = gelu2((f32x2){a[2], a[3]}, k1, k2, sg);
  f32x2 g2 = gelu2((f32x2){b[0], b[1]}, k1, k2, sg), g3 = gelu2((f32x2){b[2], b[3]}, k1, k2, sg);
  return (u32x4){cvtpk(g0.x, g0.y), cvtpk(g1.x, g1.y), cvtpk(g2.x, g2.y), cvtpk(g3.x, g3.y)};
}

template <int H, class UPD>
DI void stepA(f32x4 (&acc)[6][4], bf16x8 (&wp)[2], bf16x8 (&t)[4], const u32 (&wc)[4], const u32& n0, const u32& n1, const u32& xn, UPD&& upd) {
  bf16x8 w[8], nx[4];
  w[0] = wp[0]; w[1] = wp[1];
#pragma unroll
  for (int n = 0; n < 6; n++) {
    const int f = n + 2, ff = f < 6 ? f : f - 6;
    const u32 a = (f < 6 || H == 0) ? wc[ff & 3] : (ff ? n1 : n0); const int off = (f < 6 ? H : 1 - H) * WSLOT + (ff >> 2) * 4096;
    MMA(acc[n][0], w[n], t[0]); SB();
    s16x4 lo = rd_tr1(a, off); SB();
    MMA(acc[n][1], w[n], t[1]); SB();
    s16x4 hi = rd_tr1(a, off + 2048); SB();
    MMA(acc[n][2], w[n], t[2]); SB();
    if (n >= 4) { nx[2 * n - 8] = rd128(xn, (2 * n - 8) * 2048); SB(); }
    if (n == 1) { upd(); SB(); }
    MMA(acc[n][3], w[n], t[3]); SB();
    if (n >= 4) { nx[2 * n - 7] = rd128(xn, (2 * n - 7) * 2048); SB(); }
    if (n == 3) { BAR(); SB(); }
    w[f] = cat(lo, hi);
  }
  wp[0] = w[6]; wp[1] = w[7];
#pragma unroll
  for (int i = 0; i < 4; i++) t[i] = nx[i];
}

template <int KIND, bool ZERO = false, class FW, class FT, class FN, class MID>
DI void half(f32x4 (&acc)[3][4], bf16x8 (&w)[3], bf16x8 (&tp)[2], FW&& rw, FT&& rt_, FN&& rn, MID&& mid) {
  bf16x8 t[4], wn[3], tn[2];
  t[0] = tp[0]; t[1] = tp[1];
  s16x4 lo;
#pragma unroll
  for (int i = 0; i < 12; i++) {
    const int T = i / 3, n = i % 3;
    if constexpr (KIND == 2) MMA(acc[n][T], t[T], w[n]); else if constexpr (ZERO) MMA0(acc[n][T], w[n], t[T]); else MMA(acc[n][T], w[n], t[T]);
    SB();
    if constexpr (KIND == 0) {
      if (i == 0) t[2] = rt_(2, 0); if (i == 1) t[3] = rt_(3, 0);
      if (i >= 2 && i <= 7) { if ((i & 1) == 0) lo = rw((i - 2) >> 1, 0); else wn[(i - 2) >> 1] = cat(lo, rw((i - 2) >> 1, 1)); }
      if (i == 8) tn[0] = rn(0, 0); if (i == 9) tn[1] = rn(1, 0);
    } else if constexpr (KIND == 1) {
      if (i <= 3) { if ((i & 1) == 0) lo = rt_(2 + (i >> 1), 0); else t[2 + (i >> 1)] = cat(lo, rt_(2 + (i >> 1), 1)); }
      if (i >= 4 && i <= 6) wn[i - 4] = rw(i - 4, 0);
      if (i >= 7 && i <= 10) { if ((i & 1) == 1) lo = rn((i - 7) >> 1, 0); else tn[(i - 7) >> 1] = cat(lo, rn((i - 7) >> 1, 1)); }
    } else {
      if (i == 0) t[2] = rt_(2, 0); if (i == 2) t[3] = rt_(3, 0);
      if (i == 3 || i == 5 || i == 6) wn[i == 3 ? 0 : i - 4] = rw(i == 3 ? 0 : i - 4, 0);
      if (i == 8) tn[0] = rn(0, 0); if (i == 9) tn[1] = rn(1, 0);
    }
    mid(i);
    SB();
  }
#pragma unroll
  for (int i = 0; i < 3; i++) w[i] = wn[i];
  tp[0] = tn[0]; tp[1] = tn[1];
}

template <int I, int N, class FN> DI void sfor(FN&& f) { if constexpr (I < N) { f(std::integral_constant<int, I>{}); sfor<I + 1, N>(f); } }

DI f32x2 cmul(f32x2 a, f32x2 b) { return (f32x2){a.x * b.x - a.y * b.y, a.x * b.y + a.y * b.x}; }
DI f32x2 cfma(f32x2 a, f32x2 b, f32x2 c) { return (f32x2){__builtin_fmaf(a.x, b.x, __builtin_fmaf(-a.y, b.y, c.x)), __builtin_fmaf(a.x, b.y, __builtin_fmaf(a.y, b.x, c.y))}; }
struct CarryPre { f32x2 l16, l32, l64, L1, L4; };
DI CarryPre carry_pre(int c, u32 lds0) {
  const f32x4 pw = rdf4(lds0 + (18 & 3) * WSLOT + AX_TAB + c * 32, 16);
  CarryPre k; const f32x2 l8 = {pw[0], pw[1]};
  k.l16 = cmul(l8, l8); k.l32 = cmul(k.l16, k.l16); k.l64 = cmul(k.l32, k.l32); k.L1 = (f32x2){pw[2], pw[3]};
  const f32x2 L2 = cmul(k.L1, k.L1); k.L4 = cmul(L2, L2);
  PIN(k.l16); PIN(k.l32); PIN(k.l64); PIN(k.L1); PIN(k.L4);
  return k;
}
DI void ssm_carry(const Params& p, const CarryPre& k, int rt, int j, int c, u32 lds0) {
  const u32 scr = lds0 + TOFF + (22 & 3) * TSLOT + c * 8;
  f32x2 e[8];
#pragma unroll
  for (int s = 0; s < 8; s++) e[s] = *(LAS f32x2*)(size_t)(scr + s * 1536);
  const f32x2 a01 = cfma(k.l16, e[0], e[1]), a23 = cfma(k.l16, e[2], e[3]), a45 = cfma(k.l16, e[4], e[5]), a67 = cfma(k.l16, e[6], e[7]);
  const f32x2 a03 = cfma(k.l32, a01, a23), E = cfma(k.l64, a03, cfma(k.l32, a45, a67));
  const rsrc_t rE = mk(p.ws + WS_EG, 16 * 9216);
  const int vo = (192 * j + c) * 16 + (15 - rt) * 9216;
  st16((u32x4){__float_as_uint(E.x), 1u, __float_as_uint(E.y), 1u}, rE, vo, 0);
  u32x4 va[15], vb[15];
  auto issue = [&](u32x4 (&v)[15]) {
    asm volatile("" ::: "memory");
#pragma unroll
    for (int i = 0; i < 15; i++) v[i] = __builtin_amdgcn_raw_buffer_load_b128(rE, vo, (15 - i) * 9216, 16);
    asm volatile("" ::: "memory");
  };
  f32x2 cy;
  auto eval = [&](u32x4 (&v)[15]) {
    u32 n = 0; f32x2 q[4];
#pragma unroll
    for (int i = 0; i < 15; i++) {
      n += v[i][1] + v[i][3];
      const f32x2 x = {__uint_as_float(v[i][0]), __uint_as_float(v[i][2])};
      const int ch = (i + 1) & 3;
      q[ch] = i < 4 ? x : cfma(k.L4, q[ch], x);
    }
    cy = cfma(k.L1, cfma(k.L1, cfma(k.L1, q[0], q[1]), q[2]), q[3]);
    return __all(n == 2u * rt);
  };
  __builtin_amdgcn_s_sleep(6);
  issue(va);
  __builtin_amdgcn_s_sleep(10);
  issue(vb);
  if (!eval(va)) {
    issue(va);
    if (!eval(vb) && !eval(va)) {
#pragma nounroll
      for (u32 spin = 0; spin < SPIN_CAP; spin++) { u32x4 vc[15]; issue(vc); if (eval(vc)) break; }
    }
  }
#pragma unroll
  for (int s = 0; s < 8; s += 2) e[s] = *(LAS f32x2*)(size_t)(scr + s * 1536);
  const f32x2 c2 = cfma(k.l32, cy, a01), c4 = cfma(k.l64, cy, a03), c6 = cfma(k.l32, c4, a45);
  const f32x2 cin[8] = {cy, cfma(k.l16, cy, e[0]), c2, cfma(k.l16, c2, e[2]), c4, cfma(k.l16, c4, e[4]), c6, cfma(k.l16, c6, e[6])};
#pragma unroll
  for (int s = 0; s < 8; s++) *(LAS f32x2*)(size_t)(scr + s * 1536) = cin[s];
}

#define LBAR() PBAR()
#define PBAR() do { if (RAMP_0) { while ((int)((u32)__builtin_amdgcn_s_memtime() - tnext) < 0) {} } BAR(); \
    if (RAMP_0) { tnext = (u32)__builtin_amdgcn_s_memtime() + pace; pace = max(pace - RAMP_D, pmin); } } while (0)
DI void loader(u64x4 kv, int rt, int g, int j, int u, int lane, u32 lds0, u32 tag) {
  const bool ssm = g == 4;
  const u32 wbytes = ssm ? D * ROWB : D * F * 2;
  const int r8 = lane >> 3, gp = lane & 7;
  const int cgr = ((((gp >> 1) ^ ((r8 >> 1) & 3)) << 1) | (gp & 1));
  const int rgr = gp ^ (4 * u + (r8 >> 1));
  const int slice = 3 * g + j;
  const int kA = 16 * u + 8 * (r8 >> 2) + (r8 & 3);
  const int R = 8 * u + r8;
  const u32 voffA = ssm ? (192 * j + R) * ROWB + rgr * 16 : kA * (F * 2) + slice * 768 + cgr * 16;
  const int tok = ssm ? 16 * ((R >> 2) & 3) + 8 * (R & 1) + ((R >> 1) & 1) : R;
  const u32 voffX = (rt * 128 + tok) * ROWB + rgr * 16;
  const u32 ldsW = lds0 + u * 1024, ldsT = lds0 + TOFF;
  const u32 aKt = ssm ? 128 : 64 * F * 2, aPar = ssm ? 0 : 32 * F * 2, pstr = ssm ? 32 * ROWB : 128, a2 = ssm ? 16 * ROWB : 4 * F * 2, xstr = (ssm ? 2 : 16) * ROWB;
  const int rot = (rt >= 10 ? rt - 10 : rt) & ~1;
  asm volatile("s_waitcnt lgkmcnt(0)" : "+s"(kv) : "v"(voffA), "v"(voffX), "s"(aKt), "s"(rot));
  const rsrc_t rX = mk((void*)kv[0], S * ROWB), rA0 = mk((void*)(ssm ? kv[2] : kv[1]), wbytes), rA1 = mk((void*)(ssm ? kv[3] : kv[1]), wbytes);
  auto ktile = [&](int kap) { int kt = kap + rot; return kt >= 9 ? kt - 9 : kt; };
  auto issueA = [&](int kap, int h) { dma12(h ? rA1 : rA0, ldsW + ((2 * kap + h) & 3) * WSLOT, voffA, ktile(kap) * aKt + h * aPar, a2, pstr, pstr); };
  auto issueX = [&](int kap, int h) { dma4(rX, ldsT + (kap & 3) * TSLOT + h * 8192 + u * 1024, voffX, h * 64 * ROWB + ktile(kap) * 128, xstr); };
  issueA(0, 0); issueX(0, 0); issueX(0, 1); issueA(0, 1);
  __builtin_amdgcn_s_setprio(3);
  waitv<12>(); BAR();
  issueX(1, 0); issueX(1, 1); issueA(1, 0);
#pragma nounroll
  for (int kap = 0; kap < 7; kap++) {
    u32 l1 = ldsW + ((2 * kap + 3) & 3) * WSLOT, s1 = ktile(kap + 1) * aKt + aPar, l2 = ldsW + ((2 * kap) & 3) * WSLOT, s2 = ktile(kap + 2) * aKt;
    u32 lx = ldsT + ((kap + 2) & 3) * TSLOT + u * 1024, lx2 = lx + 8192, sx = ktile(kap + 2) * 128, sx2 = sx + 64 * ROWB;
    asm volatile("" : "+s"(l1), "+s"(s1), "+s"(l2), "+s"(s2), "+s"(lx), "+s"(lx2), "+s"(sx), "+s"(sx2));
    waitv<16>(); BAR(); dma12(rA1, l1, voffA, s1, a2, pstr, pstr); dma4(rX, lx, voffX, sx, xstr);
    waitv<16>(); BAR(); dma12(rA0, l2, voffA, s2, a2, pstr, pstr); dma4(rX, lx2, voffX, sx2, xstr);
  }
  waitv<16>(); BAR(); issueA(8, 1);
  const Params p = reload_params();
  const u32 voffL = lane * 16;
  const rsrc_t rWS = mk(p.ws, WS_END), rB0 = mk(ssm ? p.cre : p.w2, wbytes), rB1 = mk(ssm ? p.cim : p.w2, wbytes);
  const u32 voffB = ssm ? voffA : R * ROWB + j * 384 + cgr * 16;
  const u32 bJ = ssm ? 384 : 384 * ROWB, bPar = ssm ? 0 : 192 * ROWB, bI = ssm ? 128 : 32 * ROWB, b0 = ssm ? 0 : g * 1152 * ROWB;
  const int j1 = j == 2 ? 0 : j + 1, j2 = j == 0 ? 2 : j - 1;
  const u32 st3 = 96 * ROWB - 2 * pstr, pb = WS_P + (rt * 15 + 3 * g) * 98304 + u * 4096;
  auto issueB = [&](int s, int jj, int i) { dma12((i & 1) ? rB1 : rB0, ldsW + (s & 3) * WSLOT, voffB, b0 + jj * bJ + (i & 1) * bPar + (i >> 1) * bI, 16 * ROWB, pstr, st3); };
  auto issueP = [&](int s, int jj, int i) { dma8lin(rWS, ldsT + (s & 3) * TSLOT + u * 4096, voffL, pb + jj * 98304 + i * 16384); };
  waitv<12>(); BAR();
#pragma unroll
  for (int k = 0; k < 4; k++) {
    const u32 dst = ssm ? (k < 3 ? AX_TAB + (u * 3 + k) * 1024 : AX_S) : AX_T + k * 1024;
    const u32 src = ssm ? (k < 3 ? WS_TAB + j * 6144 + (u * 3 + k) * 1024 : WS_LN + rt * 5120 + 4096) : WS_LN + rt * 5120 + k * 1024;
    dma_sc1(rWS, lds0 + (18 & 3) * WSLOT + dst, voffL, src);
  }
  waitv<4>();
  BAR(); issueB(19, j, 0); waitv<12>();
  BAR(); issueB(20, j, 1); waitv<12>();
  if (ssm) { const CarryPre k = carry_pre(lane, lds0); BAR(); if (u == 1) ssm_carry(p, k, rt, j, lane, lds0); WAITL(); }
  BAR();
  issueB(21, j, 2); waitv<12>();
  if (ssm) BAR();
  BAR(); issueB(22, j, 3); waitv<12>();
  BAR(); issueB(23, j, 4); issueP(23, j, 4); waitv<20>();
  BAR(); issueB(24, j, 5); issueP(24, j, 5);
#pragma nounroll
  for (int n = 0; n < 3; n++) {
    const int i = n ? 2 * n - 2 : 4;
    u32 w1 = b0 + j1 * bJ + (i >> 1) * bI, w2 = b0 + j2 * bJ + (i >> 1) * bI, w3 = w1 + bPar, w4 = w2 + bPar;
    u32 p1 = pb + j1 * 98304 + i * 16384, p2 = pb + j2 * 98304 + i * 16384, p3 = p1 + 16384, p4 = p2 + 16384;
    asm volatile("" : "+s"(w1), "+s"(w2), "+s"(w3), "+s"(w4), "+s"(p1), "+s"(p2), "+s"(p3), "+s"(p4));
    waitv<20>(); BAR(); dma12(rB0, ldsW + 1 * WSLOT, voffB, w1, 16 * ROWB, pstr, st3); dma8lin(rWS, ldsT + 1 * TSLOT + u * 4096, voffL, p1);
    waitv<20>(); BAR(); dma12(rB0, ldsW + 2 * WSLOT, voffB, w2, 16 * ROWB, pstr, st3); dma8lin(rWS, ldsT + 2 * TSLOT + u * 4096, voffL, p2);
    waitv<20>(); BAR(); dma12(rB1, ldsW + 3 * WSLOT, voffB, w3, 16 * ROWB, pstr, st3); dma8lin(rWS, ldsT + 3 * TSLOT + u * 4096, voffL, p3);
    waitv<20>(); BAR(); dma12(rB1, ldsW + 0 * WSLOT, voffB, w4, 16 * ROWB, pstr, st3); dma8lin(rWS, ldsT + 0 * TSLOT + u * 4096, voffL, p4);
  }
  waitv<20>();
  BAR();
  {
    const u32 l = lds0 + WSLOT;
    const u32 vo = (lane & 31) < 24 ? (rt * 128 + u * 64 + (g & 3) * 16 + (lane >> 5)) * ROWB + j * 384 + (lane & 31) * 16 : 0x7fffffffu;
#pragma unroll
    for (int k = 0; k < 8; k++) dma(rX, l + (u * 8 + k) * 1024, vo, k * 2 * ROWB);
    dma(mk(u ? p.dskip : p.b2, ROWB), l + 16384 + u * 1024, voffL + j * 384, 0);
  }
  waitv<9>(); BAR(); BAR(); waitv<0>();
  BAR();
}

DI void helper(const Params& p, int rt, int g, int j, int h, int lane, u32 lds0, u32 tag) {
  const bool ssm = g == 4;
  int pace = (h == 1 && !ssm) ? RAMP_0 : 0, pmin = (h == 1 && !ssm) ? RAMP_E : 0; u32 tnext = (u32)__builtin_amdgcn_s_memtime(); (void)pace; (void)pmin; (void)tnext;
  const int slice = 3 * g + j, q = lane >> 4, jl = lane & 15;
  const rsrc_t rWS = mk(p.ws, WS_END);
  char* const hsR = p.ws + WS_RSP + blockIdx.x * 4;
  const u32 hs0 = ld_flag(hsR);
  u32 hsn = 0;
  auto startup = [&]() {
    hsn = __builtin_amdgcn_readfirstlane(hs0) + 1u;
    if (h == 0) st_word(hsR - 1024, hsn); else if (lane < 2) st_flag(p.ws, lane ? FL_P + rt * 15 + slice : FL_O + (rt * 3 + j) * 5 + g, 0u);
    if (ssm) { st16((u32x4){0u, 0u, 0u, 0u}, rWS, (192 * j + 64 * h + lane) * 16, WS_EG + (15 - rt) * 9216); st16((u32x4){0u, 0u, 0u, 0u}, rWS, (192 * j + 128 + lane) * 16, WS_EG + (15 - rt) * 9216); }
  };
  __builtin_amdgcn_s_setprio(2);
  {
    const int e = (lane >> 2) & 3, z = 2 * (q & 1) + (e >> 1);
    f32x4 c = {0.f, 0.f, 0.f, 0.f};
    u32x4 sel[12];
#pragma unroll
    for (int n = 0; n < 12; n++) { const u32 o = jl == n ? ONES : 0u; sel[n] = (u32x4){o, o, o, o}; PIN(sel[n]); }
    PIN(c);
    auto pass = [&](int sl) {
      bf16x8 f[12];
      if (ssm) {
        const u32 a = lds0 + (rt & 1) * 2 * WSLOT + h * 12288 + jl * 128 + ((q ^ (jl >> 1)) * 16);
#pragma unroll
        for (int n = 0; n < 6; n++) { f[n] = rd128(a, sl * WSLOT + n * 2048); f[6 + n] = rd128(a ^ 64, sl * WSLOT + n * 2048); }
      } else {
#pragma unroll
        for (int n = 0; n < 12; n++) f[n] = rd_tr(lds0 + (rt & 1) * 2 * WSLOT + h * 12288 + (4 * q + e) * 128 + (((n & 3) ^ z) * 32) + (lane & 3) * 8, sl * WSLOT + (n >> 2) * 4096);
      }
#pragma unroll
      for (int n = 0; n < 12; n++) PIN(f[n]);
      asm volatile("s_nop 1");
      if (ssm) {
#pragma unroll
        for (int n = 0; n < 12; n++) MMAN(c, f[n], sel[6 * sl + n % 6]);
        MFENCE();
      } else {
#pragma unroll
        for (int n = 0; n < 12; n++) MMAN(c, f[n], sel[n]);
        MFENCE();
      }
    };
    const int odd = rt & 1;
    const u32 cso = jl < 12 ? ((ssm ? (jl >= 6 ? 192 - 96 : 0) + 96 * h : 192 * h) + 16 * jl + 4 * q) * 4 : 0x7fffffffu;
    LBAR();
    if (odd) { startup(); LBAR(); LBAR(); }
    if (rt < 9) pass(0);
    if (!odd) startup();
    LBAR();
    if (rt < 9) { pass(1); st16(__builtin_bit_cast(u32x4, c), rWS, cso, WS_CS + (slice * 16 + rt) * 1536); }
    LBAR();
    if (!odd) { LBAR(); LBAR(); }
  }
  u32 pv = ld_flag(hsR);
  LBAR();
  waitv<0>(); LBAR();
  const bool early = __all(pv == hsn);
  if (h == 0 && early) st_word(hsR + 1024, 1u);
  pv = ld_flag(hsR);
  LBAR(); LBAR();
  if (h == 0 && !early) { if (!__all(pv == hsn)) spin_all(hsR, hsn); st_word(hsR + 1024, 1u); }
  LBAR(); LBAR(); LBAR();
  const char* fGo = p.ws + WS_FLAGS + FL_GO * 128;
  pv = ld_flag(fGo);
  LBAR(); LBAR();
  if (!__all(pv == 1u)) spin_all(fGo, 1u);
  const int cc = 192 * h + 4 * (lane < 48 ? lane : lane - 48);
  f32x4 cp[9];
#pragma unroll
  for (int k = 0; k < 9; k++) cp[k] = __builtin_bit_cast(f32x4, __builtin_amdgcn_raw_buffer_load_b128(rWS, cc * 4, WS_CS + (slice * 16 + k) * 1536, 16));
  const u32x2 bb = *(const GAS u32x2*)(p.b1 + (ssm ? 0 : slice * 384) + cc);
  LBAR(); LBAR(); SB();
  f32x4 cs = ((cp[0] + cp[1]) + (cp[2] + cp[3])) + ((cp[4] + cp[5]) + (cp[6] + cp[7])) + cp[8];
  u32x4 aw[4];
  PIN(cs);
  auto auxw = [&](int k) { float c1, c2, c3; split3(cs[k], c1, c2, c3); const float b = (k & 1) ? bfhi(bb[k >> 1]) : bflo(bb[k >> 1]);
    aw[k] = (u32x4){cvtpk(c1, c2), cvtpk(c1, c2), cvtpk(c3, c1), cvtpk(b, b)}; PIN(aw[k]); };
  const u32 ax = lds0 + (18 & 3) * WSLOT;
  if (!ssm) auxw(0);
  LBAR(); SB();
  if (!ssm) { auxw(1); auxw(2); }
  LBAR(); SB();
  if (ssm) *(LAS f32x4*)(size_t)(ax + AX_W + cc * 4) = cs;
  else {
    auxw(3);
#pragma unroll
    for (int k = 0; k < 4; k++) wr128(ax + AX_W + cc * 4, k * 1600, aw[k]);
  }
  WAITL(); LBAR();
  pace = pmin = 0;
  if (ssm) { const CarryPre k = carry_pre(64 + 64 * h + lane, lds0); LBAR(); ssm_carry(p, k, rt, j, 64 + 64 * h + lane, lds0); WAITL(); LBAR(); }
  LBAR();
  char* const fMine = p.ws + WS_FLAGS + (FL_P + rt * 15 + slice) * 128;
  const char* fP = p.ws + WS_FLAGS + (FL_P + rt * 15 + 3 * g + ((lane & 1) ? (j == 2 ? 0 : j + 1) : (j == 0 ? 2 : j - 1))) * 128;
  LBAR(); if (h == 1 && !ssm) st_word(fMine, 2u);
  LBAR(); if (h == 1 && ssm) st_word(fMine, 1u);
#pragma unroll
  for (u32 lv = 1; lv < 4; lv++) {
    if (lv == 1) pv = ld_flag(fP);
    LBAR(); if (h == 0 && !__all(pv >= lv)) spin_ge(fP, lv);
    LBAR(); if (h == 1 && lv == 1) st_word(fMine, 2u);
    LBAR(); pv = ld_flag(fP);
    LBAR(); if (h == 1 && lv == 1) st_word(fMine, 3u);
  }
  LBAR(); LBAR(); LBAR(); LBAR();
  LBAR();
  if (h == 1) st_flag(p.ws, FL_O + (rt * 3 + j) * 5 + g, tag);
  else if (!ssm) {
    const int k = lane & 3;
    const char* fa = p.ws + WS_FLAGS + (FL_O + (rt * 3 + j) * 5 + (k < g ? k : k + 1)) * 128;
    u32 v = 0;
#pragma nounroll
    for (u32 i = 0; i < SPIN_CAP; i++) { v = ld_flag(fa); if (__all(v == tag || k == 3)) break; }
    const bool yok = __all(v == tag);
    *(LAS u32*)(size_t)lds0 = yok; WAITL(); BAR();
    if (!yok) { spin_all(fa, tag); BAR(); }
  }
}

template <bool SSM>
DI void phaseB(const Params& p, int rt, int g, int j, int w, int lane, u32 lds0, u32 tag) {
  asm volatile("" : "+v"(lane));
  const int rg = w >> 2, cg = w & 3, q = lane >> 4, e = (lane >> 2) & 3, pp = lane & 3, jl = lane & 15;
  const int z = 2 * (q & 1) + (e >> 1);
  u32 wlo[3], whi[3], tb[2];
#pragma unroll
  for (int n = 0; n < 3; n++) {
    const int t = 3 * cg + n;
    wlo[n] = SSM ? lds0 + (16 * t + jl) * 128 + ((q ^ (jl >> 1)) * 16) : lds0 + (t >> 2) * 4096 + (4 * q + e) * 128 + (((t & 3) ^ z) * 32) + pp * 8;
    whi[n] = SSM ? (wlo[n] ^ 64) : wlo[n] + 2 * WSLOT;
  }
  tb[0] = SSM ? lds0 + TOFF + rg * 8192 + (q >> 1) * 2048 + (pp >> 1) * 1024 + (((8 * (q & 1) + e) * 16) ^ ((pp >> 1) * 64)) + 8 * (pp & 1) : lds0 + TOFF + rg * 4096 + lane * 16;
  tb[1] = tb[0] ^ 64;
  float mean = 0.f, rstd = 0.f;
  if constexpr (!SSM) { const u32 sa = lds0 + (18 & 3) * WSLOT + AX_S + (64 * rg + 16 * g + jl) * 4; mean = rdf(sa, 0); rstd = rdf(sa, 512); }
  f32x4 ob[3][4];
  auto RW = [&](int sl, int kb, int n, int part) {
    if constexpr (SSM) return rd128((kb ? whi[n] : wlo[n]) + (sl >> 1) * 2 * WSLOT, (sl & 1) * WSLOT);
    else return rd_tr1(sl < 2 ? wlo[n] : whi[n], (sl & 1) * WSLOT + kb * 12288 + part * 2048);
  };
  auto RT = [&](int sl, int kb, int T, int part) {
    if constexpr (SSM) return rd_tr1(tb[part], sl * TSLOT + kb * 4096 + T * 256);
    else return rd128(tb[0], sl * TSLOT + kb * 8192 + T * 1024);
  };
  bf16x8 wf[3], tp[2];
  auto step = [&](auto slc, auto&& mid) {
    constexpr int SL = decltype(slc)::value & 3, NS = (SL + 1) & 3;
    half<SSM ? 1 : 0, (decltype(slc)::value > 3)>(ob, wf, tp, [&](int n, int part) { return RW(SL, 1, n, part); }, [&](int T, int part) { return RT(SL, 0, T, part); }, [&](int T, int part) { return RT(SL, 1, T, part); }, mid);
    half<SSM ? 1 : 0>(ob, wf, tp, [&](int n, int part) { return RW(NS, 0, n, part); }, [&](int T, int part) { return RT(SL, 1, T, part); }, [&](int T, int part) { return RT(NS, 0, T, part); }, [](int) {});
  };
  using I0 = std::integral_constant<int, 0>; using I1 = std::integral_constant<int, 1>; using I2 = std::integral_constant<int, 2>; using I3 = std::integral_constant<int, 3>;
  if constexpr (!SSM) waitv<4>();
  __builtin_amdgcn_s_waitcnt(0xC07F); WAITL(); BAR();
#pragma unroll
  for (int n = 0; n < 3; n++) { if constexpr (SSM) wf[n] = RW(3, 0, n, 0); else wf[n] = cat(RW(3, 0, n, 0), RW(3, 0, n, 1)); }
#pragma unroll
  for (int T = 0; T < 2; T++) { if constexpr (SSM) tp[T] = cat(RT(3, 0, T, 0), RT(3, 0, T, 1)); else tp[T] = RT(3, 0, T, 0); }
  auto barm = [](int i) { if (i == 11) BAR(); };
  step(std::integral_constant<int, 7>{}, [](int) {});
  step(I0{}, [&](int i) { if (i == 3) { if constexpr (SSM) waitv<8>(); BAR(); } });
#pragma nounroll
  for (int it = 0; it < 4; it++) { step(I1{}, barm); step(I2{}, [&](int i) { if (i == 11) { waitv<4>(); BAR(); } }); step(I3{}, barm); step(I0{}, [&](int i) { if (i == 11) { waitv<0>(); BAR(); } }); }
  MFENCE();
  const rsrc_t rWS = mk(p.ws, WS_END);
  const u32 xb = WS_XB + (rt * 3 + j) * 32 * XBW + w * XBW;
  f32x4 mine[3];
#pragma unroll
  for (int T = 0; T < 4; T++) {
    if (SSM || T != g) {
      const u32 o = xb + T * 8 * XBW + g * 1536;
      const u32x4 a = {cvtpk(ob[0][T][0], ob[0][T][1]), cvtpk(ob[0][T][2], ob[0][T][3]), cvtpk(ob[1][T][0], ob[1][T][1]), cvtpk(ob[1][T][2], ob[1][T][3])};
      const u32 b0 = cvtpk(ob[2][T][0], ob[2][T][1]), b1 = cvtpk(ob[2][T][2], ob[2][T][3]);
      const auto s0 = __builtin_amdgcn_permlane32_swap(b0, b0, false, false), s1 = __builtin_amdgcn_permlane32_swap(b1, b1, false, false);
      st16(a, rWS, lane * 16, o);
      if (lane < 32) st16((u32x4){b0, b1, s0[1], s1[1]}, rWS, lane * 16, o + 1024);
    } else {
#pragma unroll
      for (int n = 0; n < 3; n++) mine[n] = ob[n][T];
    }
  }
  if constexpr (SSM) {
    waitv<0>(); BAR();
    STAMP(7);
  } else {
    waitv<0>(); BAR();
    const int tok = rt * 128 + 64 * rg + 16 * g + jl, col = 192 * j + 48 * cg + 4 * q;
    u32x2 xv[3], bv[3], dv[3];
#pragma unroll
    for (int n = 0; n < 3; n++) {
      const u32 cb = lds0 + WSLOT + (48 * cg + 4 * q + 16 * n) * 2;
      xv[n] = *(LAS u32x2*)(size_t)(cb + (16 * rg + jl) * 512); bv[n] = *(LAS u32x2*)(size_t)(cb + 16384); dv[n] = *(LAS u32x2*)(size_t)(cb + 17408);
    }
    const u32 o = xb + g * 8 * XBW;
    u32x4 a[3]; u32x2 b[3];
    BAR();
#pragma unroll
    for (int k = 0; k < 3; k++) {
      const int src = k < g ? k : k + 1;
      a[k] = __builtin_amdgcn_raw_buffer_load_b128(rWS, lane * 16, o + src * 1536, 16);
      b[k] = __builtin_amdgcn_raw_buffer_load_b64(rWS, (lane & 31) * 16 + (lane >> 5) * 8, o + src * 1536 + 1024, 16);
    }
    const bool yok = __builtin_amdgcn_readfirstlane(*(LAS u32*)(size_t)lds0);
    u32x4 ya; u32x2 yb;
    auto ldy = [&]() { ya = __builtin_amdgcn_raw_buffer_load_b128(rWS, lane * 16, o + 4 * 1536, 16); yb = __builtin_amdgcn_raw_buffer_load_b64(rWS, (lane & 31) * 16 + (lane >> 5) * 8, o + 4 * 1536 + 1024, 16); };
    if (yok) ldy();
    f32x4 gate[3], base[3];
#pragma unroll
    for (int n = 0; n < 3; n++) {
#pragma unroll
      for (int c = 0; c < 4; c++) {
        float G = mine[n][c] + ((c & 1) ? bfhi(bv[n][c >> 1]) : bflo(bv[n][c >> 1]));
#pragma unroll
        for (int k = 0; k < 3; k++) { const u32 v = n < 2 ? a[k][2 * n + (c >> 1)] : b[k][c >> 1]; G += (c & 1) ? bfhi(v) : bflo(v); }
        const float xx = (c & 1) ? bfhi(xv[n][c >> 1]) : bflo(xv[n][c >> 1]);
        const float dd = (c & 1) ? bfhi(dv[n][c >> 1]) : bflo(dv[n][c >> 1]);
        gate[n][c] = G * __builtin_amdgcn_rcpf(1.f + __builtin_amdgcn_exp2f(-1.4426950408889634f * G));
        base[n][c] = xx + (xx - mean) * rstd * dd * gate[n][c];
      }
    }
#pragma unroll
    for (int n = 0; n < 3; n++) { PIN(gate[n]); PIN(base[n]); }
    if (!yok) { BAR(); ldy(); }
#pragma unroll
    for (int n = 0; n < 3; n++) {
      const u32 y0 = n < 2 ? ya[2 * n] : yb[0], y1 = n < 2 ? ya[2 * n + 1] : yb[1];
      *(GAS f32x4*)(p.out + tok * D + col + 16 * n) = base[n] + (f32x4){bflo(y0), bfhi(y0), bflo(y1), bfhi(y1)} * gate[n];
    }
    STAMP(7);
  }
}

DI void ffn_compute(int rt, int g, int j, int w, int lane, u32 lds0, u32 tag) {
  const int rg = w >> 2, cg = w & 3, q = lane >> 4, e = (lane >> 2) & 3, pp = lane & 3, jl = lane & 15;
  const int z = 2 * (q & 1) + (e >> 1);
  u32 wc[4];
#pragma unroll
  for (int n = 0; n < 4; n++) { const int t = 6 * cg + n; wc[n] = lds0 + (t >> 2) * 4096 + (4 * q + e) * 128 + (((t & 3) ^ z) * 32) + pp * 8; }
  const u32 xbase = lds0 + TOFF + (64 * rg + jl) * 128 + ((q ^ (jl >> 1)) * 16);
  f32x4 acc[6][4];
#pragma unroll
  for (int n = 0; n < 6; n++)
#pragma unroll
    for (int T = 0; T < 4; T++) { acc[n][T] = (f32x4){0.f, 0.f, 0.f, 0.f}; asm volatile("" : "+v"(acc[n][T])); }
  bf16x8 wp[2], t[4];
  BAR();
  wp[0] = rd_tr(wc[0], 0); wp[1] = rd_tr(wc[1], 0);
#pragma unroll
  for (int T = 0; T < 4; T++) t[T] = rd128(xbase, T * 2048);
  u32 xs = 0, xn = 0, n0 = 0, n1 = 0; int dw = 2 * WSLOT;
#pragma nounroll
  for (int it = 0; it < 9; it++) {
    stepA<0>(acc, wp, t, wc, n0, n1, xn, [&]() { xn = (xbase ^ 64) + xs; PIN(xn); xs = (xs + TSLOT) & (4 * TSLOT - 1); });
    stepA<1>(acc, wp, t, wc, n0, n1, xn, [&]() { xn = xbase + xs; n0 = wc[0] + dw; n1 = wc[1] + dw; wc[2] += dw; wc[3] += dw; PIN(xn); PIN(n0); PIN(n1); PIN(wc[2]); PIN(wc[3]); });
    wc[0] = n0; wc[1] = n1; dw = -dw;
  }
  const Params p = reload_params();
  const u32 ax = lds0 + (18 & 3) * WSLOT;
  float k1[4], k2[4], sg[4];
#pragma unroll
  for (int T = 0; T < 4; T++) {
    u32x4 v = __builtin_bit_cast(u32x4, rd128(ax + AX_T + (64 * rg + jl) * 16, T * 256));
    if (q != 0) v = (u32x4){0u, 0u, 0u, 0u};
    t[T] = __builtin_bit_cast(bf16x8, v);
    const float r = rdf(ax + AX_S + 512 + (64 * rg + jl) * 4, T * 64);
    sg[T] = rdf(ax + AX_S + 1024 + (64 * rg + jl) * 4, T * 64);
    k1[T] = -2.3022081985f * r; k2[T] = -0.10294324f * r * r * r;
  }
  {
    bf16x8 a[6];
#pragma unroll
    for (int n = 0; n < 6; n++) a[n] = rd128(ax + AX_W + ((jl & 3) * 100 + 24 * cg + (jl >> 2)) * 16, n * 64);
    asm volatile("s_nop 1" : "+v"(t[0]), "+v"(t[1]), "+v"(t[2]), "+v"(t[3]));
#pragma unroll
    for (int n = 0; n < 6; n++)
#pragma unroll
      for (int T = 0; T < 4; T++) MMA(acc[n][T], a[n], t[T]);
    MFENCE();
  }
  const rsrc_t rWS = mk(p.ws, WS_END);
  const u32 pg = WS_P + (rt * 15 + 3 * g + j) * 98304 + (cg >> 1) * 16384 + (cg & 1) * 8192 + rg * 4096;
  const u32 pl = lds0 + TOFF + (cg & 1) * 8192 + rg * 4096 + lane * 16, pl0 = pl + ((19 + (cg >> 1)) & 3) * TSLOT, pl1 = pl + ((21 + (cg >> 1)) & 3) * TSLOT;
#pragma unroll
  for (int bb = 0; bb < 3; bb++) {
    const int b = (bb + 2) % 3;
#pragma unroll
    for (int T = 0; T < 4; T++) {
      const u32x4 P = gelu_frag(acc[2 * b][T], acc[2 * b + 1][T], k1[T], k2[T], sg[T]);
      st16(P, rWS, lane * 16, pg + b * 32768 + T * 1024);
      if (b < 2) wr128(b ? pl1 : pl0, T * 1024, P);
      SB();
    }
    if (bb == 0) BAR();
  }
  phaseB<false>(p, rt, g, j, w, lane, lds0, tag);
}

DI void ssm_compute(int rt, int j, int w, int lane, u32 lds0, u32 tag) {
  const int rg = w >> 2, cg = w & 3; int q = lane >> 4, jl = lane & 15;
  u32 wb = lds0 + (16 * cg + jl) * 128 + ((q ^ (jl >> 1)) * 16);
  const u32 xb0 = lds0 + TOFF + (64 * rg + jl) * 128 + ((q ^ (jl >> 1)) * 16);
  f32x4 ar[3][4], ai[3][4];
#pragma unroll
  for (int n = 0; n < 3; n++)
#pragma unroll
    for (int T = 0; T < 4; T++) { ar[n][T] = (f32x4){0.f, 0.f, 0.f, 0.f}; ai[n][T] = (f32x4){0.f, 0.f, 0.f, 0.f}; asm volatile("" : "+v"(ar[n][T]), "+v"(ai[n][T])); }
  bf16x8 wf[3], tp[2];
  auto barm = [](int i) { if (i == 11) BAR(); };
  __builtin_amdgcn_s_waitcnt(0xC07F);
  BAR();
#pragma unroll
  for (int n = 0; n < 3; n++) wf[n] = rd128(wb, n * 8192);
  tp[0] = rd128(xb0, 0); tp[1] = rd128(xb0, 2048);
  u32 xs = 0, x0 = xb0, x1 = xb0 ^ 64, wb1 = wb ^ 64; int dw = 2 * WSLOT;
#pragma nounroll
  for (int it = 0; it < 9; it++) {
    half<2>(ar, wf, tp, [&](int n, int) { return rd128(wb1, n * 8192); }, [&](int T, int) { return rd128(x0, T * 2048); }, [&](int T, int) { return rd128(x1, T * 2048); }, barm);
    half<2>(ar, wf, tp, [&](int n, int) { return rd128(wb, WSLOT + n * 8192); }, [&](int T, int) { return rd128(x1, T * 2048); }, [&](int T, int) { return rd128(x0, T * 2048); },
            [&](int i) { if (i == 10) { wb += dw; PIN(wb); xs = (xs + TSLOT) & (4 * TSLOT - 1); } });
    half<2>(ai, wf, tp, [&](int n, int) { return rd128(wb1, WSLOT + n * 8192); }, [&](int T, int) { return rd128(x0, T * 2048); }, [&](int T, int) { return rd128(x1, T * 2048); },
            [&](int i) { if (i == 7) { wb1 += dw; x0 = xb0 + xs; PIN(wb1); PIN(x0); dw = -dw; } if (i == 11) BAR(); });
    half<2>(ai, wf, tp, [&](int n, int) { return rd128(wb, n * 8192); }, [&](int T, int) { return rd128(x1, T * 2048); }, [&](int T, int) { return rd128(x0, T * 2048); },
            [&](int i) { if (i == 4) { x1 = x0 ^ 64; PIN(x1); } });
  }
  const Params p = reload_params();
  asm volatile("" : "+v"(lane));
  q = lane >> 4; jl = lane & 15;
  const u32 ax = lds0 + (18 & 3) * WSLOT;
  MFENCE();
  f32x2 lc[3];
  const u32 tb = ax + AX_TAB + (16 * cg + jl) * 32;
  const u32 scr = lds0 + TOFF + (22 & 3) * TSLOT + ((4 * rg + q) * 192 + 16 * cg + jl) * 8;
  f32x2 e0[3]; float csr[3], csi[3];
#pragma unroll
  for (int b = 0; b < 3; b++) { csr[b] = rdf(ax + AX_W + (16 * cg + jl) * 4, b * 256); csi[b] = rdf(ax + AX_W + (16 * cg + jl) * 4, 768 + b * 256); }
#pragma unroll
  for (int T = 0; T < 4; T++) {
    const f32x4 r = rdf4(ax + AX_S + 512 + (64 * rg + 4 * q) * 4, T * 64), nm = rdf4(ax + AX_S + (64 * rg + 4 * q) * 4, T * 64);
#pragma unroll
    for (int b = 0; b < 3; b++) { ar[b][T] = ar[b][T] * r + nm * csr[b]; ai[b][T] = ai[b][T] * r + nm * csi[b]; }
  }
  f32x2 l8[3];
#pragma unroll
  for (int b = 0; b < 3; b++) {
    lc[b] = *(LAS f32x2*)(size_t)(tb + b * 2048);
    l8[b] = *(LAS f32x2*)(size_t)(tb + b * 2048 + 16);
    f32x2 hr = {ar[b][0][0], ar[b][0][1]}, hi = {ai[b][0][0], ai[b][0][1]};
#pragma unroll
    for (int k = 1; k < 8; k++) {
      const f32x2 ur = {ar[b][k >> 1][(k & 1) * 2], ar[b][k >> 1][(k & 1) * 2 + 1]}, ui = {ai[b][k >> 1][(k & 1) * 2], ai[b][k >> 1][(k & 1) * 2 + 1]};
      const f32x2 nr = lc[b][0] * hr + (ur - lc[b][1] * hi), ni = lc[b][0] * hi + (ui + lc[b][1] * hr);
      hr = nr; hi = ni;
    }
    e0[b] = (f32x2){hr.x, hi.x};
    *(LAS f32x2*)(size_t)(scr + b * 512) = (f32x2){l8[b].x * hr.x - l8[b].y * hi.x + hr.y, l8[b].x * hi.x + l8[b].y * hr.x + hi.y};
  }
  WAITL(); BAR(); SB();
#pragma unroll
  for (int b = 0; b < 3; b++) {
    const f32x2 cf = *(LAS f32x2*)(size_t)(tb + b * 2048 + 8); const float c2r = cf.x, c2i = cf.y;
#pragma unroll
    for (int T = 0; T < 4; T++) {
      const f32x4 nr = c2r * ar[b][T] - c2i * ai[b][T], ni = -c2r * ai[b][T] - c2i * ar[b][T];
      ar[b][T] = nr; ai[b][T] = ni;
      asm volatile("" : "+v"(ar[b][T]), "+v"(ai[b][T]));
    }
  }
  BAR();
  const rsrc_t rWS = mk(p.ws, WS_END);
  const u32 io0 = w * 2048 + lane * 16, io1 = w * 2048 + 1024 + ((lane * 16) ^ 64);
  const u32 pg = WS_P + (rt * 15 + 12 + j) * 98304;
  f32x2 cins[3];
#pragma unroll
  for (int b = 0; b < 3; b++) { cins[b] = *(LAS f32x2*)(size_t)(scr + b * 512); l8[b] = *(LAS f32x2*)(size_t)(tb + b * 2048 + 16); }
#pragma unroll
  for (int bb = 0; bb < 3; bb++) {
    const int b = (bb + 2) % 3;
    u32x4 H[2][2];
    const f32x2 cin = cins[b];
    const f32x2 sr = {cin.x, l8[b].x * cin.x - l8[b].y * cin.y + e0[b].x}, si = {cin.y, l8[b].x * cin.y + l8[b].y * cin.x + e0[b].y};
    const f32x2 cf = *(LAS f32x2*)(size_t)(tb + b * 2048 + 8); const float c2r = cf.x, c2i = cf.y;
    f32x2 hr = c2r * sr - c2i * si, hi = -c2r * si - c2i * sr;
#pragma unroll
    for (int T = 0; T < 4; T++) {
      f32x2 pr[2], pi[2];
#pragma unroll
      for (int k = 0; k < 2; k++) {
        const f32x2 ur = {ar[b][T][2 * k], ar[b][T][2 * k + 1]}, ui = {ai[b][T][2 * k], ai[b][T][2 * k + 1]};
        const f32x2 nr = lc[b][0] * hr + (ur + lc[b][1] * hi), ni = lc[b][0] * hi + (ui - lc[b][1] * hr);
        hr = nr; hi = ni; pr[k] = nr; pi[k] = ni;
      }
      H[0][0][T] = cvtpk(pr[0].x, pr[1].x); H[0][1][T] = cvtpk(pr[0].y, pr[1].y);
      H[1][0][T] = cvtpk(pi[0].x, pi[1].x); H[1][1][T] = cvtpk(pi[0].y, pi[1].y);
    }
    if (bb == 1) BAR();
#pragma unroll
    for (int k = 0; k < 4; k++) {
      st16(H[k >> 1][k & 1], rWS, (k & 1) ? io1 : io0, pg + (2 * b + (k >> 1)) * 16384);
      if (b < 2) wr128(lds0 + TOFF + ((k & 1) ? io1 : io0), ((19 + 2 * b + (k >> 1)) & 3) * TSLOT, H[k >> 1][k & 1]);
    }
    SB();
  }
  phaseB<true>(p, rt, 4, j, w, lane, lds0, tag);
}

DI void spare(const Params& p, int sid, u32 lds0, u32 tag) {
  const int tid = threadIdx.x;
  const rsrc_t rWS = mk(p.ws, WS_END);
  const int w = __builtin_amdgcn_readfirstlane(tid >> 6), lane = tid & 63;
  if (sid == 15 && w == 11) { st_flag(p.ws, FL_GO, 0u); st16((u32x4){0u, 0u, 0u, 0u}, rWS, lane * 16, WS_ACK); waitv<0>(); hs_leader(p.ws, lane, lds0, STAMPPTR); return; }
  u32 hsn = 0;
  if (w == 11) { hsn = ld_flag(p.ws + WS_RSP + blockIdx.x * 4) + 1u; st_word(p.ws + WS_REQ + blockIdx.x * 4, hsn); spin_all(p.ws + WS_RSP + blockIdx.x * 4, hsn); }
  else if (w < 8) {
    const int row = tid >> 2, part = tid & 3;
    const GAS u32x4* src = (const GAS u32x4*)(p.x + (sid * 128 + row) * D) + part;
    u32x4 v[18];
#pragma unroll
    for (int i = 0; i < 18; i++) v[i] = src[4 * i];
    float s = 0.f, s2 = 0.f;
#pragma unroll
    for (int i = 0; i < 18; i++) { s = dot2(v[i][0], ONES, s); s2 = dot2(v[i][1], ONES, s2); s = dot2(v[i][2], ONES, s); s2 = dot2(v[i][3], ONES, s2); }
    s += s2; s += dppx<0xb1>(s); s += dppx<0x4e>(s);
    const float mean = s * (1.f / D);
    f32x2 dv = {0.f, 0.f};
#pragma unroll
    for (int i = 0; i < 72; i++) { const u32 wd = v[i >> 2][i & 3]; const f32x2 d = (f32x2){bflo(wd), bfhi(wd)} - mean; dv += d * d; }
    s = dv.x + dv.y; s += dppx<0xb1>(s); s += dppx<0x4e>(s);
    const float ve = s * (1.f / D) + 1e-5f, rstd = __builtin_amdgcn_rsqf(ve), sig = ve * rstd;
    float m1, m2, m3; split3(-mean, m1, m2, m3);
    const float g1 = rbf(sig), g2 = sig - g1;
    const int ir = (row & 64) + 16 * ((row >> 1) & 3) + 4 * ((row >> 4) & 3) + 2 * (row & 1) + ((row >> 3) & 1);
    char* ln = p.ws + WS_LN + sid * 5120;
    if (part == 0) st16((u32x4){cvtpk(m1, m1), cvtpk(m2, m2), cvtpk(m1, m3), cvtpk(g1, g2)}, rWS, row * 16, WS_LN + sid * 5120);
    else st_word(ln + 2048 + (part - 1) * 512 + row * 4, __float_as_uint(part == 1 ? mean : (part == 2 ? rstd : sig)));
    if (part >= 2) st_word(ln + 4096 + (part - 2) * 512 + ir * 4, __float_as_uint(part == 2 ? -mean * rstd : rstd));
  } else if (w == 9 && sid < 9) {
    const int c = sid * 64 + lane;
    const float lr = bf2f(p.lre[c]), li = bf2f(p.lim[c]);
    const float dt = rbf(expf(bf2f(p.logdt[c])));
    const float ex = expf(dt * lr), an = dt * li;
    const float br = ex * cosf(an), bi = ex * sinf(an);
    const float nr = br - 1.f, den = 1.f / (lr * lr + li * li);
    const float cr = (nr * lr + bi * li) * den, ci = (bi * lr - nr * li) * den;
    float ar = br, ai = bi, r8 = 0.f, i8 = 0.f;
#pragma unroll
    for (int k = 0; k < 7; k++) { const float t = ar * ar - ai * ai; ai = 2.f * ar * ai; ar = t; if (k == 2) { r8 = ar; i8 = ai; } }
    st16(__builtin_bit_cast(u32x4, (f32x4){br, bi, 2.f * cr, 2.f * ci}), rWS, c * 32, WS_TAB);
    st16(__builtin_bit_cast(u32x4, (f32x4){r8, i8, ar, ai}), rWS, c * 32 + 16, WS_TAB);
  }
  waitv<0>(); BAR();
  if (sid == 15) return;
  if (w == 11) st_word(p.ws + WS_ACK + blockIdx.x * 4, 1u);
  {
    const int js = sid % 3;
    const char* base = (const char*)(sid < 12 ? p.w2 : p.cre); const long dim = (const char*)p.cim - (const char*)p.cre;
#pragma unroll
    for (int it = 0; it < 5; it++) {
      const int L = min(it * 768 + tid, 3455), t = L / 192, e = L - t * 192;
      const int n = t < 6 ? t : (t - 6) >> 1, i = t < 6 ? t : (n < 2 ? n + 4 : n - 2);
      int jc = t < 6 ? js : js + 1 + ((t - 6) & 1); if (jc >= 3) jc -= 3;
      const int r = e / 3;
      const long off = sid < 12 ? (long)(sid * 384 + 192 * (i & 1) + 96 * (r >> 5) + 32 * (i >> 1) + (r & 31)) * ROWB + jc * 384 + (e - 3 * r) * 128
                                : (long)(192 * jc + e) * ROWB + js * 384 + (i >> 1) * 128 + ((i & 1) ? dim : 0);
      asm volatile("s_mov_b32 m0, %0\n\ts_nop 0\n\tglobal_load_lds_dword %1, off" :: "s"(lds0 + 8192), "v"(base + off) : "memory");
    }
  }
}

__global__ __launch_bounds__(768, 3) void fused_kernel(Params p) {
  extern __shared__ __attribute__((aligned(16))) char smem[];
  u64x4 kv; asm volatile("s_load_dwordx8 %0, %1, 0x0" : "=s"(kv) : "s"(__builtin_amdgcn_kernarg_segment_ptr())); SB();
  const u32 lds0 = (u32)(size_t)(LAS char*)smem;
  const int b = blockIdx.x, xcd = b & 7, idx = b >> 3;
  const u32 tag = 1u;
  if (b < 32 && (threadIdx.x >> 7) == 1) {
    u64 pc0; asm volatile("s_getpc_b64 %0" : "=s"(pc0));
    const u32 ln = min((b >> 3) * 128 + (threadIdx.x & 127), (u32)CODE_LINES);
    asm volatile("s_mov_b32 m0, %0\n\ts_nop 0\n\tglobal_load_lds_dword %1, %2" :: "s"(lds0 + TOFF + 3 * TSLOT), "v"(ln * 128), "s"(pc0) : "memory");
  }
  STAMP(0);
  const int tri = idx / 3, j = idx - 3 * tri;
  const int g = tri < 2 ? 4 : xcd >> 1, rt = 8 * (xcd & 1) + (tri < 2 ? (xcd & 6) + tri : tri - 2);
  const int lane = threadIdx.x & 63, w = __builtin_amdgcn_readfirstlane(threadIdx.x >> 6);
  const int sid = xcd * 2 + idx - 30;
  if (__builtin_expect(idx < 30 && w < 2, 1)) { loader(kv, rt, g, j, w, lane, lds0, tag); return; }
  asm volatile("s_waitcnt lgkmcnt(0)" :: "s"(kv));
  if (idx < 30 && w >= 4) {
    if (g < 4) ffn_compute(rt, g, j, w - 4, lane, lds0, tag); else ssm_compute(rt, j, w - 4, lane, lds0, tag);
    return;
  }
  const Params q = reload_params();
  if (idx < 30) helper(q, rt, g, j, w - 2, lane, lds0, tag); else { spare(q, sid, lds0, tag); STAMP(7); }
}

extern "C" void kernel_launch(void* const* d_in, const int* in_sizes, int n_in, void* d_out, int out_size, void* d_ws, size_t ws_size, hipStream_t stream) {
  Params p;
  p.x = (const u16*)d_in[0]; p.lre = (const u16*)d_in[1]; p.lim = (const u16*)d_in[2]; p.bre = (const u16*)d_in[3]; p.bim = (const u16*)d_in[4];
  p.cre = (const u16*)d_in[5]; p.cim = (const u16*)d_in[6]; p.logdt = (const u16*)d_in[7]; p.dskip = (const u16*)d_in[8];
  p.w1 = (const u16*)d_in[10]; p.b1 = (const u16*)d_in[11]; p.w2 = (const u16*)d_in[12]; p.b2 = (const u16*)d_in[13];
  p.out = (float*)d_out; p.ws = (char*)d_ws + ((ws_size - WS_END) & ~(size_t)0x1fffff);
  (void)hipFuncSetAttribute((const void*)fused_kernel, hipFuncAttributeMaxDynamicSharedMemorySize, LDS_BYTES);
  fused_kernel<<<256, 768, LDS_BYTES, stream>>>(p);
}
```
